```python
import torch

def _softmax(x, axis=-1):
    return torch.softmax(x, dim=axis)
def _gelu(x):
    return torch.nn.functional.gelu(x, approximate="tanh")
def _silu(x):
    return torch.nn.functional.silu(x)
def _sigmoid(x):
    return torch.sigmoid(x)
def _layernorm(x, axis=-1, eps=1e-5):
    m = x.mean(dim=axis, keepdim=True)
    v = x.var(dim=axis, keepdim=True, unbiased=False)
    return (x - m) / torch.sqrt(v + eps)
def _scatter_add(tgt, idx, src, axis=0):
    return tgt.clone().index_add_(axis, idx, src)
def _segment_sum(src, seg, n):
    out = torch.zeros((n,) + src.shape[1:], dtype=src.dtype)
    return out.index_add_(0, seg, src)
def _pad(x, pw):
    flat = tuple(v for pair in reversed(pw) for v in pair)
    return torch.nn.functional.pad(x, flat)

kernel_name = 'concat_heads__gqa_block_cached__gqa_block_cached__near_ridge'

def setup_inputs():

    _w = (lambda a: (lambda t: t.to(torch.bfloat16) if t.is_floating_point() else t)(torch.as_tensor(a)))
    def _A_setup():
        B, Sq, D = 16, 128, 448
        Hq, Hkv, Sk, Dh = 16, 8, 512, 256
        assert Hq % Hkv == 0, (Hq, Hkv)
        return {
            "x": _w((lambda *s: torch.randn(*s, dtype=torch.bfloat16) * 0.8)(B, Sq, D)),
            "k_cache": _w((lambda *s: torch.randn(*s, dtype=torch.bfloat16) * 0.8)(B, Hkv, Sk, Dh)),
            "v_cache": _w((lambda *s: torch.randn(*s, dtype=torch.bfloat16) * 0.05)(B, Hkv, Sk, Dh)),
            "wq": _w((lambda *s: torch.randn(*s, dtype=torch.bfloat16) * 0.8)(D, Hq * Dh) * (D ** -0.5)),
            "wo": _w((lambda *s: torch.randn(*s, dtype=torch.bfloat16) * 0.05)(Hq * Dh, D) * ((Hq * Dh) ** -0.5)),
        }
    _A_in = _A_setup()

    def _B_setup():
        B, Sq, D = 16, 128, 448
        Hq, Hkv, Sk, Dh = 16, 8, 512, 256
        assert Hq % Hkv == 0, (Hq, Hkv)
        return {
            "x": _w((lambda *s: torch.randn(*s, dtype=torch.bfloat16) * 0.8)(B, Sq, D)),
            "k_cache": _w((lambda *s: torch.randn(*s, dtype=torch.bfloat16) * 0.8)(B, Hkv, Sk, Dh)),
            "v_cache": _w((lambda *s: torch.randn(*s, dtype=torch.bfloat16) * 0.05)(B, Hkv, Sk, Dh)),
            "wq": _w((lambda *s: torch.randn(*s, dtype=torch.bfloat16) * 0.8)(D, Hq * Dh) * (D ** -0.5)),
            "wo": _w((lambda *s: torch.randn(*s, dtype=torch.bfloat16) * 0.05)(Hq * Dh, D) * ((Hq * Dh) ** -0.5)),
        }
    _B_in = _B_setup()

    return {
        "A_x": _A_in["x"],
        "A_k_cache": _A_in["k_cache"],
        "A_v_cache": _A_in["v_cache"],
        "A_wq": _A_in["wq"],
        "A_wo": _A_in["wo"],
        "B_x": _B_in["x"],
        "B_k_cache": _B_in["k_cache"],
        "B_v_cache": _B_in["v_cache"],
        "B_wq": _B_in["wq"],
        "B_wo": _B_in["wo"],
    }

def reference(A_x, A_k_cache, A_v_cache, A_wq, A_wo, B_x, B_k_cache, B_v_cache, B_wq, B_wo):
    def _A_ref(x, k_cache, v_cache, wq, wo):
        B, Sq, D = x.shape
        Hq, Dh = 16, 256
        Hkv = k_cache.shape[1]
        rep = Hq // Hkv
        q = (lambda x, *d: x.permute(*d))((x @ wq).reshape(B, Sq, Hq, Dh), 0, 2, 1, 3)
        k = (lambda x, n, axis: torch.repeat_interleave(x, n, dim=axis))(k_cache, rep, axis=1)
        v = (lambda x, n, axis: torch.repeat_interleave(x, n, dim=axis))(v_cache, rep, axis=1)
        scores = torch.einsum("bhqd,bhkd->bhqk", q, k) * (Dh ** -0.5)
        p = _softmax(scores, axis=-1)
        o = torch.einsum("bhqk,bhkd->bhqd", p, v)
        return (lambda x, *d: x.permute(*d))(o, 0, 2, 1, 3).reshape(B, Sq, Hq * Dh) @ wo

    def _B_ref(x, k_cache, v_cache, wq, wo):
        B, Sq, D = x.shape
        Hq, Dh = 16, 256
        Hkv = k_cache.shape[1]
        rep = Hq // Hkv
        q = (lambda x, *d: x.permute(*d))((x @ wq).reshape(B, Sq, Hq, Dh), 0, 2, 1, 3)
        k = (lambda x, n, axis: torch.repeat_interleave(x, n, dim=axis))(k_cache, rep, axis=1)
        v = (lambda x, n, axis: torch.repeat_interleave(x, n, dim=axis))(v_cache, rep, axis=1)
        scores = torch.einsum("bhqd,bhkd->bhqk", q, k) * (Dh ** -0.5)
        p = _softmax(scores, axis=-1)
        o = torch.einsum("bhqk,bhkd->bhqd", p, v)
        return (lambda x, *d: x.permute(*d))(o, 0, 2, 1, 3).reshape(B, Sq, Hq * Dh) @ wo

    a = _A_ref(A_x, A_k_cache, A_v_cache, A_wq, A_wo)
    b = _B_ref(B_x, B_k_cache, B_v_cache, B_wq, B_wo)
    return torch.concatenate([a, b], axis=-1)
```

```cpp
#include <hip/hip_runtime.h>
#include <hip/hip_bf16.h>
#include <stdint.h>

namespace naive {
__device__ inline float b2f(uint16_t v){ return __uint_as_float(((uint32_t)v)<<16); }
__device__ inline uint16_t f2b(float f){ uint32_t u=__float_as_uint(f); u += 0x7fff + ((u>>16)&1); return (uint16_t)(u>>16); }

__global__ void qproj(const uint16_t* x, const uint16_t* wq, uint16_t* q){
  int n = blockIdx.x*256+threadIdx.x; int row = blockIdx.y;
  float acc=0;
  for(int k=0;k<448;k++) acc += b2f(x[row*448+k])*b2f(wq[(size_t)k*4096+n]);
  q[(size_t)row*4096+n]=f2b(acc);
}
__global__ void attn(const uint16_t* q, const uint16_t* kc, const uint16_t* vc, uint16_t* o){
  int row = blockIdx.x; int h = blockIdx.y; int b = row>>7; int g = h>>1;
  __shared__ float qs[256]; __shared__ float p[512]; __shared__ float red[256];
  int t=threadIdx.x;
  qs[t]=b2f(q[(size_t)row*4096+h*256+t]);
  __syncthreads();
  const uint16_t* K = kc + ((size_t)(b*8+g)*512)*256; const uint16_t* V = vc + ((size_t)(b*8+g)*512)*256;
  float s[2];
  for(int j=0;j<2;j++){ int key=t+256*j; float a=0; for(int d=0;d<256;d++) a+=qs[d]*b2f(K[key*256+d]); s[j]=a*0.0625f; }
  float m=fmaxf(s[0],s[1]); red[t]=m; __syncthreads();
  for(int o2=128;o2>0;o2>>=1){ if(t<o2) red[t]=fmaxf(red[t],red[t+o2]); __syncthreads(); }
  m=red[0]; __syncthreads();
  float e0=expf(s[0]-m), e1=expf(s[1]-m); p[t]=e0; p[t+256]=e1; red[t]=e0+e1; __syncthreads();
  for(int o2=128;o2>0;o2>>=1){ if(t<o2) red[t]+=red[t+o2]; __syncthreads(); }
  float l=red[0];
  float a=0; for(int k=0;k<512;k++) a+=p[k]*b2f(V[k*256+t]);
  o[(size_t)row*4096+h*256+t]=f2b(a/l);
}
__global__ void oproj(const uint16_t* o, const uint16_t* wo, uint16_t* out, int br){
  int n = threadIdx.x; int row = blockIdx.x; if(n>=448) return;
  float acc=0;
  for(int k=0;k<4096;k++) acc += b2f(o[(size_t)row*4096+k])*b2f(wo[(size_t)k*448+n]);
  out[(size_t)row*896+br*448+n]=f2b(acc);
}
inline void launch(void* const* d_in, void* d_out, void* d_ws, hipStream_t stream){
  for(int br=0;br<2;br++){
    const uint16_t* x=(const uint16_t*)d_in[br*5+0]; const uint16_t* k=(const uint16_t*)d_in[br*5+1]; const uint16_t* v=(const uint16_t*)d_in[br*5+2];
    const uint16_t* wq=(const uint16_t*)d_in[br*5+3]; const uint16_t* wo=(const uint16_t*)d_in[br*5+4];
    uint16_t* q=(uint16_t*)d_ws + (size_t)br*2*2048*4096; uint16_t* o=q+(size_t)2048*4096;
    qproj<<<dim3(16,2048),256,0,stream>>>(x,wq,q);
    attn<<<dim3(2048,16),256,0,stream>>>(q,k,v,o);
    oproj<<<2048,448,0,stream>>>(o,wo,(uint16_t*)d_out,br);
  }
}
}
extern "C" void kernel_launch(void* const* d_in, const int* in_sizes, int n_in,
                              void* d_out, int out_size, void* d_ws, size_t ws_size,
                              hipStream_t stream) {
  naive::launch(d_in,d_out,d_ws,stream);
}
```

```cpp
#include <hip/hip_runtime.h>
#include <hip/hip_bf16.h>
#include <stdint.h>

__global__ __launch_bounds__(256, 1) void fused_gqa_kernel(
    const char* xA, const char* kA, const char* vA, const char* wqA, const char* woA,
    const char* xB, const char* kB, const char* vB, const char* wqB, const char* woB,
    char* out, char* ws) {
  __shared__ __attribute__((aligned(16))) char lds[163840];
  const unsigned wg = blockIdx.x;
  const unsigned xc = wg & 7, ix = wg >> 3, br = xc >> 2, gk = 2 * (xc & 3) + (ix & 1), b = ix >> 1, bb = br * 16 + b;
  const unsigned w = __builtin_amdgcn_readfirstlane(threadIdx.x >> 6);
  const char* ka = (const char*)__builtin_amdgcn_kernarg_segment_ptr();
  const unsigned toff = ((wg >> 3) * 4 + (threadIdx.x >> 6)) * 128 + (threadIdx.x & 7) * 16384;
  const unsigned j0 = (b + 3 * gk) % 7, ps = (b / 7) & 1, rho = 2 * (b % 3);
  const unsigned pk = (w | ((bb * 8 + gk) << 2) | (j0 << 10) | (rho << 13) | (ps << 16)) + (unsigned)(size_t)lds;
  asm volatile(
"L_start_%=:\n"
"s_load_dwordx8 s[4:11], %0, 0x0\n"
"s_load_dwordx2 s[12:13], %0, 0x20\n"
"s_load_dwordx4 s[16:19], %1, 0x50\n"
"s_getpc_b64 s[28:29]\n"
"L_pc0_%=:\n"
"v_min_u32 v255, L_end_%=-L_pc0_%=, %6\n"
"v_mbcnt_lo_u32_b32 v251, -1, 0\n"
"s_and_b32 s89, %7, 3\n"
"v_mbcnt_hi_u32_b32 v251, -1, v251\n"
"s_lshl_b32 s79, s89, 5\n"
"s_lshl_b32 s96, s89, 12\n"
"s_bfe_u32 s82, %7, 0x3000a\n"
"s_lshl_b32 s82, s82, 19\n"
"v_lshrrev_b32 v252, 3, v251\n"
"v_add_u32 v252, s79, v252\n"
"v_mul_u32_u24 v252, 0x380, v252\n"
"v_lshrrev_b32 v253, 4, v251\n"
"v_and_b32 v254, 7, v251\n"
"v_xor_b32 v253, v253, v254\n"
"v_lshl_add_u32 v192, v253, 4, v252\n"
"v_xor_b32 v193, 64, v192\n"
"v_add_u32 v193, 0x1c00, v193\n"
"v_add_u32 v194, 0x3800, v192\n"
"v_add_u32 v195, 0x3800, v193\n"
"s_lshl_b32 s72, s89, 10\n"
"s_bfe_u32 s94, %7, 0x10010\n"
"s_lshl_b32 s94, s94, 8\n"
"s_lshl_b32 s79, s89, 16\n"
"s_add_u32 s79, s79, s94\n"
"v_lshrrev_b32 v253, 5, v251\n"
"v_and_b32 v252, 3, v251\n"
"v_lshlrev_b32 v252, 4, v252\n"
"v_lshl_add_u32 v252, v253, 6, v252\n"
"v_bfe_u32 v253, v251, 2, 3\n"
"v_lshl_add_u32 v199, v253, 13, v252\n"
"v_add_u32 v199, s79, v199\n"
"v_add_u32 v200, 0x40000, v199\n"
"s_bfe_u32 s90, %7, 0x30002\n"
"s_lshl_b32 s79, s90, 10\n"
"s_waitcnt lgkmcnt(0)\n"
"s_add_u32 s40, s4, %2\n"
"s_addc_u32 s41, s5, 0\n"
"s_mov_b32 s42, 114688\n"
"s_mov_b32 s43, 0x20000\n"
"s_add_u32 m0, s96, 0x8400\n"
"s_lshr_b32 s75, s82, 12\n"
"buffer_load_dwordx4 v192, s[40:43], s75 offen nt lds\n"
"s_add_u32 m0, s96, 0x8800\n"
"s_nop 0\n"
"buffer_load_dwordx4 v193, s[40:43], s75 offen nt lds\n"
"s_add_u32 m0, s96, 0x8c00\n"
"s_nop 0\n"
"buffer_load_dwordx4 v194, s[40:43], s75 offen nt lds\n"
"s_add_u32 m0, s96, 0x9000\n"
"s_nop 0\n"
"buffer_load_dwordx4 v195, s[40:43], s75 offen nt lds\n"
"s_add_u32 s44, s10, s79\n"
"s_addc_u32 s45, s11, 0\n"
"s_mov_b32 s46, 3662848\n"
"s_mov_b32 s47, 0x20000\n"
"s_add_u32 m0, s72, 0x18c00\n"
"s_nop 0\n"
"buffer_load_dwordx4 v199, s[44:47], s82 offen offset:0 nt lds\n"
"s_add_u32 m0, s72, 0x19a00\n"
"s_nop 0\n"
"buffer_load_dwordx4 v199, s[44:47], s82 offen offset:512 nt lds\n"
"s_add_u32 m0, s72, 0x1ac80\n"
"s_nop 0\n"
"buffer_load_dwordx4 v199, s[44:47], s82 offen offset:128 nt lds\n"
"s_add_u32 m0, s72, 0x1ba80\n"
"s_nop 0\n"
"buffer_load_dwordx4 v199, s[44:47], s82 offen offset:640 nt lds\n"
"s_cmp_ge_u32 s89, 2\n"
"s_cbranch_scc1 L_Y_%=\n"
"v_add_u32 v202, 0x20000, v199\n"
"v_add_u32 v203, 0x20000, v200\n"
"s_add_u32 s83, s82, 0x80000\n"
"s_cmp_eq_u32 s83, 0x380000\n"
"s_cselect_b32 s83, 0, s83\n"
"s_add_u32 s84, s83, 0x80000\n"
"s_cmp_eq_u32 s84, 0x380000\n"
"s_cselect_b32 s84, 0, s84\n"
"v_and_b32 v254, 3, v253\n"
"v_lshrrev_b32 v253, 2, v253\n"
"v_lshl_add_u32 v254, v253, 4, v254\n"
"s_lshl_b32 s79, s89, 2\n"
"v_add_u32 v254, s79, v254\n"
"v_lshl_add_u32 v243, v254, 9, v252\n"
"v_and_b32 v252, 31, v251\n"
"v_lshlrev_b32 v252, 4, v252\n"
"v_xor_b32 v252, s94, v252\n"
"v_lshrrev_b32 v253, 5, v251\n"
"v_lshl_add_u32 v252, v253, 12, v252\n"
"s_lshr_b32 s79, s89, 1\n"
"s_lshl_b32 s78, s79, 13\n"
"s_lshl_b32 s79, s79, 13\n"
"s_and_b32 s94, s89, 1\n"
"s_lshl_b32 s31, s94, 11\n"
"s_add_u32 s31, s31, s78\n"
"v_add_u32 v242, s31, v252\n"
"v_and_b32 v252, 15, v251\n"
"v_lshlrev_b32 v252, 7, v252\n"
"v_bfe_u32 v253, v251, 1, 3\n"
"v_lshrrev_b32 v254, 4, v251\n"
"v_xor_b32 v253, v253, v254\n"
"v_lshl_add_u32 v252, v253, 4, v252\n"
"s_add_u32 s79, s79, 0x8400\n"
"v_add_u32 v196, s79, v252\n"
"v_xor_b32 v197, 64, v196\n"
"v_and_b32 v252, 15, v251\n"
"v_lshlrev_b32 v252, 4, v252\n"
"v_lshl_add_u32 v252, v254, 10, v252\n"
"s_lshl_b32 s94, s94, 12\n"
"v_add_u32 v198, s94, v252\n"
"v_add_u32 v244, 0x10800, v198\n"
"v_add_u32 v245, 0x21000, v198\n"
"s_add_u32 s85, s84, 0x80000\n"
"s_cmp_eq_u32 s85, 0x380000\n"
"s_cselect_b32 s85, 0, s85\n"
"s_add_u32 s86, s85, 0x80000\n"
"s_cmp_eq_u32 s86, 0x380000\n"
"s_cselect_b32 s86, 0, s86\n"
"s_add_u32 s87, s86, 0x80000\n"
"s_cmp_eq_u32 s87, 0x380000\n"
"s_cselect_b32 s87, 0, s87\n"
"s_add_u32 s88, s87, 0x80000\n"
"s_cmp_eq_u32 s88, 0x380000\n"
"s_cselect_b32 s88, 0, s88\n"
"s_add_u32 s48, s6, %3\n"
"s_addc_u32 s49, s7, 0\n"
"s_mov_b32 s50, 262144\n"
"s_mov_b32 s51, 0x20000\n"
"s_add_u32 s52, s8, %3\n"
"s_addc_u32 s53, s9, 0\n"
"s_mov_b32 s54, 262144\n"
"s_mov_b32 s55, 0x20000\n"
"s_mov_b64 s[8:9], s[28:29]\n"
"s_mov_b64 s[68:69], s[18:19]\n"
"s_mov_b32 s70, 8192\n"
"s_mov_b32 s71, 0x20000\n"
"s_bfe_u32 s79, %7, 0x50005\n"
"s_lshl_b32 s79, s79, 13\n"
"s_add_u32 s79, s79, 0x2000000\n"
"s_add_u32 s68, s68, s79\n"
"s_addc_u32 s69, s69, 0\n"
"s_lshl_b32 s30, s89, 7\n"
"s_lshl_b32 s29, s90, 2\n"
"s_add_u32 s29, s29, s30\n"
"s_add_u32 s56, s12, %4\n"
"s_addc_u32 s57, s13, 0\n"
"s_mov_b32 s58, 458752\n"
"s_mov_b32 s59, 0x20000\n"
"s_add_u32 s64, s16, %5\n"
"s_addc_u32 s65, s17, 0\n"
"s_mov_b32 s66, 228480\n"
"s_mov_b32 s67, 0x20000\n"
"s_mov_b64 s[60:61], s[18:19]\n"
"s_mov_b32 s62, 917504\n"
"s_mov_b32 s63, 0x20000\n"
"s_bfe_u32 s79, %7, 0x50005\n"
"s_lshl_b32 s79, s79, 20\n"
"s_add_u32 s60, s60, s79\n"
"s_addc_u32 s61, s61, 0\n"
"s_min_u32 s91, s90, 6\n"
"s_mov_b64 s[32:33], s[60:61]\n"
"s_mov_b32 s34, 131072\n"
"s_mov_b32 s35, 0x20000\n"
"s_lshl_b32 s79, s91, 17\n"
"s_add_u32 s32, s32, s79\n"
"s_addc_u32 s33, s33, 0\n"
"s_mov_b64 s[36:37], s[60:61]\n"
"s_mov_b32 s38, 917504\n"
"s_mov_b32 s39, 0x20000\n"
"s_cmp_lt_u32 s90, 6\n"
"s_cselect_b32 s38, 0, s38\n"
"s_cmp_eq_u32 s90, 6\n"
"s_cselect_b32 s99, 32, 0\n"
"s_bfe_u32 s92, %7, 0x3000d\n"
"s_mul_i32 s73, s89, 4224\n"
"s_mul_i32 s74, s89, 1056\n"
"s_mov_b32 s95, 0x3db8aa3b\n"
"s_mov_b32 s76, 0x10000\n"
"s_lshl_b32 s98, s91, 9\n"
"s_add_u32 s98, s98, s30\n"
"s_add_u32 s98, s98, 0x1000\n"
"s_lshl_b32 s93, s90, 14\n"
"s_add_u32 s93, s93, s96\n"
"v_and_b32 v240, 7, v251\n"
"v_mul_u32_u24 v240, 0x420, v240\n"
"v_bfe_u32 v253, v251, 3, 1\n"
"v_lshl_add_u32 v240, v253, 9, v240\n"
"v_lshrrev_b32 v254, 4, v251\n"
"v_lshl_add_u32 v240, v254, 4, v240\n"
"v_mul_u32_u24 v241, 0x420, v254\n"
"v_bfe_u32 v253, v251, 2, 2\n"
"v_lshl_add_u32 v241, v253, 6, v241\n"
"v_and_b32 v253, 3, v251\n"
"v_lshl_add_u32 v241, v253, 3, v241\n"
"v_add_u32 v241, 0x10800, v241\n"
"s_lshl_b32 s92, s92, 2\n"
"s_lshl_b32 s79, s91, 2\n"
"s_mov_b32 s14, 0x6543210\n"
"s_mov_b32 s15, 0x654321\n"
"s_add_u32 s94, s79, 4\n"
"s_lshr_b64 s[14:15], s[14:15], s94\n"
"s_and_b32 s14, s14, 0xffffff\n"
"s_lshr_b32 s15, s14, 8\n"
"s_lshl_b32 s94, s14, 24\n"
"s_or_b32 s14, s14, s94\n"
"s_lshr_b64 s[14:15], s[14:15], s92\n"
"s_and_b32 s16, s14, 0xffffff\n"
"s_mov_b32 s14, 0x60123456\n"
"s_mov_b32 s15, 0x12345\n"
"s_sub_u32 s94, 28, s79\n"
"s_lshr_b64 s[14:15], s[14:15], s94\n"
"s_and_b32 s14, s14, 0xffffff\n"
"s_lshr_b32 s15, s14, 8\n"
"s_lshl_b32 s94, s14, 24\n"
"s_or_b32 s14, s14, s94\n"
"s_lshr_b64 s[14:15], s[14:15], s92\n"
"s_and_b32 s17, s14, 0xffffff\n"
"s_lshl_b32 s79, s91, 24\n"
"s_or_b32 s16, s16, s79\n"
"s_cmp_eq_u32 s90, 7\n"
"s_cselect_b32 s79, 6, 7\n"
"s_lshl_b32 s79, s79, 24\n"
"s_or_b32 s17, s17, s79\n"
"s_lshl_b32 s4, s91, 7\n"
"s_add_u32 s5, s4, 0x7000\n"
"s_add_u32 s6, s4, 0xe000\n"
"s_add_u32 s7, s4, 0x15000\n"
"v_and_b32 v252, 7, v251\n"
"v_lshlrev_b32 v252, 2, v252\n"
"v_mov_b32 v253, 0\n"
"s_bfe_u32 s79, %7, 0x40005\n"
"s_lshl_b32 s79, s79, 10\n"
"s_and_b32 s94, %7, 7\n"
"s_lshl_b32 s94, s94, 7\n"
"s_add_u32 s79, s79, s94\n"
"s_add_u32 s79, s79, L_u13_%=-L_pc0_%=\n"
"v_and_b32 v251, 3, v251\n"
"v_lshlrev_b32 v251, 14, v251\n"
"v_add_u32 v251, s79, v251\n"
"v_min_u32 v251, L_end1_%=-L_pc0_%=, v251\n"
"global_load_dword v255, v255, s[8:9]\n"
"s_add_u32 m0, s72, 0x1ce00\n"
"s_nop 0\n"
"buffer_load_dwordx4 v200, s[44:47], s82 offen offset:0 nt lds\n"
"s_add_u32 m0, s72, 0x1d600\n"
"s_nop 0\n"
"buffer_load_dwordx4 v203, s[44:47], s82 offen offset:0 nt lds\n"
"s_add_u32 m0, s72, 0x1dc00\n"
"s_nop 0\n"
"buffer_load_dwordx4 v200, s[44:47], s82 offen offset:512 nt lds\n"
"s_add_u32 m0, s72, 0x1e400\n"
"s_nop 0\n"
"buffer_load_dwordx4 v203, s[44:47], s82 offen offset:512 nt lds\n"
"s_add_u32 m0, s72, 0x1ee80\n"
"s_nop 0\n"
"buffer_load_dwordx4 v200, s[44:47], s82 offen offset:128 nt lds\n"
"s_add_u32 m0, s72, 0x1f680\n"
"s_nop 0\n"
"buffer_load_dwordx4 v203, s[44:47], s82 offen offset:128 nt lds\n"
"s_add_u32 m0, s72, 0x1fc80\n"
"s_nop 0\n"
"buffer_load_dwordx4 v200, s[44:47], s82 offen offset:640 nt lds\n"
"s_add_u32 m0, s72, 0x20480\n"
"s_nop 0\n"
"buffer_load_dwordx4 v203, s[44:47], s82 offen offset:640 nt lds\n"
"s_waitcnt vmcnt(13)\n"
"s_barrier\n"
"ds_read_b128 a[0:3], v196 offset:0\n"
"ds_read_b128 a[4:7], v196 offset:2048\n"
"ds_read_b128 a[8:11], v196 offset:4096\n"
"ds_read_b128 a[12:15], v196 offset:6144\n"
"s_waitcnt vmcnt(11)\n"
"s_barrier\n"
"ds_read_b64_tr_b16 v[208:209], v244 offset:33792\n"
"ds_read_b64_tr_b16 v[210:211], v244 offset:34048\n"
"s_waitcnt lgkmcnt(0)\n"
"v_mfma_f32_16x16x32_bf16 v[0:3], v[208:211], a[0:3], 0\n"
"ds_read_b64_tr_b16 v[212:213], v244 offset:33800\n"
"ds_read_b64_tr_b16 v[214:215], v244 offset:34056\n"
"s_add_u32 m0, s72, 0x21000\n"
"v_mfma_f32_16x16x32_bf16 v[4:7], v[208:211], a[4:7], 0\n"
"buffer_load_dwordx4 v199, s[44:47], s83 offen offset:0 lds\n"
"ds_read_b64_tr_b16 v[216:217], v244 offset:34304\n"
"ds_read_b64_tr_b16 v[218:219], v244 offset:34560\n"
"v_mfma_f32_16x16x32_bf16 v[8:11], v[208:211], a[8:11], 0\n"
"ds_read_b64_tr_b16 v[220:221], v244 offset:34312\n"
"ds_read_b64_tr_b16 v[222:223], v244 offset:34568\n"
"v_mfma_f32_16x16x32_bf16 v[12:15], v[208:211], a[12:15], 0\n"
"s_add_u32 m0, s72, 0x21800\n"
"s_waitcnt lgkmcnt(4)\n"
"v_mfma_f32_16x16x32_bf16 v[32:35], v[212:215], a[0:3], 0\n"
"buffer_load_dwordx4 v202, s[44:47], s83 offen offset:0 lds\n"
"v_mfma_f32_16x16x32_bf16 v[36:39], v[212:215], a[4:7], 0\n"
"s_waitcnt vmcnt(11)\n"
"s_barrier\n"
"ds_read_b64_tr_b16 v[224:225], v244 offset:42240\n"
"ds_read_b64_tr_b16 v[226:227], v244 offset:42496\n"
"v_mfma_f32_16x16x32_bf16 v[40:43], v[212:215], a[8:11], 0\n"
"s_add_u32 m0, s72, 0x21e00\n"
"v_mfma_f32_16x16x32_bf16 v[44:47], v[212:215], a[12:15], 0\n"
"buffer_load_dwordx4 v199, s[44:47], s83 offen offset:512 lds\n"
"s_waitcnt lgkmcnt(4)\n"
"v_mfma_f32_16x16x32_bf16 v[16:19], v[216:219], a[0:3], 0\n"
"ds_read_b64_tr_b16 v[228:229], v244 offset:42248\n"
"ds_read_b64_tr_b16 v[230:231], v244 offset:42504\n"
"v_mfma_f32_16x16x32_bf16 v[20:23], v[216:219], a[4:7], 0\n"
"ds_read_b64_tr_b16 v[232:233], v244 offset:42752\n"
"ds_read_b64_tr_b16 v[234:235], v244 offset:43008\n"
"ds_read_b64_tr_b16 v[236:237], v244 offset:42760\n"
"ds_read_b64_tr_b16 v[238:239], v244 offset:43016\n"
"s_add_u32 m0, s72, 0x22600\n"
"v_mfma_f32_16x16x32_bf16 v[24:27], v[216:219], a[8:11], 0\n"
"buffer_load_dwordx4 v202, s[44:47], s83 offen offset:512 lds\n"
"v_mfma_f32_16x16x32_bf16 v[28:31], v[216:219], a[12:15], 0\n"
"s_waitcnt lgkmcnt(8)\n"
"v_mfma_f32_16x16x32_bf16 v[48:51], v[220:223], a[0:3], 0\n"
"s_add_u32 m0, s72, 0x23080\n"
"v_mfma_f32_16x16x32_bf16 v[52:55], v[220:223], a[4:7], 0\n"
"buffer_load_dwordx4 v199, s[44:47], s83 offen offset:128 lds\n"
"v_mfma_f32_16x16x32_bf16 v[56:59], v[220:223], a[8:11], 0\n"
"ds_read_b128 a[16:19], v197 offset:0\n"
"ds_read_b128 a[20:23], v197 offset:2048\n"
"v_mfma_f32_16x16x32_bf16 v[60:63], v[220:223], a[12:15], 0\n"
"s_add_u32 m0, s72, 0x23880\n"
"s_waitcnt lgkmcnt(2)\n"
"v_mfma_f32_16x16x32_bf16 v[64:67], v[224:227], a[0:3], 0\n"
"buffer_load_dwordx4 v202, s[44:47], s83 offen offset:128 lds\n"
"v_mfma_f32_16x16x32_bf16 v[68:71], v[224:227], a[4:7], 0\n"
"ds_read_b128 a[24:27], v197 offset:4096\n"
"ds_read_b128 a[28:31], v197 offset:6144\n"
"v_mfma_f32_16x16x32_bf16 v[72:75], v[224:227], a[8:11], 0\n"
"s_add_u32 m0, s72, 0x23e80\n"
"v_mfma_f32_16x16x32_bf16 v[76:79], v[224:227], a[12:15], 0\n"
"buffer_load_dwordx4 v199, s[44:47], s83 offen offset:640 lds\n"
"v_mfma_f32_16x16x32_bf16 v[96:99], v[228:231], a[0:3], 0\n"
"s_waitcnt vmcnt(11)\n"
"s_barrier\n"
"ds_read_b64_tr_b16 v[208:209], v244 offset:50688\n"
"ds_read_b64_tr_b16 v[210:211], v244 offset:50944\n"
"v_mfma_f32_16x16x32_bf16 v[100:103], v[228:231], a[4:7], 0\n"
"s_add_u32 m0, s72, 0x24680\n"
"v_mfma_f32_16x16x32_bf16 v[104:107], v[228:231], a[8:11], 0\n"
"buffer_load_dwordx4 v202, s[44:47], s83 offen offset:640 lds\n"
"v_mfma_f32_16x16x32_bf16 v[108:111], v[228:231], a[12:15], 0\n"
"ds_read_b64_tr_b16 v[212:213], v244 offset:50696\n"
"ds_read_b64_tr_b16 v[214:215], v244 offset:50952\n"
"v_mfma_f32_16x16x32_bf16 v[80:83], v[232:235], a[0:3], 0\n"
"s_add_u32 m0, s72, 0x18c00\n"
"v_mfma_f32_16x16x32_bf16 v[84:87], v[232:235], a[4:7], 0\n"
"buffer_load_dwordx4 v200, s[44:47], s83 offen offset:0 lds\n"
"ds_read_b64_tr_b16 v[216:217], v244 offset:51200\n"
"ds_read_b64_tr_b16 v[218:219], v244 offset:51456\n"
"ds_read_b64_tr_b16 v[220:221], v244 offset:51208\n"
"ds_read_b64_tr_b16 v[222:223], v244 offset:51464\n"
"v_mfma_f32_16x16x32_bf16 v[88:91], v[232:235], a[8:11], 0\n"
"v_mfma_f32_16x16x32_bf16 v[92:95], v[232:235], a[12:15], 0\n"
"s_add_u32 m0, s72, 0x19400\n"
"v_mfma_f32_16x16x32_bf16 v[112:115], v[236:239], a[0:3], 0\n"
"buffer_load_dwordx4 v203, s[44:47], s83 offen offset:0 lds\n"
"v_mfma_f32_16x16x32_bf16 v[116:119], v[236:239], a[4:7], 0\n"
"v_mfma_f32_16x16x32_bf16 v[120:123], v[236:239], a[8:11], 0\n"
"s_add_u32 m0, s72, 0x19a00\n"
"v_mfma_f32_16x16x32_bf16 v[124:127], v[236:239], a[12:15], 0\n"
"buffer_load_dwordx4 v200, s[44:47], s83 offen offset:512 lds\n"
"s_waitcnt lgkmcnt(0)\n"
"v_mfma_f32_16x16x32_bf16 v[0:3], v[208:211], a[16:19], v[0:3]\n"
"v_mfma_f32_16x16x32_bf16 v[4:7], v[208:211], a[20:23], v[4:7]\n"
"s_add_u32 m0, s72, 0x1a200\n"
"v_mfma_f32_16x16x32_bf16 v[8:11], v[208:211], a[24:27], v[8:11]\n"
"buffer_load_dwordx4 v203, s[44:47], s83 offen offset:512 lds\n"
"v_mfma_f32_16x16x32_bf16 v[12:15], v[208:211], a[28:31], v[12:15]\n"
"v_mfma_f32_16x16x32_bf16 v[32:35], v[212:215], a[16:19], v[32:35]\n"
"s_add_u32 m0, s72, 0x1ac80\n"
"v_mfma_f32_16x16x32_bf16 v[36:39], v[212:215], a[20:23], v[36:39]\n"
"buffer_load_dwordx4 v200, s[44:47], s83 offen offset:128 lds\n"
"v_mfma_f32_16x16x32_bf16 v[40:43], v[212:215], a[24:27], v[40:43]\n"
"s_waitcnt vmcnt(13)\n"
"s_barrier\n"
"ds_read_b64_tr_b16 v[224:225], v244 offset:59136\n"
"ds_read_b64_tr_b16 v[226:227], v244 offset:59392\n"
"v_mfma_f32_16x16x32_bf16 v[44:47], v[212:215], a[28:31], v[44:47]\n"
"s_add_u32 m0, s72, 0x1b480\n"
"v_mfma_f32_16x16x32_bf16 v[16:19], v[216:219], a[16:19], v[16:19]\n"
"buffer_load_dwordx4 v203, s[44:47], s83 offen offset:128 lds\n"
"v_mfma_f32_16x16x32_bf16 v[20:23], v[216:219], a[20:23], v[20:23]\n"
"ds_read_b64_tr_b16 v[228:229], v244 offset:59144\n"
"ds_read_b64_tr_b16 v[230:231], v244 offset:59400\n"
"ds_read_b64_tr_b16 v[232:233], v244 offset:59648\n"
"ds_read_b64_tr_b16 v[234:235], v244 offset:59904\n"
"ds_read_b64_tr_b16 v[236:237], v244 offset:59656\n"
"ds_read_b64_tr_b16 v[238:239], v244 offset:59912\n"
"v_mfma_f32_16x16x32_bf16 v[24:27], v[216:219], a[24:27], v[24:27]\n"
"s_add_u32 m0, s72, 0x1ba80\n"
"v_mfma_f32_16x16x32_bf16 v[28:31], v[216:219], a[28:31], v[28:31]\n"
"buffer_load_dwordx4 v200, s[44:47], s83 offen offset:640 lds\n"
"v_mfma_f32_16x16x32_bf16 v[48:51], v[220:223], a[16:19], v[48:51]\n"
"v_mfma_f32_16x16x32_bf16 v[52:55], v[220:223], a[20:23], v[52:55]\n"
"s_add_u32 m0, s72, 0x1c280\n"
"v_mfma_f32_16x16x32_bf16 v[56:59], v[220:223], a[24:27], v[56:59]\n"
"buffer_load_dwordx4 v203, s[44:47], s83 offen offset:640 lds\n"
"v_mfma_f32_16x16x32_bf16 v[60:63], v[220:223], a[28:31], v[60:63]\n"
"s_waitcnt lgkmcnt(0)\n"
"v_mfma_f32_16x16x32_bf16 v[64:67], v[224:227], a[16:19], v[64:67]\n"
"s_add_u32 m0, s72, 0x1ce00\n"
"v_mfma_f32_16x16x32_bf16 v[68:71], v[224:227], a[20:23], v[68:71]\n"
"buffer_load_dwordx4 v199, s[44:47], s84 offen offset:0 lds\n"
"v_mfma_f32_16x16x32_bf16 v[72:75], v[224:227], a[24:27], v[72:75]\n"
"v_mfma_f32_16x16x32_bf16 v[76:79], v[224:227], a[28:31], v[76:79]\n"
"s_add_u32 m0, s72, 0x1d600\n"
"v_mfma_f32_16x16x32_bf16 v[96:99], v[228:231], a[16:19], v[96:99]\n"
"buffer_load_dwordx4 v202, s[44:47], s84 offen offset:0 lds\n"
"v_mfma_f32_16x16x32_bf16 v[100:103], v[228:231], a[20:23], v[100:103]\n"
"s_barrier\n"
"ds_read_b128 a[32:35], v196 offset:16896\n"
"v_mfma_f32_16x16x32_bf16 v[104:107], v[228:231], a[24:27], v[104:107]\n"
"ds_read_b128 a[36:39], v196 offset:18944\n"
"s_add_u32 m0, s72, 0x1dc00\n"
"v_mfma_f32_16x16x32_bf16 v[108:111], v[228:231], a[28:31], v[108:111]\n"
"buffer_load_dwordx4 v199, s[44:47], s84 offen offset:512 lds\n"
"v_mfma_f32_16x16x32_bf16 v[80:83], v[232:235], a[16:19], v[80:83]\n"
"ds_read_b128 a[40:43], v196 offset:20992\n"
"ds_read_b128 a[44:47], v196 offset:23040\n"
"v_mfma_f32_16x16x32_bf16 v[84:87], v[232:235], a[20:23], v[84:87]\n"
"s_waitcnt vmcnt(15)\n"
"s_barrier\n"
"ds_read_b64_tr_b16 v[208:209], v245 offset:0\n"
"ds_read_b64_tr_b16 v[210:211], v245 offset:256\n"
"ds_read_b64_tr_b16 v[212:213], v245 offset:8\n"
"ds_read_b64_tr_b16 v[214:215], v245 offset:264\n"
"ds_read_b64_tr_b16 v[216:217], v245 offset:512\n"
"ds_read_b64_tr_b16 v[218:219], v245 offset:768\n"
"ds_read_b64_tr_b16 v[220:221], v245 offset:520\n"
"ds_read_b64_tr_b16 v[222:223], v245 offset:776\n"
"s_add_u32 m0, s72, 0x1e400\n"
"v_mfma_f32_16x16x32_bf16 v[88:91], v[232:235], a[24:27], v[88:91]\n"
"buffer_load_dwordx4 v202, s[44:47], s84 offen offset:512 lds\n"
"v_mfma_f32_16x16x32_bf16 v[92:95], v[232:235], a[28:31], v[92:95]\n"
"v_mfma_f32_16x16x32_bf16 v[112:115], v[236:239], a[16:19], v[112:115]\n"
"s_add_u32 m0, s72, 0x1ee80\n"
"v_mfma_f32_16x16x32_bf16 v[116:119], v[236:239], a[20:23], v[116:119]\n"
"buffer_load_dwordx4 v199, s[44:47], s84 offen offset:128 lds\n"
"v_mfma_f32_16x16x32_bf16 v[120:123], v[236:239], a[24:27], v[120:123]\n"
"v_mfma_f32_16x16x32_bf16 v[124:127], v[236:239], a[28:31], v[124:127]\n"
"s_add_u32 m0, s72, 0x1f680\n"
"s_waitcnt lgkmcnt(0)\n"
"v_mfma_f32_16x16x32_bf16 v[0:3], v[208:211], a[32:35], v[0:3]\n"
"buffer_load_dwordx4 v202, s[44:47], s84 offen offset:128 lds\n"
"v_mfma_f32_16x16x32_bf16 v[4:7], v[208:211], a[36:39], v[4:7]\n"
"v_mfma_f32_16x16x32_bf16 v[8:11], v[208:211], a[40:43], v[8:11]\n"
"s_add_u32 m0, s72, 0x1fc80\n"
"v_mfma_f32_16x16x32_bf16 v[12:15], v[208:211], a[44:47], v[12:15]\n"
"buffer_load_dwordx4 v199, s[44:47], s84 offen offset:640 lds\n"
"v_mfma_f32_16x16x32_bf16 v[32:35], v[212:215], a[32:35], v[32:35]\n"
"s_waitcnt vmcnt(15)\n"
"s_barrier\n"
"ds_read_b64_tr_b16 v[224:225], v245 offset:8448\n"
"ds_read_b64_tr_b16 v[226:227], v245 offset:8704\n"
"v_mfma_f32_16x16x32_bf16 v[36:39], v[212:215], a[36:39], v[36:39]\n"
"s_add_u32 m0, s72, 0x20480\n"
"v_mfma_f32_16x16x32_bf16 v[40:43], v[212:215], a[40:43], v[40:43]\n"
"buffer_load_dwordx4 v202, s[44:47], s84 offen offset:640 lds\n"
"v_mfma_f32_16x16x32_bf16 v[44:47], v[212:215], a[44:47], v[44:47]\n"
"ds_read_b64_tr_b16 v[228:229], v245 offset:8456\n"
"ds_read_b64_tr_b16 v[230:231], v245 offset:8712\n"
"v_mfma_f32_16x16x32_bf16 v[16:19], v[216:219], a[32:35], v[16:19]\n"
"s_add_u32 m0, s72, 0x21000\n"
"v_mfma_f32_16x16x32_bf16 v[20:23], v[216:219], a[36:39], v[20:23]\n"
"buffer_load_dwordx4 v200, s[44:47], s84 offen offset:0 lds\n"
"ds_read_b64_tr_b16 v[232:233], v245 offset:8960\n"
"ds_read_b64_tr_b16 v[234:235], v245 offset:9216\n"
"ds_read_b64_tr_b16 v[236:237], v245 offset:8968\n"
"ds_read_b64_tr_b16 v[238:239], v245 offset:9224\n"
"v_mfma_f32_16x16x32_bf16 v[24:27], v[216:219], a[40:43], v[24:27]\n"
"v_mfma_f32_16x16x32_bf16 v[28:31], v[216:219], a[44:47], v[28:31]\n"
"s_add_u32 m0, s72, 0x21800\n"
"v_mfma_f32_16x16x32_bf16 v[48:51], v[220:223], a[32:35], v[48:51]\n"
"buffer_load_dwordx4 v203, s[44:47], s84 offen offset:0 lds\n"
"v_mfma_f32_16x16x32_bf16 v[52:55], v[220:223], a[36:39], v[52:55]\n"
"ds_read_b128 a[48:51], v197 offset:16896\n"
"ds_read_b128 a[52:55], v197 offset:18944\n"
"v_mfma_f32_16x16x32_bf16 v[56:59], v[220:223], a[40:43], v[56:59]\n"
"ds_read_b128 a[56:59], v197 offset:20992\n"
"s_add_u32 m0, s72, 0x21e00\n"
"v_mfma_f32_16x16x32_bf16 v[60:63], v[220:223], a[44:47], v[60:63]\n"
"buffer_load_dwordx4 v200, s[44:47], s84 offen offset:512 lds\n"
"s_waitcnt lgkmcnt(3)\n"
"v_mfma_f32_16x16x32_bf16 v[64:67], v[224:227], a[32:35], v[64:67]\n"
"ds_read_b128 a[60:63], v197 offset:23040\n"
"v_mfma_f32_16x16x32_bf16 v[68:71], v[224:227], a[36:39], v[68:71]\n"
"s_add_u32 m0, s72, 0x22600\n"
"v_mfma_f32_16x16x32_bf16 v[72:75], v[224:227], a[40:43], v[72:75]\n"
"buffer_load_dwordx4 v203, s[44:47], s84 offen offset:512 lds\n"
"v_mfma_f32_16x16x32_bf16 v[76:79], v[224:227], a[44:47], v[76:79]\n"
"v_mfma_f32_16x16x32_bf16 v[96:99], v[228:231], a[32:35], v[96:99]\n"
"s_add_u32 m0, s72, 0x25180\n"
"v_mfma_f32_16x16x32_bf16 v[100:103], v[228:231], a[36:39], v[100:103]\n"
"buffer_load_dwordx4 v200, s[44:47], s84 offen offset:128 lds\n"
"v_mfma_f32_16x16x32_bf16 v[104:107], v[228:231], a[40:43], v[104:107]\n"
"s_waitcnt vmcnt(17)\n"
"s_barrier\n"
"ds_read_b64_tr_b16 v[208:209], v244 offset:33792\n"
"ds_read_b64_tr_b16 v[210:211], v244 offset:34048\n"
"v_mfma_f32_16x16x32_bf16 v[108:111], v[228:231], a[44:47], v[108:111]\n"
"s_add_u32 m0, s72, 0x25980\n"
"v_mfma_f32_16x16x32_bf16 v[80:83], v[232:235], a[32:35], v[80:83]\n"
"buffer_load_dwordx4 v203, s[44:47], s84 offen offset:128 lds\n"
"v_mfma_f32_16x16x32_bf16 v[84:87], v[232:235], a[36:39], v[84:87]\n"
"ds_read_b64_tr_b16 v[212:213], v244 offset:33800\n"
"ds_read_b64_tr_b16 v[214:215], v244 offset:34056\n"
"ds_read_b64_tr_b16 v[216:217], v244 offset:34304\n"
"ds_read_b64_tr_b16 v[218:219], v244 offset:34560\n"
"ds_read_b64_tr_b16 v[220:221], v244 offset:34312\n"
"ds_read_b64_tr_b16 v[222:223], v244 offset:34568\n"
"v_mfma_f32_16x16x32_bf16 v[88:91], v[232:235], a[40:43], v[88:91]\n"
"s_add_u32 m0, s72, 0x25f80\n"
"v_mfma_f32_16x16x32_bf16 v[92:95], v[232:235], a[44:47], v[92:95]\n"
"buffer_load_dwordx4 v200, s[44:47], s84 offen offset:640 lds\n"
"v_mfma_f32_16x16x32_bf16 v[112:115], v[236:239], a[32:35], v[112:115]\n"
"v_mfma_f32_16x16x32_bf16 v[116:119], v[236:239], a[36:39], v[116:119]\n"
"s_add_u32 m0, s72, 0x26780\n"
"v_mfma_f32_16x16x32_bf16 v[120:123], v[236:239], a[40:43], v[120:123]\n"
"buffer_load_dwordx4 v203, s[44:47], s84 offen offset:640 lds\n"
"v_mfma_f32_16x16x32_bf16 v[124:127], v[236:239], a[44:47], v[124:127]\n"
"s_waitcnt lgkmcnt(0)\n"
"v_mfma_f32_16x16x32_bf16 v[0:3], v[208:211], a[48:51], v[0:3]\n"
"s_add_u32 m0, s72, 0x23100\n"
"v_mfma_f32_16x16x32_bf16 v[4:7], v[208:211], a[52:55], v[4:7]\n"
"buffer_load_dwordx4 v199, s[44:47], s85 offen offset:0 lds\n"
"v_mfma_f32_16x16x32_bf16 v[8:11], v[208:211], a[56:59], v[8:11]\n"
"v_mfma_f32_16x16x32_bf16 v[12:15], v[208:211], a[60:63], v[12:15]\n"
"s_add_u32 m0, s72, 0x23900\n"
"v_mfma_f32_16x16x32_bf16 v[32:35], v[212:215], a[48:51], v[32:35]\n"
"buffer_load_dwordx4 v202, s[44:47], s85 offen offset:0 lds\n"
"v_mfma_f32_16x16x32_bf16 v[36:39], v[212:215], a[52:55], v[36:39]\n"
"s_waitcnt vmcnt(18)\n"
"s_barrier\n"
"ds_read_b64_tr_b16 v[224:225], v244 offset:42240\n"
"ds_read_b64_tr_b16 v[226:227], v244 offset:42496\n"
"v_mfma_f32_16x16x32_bf16 v[40:43], v[212:215], a[56:59], v[40:43]\n"
"s_add_u32 m0, s72, 0x23f00\n"
"v_mfma_f32_16x16x32_bf16 v[44:47], v[212:215], a[60:63], v[44:47]\n"
"buffer_load_dwordx4 v199, s[44:47], s85 offen offset:512 lds\n"
"v_mfma_f32_16x16x32_bf16 v[16:19], v[216:219], a[48:51], v[16:19]\n"
"ds_read_b64_tr_b16 v[228:229], v244 offset:42248\n"
"ds_read_b64_tr_b16 v[230:231], v244 offset:42504\n"
"v_mfma_f32_16x16x32_bf16 v[20:23], v[216:219], a[52:55], v[20:23]\n"
"ds_read_b64_tr_b16 v[232:233], v244 offset:42752\n"
"ds_read_b64_tr_b16 v[234:235], v244 offset:43008\n"
"ds_read_b64_tr_b16 v[236:237], v244 offset:42760\n"
"ds_read_b64_tr_b16 v[238:239], v244 offset:43016\n"
"s_add_u32 m0, s72, 0x24700\n"
"v_mfma_f32_16x16x32_bf16 v[24:27], v[216:219], a[56:59], v[24:27]\n"
"buffer_load_dwordx4 v202, s[44:47], s85 offen offset:512 lds\n"
"v_mfma_f32_16x16x32_bf16 v[28:31], v[216:219], a[60:63], v[28:31]\n"
"v_mfma_f32_16x16x32_bf16 v[48:51], v[220:223], a[48:51], v[48:51]\n"
"ds_read_b128 a[64:67], v196 offset:33792\n"
"s_add_u32 m0, s72, 0x18b80\n"
"v_mfma_f32_16x16x32_bf16 v[52:55], v[220:223], a[52:55], v[52:55]\n"
"buffer_load_dwordx4 v199, s[44:47], s85 offen offset:128 lds\n"
"v_mfma_f32_16x16x32_bf16 v[56:59], v[220:223], a[56:59], v[56:59]\n"
"ds_read_b128 a[68:71], v196 offset:35840\n"
"ds_read_b128 a[72:75], v196 offset:37888\n"
"v_mfma_f32_16x16x32_bf16 v[60:63], v[220:223], a[60:63], v[60:63]\n"
"s_add_u32 m0, s72, 0x19380\n"
"s_waitcnt lgkmcnt(3)\n"
"v_mfma_f32_16x16x32_bf16 v[64:67], v[224:227], a[48:51], v[64:67]\n"
"buffer_load_dwordx4 v202, s[44:47], s85 offen offset:128 lds\n"
"v_mfma_f32_16x16x32_bf16 v[68:71], v[224:227], a[52:55], v[68:71]\n"
"ds_read_b128 a[76:79], v196 offset:39936\n"
"v_mfma_f32_16x16x32_bf16 v[72:75], v[224:227], a[56:59], v[72:75]\n"
"s_add_u32 m0, s72, 0x19980\n"
"v_mfma_f32_16x16x32_bf16 v[76:79], v[224:227], a[60:63], v[76:79]\n"
"buffer_load_dwordx4 v199, s[44:47], s85 offen offset:640 lds\n"
"v_mfma_f32_16x16x32_bf16 v[96:99], v[228:231], a[48:51], v[96:99]\n"
"s_waitcnt vmcnt(19)\n"
"s_barrier\n"
"ds_read_b64_tr_b16 v[208:209], v244 offset:50688\n"
"ds_read_b64_tr_b16 v[210:211], v244 offset:50944\n"
"v_mfma_f32_16x16x32_bf16 v[100:103], v[228:231], a[52:55], v[100:103]\n"
"s_add_u32 m0, s72, 0x1a180\n"
"v_mfma_f32_16x16x32_bf16 v[104:107], v[228:231], a[56:59], v[104:107]\n"
"buffer_load_dwordx4 v202, s[44:47], s85 offen offset:640 lds\n"
"v_mfma_f32_16x16x32_bf16 v[108:111], v[228:231], a[60:63], v[108:111]\n"
"ds_read_b64_tr_b16 v[212:213], v244 offset:50696\n"
"ds_read_b64_tr_b16 v[214:215], v244 offset:50952\n"
"v_mfma_f32_16x16x32_bf16 v[80:83], v[232:235], a[48:51], v[80:83]\n"
"s_add_u32 m0, s72, 0x1ad00\n"
"v_mfma_f32_16x16x32_bf16 v[84:87], v[232:235], a[52:55], v[84:87]\n"
"buffer_load_dwordx4 v200, s[44:47], s85 offen offset:0 lds\n"
"ds_read_b64_tr_b16 v[216:217], v244 offset:51200\n"
"ds_read_b64_tr_b16 v[218:219], v244 offset:51456\n"
"ds_read_b64_tr_b16 v[220:221], v244 offset:51208\n"
"ds_read_b64_tr_b16 v[222:223], v244 offset:51464\n"
"v_mfma_f32_16x16x32_bf16 v[88:91], v[232:235], a[56:59], v[88:91]\n"
"v_mfma_f32_16x16x32_bf16 v[92:95], v[232:235], a[60:63], v[92:95]\n"
"s_add_u32 m0, s72, 0x1b500\n"
"v_mfma_f32_16x16x32_bf16 v[112:115], v[236:239], a[48:51], v[112:115]\n"
"buffer_load_dwordx4 v203, s[44:47], s85 offen offset:0 lds\n"
"v_mfma_f32_16x16x32_bf16 v[116:119], v[236:239], a[52:55], v[116:119]\n"
"v_mfma_f32_16x16x32_bf16 v[120:123], v[236:239], a[56:59], v[120:123]\n"
"s_add_u32 m0, s72, 0x1bb00\n"
"v_mfma_f32_16x16x32_bf16 v[124:127], v[236:239], a[60:63], v[124:127]\n"
"buffer_load_dwordx4 v200, s[44:47], s85 offen offset:512 lds\n"
"s_waitcnt lgkmcnt(0)\n"
"v_mfma_f32_16x16x32_bf16 v[0:3], v[208:211], a[64:67], v[0:3]\n"
"v_mfma_f32_16x16x32_bf16 v[4:7], v[208:211], a[68:71], v[4:7]\n"
"s_add_u32 m0, s72, 0x1c300\n"
"v_mfma_f32_16x16x32_bf16 v[8:11], v[208:211], a[72:75], v[8:11]\n"
"buffer_load_dwordx4 v203, s[44:47], s85 offen offset:512 lds\n"
"v_mfma_f32_16x16x32_bf16 v[12:15], v[208:211], a[76:79], v[12:15]\n"
"v_mfma_f32_16x16x32_bf16 v[32:35], v[212:215], a[64:67], v[32:35]\n"
"s_add_u32 m0, s72, 0x2080\n"
"v_mfma_f32_16x16x32_bf16 v[36:39], v[212:215], a[68:71], v[36:39]\n"
"buffer_load_dwordx4 v200, s[44:47], s85 offen offset:128 lds\n"
"v_mfma_f32_16x16x32_bf16 v[40:43], v[212:215], a[72:75], v[40:43]\n"
"s_waitcnt vmcnt(21)\n"
"s_barrier\n"
"ds_read_b64_tr_b16 v[224:225], v244 offset:59136\n"
"ds_read_b64_tr_b16 v[226:227], v244 offset:59392\n"
"v_mfma_f32_16x16x32_bf16 v[44:47], v[212:215], a[76:79], v[44:47]\n"
"s_add_u32 m0, s72, 0x2880\n"
"v_mfma_f32_16x16x32_bf16 v[16:19], v[216:219], a[64:67], v[16:19]\n"
"buffer_load_dwordx4 v203, s[44:47], s85 offen offset:128 lds\n"
"v_mfma_f32_16x16x32_bf16 v[20:23], v[216:219], a[68:71], v[20:23]\n"
"ds_read_b64_tr_b16 v[228:229], v244 offset:59144\n"
"ds_read_b64_tr_b16 v[230:231], v244 offset:59400\n"
"ds_read_b64_tr_b16 v[232:233], v244 offset:59648\n"
"ds_read_b64_tr_b16 v[234:235], v244 offset:59904\n"
"ds_read_b64_tr_b16 v[236:237], v244 offset:59656\n"
"ds_read_b64_tr_b16 v[238:239], v244 offset:59912\n"
"v_mfma_f32_16x16x32_bf16 v[24:27], v[216:219], a[72:75], v[24:27]\n"
"s_add_u32 m0, s72, 0x2e80\n"
"v_mfma_f32_16x16x32_bf16 v[28:31], v[216:219], a[76:79], v[28:31]\n"
"buffer_load_dwordx4 v200, s[44:47], s85 offen offset:640 lds\n"
"v_mfma_f32_16x16x32_bf16 v[48:51], v[220:223], a[64:67], v[48:51]\n"
"ds_read_b128 a[80:83], v197 offset:33792\n"
"ds_read_b128 a[84:87], v197 offset:35840\n"
"v_mfma_f32_16x16x32_bf16 v[52:55], v[220:223], a[68:71], v[52:55]\n"
"ds_read_b128 a[88:91], v197 offset:37888\n"
"s_add_u32 m0, s72, 0x3680\n"
"v_mfma_f32_16x16x32_bf16 v[56:59], v[220:223], a[72:75], v[56:59]\n"
"buffer_load_dwordx4 v203, s[44:47], s85 offen offset:640 lds\n"
"v_mfma_f32_16x16x32_bf16 v[60:63], v[220:223], a[76:79], v[60:63]\n"
"ds_read_b128 a[92:95], v197 offset:39936\n"
"s_waitcnt lgkmcnt(4)\n"
"v_mfma_f32_16x16x32_bf16 v[64:67], v[224:227], a[64:67], v[64:67]\n"
"s_add_u32 m0, s72, 0x1ce00\n"
"v_mfma_f32_16x16x32_bf16 v[68:71], v[224:227], a[68:71], v[68:71]\n"
"buffer_load_dwordx4 v199, s[44:47], s86 offen offset:0 lds\n"
"v_mfma_f32_16x16x32_bf16 v[72:75], v[224:227], a[72:75], v[72:75]\n"
"v_mfma_f32_16x16x32_bf16 v[76:79], v[224:227], a[76:79], v[76:79]\n"
"s_add_u32 m0, s72, 0x1d600\n"
"v_mfma_f32_16x16x32_bf16 v[96:99], v[228:231], a[64:67], v[96:99]\n"
"buffer_load_dwordx4 v202, s[44:47], s86 offen offset:0 lds\n"
"v_mfma_f32_16x16x32_bf16 v[100:103], v[228:231], a[68:71], v[100:103]\n"
"s_waitcnt vmcnt(22)\n"
"s_barrier\n"
"ds_read_b64_tr_b16 v[208:209], v245 offset:0\n"
"ds_read_b64_tr_b16 v[210:211], v245 offset:256\n"
"v_mfma_f32_16x16x32_bf16 v[104:107], v[228:231], a[72:75], v[104:107]\n"
"s_add_u32 m0, s72, 0x1dc00\n"
"v_mfma_f32_16x16x32_bf16 v[108:111], v[228:231], a[76:79], v[108:111]\n"
"buffer_load_dwordx4 v199, s[44:47], s86 offen offset:512 lds\n"
"v_mfma_f32_16x16x32_bf16 v[80:83], v[232:235], a[64:67], v[80:83]\n"
"ds_read_b64_tr_b16 v[212:213], v245 offset:8\n"
"ds_read_b64_tr_b16 v[214:215], v245 offset:264\n"
"v_mfma_f32_16x16x32_bf16 v[84:87], v[232:235], a[68:71], v[84:87]\n"
"ds_read_b64_tr_b16 v[216:217], v245 offset:512\n"
"ds_read_b64_tr_b16 v[218:219], v245 offset:768\n"
"ds_read_b64_tr_b16 v[220:221], v245 offset:520\n"
"ds_read_b64_tr_b16 v[222:223], v245 offset:776\n"
"s_add_u32 m0, s72, 0x1e400\n"
"v_mfma_f32_16x16x32_bf16 v[88:91], v[232:235], a[72:75], v[88:91]\n"
"buffer_load_dwordx4 v202, s[44:47], s86 offen offset:512 lds\n"
"v_mfma_f32_16x16x32_bf16 v[92:95], v[232:235], a[76:79], v[92:95]\n"
"v_mfma_f32_16x16x32_bf16 v[112:115], v[236:239], a[64:67], v[112:115]\n"
"s_add_u32 m0, s72, 0x1ee80\n"
"v_mfma_f32_16x16x32_bf16 v[116:119], v[236:239], a[68:71], v[116:119]\n"
"buffer_load_dwordx4 v199, s[44:47], s86 offen offset:128 lds\n"
"v_mfma_f32_16x16x32_bf16 v[120:123], v[236:239], a[72:75], v[120:123]\n"
"v_mfma_f32_16x16x32_bf16 v[124:127], v[236:239], a[76:79], v[124:127]\n"
"s_add_u32 m0, s72, 0x1f680\n"
"s_waitcnt lgkmcnt(0)\n"
"v_mfma_f32_16x16x32_bf16 v[0:3], v[208:211], a[80:83], v[0:3]\n"
"buffer_load_dwordx4 v202, s[44:47], s86 offen offset:128 lds\n"
"v_mfma_f32_16x16x32_bf16 v[4:7], v[208:211], a[84:87], v[4:7]\n"
"v_mfma_f32_16x16x32_bf16 v[8:11], v[208:211], a[88:91], v[8:11]\n"
"s_add_u32 m0, s72, 0x1fc80\n"
"v_mfma_f32_16x16x32_bf16 v[12:15], v[208:211], a[92:95], v[12:15]\n"
"buffer_load_dwordx4 v199, s[44:47], s86 offen offset:640 lds\n"
"v_mfma_f32_16x16x32_bf16 v[32:35], v[212:215], a[80:83], v[32:35]\n"
"s_waitcnt vmcnt(23)\n"
"s_barrier\n"
"ds_read_b64_tr_b16 v[224:225], v245 offset:16896\n"
"ds_read_b64_tr_b16 v[226:227], v245 offset:17152\n"
"v_mfma_f32_16x16x32_bf16 v[36:39], v[212:215], a[84:87], v[36:39]\n"
"s_add_u32 m0, s72, 0x20480\n"
"v_mfma_f32_16x16x32_bf16 v[40:43], v[212:215], a[88:91], v[40:43]\n"
"buffer_load_dwordx4 v202, s[44:47], s86 offen offset:640 lds\n"
"v_mfma_f32_16x16x32_bf16 v[44:47], v[212:215], a[92:95], v[44:47]\n"
"ds_read_b64_tr_b16 v[228:229], v245 offset:16904\n"
"ds_read_b64_tr_b16 v[230:231], v245 offset:17160\n"
"v_mfma_f32_16x16x32_bf16 v[16:19], v[216:219], a[80:83], v[16:19]\n"
"s_add_u32 m0, s72, 0x21000\n"
"v_mfma_f32_16x16x32_bf16 v[20:23], v[216:219], a[84:87], v[20:23]\n"
"buffer_load_dwordx4 v200, s[44:47], s86 offen offset:0 lds\n"
"ds_read_b64_tr_b16 v[232:233], v245 offset:17408\n"
"ds_read_b64_tr_b16 v[234:235], v245 offset:17664\n"
"ds_read_b64_tr_b16 v[236:237], v245 offset:17416\n"
"ds_read_b64_tr_b16 v[238:239], v245 offset:17672\n"
"v_mfma_f32_16x16x32_bf16 v[24:27], v[216:219], a[88:91], v[24:27]\n"
"v_mfma_f32_16x16x32_bf16 v[28:31], v[216:219], a[92:95], v[28:31]\n"
"s_add_u32 m0, s72, 0x21800\n"
"v_mfma_f32_16x16x32_bf16 v[48:51], v[220:223], a[80:83], v[48:51]\n"
"buffer_load_dwordx4 v203, s[44:47], s86 offen offset:0 lds\n"
"v_mfma_f32_16x16x32_bf16 v[52:55], v[220:223], a[84:87], v[52:55]\n"
"ds_read_b128 a[96:99], v196 offset:0\n"
"ds_read_b128 a[100:103], v196 offset:2048\n"
"v_mfma_f32_16x16x32_bf16 v[56:59], v[220:223], a[88:91], v[56:59]\n"
"ds_read_b128 a[104:107], v196 offset:4096\n"
"s_add_u32 m0, s72, 0x21e00\n"
"v_mfma_f32_16x16x32_bf16 v[60:63], v[220:223], a[92:95], v[60:63]\n"
"buffer_load_dwordx4 v200, s[44:47], s86 offen offset:512 lds\n"
"s_waitcnt lgkmcnt(3)\n"
"v_mfma_f32_16x16x32_bf16 v[64:67], v[224:227], a[80:83], v[64:67]\n"
"ds_read_b128 a[108:111], v196 offset:6144\n"
"v_mfma_f32_16x16x32_bf16 v[68:71], v[224:227], a[84:87], v[68:71]\n"
"s_add_u32 m0, s72, 0x22600\n"
"v_mfma_f32_16x16x32_bf16 v[72:75], v[224:227], a[88:91], v[72:75]\n"
"buffer_load_dwordx4 v203, s[44:47], s86 offen offset:512 lds\n"
"v_mfma_f32_16x16x32_bf16 v[76:79], v[224:227], a[92:95], v[76:79]\n"
"v_mfma_f32_16x16x32_bf16 v[96:99], v[228:231], a[80:83], v[96:99]\n"
"s_waitcnt vmcnt(24)\n"
"s_barrier\n"
"s_add_u32 m0, s72, 0x25180\n"
"v_mfma_f32_16x16x32_bf16 v[100:103], v[228:231], a[84:87], v[100:103]\n"
"buffer_load_dwordx4 v200, s[44:47], s86 offen offset:128 lds\n"
"v_mfma_f32_16x16x32_bf16 v[104:107], v[228:231], a[88:91], v[104:107]\n"
"ds_read_b64_tr_b16 v[208:209], v245 offset:8448\n"
"ds_read_b64_tr_b16 v[210:211], v245 offset:8704\n"
"v_mfma_f32_16x16x32_bf16 v[108:111], v[228:231], a[92:95], v[108:111]\n"
"s_add_u32 m0, s72, 0x25980\n"
"v_mfma_f32_16x16x32_bf16 v[80:83], v[232:235], a[80:83], v[80:83]\n"
"buffer_load_dwordx4 v203, s[44:47], s86 offen offset:128 lds\n"
"v_mfma_f32_16x16x32_bf16 v[84:87], v[232:235], a[84:87], v[84:87]\n"
"ds_read_b64_tr_b16 v[212:213], v245 offset:8456\n"
"ds_read_b64_tr_b16 v[214:215], v245 offset:8712\n"
"ds_read_b64_tr_b16 v[216:217], v245 offset:8960\n"
"ds_read_b64_tr_b16 v[218:219], v245 offset:9216\n"
"ds_read_b64_tr_b16 v[220:221], v245 offset:8968\n"
"ds_read_b64_tr_b16 v[222:223], v245 offset:9224\n"
"v_mfma_f32_16x16x32_bf16 v[88:91], v[232:235], a[88:91], v[88:91]\n"
"s_add_u32 m0, s72, 0x25f80\n"
"v_mfma_f32_16x16x32_bf16 v[92:95], v[232:235], a[92:95], v[92:95]\n"
"buffer_load_dwordx4 v200, s[44:47], s86 offen offset:640 lds\n"
"v_mfma_f32_16x16x32_bf16 v[112:115], v[236:239], a[80:83], v[112:115]\n"
"v_mfma_f32_16x16x32_bf16 v[116:119], v[236:239], a[84:87], v[116:119]\n"
"s_add_u32 m0, s72, 0x26780\n"
"v_mfma_f32_16x16x32_bf16 v[120:123], v[236:239], a[88:91], v[120:123]\n"
"buffer_load_dwordx4 v203, s[44:47], s86 offen offset:640 lds\n"
"v_mfma_f32_16x16x32_bf16 v[124:127], v[236:239], a[92:95], v[124:127]\n"
"s_waitcnt lgkmcnt(0)\n"
"v_mfma_f32_16x16x32_bf16 v[0:3], v[208:211], a[96:99], v[0:3]\n"
"s_waitcnt vmcnt(24)\n"
"s_barrier\n"
"s_add_u32 m0, s72, 0x23100\n"
"v_mfma_f32_16x16x32_bf16 v[4:7], v[208:211], a[100:103], v[4:7]\n"
"buffer_load_dwordx4 v199, s[44:47], s87 offen offset:0 lds\n"
"v_mfma_f32_16x16x32_bf16 v[8:11], v[208:211], a[104:107], v[8:11]\n"
"ds_read_b64_tr_b16 v[224:225], v244 offset:33792\n"
"ds_read_b64_tr_b16 v[226:227], v244 offset:34048\n"
"v_mfma_f32_16x16x32_bf16 v[12:15], v[208:211], a[108:111], v[12:15]\n"
"s_add_u32 m0, s72, 0x23900\n"
"v_mfma_f32_16x16x32_bf16 v[32:35], v[212:215], a[96:99], v[32:35]\n"
"buffer_load_dwordx4 v202, s[44:47], s87 offen offset:0 lds\n"
"v_mfma_f32_16x16x32_bf16 v[36:39], v[212:215], a[100:103], v[36:39]\n"
"ds_read_b64_tr_b16 v[228:229], v244 offset:33800\n"
"ds_read_b64_tr_b16 v[230:231], v244 offset:34056\n"
"v_mfma_f32_16x16x32_bf16 v[40:43], v[212:215], a[104:107], v[40:43]\n"
"s_add_u32 m0, s72, 0x23f00\n"
"v_mfma_f32_16x16x32_bf16 v[44:47], v[212:215], a[108:111], v[44:47]\n"
"buffer_load_dwordx4 v199, s[44:47], s87 offen offset:512 lds\n"
"v_mfma_f32_16x16x32_bf16 v[16:19], v[216:219], a[96:99], v[16:19]\n"
"ds_read_b64_tr_b16 v[232:233], v244 offset:34304\n"
"ds_read_b64_tr_b16 v[234:235], v244 offset:34560\n"
"v_mfma_f32_16x16x32_bf16 v[20:23], v[216:219], a[100:103], v[20:23]\n"
"ds_read_b64_tr_b16 v[236:237], v244 offset:34312\n"
"ds_read_b64_tr_b16 v[238:239], v244 offset:34568\n"
"s_add_u32 m0, s72, 0x24700\n"
"v_mfma_f32_16x16x32_bf16 v[24:27], v[216:219], a[104:107], v[24:27]\n"
"buffer_load_dwordx4 v202, s[44:47], s87 offen offset:512 lds\n"
"v_mfma_f32_16x16x32_bf16 v[28:31], v[216:219], a[108:111], v[28:31]\n"
"v_mfma_f32_16x16x32_bf16 v[48:51], v[220:223], a[96:99], v[48:51]\n"
"ds_read_b128 a[112:115], v197 offset:0\n"
"ds_read_b128 a[116:119], v197 offset:2048\n"
"v_mfma_f32_16x16x32_bf16 v[52:55], v[220:223], a[100:103], v[52:55]\n"
"ds_read_b128 a[120:123], v197 offset:4096\n"
"ds_read_b128 a[124:127], v197 offset:6144\n"
"v_mfma_f32_16x16x32_bf16 v[56:59], v[220:223], a[104:107], v[56:59]\n"
"v_mfma_f32_16x16x32_bf16 v[60:63], v[220:223], a[108:111], v[60:63]\n"
"s_waitcnt lgkmcnt(4)\n"
"v_mfma_f32_16x16x32_bf16 v[64:67], v[224:227], a[96:99], v[64:67]\n"
"v_mfma_f32_16x16x32_bf16 v[68:71], v[224:227], a[100:103], v[68:71]\n"
"v_mfma_f32_16x16x32_bf16 v[72:75], v[224:227], a[104:107], v[72:75]\n"
"s_waitcnt vmcnt(20)\n"
"s_barrier\n"
"s_add_u32 m0, s72, 0x18b80\n"
"v_mfma_f32_16x16x32_bf16 v[76:79], v[224:227], a[108:111], v[76:79]\n"
"buffer_load_dwordx4 v199, s[44:47], s87 offen offset:128 lds\n"
"v_mfma_f32_16x16x32_bf16 v[96:99], v[228:231], a[96:99], v[96:99]\n"
"ds_read_b64_tr_b16 v[208:209], v244 offset:42240\n"
"ds_read_b64_tr_b16 v[210:211], v244 offset:42496\n"
"v_mfma_f32_16x16x32_bf16 v[100:103], v[228:231], a[100:103], v[100:103]\n"
"s_add_u32 m0, s72, 0x19380\n"
"v_mfma_f32_16x16x32_bf16 v[104:107], v[228:231], a[104:107], v[104:107]\n"
"buffer_load_dwordx4 v202, s[44:47], s87 offen offset:128 lds\n"
"v_mfma_f32_16x16x32_bf16 v[108:111], v[228:231], a[108:111], v[108:111]\n"
"ds_read_b64_tr_b16 v[212:213], v244 offset:42248\n"
"ds_read_b64_tr_b16 v[214:215], v244 offset:42504\n"
"v_mfma_f32_16x16x32_bf16 v[80:83], v[232:235], a[96:99], v[80:83]\n"
"s_add_u32 m0, s72, 0x19980\n"
"v_mfma_f32_16x16x32_bf16 v[84:87], v[232:235], a[100:103], v[84:87]\n"
"buffer_load_dwordx4 v199, s[44:47], s87 offen offset:640 lds\n"
"ds_read_b64_tr_b16 v[216:217], v244 offset:42752\n"
"ds_read_b64_tr_b16 v[218:219], v244 offset:43008\n"
"ds_read_b64_tr_b16 v[220:221], v244 offset:42760\n"
"ds_read_b64_tr_b16 v[222:223], v244 offset:43016\n"
"v_mfma_f32_16x16x32_bf16 v[88:91], v[232:235], a[104:107], v[88:91]\n"
"v_mfma_f32_16x16x32_bf16 v[92:95], v[232:235], a[108:111], v[92:95]\n"
"s_add_u32 m0, s72, 0x1a180\n"
"v_mfma_f32_16x16x32_bf16 v[112:115], v[236:239], a[96:99], v[112:115]\n"
"buffer_load_dwordx4 v202, s[44:47], s87 offen offset:640 lds\n"
"v_mfma_f32_16x16x32_bf16 v[116:119], v[236:239], a[100:103], v[116:119]\n"
"ds_read_b64_tr_b16 v[224:225], v198 offset:8448\n"
"ds_read_b64_tr_b16 v[226:227], v198 offset:8704\n"
"v_mfma_f32_16x16x32_bf16 v[120:123], v[236:239], a[104:107], v[120:123]\n"
"v_mfma_f32_16x16x32_bf16 v[124:127], v[236:239], a[108:111], v[124:127]\n"
"s_waitcnt lgkmcnt(2)\n"
"v_mfma_f32_16x16x32_bf16 v[0:3], v[208:211], a[112:115], v[0:3]\n"
"ds_read_b64_tr_b16 v[228:229], v198 offset:8456\n"
"ds_read_b64_tr_b16 v[230:231], v198 offset:8712\n"
"v_mfma_f32_16x16x32_bf16 v[4:7], v[208:211], a[116:119], v[4:7]\n"
"s_waitcnt vmcnt(20)\n"
"s_barrier\n"
"s_add_u32 m0, s72, 0x1ad00\n"
"v_mfma_f32_16x16x32_bf16 v[8:11], v[208:211], a[120:123], v[8:11]\n"
"buffer_load_dwordx4 v200, s[44:47], s87 offen offset:0 lds\n"
"v_mfma_f32_16x16x32_bf16 v[12:15], v[208:211], a[124:127], v[12:15]\n"
"ds_read_b64_tr_b16 v[232:233], v198 offset:8960\n"
"ds_read_b64_tr_b16 v[234:235], v198 offset:9216\n"
"v_mfma_f32_16x16x32_bf16 v[32:35], v[212:215], a[112:115], v[32:35]\n"
"s_add_u32 m0, s72, 0x1b500\n"
"v_mfma_f32_16x16x32_bf16 v[36:39], v[212:215], a[116:119], v[36:39]\n"
"buffer_load_dwordx4 v203, s[44:47], s87 offen offset:0 lds\n"
"v_mfma_f32_16x16x32_bf16 v[40:43], v[212:215], a[120:123], v[40:43]\n"
"ds_read_b64_tr_b16 v[236:237], v198 offset:8968\n"
"ds_read_b64_tr_b16 v[238:239], v198 offset:9224\n"
"v_mfma_f32_16x16x32_bf16 v[44:47], v[212:215], a[124:127], v[44:47]\n"
"s_add_u32 m0, s72, 0x1bb00\n"
"v_mfma_f32_16x16x32_bf16 v[16:19], v[216:219], a[112:115], v[16:19]\n"
"buffer_load_dwordx4 v200, s[44:47], s87 offen offset:512 lds\n"
"v_mfma_f32_16x16x32_bf16 v[20:23], v[216:219], a[116:119], v[20:23]\n"
"v_mfma_f32_16x16x32_bf16 v[24:27], v[216:219], a[120:123], v[24:27]\n"
"s_add_u32 m0, s72, 0x1c300\n"
"v_mfma_f32_16x16x32_bf16 v[28:31], v[216:219], a[124:127], v[28:31]\n"
"buffer_load_dwordx4 v203, s[44:47], s87 offen offset:512 lds\n"
"v_mfma_f32_16x16x32_bf16 v[48:51], v[220:223], a[112:115], v[48:51]\n"
"ds_read_b128 a[128:131], v196 offset:16896\n"
"ds_read_b128 a[132:135], v196 offset:18944\n"
"v_mfma_f32_16x16x32_bf16 v[52:55], v[220:223], a[116:119], v[52:55]\n"
"v_mfma_f32_16x16x32_bf16 v[56:59], v[220:223], a[120:123], v[56:59]\n"
"v_mfma_f32_16x16x32_bf16 v[60:63], v[220:223], a[124:127], v[60:63]\n"
"ds_read_b128 a[136:139], v196 offset:20992\n"
"s_waitcnt lgkmcnt(3)\n"
"v_mfma_f32_16x16x32_bf16 v[64:67], v[224:227], a[112:115], v[64:67]\n"
"ds_read_b128 a[140:143], v196 offset:23040\n"
"v_mfma_f32_16x16x32_bf16 v[68:71], v[224:227], a[116:119], v[68:71]\n"
"v_mfma_f32_16x16x32_bf16 v[72:75], v[224:227], a[120:123], v[72:75]\n"
"ds_read_b64_tr_b16 v[208:209], v244 offset:50688\n"
"ds_read_b64_tr_b16 v[210:211], v244 offset:50944\n"
"v_mfma_f32_16x16x32_bf16 v[76:79], v[224:227], a[124:127], v[76:79]\n"
"v_mfma_f32_16x16x32_bf16 v[96:99], v[228:231], a[112:115], v[96:99]\n"
"v_mfma_f32_16x16x32_bf16 v[100:103], v[228:231], a[116:119], v[100:103]\n"
"ds_read_b64_tr_b16 v[212:213], v244 offset:50696\n"
"ds_read_b64_tr_b16 v[214:215], v244 offset:50952\n"
"v_mfma_f32_16x16x32_bf16 v[104:107], v[228:231], a[120:123], v[104:107]\n"
"v_mfma_f32_16x16x32_bf16 v[108:111], v[228:231], a[124:127], v[108:111]\n"
"v_mfma_f32_16x16x32_bf16 v[80:83], v[232:235], a[112:115], v[80:83]\n"
"ds_read_b64_tr_b16 v[216:217], v244 offset:51200\n"
"ds_read_b64_tr_b16 v[218:219], v244 offset:51456\n"
"v_mfma_f32_16x16x32_bf16 v[84:87], v[232:235], a[116:119], v[84:87]\n"
"ds_read_b64_tr_b16 v[220:221], v244 offset:51208\n"
"ds_read_b64_tr_b16 v[222:223], v244 offset:51464\n"
"v_mfma_f32_16x16x32_bf16 v[88:91], v[232:235], a[120:123], v[88:91]\n"
"v_mfma_f32_16x16x32_bf16 v[92:95], v[232:235], a[124:127], v[92:95]\n"
"v_mfma_f32_16x16x32_bf16 v[112:115], v[236:239], a[112:115], v[112:115]\n"
"v_mfma_f32_16x16x32_bf16 v[116:119], v[236:239], a[116:119], v[116:119]\n"
"v_mfma_f32_16x16x32_bf16 v[120:123], v[236:239], a[120:123], v[120:123]\n"
"v_mfma_f32_16x16x32_bf16 v[124:127], v[236:239], a[124:127], v[124:127]\n"
"s_waitcnt lgkmcnt(0)\n"
"v_mfma_f32_16x16x32_bf16 v[0:3], v[208:211], a[128:131], v[0:3]\n"
"v_mfma_f32_16x16x32_bf16 v[4:7], v[208:211], a[132:135], v[4:7]\n"
"v_mfma_f32_16x16x32_bf16 v[8:11], v[208:211], a[136:139], v[8:11]\n"
"s_waitcnt vmcnt(12)\n"
"s_barrier\n"
"s_add_u32 m0, s72, 0x1cd80\n"
"v_mfma_f32_16x16x32_bf16 v[12:15], v[208:211], a[140:143], v[12:15]\n"
"buffer_load_dwordx4 v200, s[44:47], s87 offen offset:128 lds\n"
"v_mfma_f32_16x16x32_bf16 v[32:35], v[212:215], a[128:131], v[32:35]\n"
"ds_read_b64_tr_b16 v[224:225], v244 offset:59136\n"
"ds_read_b64_tr_b16 v[226:227], v244 offset:59392\n"
"v_mfma_f32_16x16x32_bf16 v[36:39], v[212:215], a[132:135], v[36:39]\n"
"s_add_u32 m0, s72, 0x1d580\n"
"v_mfma_f32_16x16x32_bf16 v[40:43], v[212:215], a[136:139], v[40:43]\n"
"buffer_load_dwordx4 v203, s[44:47], s87 offen offset:128 lds\n"
"v_mfma_f32_16x16x32_bf16 v[44:47], v[212:215], a[140:143], v[44:47]\n"
"ds_read_b64_tr_b16 v[228:229], v244 offset:59144\n"
"ds_read_b64_tr_b16 v[230:231], v244 offset:59400\n"
"v_mfma_f32_16x16x32_bf16 v[16:19], v[216:219], a[128:131], v[16:19]\n"
"s_add_u32 m0, s72, 0x1db80\n"
"v_mfma_f32_16x16x32_bf16 v[20:23], v[216:219], a[132:135], v[20:23]\n"
"buffer_load_dwordx4 v200, s[44:47], s87 offen offset:640 lds\n"
"ds_read_b64_tr_b16 v[232:233], v244 offset:59648\n"
"ds_read_b64_tr_b16 v[234:235], v244 offset:59904\n"
"ds_read_b64_tr_b16 v[236:237], v244 offset:59656\n"
"ds_read_b64_tr_b16 v[238:239], v244 offset:59912\n"
"v_mfma_f32_16x16x32_bf16 v[24:27], v[216:219], a[136:139], v[24:27]\n"
"v_mfma_f32_16x16x32_bf16 v[28:31], v[216:219], a[140:143], v[28:31]\n"
"s_add_u32 m0, s72, 0x1e380\n"
"v_mfma_f32_16x16x32_bf16 v[48:51], v[220:223], a[128:131], v[48:51]\n"
"buffer_load_dwordx4 v203, s[44:47], s87 offen offset:640 lds\n"
"v_mfma_f32_16x16x32_bf16 v[52:55], v[220:223], a[132:135], v[52:55]\n"
"ds_read_b128 a[144:147], v197 offset:16896\n"
"ds_read_b128 a[148:151], v197 offset:18944\n"
"v_mfma_f32_16x16x32_bf16 v[56:59], v[220:223], a[136:139], v[56:59]\n"
"ds_read_b128 a[152:155], v197 offset:20992\n"
"v_mfma_f32_16x16x32_bf16 v[60:63], v[220:223], a[140:143], v[60:63]\n"
"s_waitcnt lgkmcnt(3)\n"
"v_mfma_f32_16x16x32_bf16 v[64:67], v[224:227], a[128:131], v[64:67]\n"
"ds_read_b128 a[156:159], v197 offset:23040\n"
"v_mfma_f32_16x16x32_bf16 v[68:71], v[224:227], a[132:135], v[68:71]\n"
"s_waitcnt vmcnt(12)\n"
"s_barrier\n"
"s_add_u32 m0, s72, 0x1ef00\n"
"v_mfma_f32_16x16x32_bf16 v[72:75], v[224:227], a[136:139], v[72:75]\n"
"buffer_load_dwordx4 v199, s[44:47], s88 offen offset:0 lds\n"
"v_mfma_f32_16x16x32_bf16 v[76:79], v[224:227], a[140:143], v[76:79]\n"
"ds_read_b64_tr_b16 v[208:209], v245 offset:0\n"
"ds_read_b64_tr_b16 v[210:211], v245 offset:256\n"
"v_mfma_f32_16x16x32_bf16 v[96:99], v[228:231], a[128:131], v[96:99]\n"
"s_add_u32 m0, s72, 0x1f700\n"
"v_mfma_f32_16x16x32_bf16 v[100:103], v[228:231], a[132:135], v[100:103]\n"
"buffer_load_dwordx4 v202, s[44:47], s88 offen offset:0 lds\n"
"v_mfma_f32_16x16x32_bf16 v[104:107], v[228:231], a[136:139], v[104:107]\n"
"ds_read_b64_tr_b16 v[212:213], v245 offset:8\n"
"ds_read_b64_tr_b16 v[214:215], v245 offset:264\n"
"v_mfma_f32_16x16x32_bf16 v[108:111], v[228:231], a[140:143], v[108:111]\n"
"s_add_u32 m0, s72, 0x1fd00\n"
"v_mfma_f32_16x16x32_bf16 v[80:83], v[232:235], a[128:131], v[80:83]\n"
"buffer_load_dwordx4 v199, s[44:47], s88 offen offset:512 lds\n"
"v_mfma_f32_16x16x32_bf16 v[84:87], v[232:235], a[132:135], v[84:87]\n"
"ds_read_b64_tr_b16 v[216:217], v245 offset:512\n"
"ds_read_b64_tr_b16 v[218:219], v245 offset:768\n"
"ds_read_b64_tr_b16 v[220:221], v245 offset:520\n"
"ds_read_b64_tr_b16 v[222:223], v245 offset:776\n"
"v_mfma_f32_16x16x32_bf16 v[88:91], v[232:235], a[136:139], v[88:91]\n"
"s_add_u32 m0, s72, 0x20500\n"
"v_mfma_f32_16x16x32_bf16 v[92:95], v[232:235], a[140:143], v[92:95]\n"
"buffer_load_dwordx4 v202, s[44:47], s88 offen offset:512 lds\n"
"v_mfma_f32_16x16x32_bf16 v[112:115], v[236:239], a[128:131], v[112:115]\n"
"ds_read_b64_tr_b16 v[224:225], v245 offset:16896\n"
"ds_read_b64_tr_b16 v[226:227], v245 offset:17152\n"
"v_mfma_f32_16x16x32_bf16 v[116:119], v[236:239], a[132:135], v[116:119]\n"
"v_mfma_f32_16x16x32_bf16 v[120:123], v[236:239], a[136:139], v[120:123]\n"
"v_mfma_f32_16x16x32_bf16 v[124:127], v[236:239], a[140:143], v[124:127]\n"
"s_waitcnt lgkmcnt(2)\n"
"v_mfma_f32_16x16x32_bf16 v[0:3], v[208:211], a[144:147], v[0:3]\n"
"s_waitcnt vmcnt(12)\n"
"s_barrier\n"
"s_add_u32 m0, s72, 0x21000\n"
"v_mfma_f32_16x16x32_bf16 v[4:7], v[208:211], a[148:151], v[4:7]\n"
"buffer_load_dwordx4 v200, s[44:47], s88 offen offset:0 lds\n"
"v_mfma_f32_16x16x32_bf16 v[8:11], v[208:211], a[152:155], v[8:11]\n"
"ds_read_b64_tr_b16 v[228:229], v245 offset:16904\n"
"ds_read_b64_tr_b16 v[230:231], v245 offset:17160\n"
"v_mfma_f32_16x16x32_bf16 v[12:15], v[208:211], a[156:159], v[12:15]\n"
"s_add_u32 m0, s72, 0x21800\n"
"v_mfma_f32_16x16x32_bf16 v[32:35], v[212:215], a[144:147], v[32:35]\n"
"buffer_load_dwordx4 v203, s[44:47], s88 offen offset:0 lds\n"
"v_mfma_f32_16x16x32_bf16 v[36:39], v[212:215], a[148:151], v[36:39]\n"
"ds_read_b64_tr_b16 v[232:233], v245 offset:17408\n"
"ds_read_b64_tr_b16 v[234:235], v245 offset:17664\n"
"v_mfma_f32_16x16x32_bf16 v[40:43], v[212:215], a[152:155], v[40:43]\n"
"s_add_u32 m0, s72, 0x21e00\n"
"v_mfma_f32_16x16x32_bf16 v[44:47], v[212:215], a[156:159], v[44:47]\n"
"buffer_load_dwordx4 v200, s[44:47], s88 offen offset:512 lds\n"
"v_mfma_f32_16x16x32_bf16 v[16:19], v[216:219], a[144:147], v[16:19]\n"
"ds_read_b64_tr_b16 v[236:237], v245 offset:17416\n"
"ds_read_b64_tr_b16 v[238:239], v245 offset:17672\n"
"v_mfma_f32_16x16x32_bf16 v[20:23], v[216:219], a[148:151], v[20:23]\n"
"s_add_u32 m0, s72, 0x22600\n"
"v_mfma_f32_16x16x32_bf16 v[24:27], v[216:219], a[152:155], v[24:27]\n"
"buffer_load_dwordx4 v203, s[44:47], s88 offen offset:512 lds\n"
"v_mfma_f32_16x16x32_bf16 v[28:31], v[216:219], a[156:159], v[28:31]\n"
"v_mfma_f32_16x16x32_bf16 v[48:51], v[220:223], a[144:147], v[48:51]\n"
"ds_read_b128 a[160:163], v196 offset:33792\n"
"ds_read_b128 a[164:167], v196 offset:35840\n"
"v_mfma_f32_16x16x32_bf16 v[52:55], v[220:223], a[148:151], v[52:55]\n"
"ds_read_b128 a[168:171], v196 offset:37888\n"
"ds_read_b128 a[172:175], v196 offset:39936\n"
"v_mfma_f32_16x16x32_bf16 v[56:59], v[220:223], a[152:155], v[56:59]\n"
"ds_read_b64_tr_b16 v[208:209], v245 offset:8448\n"
"ds_read_b64_tr_b16 v[210:211], v245 offset:8704\n"
"v_mfma_f32_16x16x32_bf16 v[60:63], v[220:223], a[156:159], v[60:63]\n"
"s_waitcnt lgkmcnt(6)\n"
"v_mfma_f32_16x16x32_bf16 v[64:67], v[224:227], a[144:147], v[64:67]\n"
"ds_read_b64_tr_b16 v[212:213], v245 offset:8456\n"
"ds_read_b64_tr_b16 v[214:215], v245 offset:8712\n"
"v_mfma_f32_16x16x32_bf16 v[68:71], v[224:227], a[148:151], v[68:71]\n"
"ds_read_b64_tr_b16 v[216:217], v245 offset:8960\n"
"ds_read_b64_tr_b16 v[218:219], v245 offset:9216\n"
"v_mfma_f32_16x16x32_bf16 v[72:75], v[224:227], a[152:155], v[72:75]\n"
"s_waitcnt vmcnt(12)\n"
"s_barrier\n"
"s_add_u32 m0, s72, 0x25180\n"
"v_mfma_f32_16x16x32_bf16 v[76:79], v[224:227], a[156:159], v[76:79]\n"
"buffer_load_dwordx4 v199, s[44:47], s88 offen offset:128 lds\n"
"v_mfma_f32_16x16x32_bf16 v[96:99], v[228:231], a[144:147], v[96:99]\n"
"ds_read_b64_tr_b16 v[220:221], v245 offset:8968\n"
"ds_read_b64_tr_b16 v[222:223], v245 offset:9224\n"
"v_mfma_f32_16x16x32_bf16 v[100:103], v[228:231], a[148:151], v[100:103]\n"
"s_add_u32 m0, s72, 0x25980\n"
"v_mfma_f32_16x16x32_bf16 v[104:107], v[228:231], a[152:155], v[104:107]\n"
"buffer_load_dwordx4 v202, s[44:47], s88 offen offset:128 lds\n"
"v_mfma_f32_16x16x32_bf16 v[108:111], v[228:231], a[156:159], v[108:111]\n"
"v_mfma_f32_16x16x32_bf16 v[80:83], v[232:235], a[144:147], v[80:83]\n"
"s_add_u32 m0, s72, 0x25f80\n"
"v_mfma_f32_16x16x32_bf16 v[84:87], v[232:235], a[148:151], v[84:87]\n"
"buffer_load_dwordx4 v199, s[44:47], s88 offen offset:640 lds\n"
"v_mfma_f32_16x16x32_bf16 v[88:91], v[232:235], a[152:155], v[88:91]\n"
"v_mfma_f32_16x16x32_bf16 v[92:95], v[232:235], a[156:159], v[92:95]\n"
"s_add_u32 m0, s72, 0x26780\n"
"v_mfma_f32_16x16x32_bf16 v[112:115], v[236:239], a[144:147], v[112:115]\n"
"buffer_load_dwordx4 v202, s[44:47], s88 offen offset:640 lds\n"
"v_mfma_f32_16x16x32_bf16 v[116:119], v[236:239], a[148:151], v[116:119]\n"
"ds_read_b64_tr_b16 v[224:225], v244 offset:33792\n"
"ds_read_b64_tr_b16 v[226:227], v244 offset:34048\n"
"v_mfma_f32_16x16x32_bf16 v[120:123], v[236:239], a[152:155], v[120:123]\n"
"v_mfma_f32_16x16x32_bf16 v[124:127], v[236:239], a[156:159], v[124:127]\n"
"s_waitcnt lgkmcnt(2)\n"
"v_mfma_f32_16x16x32_bf16 v[0:3], v[208:211], a[160:163], v[0:3]\n"
"ds_read_b64_tr_b16 v[228:229], v244 offset:33800\n"
"ds_read_b64_tr_b16 v[230:231], v244 offset:34056\n"
"v_mfma_f32_16x16x32_bf16 v[4:7], v[208:211], a[164:167], v[4:7]\n"
"s_barrier\n"
"s_add_u32 m0, s72, 0x23080\n"
"v_mfma_f32_16x16x32_bf16 v[8:11], v[208:211], a[168:171], v[8:11]\n"
"buffer_load_dwordx4 v200, s[44:47], s88 offen offset:128 lds\n"
"v_mfma_f32_16x16x32_bf16 v[12:15], v[208:211], a[172:175], v[12:15]\n"
"ds_read_b64_tr_b16 v[232:233], v244 offset:34304\n"
"ds_read_b64_tr_b16 v[234:235], v244 offset:34560\n"
"v_mfma_f32_16x16x32_bf16 v[32:35], v[212:215], a[160:163], v[32:35]\n"
"s_add_u32 m0, s72, 0x23880\n"
"v_mfma_f32_16x16x32_bf16 v[36:39], v[212:215], a[164:167], v[36:39]\n"
"buffer_load_dwordx4 v203, s[44:47], s88 offen offset:128 lds\n"
"v_mfma_f32_16x16x32_bf16 v[40:43], v[212:215], a[168:171], v[40:43]\n"
"ds_read_b64_tr_b16 v[236:237], v244 offset:34312\n"
"ds_read_b64_tr_b16 v[238:239], v244 offset:34568\n"
"v_mfma_f32_16x16x32_bf16 v[44:47], v[212:215], a[172:175], v[44:47]\n"
"s_add_u32 m0, s72, 0x23e80\n"
"v_mfma_f32_16x16x32_bf16 v[16:19], v[216:219], a[160:163], v[16:19]\n"
"buffer_load_dwordx4 v200, s[44:47], s88 offen offset:640 lds\n"
"v_mfma_f32_16x16x32_bf16 v[20:23], v[216:219], a[164:167], v[20:23]\n"
"v_mfma_f32_16x16x32_bf16 v[24:27], v[216:219], a[168:171], v[24:27]\n"
"s_add_u32 m0, s72, 0x24680\n"
"v_mfma_f32_16x16x32_bf16 v[28:31], v[216:219], a[172:175], v[28:31]\n"
"buffer_load_dwordx4 v203, s[44:47], s88 offen offset:640 lds\n"
"v_mfma_f32_16x16x32_bf16 v[48:51], v[220:223], a[160:163], v[48:51]\n"
"ds_read_b128 a[176:179], v197 offset:33792\n"
"ds_read_b128 a[180:183], v197 offset:35840\n"
"v_mfma_f32_16x16x32_bf16 v[52:55], v[220:223], a[164:167], v[52:55]\n"
"ds_read_b128 a[184:187], v197 offset:37888\n"
"ds_read_b128 a[188:191], v197 offset:39936\n"
"v_mfma_f32_16x16x32_bf16 v[56:59], v[220:223], a[168:171], v[56:59]\n"
"ds_read_b64_tr_b16 v[208:209], v244 offset:42240\n"
"ds_read_b64_tr_b16 v[210:211], v244 offset:42496\n"
"v_mfma_f32_16x16x32_bf16 v[60:63], v[220:223], a[172:175], v[60:63]\n"
"s_waitcnt lgkmcnt(6)\n"
"v_mfma_f32_16x16x32_bf16 v[64:67], v[224:227], a[160:163], v[64:67]\n"
"s_waitcnt vmcnt(16)\n"
"s_barrier\n"
"v_xor_b32 v199, 0x100, v199\n"
"v_xor_b32 v200, 0x100, v200\n"
"v_xor_b32 v202, 0x100, v202\n"
"v_xor_b32 v203, 0x100, v203\n"
"s_add_u32 m0, s72, 0x18c00\n"
"v_mfma_f32_16x16x32_bf16 v[68:71], v[224:227], a[164:167], v[68:71]\n"
"buffer_load_dwordx4 v199, s[44:47], s82 offen offset:0 lds\n"
"v_mfma_f32_16x16x32_bf16 v[72:75], v[224:227], a[168:171], v[72:75]\n"
"ds_read_b64_tr_b16 v[212:213], v244 offset:42248\n"
"ds_read_b64_tr_b16 v[214:215], v244 offset:42504\n"
"v_mfma_f32_16x16x32_bf16 v[76:79], v[224:227], a[172:175], v[76:79]\n"
"s_add_u32 m0, s72, 0x19400\n"
"v_mfma_f32_16x16x32_bf16 v[96:99], v[228:231], a[160:163], v[96:99]\n"
"buffer_load_dwordx4 v202, s[44:47], s82 offen offset:0 lds\n"
"v_mfma_f32_16x16x32_bf16 v[100:103], v[228:231], a[164:167], v[100:103]\n"
"ds_read_b64_tr_b16 v[216:217], v244 offset:42752\n"
"ds_read_b64_tr_b16 v[218:219], v244 offset:43008\n"
"v_mfma_f32_16x16x32_bf16 v[104:107], v[228:231], a[168:171], v[104:107]\n"
"s_add_u32 m0, s72, 0x19a00\n"
"v_mfma_f32_16x16x32_bf16 v[108:111], v[228:231], a[172:175], v[108:111]\n"
"buffer_load_dwordx4 v199, s[44:47], s82 offen offset:512 lds\n"
"v_mfma_f32_16x16x32_bf16 v[80:83], v[232:235], a[160:163], v[80:83]\n"
"ds_read_b64_tr_b16 v[220:221], v244 offset:42760\n"
"ds_read_b64_tr_b16 v[222:223], v244 offset:43016\n"
"v_mfma_f32_16x16x32_bf16 v[84:87], v[232:235], a[164:167], v[84:87]\n"
"s_add_u32 m0, s72, 0x1a200\n"
"v_mfma_f32_16x16x32_bf16 v[88:91], v[232:235], a[168:171], v[88:91]\n"
"buffer_load_dwordx4 v202, s[44:47], s82 offen offset:512 lds\n"
"v_mfma_f32_16x16x32_bf16 v[92:95], v[232:235], a[172:175], v[92:95]\n"
"v_mfma_f32_16x16x32_bf16 v[112:115], v[236:239], a[160:163], v[112:115]\n"
"ds_read_b64_tr_b16 v[224:225], v244 offset:50688\n"
"ds_read_b64_tr_b16 v[226:227], v244 offset:50944\n"
"v_mfma_f32_16x16x32_bf16 v[116:119], v[236:239], a[164:167], v[116:119]\n"
"v_mfma_f32_16x16x32_bf16 v[120:123], v[236:239], a[168:171], v[120:123]\n"
"v_mfma_f32_16x16x32_bf16 v[124:127], v[236:239], a[172:175], v[124:127]\n"
"s_waitcnt lgkmcnt(2)\n"
"v_mfma_f32_16x16x32_bf16 v[0:3], v[208:211], a[176:179], v[0:3]\n"
"ds_read_b64_tr_b16 v[228:229], v244 offset:50696\n"
"ds_read_b64_tr_b16 v[230:231], v244 offset:50952\n"
"v_mfma_f32_16x16x32_bf16 v[4:7], v[208:211], a[180:183], v[4:7]\n"
"ds_read_b64_tr_b16 v[232:233], v244 offset:51200\n"
"ds_read_b64_tr_b16 v[234:235], v244 offset:51456\n"
"v_mfma_f32_16x16x32_bf16 v[8:11], v[208:211], a[184:187], v[8:11]\n"
"s_waitcnt vmcnt(16)\n"
"s_barrier\n"
"s_add_u32 m0, s72, 0x1ad00\n"
"v_mfma_f32_16x16x32_bf16 v[12:15], v[208:211], a[188:191], v[12:15]\n"
"buffer_load_dwordx4 v200, s[44:47], s82 offen offset:0 lds\n"
"v_mfma_f32_16x16x32_bf16 v[32:35], v[212:215], a[176:179], v[32:35]\n"
"ds_read_b64_tr_b16 v[236:237], v244 offset:51208\n"
"ds_read_b64_tr_b16 v[238:239], v244 offset:51464\n"
"v_mfma_f32_16x16x32_bf16 v[36:39], v[212:215], a[180:183], v[36:39]\n"
"s_add_u32 m0, s72, 0x1b500\n"
"v_mfma_f32_16x16x32_bf16 v[40:43], v[212:215], a[184:187], v[40:43]\n"
"buffer_load_dwordx4 v203, s[44:47], s82 offen offset:0 lds\n"
"v_mfma_f32_16x16x32_bf16 v[44:47], v[212:215], a[188:191], v[44:47]\n"
"v_mfma_f32_16x16x32_bf16 v[16:19], v[216:219], a[176:179], v[16:19]\n"
"s_add_u32 m0, s72, 0x1bb00\n"
"v_mfma_f32_16x16x32_bf16 v[20:23], v[216:219], a[180:183], v[20:23]\n"
"buffer_load_dwordx4 v200, s[44:47], s82 offen offset:512 lds\n"
"v_mfma_f32_16x16x32_bf16 v[24:27], v[216:219], a[184:187], v[24:27]\n"
"v_mfma_f32_16x16x32_bf16 v[28:31], v[216:219], a[188:191], v[28:31]\n"
"s_add_u32 m0, s72, 0x1c300\n"
"v_mfma_f32_16x16x32_bf16 v[48:51], v[220:223], a[176:179], v[48:51]\n"
"buffer_load_dwordx4 v203, s[44:47], s82 offen offset:512 lds\n"
"v_mfma_f32_16x16x32_bf16 v[52:55], v[220:223], a[180:183], v[52:55]\n"
"ds_read_b128 a[192:195], v196 offset:0\n"
"ds_read_b128 a[196:199], v196 offset:2048\n"
"v_mfma_f32_16x16x32_bf16 v[56:59], v[220:223], a[184:187], v[56:59]\n"
"ds_read_b128 a[200:203], v196 offset:4096\n"
"v_mfma_f32_16x16x32_bf16 v[60:63], v[220:223], a[188:191], v[60:63]\n"
"s_waitcnt lgkmcnt(3)\n"
"v_mfma_f32_16x16x32_bf16 v[64:67], v[224:227], a[176:179], v[64:67]\n"
"ds_read_b128 a[204:207], v196 offset:6144\n"
"v_mfma_f32_16x16x32_bf16 v[68:71], v[224:227], a[180:183], v[68:71]\n"
"s_waitcnt vmcnt(16)\n"
"s_barrier\n"
"s_add_u32 m0, s72, 0x1ce00\n"
"v_mfma_f32_16x16x32_bf16 v[72:75], v[224:227], a[184:187], v[72:75]\n"
"buffer_load_dwordx4 v199, s[44:47], s83 offen offset:0 lds\n"
"v_mfma_f32_16x16x32_bf16 v[76:79], v[224:227], a[188:191], v[76:79]\n"
"ds_read_b64_tr_b16 v[208:209], v244 offset:59136\n"
"ds_read_b64_tr_b16 v[210:211], v244 offset:59392\n"
"v_mfma_f32_16x16x32_bf16 v[96:99], v[228:231], a[176:179], v[96:99]\n"
"s_add_u32 m0, s72, 0x1d600\n"
"v_mfma_f32_16x16x32_bf16 v[100:103], v[228:231], a[180:183], v[100:103]\n"
"buffer_load_dwordx4 v202, s[44:47], s83 offen offset:0 lds\n"
"v_mfma_f32_16x16x32_bf16 v[104:107], v[228:231], a[184:187], v[104:107]\n"
"ds_read_b64_tr_b16 v[212:213], v244 offset:59144\n"
"ds_read_b64_tr_b16 v[214:215], v244 offset:59400\n"
"v_mfma_f32_16x16x32_bf16 v[108:111], v[228:231], a[188:191], v[108:111]\n"
"s_add_u32 m0, s72, 0x1dc00\n"
"v_mfma_f32_16x16x32_bf16 v[80:83], v[232:235], a[176:179], v[80:83]\n"
"buffer_load_dwordx4 v199, s[44:47], s83 offen offset:512 lds\n"
"v_mfma_f32_16x16x32_bf16 v[84:87], v[232:235], a[180:183], v[84:87]\n"
"ds_read_b64_tr_b16 v[216:217], v244 offset:59648\n"
"ds_read_b64_tr_b16 v[218:219], v244 offset:59904\n"
"ds_read_b64_tr_b16 v[220:221], v244 offset:59656\n"
"ds_read_b64_tr_b16 v[222:223], v244 offset:59912\n"
"v_mfma_f32_16x16x32_bf16 v[88:91], v[232:235], a[184:187], v[88:91]\n"
"s_add_u32 m0, s72, 0x1e400\n"
"v_mfma_f32_16x16x32_bf16 v[92:95], v[232:235], a[188:191], v[92:95]\n"
"buffer_load_dwordx4 v202, s[44:47], s83 offen offset:512 lds\n"
"v_mfma_f32_16x16x32_bf16 v[112:115], v[236:239], a[176:179], v[112:115]\n"
"ds_read_b128 a[208:211], v197 offset:0\n"
"ds_read_b128 a[212:215], v197 offset:2048\n"
"v_mfma_f32_16x16x32_bf16 v[116:119], v[236:239], a[180:183], v[116:119]\n"
"ds_read_b128 a[216:219], v197 offset:4096\n"
"v_mfma_f32_16x16x32_bf16 v[120:123], v[236:239], a[184:187], v[120:123]\n"
"v_mfma_f32_16x16x32_bf16 v[124:127], v[236:239], a[188:191], v[124:127]\n"
"s_waitcnt lgkmcnt(3)\n"
"v_mfma_f32_16x16x32_bf16 v[0:3], v[208:211], a[192:195], v[0:3]\n"
"s_waitcnt vmcnt(16)\n"
"s_barrier\n"
"s_add_u32 m0, s72, 0x1ef00\n"
"v_mfma_f32_16x16x32_bf16 v[4:7], v[208:211], a[196:199], v[4:7]\n"
"buffer_load_dwordx4 v200, s[44:47], s83 offen offset:0 lds\n"
"v_mfma_f32_16x16x32_bf16 v[8:11], v[208:211], a[200:203], v[8:11]\n"
"ds_read_b128 a[220:223], v197 offset:6144\n"
"v_mfma_f32_16x16x32_bf16 v[12:15], v[208:211], a[204:207], v[12:15]\n"
"s_add_u32 m0, s72, 0x1f700\n"
"v_mfma_f32_16x16x32_bf16 v[32:35], v[212:215], a[192:195], v[32:35]\n"
"buffer_load_dwordx4 v203, s[44:47], s83 offen offset:0 lds\n"
"v_mfma_f32_16x16x32_bf16 v[36:39], v[212:215], a[196:199], v[36:39]\n"
"ds_read_b64_tr_b16 v[224:225], v245 offset:0\n"
"ds_read_b64_tr_b16 v[226:227], v245 offset:256\n"
"v_mfma_f32_16x16x32_bf16 v[40:43], v[212:215], a[200:203], v[40:43]\n"
"s_add_u32 m0, s72, 0x1fd00\n"
"v_mfma_f32_16x16x32_bf16 v[44:47], v[212:215], a[204:207], v[44:47]\n"
"buffer_load_dwordx4 v200, s[44:47], s83 offen offset:512 lds\n"
"v_mfma_f32_16x16x32_bf16 v[16:19], v[216:219], a[192:195], v[16:19]\n"
"ds_read_b64_tr_b16 v[228:229], v245 offset:8\n"
"ds_read_b64_tr_b16 v[230:231], v245 offset:264\n"
"v_mfma_f32_16x16x32_bf16 v[20:23], v[216:219], a[196:199], v[20:23]\n"
"ds_read_b64_tr_b16 v[232:233], v245 offset:512\n"
"ds_read_b64_tr_b16 v[234:235], v245 offset:768\n"
"ds_read_b64_tr_b16 v[236:237], v245 offset:520\n"
"ds_read_b64_tr_b16 v[238:239], v245 offset:776\n"
"s_add_u32 m0, s72, 0x20500\n"
"v_mfma_f32_16x16x32_bf16 v[24:27], v[216:219], a[200:203], v[24:27]\n"
"buffer_load_dwordx4 v203, s[44:47], s83 offen offset:512 lds\n"
"v_mfma_f32_16x16x32_bf16 v[28:31], v[216:219], a[204:207], v[28:31]\n"
"v_mfma_f32_16x16x32_bf16 v[48:51], v[220:223], a[192:195], v[48:51]\n"
"ds_read_b64_tr_b16 v[208:209], v245 offset:16896\n"
"ds_read_b64_tr_b16 v[210:211], v245 offset:17152\n"
"v_mfma_f32_16x16x32_bf16 v[52:55], v[220:223], a[196:199], v[52:55]\n"
"v_mfma_f32_16x16x32_bf16 v[56:59], v[220:223], a[200:203], v[56:59]\n"
"v_mfma_f32_16x16x32_bf16 v[60:63], v[220:223], a[204:207], v[60:63]\n"
"s_waitcnt lgkmcnt(2)\n"
"v_mfma_f32_16x16x32_bf16 v[0:3], v[224:227], a[208:211], v[0:3]\n"
"ds_read_b64_tr_b16 v[212:213], v245 offset:16904\n"
"ds_read_b64_tr_b16 v[214:215], v245 offset:17160\n"
"v_mfma_f32_16x16x32_bf16 v[4:7], v[224:227], a[212:215], v[4:7]\n"
"ds_read_b64_tr_b16 v[216:217], v245 offset:17408\n"
"ds_read_b64_tr_b16 v[218:219], v245 offset:17664\n"
"v_mfma_f32_16x16x32_bf16 v[8:11], v[224:227], a[216:219], v[8:11]\n"
"s_waitcnt vmcnt(16)\n"
"s_barrier\n"
"s_add_u32 m0, s72, 0x21000\n"
"v_mfma_f32_16x16x32_bf16 v[12:15], v[224:227], a[220:223], v[12:15]\n"
"buffer_load_dwordx4 v199, s[44:47], s84 offen offset:0 lds\n"
"v_mfma_f32_16x16x32_bf16 v[32:35], v[228:231], a[208:211], v[32:35]\n"
"ds_read_b64_tr_b16 v[220:221], v245 offset:17416\n"
"ds_read_b64_tr_b16 v[222:223], v245 offset:17672\n"
"v_mfma_f32_16x16x32_bf16 v[36:39], v[228:231], a[212:215], v[36:39]\n"
"v_mul_f32 v0, s95, v0\n"
"s_add_u32 m0, s72, 0x21800\n"
"v_mfma_f32_16x16x32_bf16 v[40:43], v[228:231], a[216:219], v[40:43]\n"
"buffer_load_dwordx4 v202, s[44:47], s84 offen offset:0 lds\n"
"v_mfma_f32_16x16x32_bf16 v[44:47], v[228:231], a[220:223], v[44:47]\n"
"v_mul_f32 v1, s95, v1\n"
"v_mul_f32 v2, s95, v2\n"
"v_mfma_f32_16x16x32_bf16 v[16:19], v[232:235], a[208:211], v[16:19]\n"
"v_mul_f32 v3, s95, v3\n"
"s_add_u32 m0, s72, 0x21e00\n"
"v_mfma_f32_16x16x32_bf16 v[20:23], v[232:235], a[212:215], v[20:23]\n"
"buffer_load_dwordx4 v199, s[44:47], s84 offen offset:512 lds\n"
"v_mfma_f32_16x16x32_bf16 v[24:27], v[232:235], a[216:219], v[24:27]\n"
"v_mul_f32 v32, s95, v32\n"
"v_mul_f32 v33, s95, v33\n"
"v_mfma_f32_16x16x32_bf16 v[28:31], v[232:235], a[220:223], v[28:31]\n"
"v_mul_f32 v34, s95, v34\n"
"s_add_u32 m0, s72, 0x22600\n"
"v_mfma_f32_16x16x32_bf16 v[48:51], v[236:239], a[208:211], v[48:51]\n"
"buffer_load_dwordx4 v202, s[44:47], s84 offen offset:512 lds\n"
"v_mfma_f32_16x16x32_bf16 v[52:55], v[236:239], a[212:215], v[52:55]\n"
"ds_read_b64_tr_b16 v[224:225], v245 offset:8448\n"
"ds_read_b64_tr_b16 v[226:227], v245 offset:8704\n"
"v_mfma_f32_16x16x32_bf16 v[56:59], v[236:239], a[216:219], v[56:59]\n"
"v_mul_f32 v35, s95, v35\n"
"v_mfma_f32_16x16x32_bf16 v[60:63], v[236:239], a[220:223], v[60:63]\n"
"s_waitcnt lgkmcnt(2)\n"
"v_mfma_f32_16x16x32_bf16 v[64:67], v[208:211], a[192:195], v[64:67]\n"
"ds_read_b64_tr_b16 v[228:229], v245 offset:8456\n"
"ds_read_b64_tr_b16 v[230:231], v245 offset:8712\n"
"v_mfma_f32_16x16x32_bf16 v[68:71], v[208:211], a[196:199], v[68:71]\n"
"s_waitcnt vmcnt(16)\n"
"s_barrier\n"
"s_add_u32 m0, s72, 0x25200\n"
"v_mfma_f32_16x16x32_bf16 v[72:75], v[208:211], a[200:203], v[72:75]\n"
"buffer_load_dwordx4 v200, s[44:47], s84 offen offset:0 lds\n"
"v_mfma_f32_16x16x32_bf16 v[76:79], v[208:211], a[204:207], v[76:79]\n"
"ds_read_b64_tr_b16 v[232:233], v245 offset:8960\n"
"ds_read_b64_tr_b16 v[234:235], v245 offset:9216\n"
"v_mfma_f32_16x16x32_bf16 v[96:99], v[212:215], a[192:195], v[96:99]\n"
"v_cvt_pk_bf16_f32 v0, v0, v1\n"
"s_add_u32 m0, s72, 0x25a00\n"
"v_mfma_f32_16x16x32_bf16 v[100:103], v[212:215], a[196:199], v[100:103]\n"
"buffer_load_dwordx4 v203, s[44:47], s84 offen offset:0 lds\n"
"v_mfma_f32_16x16x32_bf16 v[104:107], v[212:215], a[200:203], v[104:107]\n"
"ds_read_b64_tr_b16 v[236:237], v245 offset:8968\n"
"ds_read_b64_tr_b16 v[238:239], v245 offset:9224\n"
"v_mfma_f32_16x16x32_bf16 v[108:111], v[212:215], a[204:207], v[108:111]\n"
"v_cvt_pk_bf16_f32 v1, v2, v3\n"
"s_add_u32 m0, s72, 0x26000\n"
"v_mfma_f32_16x16x32_bf16 v[80:83], v[216:219], a[192:195], v[80:83]\n"
"buffer_load_dwordx4 v200, s[44:47], s84 offen offset:512 lds\n"
"v_mfma_f32_16x16x32_bf16 v[84:87], v[216:219], a[196:199], v[84:87]\n"
"v_cvt_pk_bf16_f32 v2, v32, v33\n"
"v_cvt_pk_bf16_f32 v3, v34, v35\n"
"v_mfma_f32_16x16x32_bf16 v[88:91], v[216:219], a[200:203], v[88:91]\n"
"v_mul_f32 v4, s95, v4\n"
"s_add_u32 m0, s72, 0x26800\n"
"v_mfma_f32_16x16x32_bf16 v[92:95], v[216:219], a[204:207], v[92:95]\n"
"buffer_load_dwordx4 v203, s[44:47], s84 offen offset:512 lds\n"
"v_mfma_f32_16x16x32_bf16 v[112:115], v[220:223], a[192:195], v[112:115]\n"
"ds_read_b64_tr_b16 v[208:209], v244 offset:33792\n"
"ds_read_b64_tr_b16 v[210:211], v244 offset:34048\n"
"v_mfma_f32_16x16x32_bf16 v[116:119], v[220:223], a[196:199], v[116:119]\n"
"ds_read_b64_tr_b16 v[212:213], v244 offset:33800\n"
"ds_read_b64_tr_b16 v[214:215], v244 offset:34056\n"
"v_mfma_f32_16x16x32_bf16 v[120:123], v[220:223], a[200:203], v[120:123]\n"
"ds_read_b64_tr_b16 v[216:217], v244 offset:34304\n"
"ds_read_b64_tr_b16 v[218:219], v244 offset:34560\n"
"v_mfma_f32_16x16x32_bf16 v[124:127], v[220:223], a[204:207], v[124:127]\n"
"v_mul_f32 v5, s95, v5\n"
"s_waitcnt lgkmcnt(6)\n"
"v_mfma_f32_16x16x32_bf16 v[64:67], v[224:227], a[208:211], v[64:67]\n"
"s_waitcnt vmcnt(16)\n"
"s_barrier\n"
"s_add_u32 m0, s72, 0x23100\n"
"v_mfma_f32_16x16x32_bf16 v[68:71], v[224:227], a[212:215], v[68:71]\n"
"buffer_load_dwordx4 v199, s[44:47], s85 offen offset:0 lds\n"
"v_mfma_f32_16x16x32_bf16 v[72:75], v[224:227], a[216:219], v[72:75]\n"
"ds_read_b64_tr_b16 v[220:221], v244 offset:34312\n"
"ds_read_b64_tr_b16 v[222:223], v244 offset:34568\n"
"v_mfma_f32_16x16x32_bf16 v[76:79], v[224:227], a[220:223], v[76:79]\n"
"v_mul_f32 v6, s95, v6\n"
"s_add_u32 m0, s72, 0x23900\n"
"v_mfma_f32_16x16x32_bf16 v[96:99], v[228:231], a[208:211], v[96:99]\n"
"buffer_load_dwordx4 v202, s[44:47], s85 offen offset:0 lds\n"
"v_mfma_f32_16x16x32_bf16 v[100:103], v[228:231], a[212:215], v[100:103]\n"
"v_mul_f32 v7, s95, v7\n"
"v_mul_f32 v36, s95, v36\n"
"v_mfma_f32_16x16x32_bf16 v[104:107], v[228:231], a[216:219], v[104:107]\n"
"v_mul_f32 v37, s95, v37\n"
"s_add_u32 m0, s72, 0x23f00\n"
"v_mfma_f32_16x16x32_bf16 v[108:111], v[228:231], a[220:223], v[108:111]\n"
"buffer_load_dwordx4 v199, s[44:47], s85 offen offset:512 lds\n"
"v_mfma_f32_16x16x32_bf16 v[80:83], v[232:235], a[208:211], v[80:83]\n"
"v_mul_f32 v38, s95, v38\n"
"v_mul_f32 v39, s95, v39\n"
"v_mfma_f32_16x16x32_bf16 v[84:87], v[232:235], a[212:215], v[84:87]\n"
"v_cvt_pk_bf16_f32 v4, v4, v5\n"
"s_add_u32 m0, s72, 0x24700\n"
"v_mfma_f32_16x16x32_bf16 v[88:91], v[232:235], a[216:219], v[88:91]\n"
"buffer_load_dwordx4 v202, s[44:47], s85 offen offset:512 lds\n"
"v_mfma_f32_16x16x32_bf16 v[92:95], v[232:235], a[220:223], v[92:95]\n"
"v_cvt_pk_bf16_f32 v5, v6, v7\n"
"v_cvt_pk_bf16_f32 v6, v36, v37\n"
"v_mfma_f32_16x16x32_bf16 v[112:115], v[236:239], a[208:211], v[112:115]\n"
"ds_read_b64_tr_b16 v[224:225], v244 offset:42240\n"
"ds_read_b64_tr_b16 v[226:227], v244 offset:42496\n"
"v_mfma_f32_16x16x32_bf16 v[116:119], v[236:239], a[212:215], v[116:119]\n"
"ds_read_b64_tr_b16 v[228:229], v244 offset:42248\n"
"ds_read_b64_tr_b16 v[230:231], v244 offset:42504\n"
"v_mfma_f32_16x16x32_bf16 v[120:123], v[236:239], a[216:219], v[120:123]\n"
"ds_read_b64_tr_b16 v[232:233], v244 offset:42752\n"
"ds_read_b64_tr_b16 v[234:235], v244 offset:43008\n"
"v_mfma_f32_16x16x32_bf16 v[124:127], v[236:239], a[220:223], v[124:127]\n"
"v_cvt_pk_bf16_f32 v7, v38, v39\n"
"s_waitcnt lgkmcnt(6)\n"
"v_mfma_f32_16x16x32_bf16 v[128:131], v[208:211], a[0:3], 0\n"
"ds_read_b64_tr_b16 v[236:237], v244 offset:42760\n"
"ds_read_b64_tr_b16 v[238:239], v244 offset:43016\n"
"v_mfma_f32_16x16x32_bf16 v[132:135], v[208:211], a[4:7], 0\n"
"v_mul_f32 v8, s95, v8\n"
"v_mul_f32 v9, s95, v9\n"
"v_mfma_f32_16x16x32_bf16 v[136:139], v[208:211], a[8:11], 0\n"
"s_waitcnt vmcnt(16)\n"
"s_barrier\n"
"s_add_u32 m0, s72, 0x18c00\n"
"v_mfma_f32_16x16x32_bf16 v[140:143], v[208:211], a[12:15], 0\n"
"buffer_load_dwordx4 v200, s[44:47], s85 offen offset:0 lds\n"
"v_mfma_f32_16x16x32_bf16 v[160:163], v[212:215], a[0:3], 0\n"
"v_mul_f32 v10, s95, v10\n"
"v_mul_f32 v11, s95, v11\n"
"v_mfma_f32_16x16x32_bf16 v[164:167], v[212:215], a[4:7], 0\n"
"v_mul_f32 v40, s95, v40\n"
"s_add_u32 m0, s72, 0x19400\n"
"v_mfma_f32_16x16x32_bf16 v[168:171], v[212:215], a[8:11], 0\n"
"buffer_load_dwordx4 v203, s[44:47], s85 offen offset:0 lds\n"
"v_mfma_f32_16x16x32_bf16 v[172:175], v[212:215], a[12:15], 0\n"
"v_mul_f32 v41, s95, v41\n"
"v_mul_f32 v42, s95, v42\n"
"v_mfma_f32_16x16x32_bf16 v[144:147], v[216:219], a[0:3], 0\n"
"v_mul_f32 v43, s95, v43\n"
"s_add_u32 m0, s72, 0x19a00\n"
"v_mfma_f32_16x16x32_bf16 v[148:151], v[216:219], a[4:7], 0\n"
"buffer_load_dwordx4 v200, s[44:47], s85 offen offset:512 lds\n"
"v_mfma_f32_16x16x32_bf16 v[152:155], v[216:219], a[8:11], 0\n"
"v_cvt_pk_bf16_f32 v8, v8, v9\n"
"v_cvt_pk_bf16_f32 v9, v10, v11\n"
"v_mfma_f32_16x16x32_bf16 v[156:159], v[216:219], a[12:15], 0\n"
"v_cvt_pk_bf16_f32 v10, v40, v41\n"
"s_add_u32 m0, s72, 0x1a200\n"
"v_mfma_f32_16x16x32_bf16 v[176:179], v[220:223], a[0:3], 0\n"
"buffer_load_dwordx4 v203, s[44:47], s85 offen offset:512 lds\n"
"v_mfma_f32_16x16x32_bf16 v[180:183], v[220:223], a[4:7], 0\n"
"ds_read_b64_tr_b16 v[208:209], v244 offset:50688\n"
"ds_read_b64_tr_b16 v[210:211], v244 offset:50944\n"
"v_mfma_f32_16x16x32_bf16 v[184:187], v[220:223], a[8:11], 0\n"
"v_cvt_pk_bf16_f32 v11, v42, v43\n"
"v_mfma_f32_16x16x32_bf16 v[188:191], v[220:223], a[12:15], 0\n"
"s_waitcnt lgkmcnt(2)\n"
"v_mfma_f32_16x16x32_bf16 v[128:131], v[224:227], a[16:19], v[128:131]\n"
"ds_read_b64_tr_b16 v[212:213], v244 offset:50696\n"
"ds_read_b64_tr_b16 v[214:215], v244 offset:50952\n"
"v_mfma_f32_16x16x32_bf16 v[132:135], v[224:227], a[20:23], v[132:135]\n"
"s_waitcnt vmcnt(16)\n"
"s_barrier\n"
"s_add_u32 m0, s72, 0x1ad00\n"
"v_mfma_f32_16x16x32_bf16 v[136:139], v[224:227], a[24:27], v[136:139]\n"
"buffer_load_dwordx4 v199, s[44:47], s86 offen offset:0 lds\n"
"v_mfma_f32_16x16x32_bf16 v[140:143], v[224:227], a[28:31], v[140:143]\n"
"ds_read_b64_tr_b16 v[216:217], v244 offset:51200\n"
"ds_read_b64_tr_b16 v[218:219], v244 offset:51456\n"
"v_mfma_f32_16x16x32_bf16 v[160:163], v[228:231], a[16:19], v[160:163]\n"
"v_mul_f32 v12, s95, v12\n"
"s_add_u32 m0, s72, 0x1b500\n"
"v_mfma_f32_16x16x32_bf16 v[164:167], v[228:231], a[20:23], v[164:167]\n"
"buffer_load_dwordx4 v202, s[44:47], s86 offen offset:0 lds\n"
"v_mfma_f32_16x16x32_bf16 v[168:171], v[228:231], a[24:27], v[168:171]\n"
"ds_read_b64_tr_b16 v[220:221], v244 offset:51208\n"
"ds_read_b64_tr_b16 v[222:223], v244 offset:51464\n"
"v_mfma_f32_16x16x32_bf16 v[172:175], v[228:231], a[28:31], v[172:175]\n"
"v_mul_f32 v13, s95, v13\n"
"s_add_u32 m0, s72, 0x1bb00\n"
"v_mfma_f32_16x16x32_bf16 v[144:147], v[232:235], a[16:19], v[144:147]\n"
"buffer_load_dwordx4 v199, s[44:47], s86 offen offset:512 lds\n"
"v_mfma_f32_16x16x32_bf16 v[148:151], v[232:235], a[20:23], v[148:151]\n"
"v_mul_f32 v14, s95, v14\n"
"v_mul_f32 v15, s95, v15\n"
"v_mfma_f32_16x16x32_bf16 v[152:155], v[232:235], a[24:27], v[152:155]\n"
"v_mul_f32 v44, s95, v44\n"
"s_add_u32 m0, s72, 0x1c300\n"
"v_mfma_f32_16x16x32_bf16 v[156:159], v[232:235], a[28:31], v[156:159]\n"
"buffer_load_dwordx4 v202, s[44:47], s86 offen offset:512 lds\n"
"v_mfma_f32_16x16x32_bf16 v[176:179], v[236:239], a[16:19], v[176:179]\n"
"ds_read_b64_tr_b16 v[224:225], v244 offset:59136\n"
"ds_read_b64_tr_b16 v[226:227], v244 offset:59392\n"
"v_mfma_f32_16x16x32_bf16 v[180:183], v[236:239], a[20:23], v[180:183]\n"
"ds_read_b64_tr_b16 v[228:229], v244 offset:59144\n"
"ds_read_b64_tr_b16 v[230:231], v244 offset:59400\n"
"v_mfma_f32_16x16x32_bf16 v[184:187], v[236:239], a[24:27], v[184:187]\n"
"ds_read_b64_tr_b16 v[232:233], v244 offset:59648\n"
"ds_read_b64_tr_b16 v[234:235], v244 offset:59904\n"
"v_mfma_f32_16x16x32_bf16 v[188:191], v[236:239], a[28:31], v[188:191]\n"
"v_mul_f32 v45, s95, v45\n"
"s_waitcnt lgkmcnt(6)\n"
"v_mfma_f32_16x16x32_bf16 v[128:131], v[208:211], a[32:35], v[128:131]\n"
"s_waitcnt vmcnt(16)\n"
"s_barrier\n"
"s_add_u32 m0, s72, 0x1ce00\n"
"v_mfma_f32_16x16x32_bf16 v[132:135], v[208:211], a[36:39], v[132:135]\n"
"buffer_load_dwordx4 v200, s[44:47], s86 offen offset:0 lds\n"
"v_mfma_f32_16x16x32_bf16 v[136:139], v[208:211], a[40:43], v[136:139]\n"
"ds_read_b64_tr_b16 v[236:237], v244 offset:59656\n"
"ds_read_b64_tr_b16 v[238:239], v244 offset:59912\n"
"v_mfma_f32_16x16x32_bf16 v[140:143], v[208:211], a[44:47], v[140:143]\n"
"v_mul_f32 v46, s95, v46\n"
"s_add_u32 m0, s72, 0x1d600\n"
"v_mfma_f32_16x16x32_bf16 v[160:163], v[212:215], a[32:35], v[160:163]\n"
"buffer_load_dwordx4 v203, s[44:47], s86 offen offset:0 lds\n"
"v_mfma_f32_16x16x32_bf16 v[164:167], v[212:215], a[36:39], v[164:167]\n"
"v_mul_f32 v47, s95, v47\n"
"v_cvt_pk_bf16_f32 v12, v12, v13\n"
"v_mfma_f32_16x16x32_bf16 v[168:171], v[212:215], a[40:43], v[168:171]\n"
"v_cvt_pk_bf16_f32 v13, v14, v15\n"
"s_add_u32 m0, s72, 0x1dc00\n"
"v_mfma_f32_16x16x32_bf16 v[172:175], v[212:215], a[44:47], v[172:175]\n"
"buffer_load_dwordx4 v200, s[44:47], s86 offen offset:512 lds\n"
"v_mfma_f32_16x16x32_bf16 v[144:147], v[216:219], a[32:35], v[144:147]\n"
"v_cvt_pk_bf16_f32 v14, v44, v45\n"
"v_cvt_pk_bf16_f32 v15, v46, v47\n"
"v_mfma_f32_16x16x32_bf16 v[148:151], v[216:219], a[36:39], v[148:151]\n"
"v_mul_f32 v16, s95, v16\n"
"s_add_u32 m0, s72, 0x1e400\n"
"v_mfma_f32_16x16x32_bf16 v[152:155], v[216:219], a[40:43], v[152:155]\n"
"buffer_load_dwordx4 v203, s[44:47], s86 offen offset:512 lds\n"
"v_mfma_f32_16x16x32_bf16 v[156:159], v[216:219], a[44:47], v[156:159]\n"
"v_mul_f32 v17, s95, v17\n"
"v_mul_f32 v18, s95, v18\n"
"v_mfma_f32_16x16x32_bf16 v[176:179], v[220:223], a[32:35], v[176:179]\n"
"ds_read_b64_tr_b16 v[208:209], v245 offset:0\n"
"ds_read_b64_tr_b16 v[210:211], v245 offset:256\n"
"v_mfma_f32_16x16x32_bf16 v[180:183], v[220:223], a[36:39], v[180:183]\n"
"ds_read_b64_tr_b16 v[212:213], v245 offset:8\n"
"ds_read_b64_tr_b16 v[214:215], v245 offset:264\n"
"v_mfma_f32_16x16x32_bf16 v[184:187], v[220:223], a[40:43], v[184:187]\n"
"ds_read_b64_tr_b16 v[216:217], v245 offset:512\n"
"ds_read_b64_tr_b16 v[218:219], v245 offset:768\n"
"v_mfma_f32_16x16x32_bf16 v[188:191], v[220:223], a[44:47], v[188:191]\n"
"v_mul_f32 v19, s95, v19\n"
"s_waitcnt lgkmcnt(6)\n"
"v_mfma_f32_16x16x32_bf16 v[128:131], v[224:227], a[48:51], v[128:131]\n"
"ds_read_b64_tr_b16 v[220:221], v245 offset:520\n"
"ds_read_b64_tr_b16 v[222:223], v245 offset:776\n"
"v_mfma_f32_16x16x32_bf16 v[132:135], v[224:227], a[52:55], v[132:135]\n"
"v_mul_f32 v48, s95, v48\n"
"v_mul_f32 v49, s95, v49\n"
"v_mfma_f32_16x16x32_bf16 v[136:139], v[224:227], a[56:59], v[136:139]\n"
"s_waitcnt vmcnt(16)\n"
"s_barrier\n"
"s_add_u32 m0, s72, 0x1ef00\n"
"v_mfma_f32_16x16x32_bf16 v[140:143], v[224:227], a[60:63], v[140:143]\n"
"buffer_load_dwordx4 v199, s[44:47], s87 offen offset:0 lds\n"
"v_mfma_f32_16x16x32_bf16 v[160:163], v[228:231], a[48:51], v[160:163]\n"
"v_mul_f32 v50, s95, v50\n"
"v_mul_f32 v51, s95, v51\n"
"v_mfma_f32_16x16x32_bf16 v[164:167], v[228:231], a[52:55], v[164:167]\n"
"v_cvt_pk_bf16_f32 v16, v16, v17\n"
"s_add_u32 m0, s72, 0x1f700\n"
"v_mfma_f32_16x16x32_bf16 v[168:171], v[228:231], a[56:59], v[168:171]\n"
"buffer_load_dwordx4 v202, s[44:47], s87 offen offset:0 lds\n"
"v_mfma_f32_16x16x32_bf16 v[172:175], v[228:231], a[60:63], v[172:175]\n"
"v_cvt_pk_bf16_f32 v17, v18, v19\n"
"v_cvt_pk_bf16_f32 v18, v48, v49\n"
"v_mfma_f32_16x16x32_bf16 v[144:147], v[232:235], a[48:51], v[144:147]\n"
"v_cvt_pk_bf16_f32 v19, v50, v51\n"
"s_add_u32 m0, s72, 0x1fd00\n"
"v_mfma_f32_16x16x32_bf16 v[148:151], v[232:235], a[52:55], v[148:151]\n"
"buffer_load_dwordx4 v199, s[44:47], s87 offen offset:512 lds\n"
"v_mfma_f32_16x16x32_bf16 v[152:155], v[232:235], a[56:59], v[152:155]\n"
"v_mul_f32 v20, s95, v20\n"
"v_mul_f32 v21, s95, v21\n"
"v_mfma_f32_16x16x32_bf16 v[156:159], v[232:235], a[60:63], v[156:159]\n"
"v_mul_f32 v22, s95, v22\n"
"s_add_u32 m0, s72, 0x20500\n"
"v_mfma_f32_16x16x32_bf16 v[176:179], v[236:239], a[48:51], v[176:179]\n"
"buffer_load_dwordx4 v202, s[44:47], s87 offen offset:512 lds\n"
"v_mfma_f32_16x16x32_bf16 v[180:183], v[236:239], a[52:55], v[180:183]\n"
"ds_read_b64_tr_b16 v[224:225], v245 offset:16896\n"
"ds_read_b64_tr_b16 v[226:227], v245 offset:17152\n"
"v_mfma_f32_16x16x32_bf16 v[184:187], v[236:239], a[56:59], v[184:187]\n"
"v_mul_f32 v23, s95, v23\n"
"v_mfma_f32_16x16x32_bf16 v[188:191], v[236:239], a[60:63], v[188:191]\n"
"s_waitcnt lgkmcnt(2)\n"
"v_mfma_f32_16x16x32_bf16 v[128:131], v[208:211], a[64:67], v[128:131]\n"
"ds_read_b64_tr_b16 v[228:229], v245 offset:16904\n"
"ds_read_b64_tr_b16 v[230:231], v245 offset:17160\n"
"v_mfma_f32_16x16x32_bf16 v[132:135], v[208:211], a[68:71], v[132:135]\n"
"s_waitcnt vmcnt(16)\n"
"s_barrier\n"
"s_add_u32 m0, s72, 0x21000\n"
"v_mfma_f32_16x16x32_bf16 v[136:139], v[208:211], a[72:75], v[136:139]\n"
"buffer_load_dwordx4 v200, s[44:47], s87 offen offset:0 lds\n"
"v_mfma_f32_16x16x32_bf16 v[140:143], v[208:211], a[76:79], v[140:143]\n"
"ds_read_b64_tr_b16 v[232:233], v245 offset:17408\n"
"ds_read_b64_tr_b16 v[234:235], v245 offset:17664\n"
"v_mfma_f32_16x16x32_bf16 v[160:163], v[212:215], a[64:67], v[160:163]\n"
"v_mul_f32 v52, s95, v52\n"
"s_add_u32 m0, s72, 0x21800\n"
"v_mfma_f32_16x16x32_bf16 v[164:167], v[212:215], a[68:71], v[164:167]\n"
"buffer_load_dwordx4 v203, s[44:47], s87 offen offset:0 lds\n"
"v_mfma_f32_16x16x32_bf16 v[168:171], v[212:215], a[72:75], v[168:171]\n"
"ds_read_b64_tr_b16 v[236:237], v245 offset:17416\n"
"ds_read_b64_tr_b16 v[238:239], v245 offset:17672\n"
"v_mfma_f32_16x16x32_bf16 v[172:175], v[212:215], a[76:79], v[172:175]\n"
"v_mul_f32 v53, s95, v53\n"
"s_add_u32 m0, s72, 0x21e00\n"
"v_mfma_f32_16x16x32_bf16 v[144:147], v[216:219], a[64:67], v[144:147]\n"
"buffer_load_dwordx4 v200, s[44:47], s87 offen offset:512 lds\n"
"v_mfma_f32_16x16x32_bf16 v[148:151], v[216:219], a[68:71], v[148:151]\n"
"v_mul_f32 v54, s95, v54\n"
"v_mul_f32 v55, s95, v55\n"
"v_mfma_f32_16x16x32_bf16 v[152:155], v[216:219], a[72:75], v[152:155]\n"
"v_cvt_pk_bf16_f32 v20, v20, v21\n"
"s_add_u32 m0, s72, 0x22600\n"
"v_mfma_f32_16x16x32_bf16 v[156:159], v[216:219], a[76:79], v[156:159]\n"
"buffer_load_dwordx4 v203, s[44:47], s87 offen offset:512 lds\n"
"v_mfma_f32_16x16x32_bf16 v[176:179], v[220:223], a[64:67], v[176:179]\n"
"ds_read_b64_tr_b16 v[208:209], v245 offset:8448\n"
"ds_read_b64_tr_b16 v[210:211], v245 offset:8704\n"
"v_mfma_f32_16x16x32_bf16 v[180:183], v[220:223], a[68:71], v[180:183]\n"
"ds_read_b64_tr_b16 v[212:213], v245 offset:8456\n"
"ds_read_b64_tr_b16 v[214:215], v245 offset:8712\n"
"v_mfma_f32_16x16x32_bf16 v[184:187], v[220:223], a[72:75], v[184:187]\n"
"ds_read_b64_tr_b16 v[216:217], v245 offset:8960\n"
"ds_read_b64_tr_b16 v[218:219], v245 offset:9216\n"
"v_mfma_f32_16x16x32_bf16 v[188:191], v[220:223], a[76:79], v[188:191]\n"
"v_cvt_pk_bf16_f32 v21, v22, v23\n"
"s_waitcnt lgkmcnt(6)\n"
"v_mfma_f32_16x16x32_bf16 v[128:131], v[224:227], a[80:83], v[128:131]\n"
"s_waitcnt vmcnt(16)\n"
"s_barrier\n"
"s_add_u32 m0, s72, 0x25200\n"
"v_mfma_f32_16x16x32_bf16 v[132:135], v[224:227], a[84:87], v[132:135]\n"
"buffer_load_dwordx4 v199, s[44:47], s88 offen offset:0 lds\n"
"v_mfma_f32_16x16x32_bf16 v[136:139], v[224:227], a[88:91], v[136:139]\n"
"ds_read_b64_tr_b16 v[220:221], v245 offset:8968\n"
"ds_read_b64_tr_b16 v[222:223], v245 offset:9224\n"
"v_mfma_f32_16x16x32_bf16 v[140:143], v[224:227], a[92:95], v[140:143]\n"
"v_cvt_pk_bf16_f32 v22, v52, v53\n"
"s_add_u32 m0, s72, 0x25a00\n"
"v_mfma_f32_16x16x32_bf16 v[160:163], v[228:231], a[80:83], v[160:163]\n"
"buffer_load_dwordx4 v202, s[44:47], s88 offen offset:0 lds\n"
"v_mfma_f32_16x16x32_bf16 v[164:167], v[228:231], a[84:87], v[164:167]\n"
"v_cvt_pk_bf16_f32 v23, v54, v55\n"
"v_mul_f32 v24, s95, v24\n"
"v_mfma_f32_16x16x32_bf16 v[168:171], v[228:231], a[88:91], v[168:171]\n"
"v_mul_f32 v25, s95, v25\n"
"s_add_u32 m0, s72, 0x26000\n"
"v_mfma_f32_16x16x32_bf16 v[172:175], v[228:231], a[92:95], v[172:175]\n"
"buffer_load_dwordx4 v199, s[44:47], s88 offen offset:512 lds\n"
"v_mfma_f32_16x16x32_bf16 v[144:147], v[232:235], a[80:83], v[144:147]\n"
"v_mul_f32 v26, s95, v26\n"
"v_mul_f32 v27, s95, v27\n"
"v_mfma_f32_16x16x32_bf16 v[148:151], v[232:235], a[84:87], v[148:151]\n"
"v_mul_f32 v56, s95, v56\n"
"s_add_u32 m0, s72, 0x26800\n"
"v_mfma_f32_16x16x32_bf16 v[152:155], v[232:235], a[88:91], v[152:155]\n"
"buffer_load_dwordx4 v202, s[44:47], s88 offen offset:512 lds\n"
"v_mfma_f32_16x16x32_bf16 v[156:159], v[232:235], a[92:95], v[156:159]\n"
"v_mul_f32 v57, s95, v57\n"
"v_mul_f32 v58, s95, v58\n"
"v_mfma_f32_16x16x32_bf16 v[176:179], v[236:239], a[80:83], v[176:179]\n"
"ds_read_b64_tr_b16 v[224:225], v244 offset:33792\n"
"ds_read_b64_tr_b16 v[226:227], v244 offset:34048\n"
"v_mfma_f32_16x16x32_bf16 v[180:183], v[236:239], a[84:87], v[180:183]\n"
"ds_read_b64_tr_b16 v[228:229], v244 offset:33800\n"
"ds_read_b64_tr_b16 v[230:231], v244 offset:34056\n"
"v_mfma_f32_16x16x32_bf16 v[184:187], v[236:239], a[88:91], v[184:187]\n"
"ds_read_b64_tr_b16 v[232:233], v244 offset:34304\n"
"ds_read_b64_tr_b16 v[234:235], v244 offset:34560\n"
"v_mfma_f32_16x16x32_bf16 v[188:191], v[236:239], a[92:95], v[188:191]\n"
"v_mul_f32 v59, s95, v59\n"
"s_waitcnt lgkmcnt(6)\n"
"v_mfma_f32_16x16x32_bf16 v[128:131], v[208:211], a[96:99], v[128:131]\n"
"ds_read_b64_tr_b16 v[236:237], v244 offset:34312\n"
"ds_read_b64_tr_b16 v[238:239], v244 offset:34568\n"
"v_mfma_f32_16x16x32_bf16 v[132:135], v[208:211], a[100:103], v[132:135]\n"
"v_cvt_pk_bf16_f32 v24, v24, v25\n"
"v_cvt_pk_bf16_f32 v25, v26, v27\n"
"v_mfma_f32_16x16x32_bf16 v[136:139], v[208:211], a[104:107], v[136:139]\n"
"s_waitcnt vmcnt(16)\n"
"s_barrier\n"
"s_add_u32 m0, s72, 0x23100\n"
"v_mfma_f32_16x16x32_bf16 v[140:143], v[208:211], a[108:111], v[140:143]\n"
"buffer_load_dwordx4 v200, s[44:47], s88 offen offset:0 lds\n"
"v_mfma_f32_16x16x32_bf16 v[160:163], v[212:215], a[96:99], v[160:163]\n"
"v_cvt_pk_bf16_f32 v26, v56, v57\n"
"v_cvt_pk_bf16_f32 v27, v58, v59\n"
"v_mfma_f32_16x16x32_bf16 v[164:167], v[212:215], a[100:103], v[164:167]\n"
"v_mul_f32 v28, s95, v28\n"
"s_add_u32 m0, s72, 0x23900\n"
"v_mfma_f32_16x16x32_bf16 v[168:171], v[212:215], a[104:107], v[168:171]\n"
"buffer_load_dwordx4 v203, s[44:47], s88 offen offset:0 lds\n"
"v_mfma_f32_16x16x32_bf16 v[172:175], v[212:215], a[108:111], v[172:175]\n"
"v_mul_f32 v29, s95, v29\n"
"v_mul_f32 v30, s95, v30\n"
"v_mfma_f32_16x16x32_bf16 v[144:147], v[216:219], a[96:99], v[144:147]\n"
"v_mul_f32 v31, s95, v31\n"
"s_add_u32 m0, s72, 0x23f00\n"
"v_mfma_f32_16x16x32_bf16 v[148:151], v[216:219], a[100:103], v[148:151]\n"
"buffer_load_dwordx4 v200, s[44:47], s88 offen offset:512 lds\n"
"v_mfma_f32_16x16x32_bf16 v[152:155], v[216:219], a[104:107], v[152:155]\n"
"v_mul_f32 v60, s95, v60\n"
"v_mul_f32 v61, s95, v61\n"
"v_mfma_f32_16x16x32_bf16 v[156:159], v[216:219], a[108:111], v[156:159]\n"
"v_mul_f32 v62, s95, v62\n"
"s_add_u32 m0, s72, 0x24700\n"
"v_mfma_f32_16x16x32_bf16 v[176:179], v[220:223], a[96:99], v[176:179]\n"
"buffer_load_dwordx4 v203, s[44:47], s88 offen offset:512 lds\n"
"v_mfma_f32_16x16x32_bf16 v[180:183], v[220:223], a[100:103], v[180:183]\n"
"ds_read_b64_tr_b16 v[208:209], v244 offset:42240\n"
"ds_read_b64_tr_b16 v[210:211], v244 offset:42496\n"
"v_mfma_f32_16x16x32_bf16 v[184:187], v[220:223], a[104:107], v[184:187]\n"
"v_mul_f32 v63, s95, v63\n"
"v_mfma_f32_16x16x32_bf16 v[188:191], v[220:223], a[108:111], v[188:191]\n"
"s_waitcnt lgkmcnt(2)\n"
"v_mfma_f32_16x16x32_bf16 v[128:131], v[224:227], a[112:115], v[128:131]\n"
"ds_read_b64_tr_b16 v[212:213], v244 offset:42248\n"
"ds_read_b64_tr_b16 v[214:215], v244 offset:42504\n"
"v_mfma_f32_16x16x32_bf16 v[132:135], v[224:227], a[116:119], v[132:135]\n"
"s_waitcnt vmcnt(16)\n"
"s_barrier\n"
"s_add_u32 m0, s72, 0x18b80\n"
"v_mfma_f32_16x16x32_bf16 v[136:139], v[224:227], a[120:123], v[136:139]\n"
"buffer_load_dwordx4 v199, s[44:47], s82 offen offset:128 lds\n"
"v_mfma_f32_16x16x32_bf16 v[140:143], v[224:227], a[124:127], v[140:143]\n"
"ds_read_b64_tr_b16 v[216:217], v244 offset:42752\n"
"ds_read_b64_tr_b16 v[218:219], v244 offset:43008\n"
"v_mfma_f32_16x16x32_bf16 v[160:163], v[228:231], a[112:115], v[160:163]\n"
"v_cvt_pk_bf16_f32 v28, v28, v29\n"
"s_add_u32 m0, s72, 0x19380\n"
"v_mfma_f32_16x16x32_bf16 v[164:167], v[228:231], a[116:119], v[164:167]\n"
"buffer_load_dwordx4 v202, s[44:47], s82 offen offset:128 lds\n"
"v_mfma_f32_16x16x32_bf16 v[168:171], v[228:231], a[120:123], v[168:171]\n"
"ds_read_b64_tr_b16 v[220:221], v244 offset:42760\n"
"ds_read_b64_tr_b16 v[222:223], v244 offset:43016\n"
"v_mfma_f32_16x16x32_bf16 v[172:175], v[228:231], a[124:127], v[172:175]\n"
"v_cvt_pk_bf16_f32 v29, v30, v31\n"
"s_add_u32 m0, s72, 0x19980\n"
"v_mfma_f32_16x16x32_bf16 v[144:147], v[232:235], a[112:115], v[144:147]\n"
"buffer_load_dwordx4 v199, s[44:47], s82 offen offset:640 lds\n"
"v_mfma_f32_16x16x32_bf16 v[148:151], v[232:235], a[116:119], v[148:151]\n"
"v_cvt_pk_bf16_f32 v30, v60, v61\n"
"v_cvt_pk_bf16_f32 v31, v62, v63\n"
"v_mfma_f32_16x16x32_bf16 v[152:155], v[232:235], a[120:123], v[152:155]\n"
"v_mul_f32 v64, s95, v64\n"
"s_add_u32 m0, s72, 0x1a180\n"
"v_mfma_f32_16x16x32_bf16 v[156:159], v[232:235], a[124:127], v[156:159]\n"
"buffer_load_dwordx4 v202, s[44:47], s82 offen offset:640 lds\n"
"v_mfma_f32_16x16x32_bf16 v[176:179], v[236:239], a[112:115], v[176:179]\n"
"ds_read_b64_tr_b16 v[224:225], v244 offset:50688\n"
"ds_read_b64_tr_b16 v[226:227], v244 offset:50944\n"
"v_mfma_f32_16x16x32_bf16 v[180:183], v[236:239], a[116:119], v[180:183]\n"
"ds_read_b64_tr_b16 v[228:229], v244 offset:50696\n"
"ds_read_b64_tr_b16 v[230:231], v244 offset:50952\n"
"v_mfma_f32_16x16x32_bf16 v[184:187], v[236:239], a[120:123], v[184:187]\n"
"ds_read_b64_tr_b16 v[232:233], v244 offset:51200\n"
"ds_read_b64_tr_b16 v[234:235], v244 offset:51456\n"
"v_mfma_f32_16x16x32_bf16 v[188:191], v[236:239], a[124:127], v[188:191]\n"
"v_mul_f32 v65, s95, v65\n"
"s_waitcnt lgkmcnt(6)\n"
"v_mfma_f32_16x16x32_bf16 v[128:131], v[208:211], a[128:131], v[128:131]\n"
"s_waitcnt vmcnt(16)\n"
"s_barrier\n"
"s_add_u32 m0, s72, 0x1ac80\n"
"v_mfma_f32_16x16x32_bf16 v[132:135], v[208:211], a[132:135], v[132:135]\n"
"buffer_load_dwordx4 v200, s[44:47], s82 offen offset:128 lds\n"
"v_mfma_f32_16x16x32_bf16 v[136:139], v[208:211], a[136:139], v[136:139]\n"
"ds_read_b64_tr_b16 v[236:237], v244 offset:51208\n"
"ds_read_b64_tr_b16 v[238:239], v244 offset:51464\n"
"v_mfma_f32_16x16x32_bf16 v[140:143], v[208:211], a[140:143], v[140:143]\n"
"v_mul_f32 v66, s95, v66\n"
"s_add_u32 m0, s72, 0x1b480\n"
"v_mfma_f32_16x16x32_bf16 v[160:163], v[212:215], a[128:131], v[160:163]\n"
"buffer_load_dwordx4 v203, s[44:47], s82 offen offset:128 lds\n"
"v_mfma_f32_16x16x32_bf16 v[164:167], v[212:215], a[132:135], v[164:167]\n"
"v_mul_f32 v67, s95, v67\n"
"v_mul_f32 v96, s95, v96\n"
"v_mfma_f32_16x16x32_bf16 v[168:171], v[212:215], a[136:139], v[168:171]\n"
"v_mul_f32 v97, s95, v97\n"
"s_add_u32 m0, s72, 0x1ba80\n"
"v_mfma_f32_16x16x32_bf16 v[172:175], v[212:215], a[140:143], v[172:175]\n"
"buffer_load_dwordx4 v200, s[44:47], s82 offen offset:640 lds\n"
"v_mfma_f32_16x16x32_bf16 v[144:147], v[216:219], a[128:131], v[144:147]\n"
"v_mul_f32 v98, s95, v98\n"
"v_mul_f32 v99, s95, v99\n"
"v_mfma_f32_16x16x32_bf16 v[148:151], v[216:219], a[132:135], v[148:151]\n"
"v_cvt_pk_bf16_f32 v64, v64, v65\n"
"s_add_u32 m0, s72, 0x1c280\n"
"v_mfma_f32_16x16x32_bf16 v[152:155], v[216:219], a[136:139], v[152:155]\n"
"buffer_load_dwordx4 v203, s[44:47], s82 offen offset:640 lds\n"
"v_mfma_f32_16x16x32_bf16 v[156:159], v[216:219], a[140:143], v[156:159]\n"
"v_cvt_pk_bf16_f32 v65, v66, v67\n"
"v_cvt_pk_bf16_f32 v66, v96, v97\n"
"v_mfma_f32_16x16x32_bf16 v[176:179], v[220:223], a[128:131], v[176:179]\n"
"ds_read_b64_tr_b16 v[208:209], v244 offset:59136\n"
"ds_read_b64_tr_b16 v[210:211], v244 offset:59392\n"
"v_mfma_f32_16x16x32_bf16 v[180:183], v[220:223], a[132:135], v[180:183]\n"
"ds_read_b64_tr_b16 v[212:213], v244 offset:59144\n"
"ds_read_b64_tr_b16 v[214:215], v244 offset:59400\n"
"v_mfma_f32_16x16x32_bf16 v[184:187], v[220:223], a[136:139], v[184:187]\n"
"ds_read_b64_tr_b16 v[216:217], v244 offset:59648\n"
"ds_read_b64_tr_b16 v[218:219], v244 offset:59904\n"
"v_mfma_f32_16x16x32_bf16 v[188:191], v[220:223], a[140:143], v[188:191]\n"
"v_cvt_pk_bf16_f32 v67, v98, v99\n"
"s_waitcnt lgkmcnt(6)\n"
"v_mfma_f32_16x16x32_bf16 v[128:131], v[224:227], a[144:147], v[128:131]\n"
"ds_read_b64_tr_b16 v[220:221], v244 offset:59656\n"
"ds_read_b64_tr_b16 v[222:223], v244 offset:59912\n"
"v_mfma_f32_16x16x32_bf16 v[132:135], v[224:227], a[148:151], v[132:135]\n"
"v_mul_f32 v68, s95, v68\n"
"v_mul_f32 v69, s95, v69\n"
"v_mfma_f32_16x16x32_bf16 v[136:139], v[224:227], a[152:155], v[136:139]\n"
"s_waitcnt vmcnt(16)\n"
"s_barrier\n"
"s_add_u32 m0, s72, 0x1cd80\n"
"v_mfma_f32_16x16x32_bf16 v[140:143], v[224:227], a[156:159], v[140:143]\n"
"buffer_load_dwordx4 v199, s[44:47], s83 offen offset:128 lds\n"
"v_mfma_f32_16x16x32_bf16 v[160:163], v[228:231], a[144:147], v[160:163]\n"
"v_mul_f32 v70, s95, v70\n"
"v_mul_f32 v71, s95, v71\n"
"v_mfma_f32_16x16x32_bf16 v[164:167], v[228:231], a[148:151], v[164:167]\n"
"v_mul_f32 v100, s95, v100\n"
"s_add_u32 m0, s72, 0x1d580\n"
"v_mfma_f32_16x16x32_bf16 v[168:171], v[228:231], a[152:155], v[168:171]\n"
"buffer_load_dwordx4 v202, s[44:47], s83 offen offset:128 lds\n"
"v_mfma_f32_16x16x32_bf16 v[172:175], v[228:231], a[156:159], v[172:175]\n"
"v_mul_f32 v101, s95, v101\n"
"v_mul_f32 v102, s95, v102\n"
"v_mfma_f32_16x16x32_bf16 v[144:147], v[232:235], a[144:147], v[144:147]\n"
"v_mul_f32 v103, s95, v103\n"
"s_add_u32 m0, s72, 0x1db80\n"
"v_mfma_f32_16x16x32_bf16 v[148:151], v[232:235], a[148:151], v[148:151]\n"
"buffer_load_dwordx4 v199, s[44:47], s83 offen offset:640 lds\n"
"v_mfma_f32_16x16x32_bf16 v[152:155], v[232:235], a[152:155], v[152:155]\n"
"v_cvt_pk_bf16_f32 v68, v68, v69\n"
"v_cvt_pk_bf16_f32 v69, v70, v71\n"
"v_mfma_f32_16x16x32_bf16 v[156:159], v[232:235], a[156:159], v[156:159]\n"
"v_cvt_pk_bf16_f32 v70, v100, v101\n"
"s_add_u32 m0, s72, 0x1e380\n"
"v_mfma_f32_16x16x32_bf16 v[176:179], v[236:239], a[144:147], v[176:179]\n"
"buffer_load_dwordx4 v202, s[44:47], s83 offen offset:640 lds\n"
"v_mfma_f32_16x16x32_bf16 v[180:183], v[236:239], a[148:151], v[180:183]\n"
"ds_read_b64_tr_b16 v[224:225], v245 offset:0\n"
"ds_read_b64_tr_b16 v[226:227], v245 offset:256\n"
"v_mfma_f32_16x16x32_bf16 v[184:187], v[236:239], a[152:155], v[184:187]\n"
"v_cvt_pk_bf16_f32 v71, v102, v103\n"
"v_mfma_f32_16x16x32_bf16 v[188:191], v[236:239], a[156:159], v[188:191]\n"
"s_waitcnt lgkmcnt(2)\n"
"v_mfma_f32_16x16x32_bf16 v[128:131], v[208:211], a[160:163], v[128:131]\n"
"ds_read_b64_tr_b16 v[228:229], v245 offset:8\n"
"ds_read_b64_tr_b16 v[230:231], v245 offset:264\n"
"v_mfma_f32_16x16x32_bf16 v[132:135], v[208:211], a[164:167], v[132:135]\n"
"s_waitcnt vmcnt(16)\n"
"s_barrier\n"
"s_add_u32 m0, s72, 0x1ee80\n"
"v_mfma_f32_16x16x32_bf16 v[136:139], v[208:211], a[168:171], v[136:139]\n"
"buffer_load_dwordx4 v200, s[44:47], s83 offen offset:128 lds\n"
"v_mfma_f32_16x16x32_bf16 v[140:143], v[208:211], a[172:175], v[140:143]\n"
"ds_read_b64_tr_b16 v[232:233], v245 offset:512\n"
"ds_read_b64_tr_b16 v[234:235], v245 offset:768\n"
"v_mfma_f32_16x16x32_bf16 v[160:163], v[212:215], a[160:163], v[160:163]\n"
"v_mul_f32 v72, s95, v72\n"
"s_add_u32 m0, s72, 0x1f680\n"
"v_mfma_f32_16x16x32_bf16 v[164:167], v[212:215], a[164:167], v[164:167]\n"
"buffer_load_dwordx4 v203, s[44:47], s83 offen offset:128 lds\n"
"v_mfma_f32_16x16x32_bf16 v[168:171], v[212:215], a[168:171], v[168:171]\n"
"ds_read_b64_tr_b16 v[236:237], v245 offset:520\n"
"ds_read_b64_tr_b16 v[238:239], v245 offset:776\n"
"v_mfma_f32_16x16x32_bf16 v[172:175], v[212:215], a[172:175], v[172:175]\n"
"v_mul_f32 v73, s95, v73\n"
"s_add_u32 m0, s72, 0x1fc80\n"
"v_mfma_f32_16x16x32_bf16 v[144:147], v[216:219], a[160:163], v[144:147]\n"
"buffer_load_dwordx4 v200, s[44:47], s83 offen offset:640 lds\n"
"v_mfma_f32_16x16x32_bf16 v[148:151], v[216:219], a[164:167], v[148:151]\n"
"v_mul_f32 v74, s95, v74\n"
"v_mul_f32 v75, s95, v75\n"
"v_mfma_f32_16x16x32_bf16 v[152:155], v[216:219], a[168:171], v[152:155]\n"
"v_mul_f32 v104, s95, v104\n"
"s_add_u32 m0, s72, 0x20480\n"
"v_mfma_f32_16x16x32_bf16 v[156:159], v[216:219], a[172:175], v[156:159]\n"
"buffer_load_dwordx4 v203, s[44:47], s83 offen offset:640 lds\n"
"v_mfma_f32_16x16x32_bf16 v[176:179], v[220:223], a[160:163], v[176:179]\n"
"ds_read_b64_tr_b16 v[208:209], v245 offset:16896\n"
"ds_read_b64_tr_b16 v[210:211], v245 offset:17152\n"
"v_mfma_f32_16x16x32_bf16 v[180:183], v[220:223], a[164:167], v[180:183]\n"
"ds_read_b64_tr_b16 v[212:213], v245 offset:16904\n"
"ds_read_b64_tr_b16 v[214:215], v245 offset:17160\n"
"v_mfma_f32_16x16x32_bf16 v[184:187], v[220:223], a[168:171], v[184:187]\n"
"ds_read_b64_tr_b16 v[216:217], v245 offset:17408\n"
"ds_read_b64_tr_b16 v[218:219], v245 offset:17664\n"
"v_mfma_f32_16x16x32_bf16 v[188:191], v[220:223], a[172:175], v[188:191]\n"
"v_mul_f32 v105, s95, v105\n"
"s_waitcnt lgkmcnt(6)\n"
"v_mfma_f32_16x16x32_bf16 v[128:131], v[224:227], a[176:179], v[128:131]\n"
"s_waitcnt vmcnt(16)\n"
"s_barrier\n"
"s_add_u32 m0, s72, 0x20f80\n"
"v_mfma_f32_16x16x32_bf16 v[132:135], v[224:227], a[180:183], v[132:135]\n"
"buffer_load_dwordx4 v199, s[44:47], s84 offen offset:128 lds\n"
"v_mfma_f32_16x16x32_bf16 v[136:139], v[224:227], a[184:187], v[136:139]\n"
"ds_read_b64_tr_b16 v[220:221], v245 offset:17416\n"
"ds_read_b64_tr_b16 v[222:223], v245 offset:17672\n"
"v_mfma_f32_16x16x32_bf16 v[140:143], v[224:227], a[188:191], v[140:143]\n"
"v_mul_f32 v106, s95, v106\n"
"s_add_u32 m0, s72, 0x21780\n"
"v_mfma_f32_16x16x32_bf16 v[160:163], v[228:231], a[176:179], v[160:163]\n"
"buffer_load_dwordx4 v202, s[44:47], s84 offen offset:128 lds\n"
"v_mfma_f32_16x16x32_bf16 v[164:167], v[228:231], a[180:183], v[164:167]\n"
"v_mul_f32 v107, s95, v107\n"
"v_cvt_pk_bf16_f32 v72, v72, v73\n"
"v_mfma_f32_16x16x32_bf16 v[168:171], v[228:231], a[184:187], v[168:171]\n"
"v_cvt_pk_bf16_f32 v73, v74, v75\n"
"s_add_u32 m0, s72, 0x21d80\n"
"v_mfma_f32_16x16x32_bf16 v[172:175], v[228:231], a[188:191], v[172:175]\n"
"buffer_load_dwordx4 v199, s[44:47], s84 offen offset:640 lds\n"
"v_mfma_f32_16x16x32_bf16 v[144:147], v[232:235], a[176:179], v[144:147]\n"
"v_cvt_pk_bf16_f32 v74, v104, v105\n"
"v_cvt_pk_bf16_f32 v75, v106, v107\n"
"v_mfma_f32_16x16x32_bf16 v[148:151], v[232:235], a[180:183], v[148:151]\n"
"v_mul_f32 v76, s95, v76\n"
"s_add_u32 m0, s72, 0x22580\n"
"v_mfma_f32_16x16x32_bf16 v[152:155], v[232:235], a[184:187], v[152:155]\n"
"buffer_load_dwordx4 v202, s[44:47], s84 offen offset:640 lds\n"
"v_mfma_f32_16x16x32_bf16 v[156:159], v[232:235], a[188:191], v[156:159]\n"
"v_mul_f32 v77, s95, v77\n"
"v_mul_f32 v78, s95, v78\n"
"v_mfma_f32_16x16x32_bf16 v[176:179], v[236:239], a[176:179], v[176:179]\n"
"ds_read_b64_tr_b16 v[224:225], v245 offset:8448\n"
"ds_read_b64_tr_b16 v[226:227], v245 offset:8704\n"
"v_mfma_f32_16x16x32_bf16 v[180:183], v[236:239], a[180:183], v[180:183]\n"
"ds_read_b64_tr_b16 v[228:229], v245 offset:8456\n"
"ds_read_b64_tr_b16 v[230:231], v245 offset:8712\n"
"v_mfma_f32_16x16x32_bf16 v[184:187], v[236:239], a[184:187], v[184:187]\n"
"ds_read_b64_tr_b16 v[232:233], v245 offset:8960\n"
"ds_read_b64_tr_b16 v[234:235], v245 offset:9216\n"
"v_mfma_f32_16x16x32_bf16 v[188:191], v[236:239], a[188:191], v[188:191]\n"
"v_mul_f32 v79, s95, v79\n"
"s_waitcnt lgkmcnt(6)\n"
"v_mfma_f32_16x16x32_bf16 v[128:131], v[208:211], a[192:195], v[128:131]\n"
"ds_read_b64_tr_b16 v[236:237], v245 offset:8968\n"
"ds_read_b64_tr_b16 v[238:239], v245 offset:9224\n"
"v_mfma_f32_16x16x32_bf16 v[132:135], v[208:211], a[196:199], v[132:135]\n"
"v_mul_f32 v108, s95, v108\n"
"v_mul_f32 v109, s95, v109\n"
"v_mfma_f32_16x16x32_bf16 v[136:139], v[208:211], a[200:203], v[136:139]\n"
"s_waitcnt vmcnt(16)\n"
"s_barrier\n"
"s_add_u32 m0, s72, 0x25180\n"
"v_mfma_f32_16x16x32_bf16 v[140:143], v[208:211], a[204:207], v[140:143]\n"
"buffer_load_dwordx4 v200, s[44:47], s84 offen offset:128 lds\n"
"v_mfma_f32_16x16x32_bf16 v[160:163], v[212:215], a[192:195], v[160:163]\n"
"v_mul_f32 v110, s95, v110\n"
"v_mul_f32 v111, s95, v111\n"
"v_mfma_f32_16x16x32_bf16 v[164:167], v[212:215], a[196:199], v[164:167]\n"
"v_cvt_pk_bf16_f32 v76, v76, v77\n"
"s_add_u32 m0, s72, 0x25980\n"
"v_mfma_f32_16x16x32_bf16 v[168:171], v[212:215], a[200:203], v[168:171]\n"
"buffer_load_dwordx4 v203, s[44:47], s84 offen offset:128 lds\n"
"v_mfma_f32_16x16x32_bf16 v[172:175], v[212:215], a[204:207], v[172:175]\n"
"v_cvt_pk_bf16_f32 v77, v78, v79\n"
"v_cvt_pk_bf16_f32 v78, v108, v109\n"
"v_mfma_f32_16x16x32_bf16 v[144:147], v[216:219], a[192:195], v[144:147]\n"
"v_cvt_pk_bf16_f32 v79, v110, v111\n"
"s_add_u32 m0, s72, 0x25f80\n"
"v_mfma_f32_16x16x32_bf16 v[148:151], v[216:219], a[196:199], v[148:151]\n"
"buffer_load_dwordx4 v200, s[44:47], s84 offen offset:640 lds\n"
"v_mfma_f32_16x16x32_bf16 v[152:155], v[216:219], a[200:203], v[152:155]\n"
"v_mul_f32 v80, s95, v80\n"
"v_mul_f32 v81, s95, v81\n"
"v_mfma_f32_16x16x32_bf16 v[156:159], v[216:219], a[204:207], v[156:159]\n"
"v_mul_f32 v82, s95, v82\n"
"s_add_u32 m0, s72, 0x26780\n"
"v_mfma_f32_16x16x32_bf16 v[176:179], v[220:223], a[192:195], v[176:179]\n"
"buffer_load_dwordx4 v203, s[44:47], s84 offen offset:640 lds\n"
"v_mfma_f32_16x16x32_bf16 v[180:183], v[220:223], a[196:199], v[180:183]\n"
"ds_read_b64_tr_b16 v[208:209], v244 offset:33792\n"
"ds_read_b64_tr_b16 v[210:211], v244 offset:34048\n"
"v_mfma_f32_16x16x32_bf16 v[184:187], v[220:223], a[200:203], v[184:187]\n"
"v_mul_f32 v83, s95, v83\n"
"v_mfma_f32_16x16x32_bf16 v[188:191], v[220:223], a[204:207], v[188:191]\n"
"s_waitcnt lgkmcnt(2)\n"
"v_mfma_f32_16x16x32_bf16 v[128:131], v[224:227], a[208:211], v[128:131]\n"
"ds_read_b64_tr_b16 v[212:213], v244 offset:33800\n"
"ds_read_b64_tr_b16 v[214:215], v244 offset:34056\n"
"v_mfma_f32_16x16x32_bf16 v[132:135], v[224:227], a[212:215], v[132:135]\n"
"s_waitcnt vmcnt(16)\n"
"s_barrier\n"
"s_add_u32 m0, s72, 0x23080\n"
"v_mfma_f32_16x16x32_bf16 v[136:139], v[224:227], a[216:219], v[136:139]\n"
"buffer_load_dwordx4 v199, s[44:47], s85 offen offset:128 lds\n"
"v_mfma_f32_16x16x32_bf16 v[140:143], v[224:227], a[220:223], v[140:143]\n"
"ds_read_b64_tr_b16 v[216:217], v244 offset:34304\n"
"ds_read_b64_tr_b16 v[218:219], v244 offset:34560\n"
"v_mfma_f32_16x16x32_bf16 v[160:163], v[228:231], a[208:211], v[160:163]\n"
"v_mul_f32 v112, s95, v112\n"
"s_add_u32 m0, s72, 0x23880\n"
"v_mfma_f32_16x16x32_bf16 v[164:167], v[228:231], a[212:215], v[164:167]\n"
"buffer_load_dwordx4 v202, s[44:47], s85 offen offset:128 lds\n"
"v_mfma_f32_16x16x32_bf16 v[168:171], v[228:231], a[216:219], v[168:171]\n"
"ds_read_b64_tr_b16 v[220:221], v244 offset:34312\n"
"ds_read_b64_tr_b16 v[222:223], v244 offset:34568\n"
"v_mfma_f32_16x16x32_bf16 v[172:175], v[228:231], a[220:223], v[172:175]\n"
"v_mul_f32 v113, s95, v113\n"
"s_add_u32 m0, s72, 0x23e80\n"
"v_mfma_f32_16x16x32_bf16 v[144:147], v[232:235], a[208:211], v[144:147]\n"
"buffer_load_dwordx4 v199, s[44:47], s85 offen offset:640 lds\n"
"v_mfma_f32_16x16x32_bf16 v[148:151], v[232:235], a[212:215], v[148:151]\n"
"v_mul_f32 v114, s95, v114\n"
"v_mul_f32 v115, s95, v115\n"
"v_mfma_f32_16x16x32_bf16 v[152:155], v[232:235], a[216:219], v[152:155]\n"
"v_cvt_pk_bf16_f32 v80, v80, v81\n"
"s_add_u32 m0, s72, 0x24680\n"
"v_mfma_f32_16x16x32_bf16 v[156:159], v[232:235], a[220:223], v[156:159]\n"
"buffer_load_dwordx4 v202, s[44:47], s85 offen offset:640 lds\n"
"v_mfma_f32_16x16x32_bf16 v[176:179], v[236:239], a[208:211], v[176:179]\n"
"ds_read_b64_tr_b16 v[224:225], v244 offset:42240\n"
"ds_read_b64_tr_b16 v[226:227], v244 offset:42496\n"
"v_mfma_f32_16x16x32_bf16 v[180:183], v[236:239], a[212:215], v[180:183]\n"
"ds_read_b64_tr_b16 v[228:229], v244 offset:42248\n"
"ds_read_b64_tr_b16 v[230:231], v244 offset:42504\n"
"v_mfma_f32_16x16x32_bf16 v[184:187], v[236:239], a[216:219], v[184:187]\n"
"ds_read_b64_tr_b16 v[232:233], v244 offset:42752\n"
"ds_read_b64_tr_b16 v[234:235], v244 offset:43008\n"
"v_mfma_f32_16x16x32_bf16 v[188:191], v[236:239], a[220:223], v[188:191]\n"
"v_cvt_pk_bf16_f32 v81, v82, v83\n"
"s_waitcnt lgkmcnt(6)\n"
"v_mfma_f32_16x16x32_bf16 v[32:35], v[208:211], a[0:3], 0\n"
"s_waitcnt vmcnt(16)\n"
"s_barrier\n"
"s_add_u32 m0, s72, 0x18b80\n"
"v_mfma_f32_16x16x32_bf16 v[36:39], v[208:211], a[4:7], 0\n"
"buffer_load_dwordx4 v200, s[44:47], s85 offen offset:128 lds\n"
"v_mfma_f32_16x16x32_bf16 v[40:43], v[208:211], a[8:11], 0\n"
"ds_read_b64_tr_b16 v[236:237], v244 offset:42760\n"
"ds_read_b64_tr_b16 v[238:239], v244 offset:43016\n"
"v_mfma_f32_16x16x32_bf16 v[44:47], v[208:211], a[12:15], 0\n"
"v_cvt_pk_bf16_f32 v82, v112, v113\n"
"s_add_u32 m0, s72, 0x19380\n"
"v_mfma_f32_16x16x32_bf16 v[96:99], v[212:215], a[0:3], 0\n"
"buffer_load_dwordx4 v203, s[44:47], s85 offen offset:128 lds\n"
"v_mfma_f32_16x16x32_bf16 v[100:103], v[212:215], a[4:7], 0\n"
"v_cvt_pk_bf16_f32 v83, v114, v115\n"
"v_mul_f32 v84, s95, v84\n"
"v_mfma_f32_16x16x32_bf16 v[104:107], v[212:215], a[8:11], 0\n"
"v_mul_f32 v85, s95, v85\n"
"s_add_u32 m0, s72, 0x19980\n"
"v_mfma_f32_16x16x32_bf16 v[108:111], v[212:215], a[12:15], 0\n"
"buffer_load_dwordx4 v200, s[44:47], s85 offen offset:640 lds\n"
"v_mfma_f32_16x16x32_bf16 v[48:51], v[216:219], a[0:3], 0\n"
"v_mul_f32 v86, s95, v86\n"
"v_mul_f32 v87, s95, v87\n"
"v_mfma_f32_16x16x32_bf16 v[52:55], v[216:219], a[4:7], 0\n"
"v_mul_f32 v116, s95, v116\n"
"s_add_u32 m0, s72, 0x1a180\n"
"v_mfma_f32_16x16x32_bf16 v[56:59], v[216:219], a[8:11], 0\n"
"buffer_load_dwordx4 v203, s[44:47], s85 offen offset:640 lds\n"
"v_mfma_f32_16x16x32_bf16 v[60:63], v[216:219], a[12:15], 0\n"
"v_mul_f32 v117, s95, v117\n"
"v_mul_f32 v118, s95, v118\n"
"v_mfma_f32_16x16x32_bf16 v[112:115], v[220:223], a[0:3], 0\n"
"ds_read_b64_tr_b16 v[208:209], v244 offset:50688\n"
"ds_read_b64_tr_b16 v[210:211], v244 offset:50944\n"
"v_mul_f32 v119, s95, v119\n"
"v_cvt_pk_bf16_f32 v84, v84, v85\n"
"v_cvt_pk_bf16_f32 v85, v86, v87\n"
"v_cvt_pk_bf16_f32 v86, v116, v117\n"
"v_cvt_pk_bf16_f32 v87, v118, v119\n"
"v_mfma_f32_16x16x32_bf16 v[116:119], v[220:223], a[4:7], 0\n"
"ds_read_b64_tr_b16 v[212:213], v244 offset:50696\n"
"ds_read_b64_tr_b16 v[214:215], v244 offset:50952\n"
"v_mul_f32 v88, s95, v88\n"
"v_mul_f32 v89, s95, v89\n"
"v_mul_f32 v90, s95, v90\n"
"v_mul_f32 v91, s95, v91\n"
"v_mul_f32 v120, s95, v120\n"
"v_mul_f32 v121, s95, v121\n"
"v_mul_f32 v122, s95, v122\n"
"v_mul_f32 v123, s95, v123\n"
"v_cvt_pk_bf16_f32 v88, v88, v89\n"
"v_cvt_pk_bf16_f32 v89, v90, v91\n"
"v_cvt_pk_bf16_f32 v90, v120, v121\n"
"v_cvt_pk_bf16_f32 v91, v122, v123\n"
"v_mfma_f32_16x16x32_bf16 v[120:123], v[220:223], a[8:11], 0\n"
"ds_read_b64_tr_b16 v[216:217], v244 offset:51200\n"
"ds_read_b64_tr_b16 v[218:219], v244 offset:51456\n"
"v_mul_f32 v92, s95, v92\n"
"v_mul_f32 v93, s95, v93\n"
"v_mul_f32 v94, s95, v94\n"
"v_mul_f32 v95, s95, v95\n"
"v_mul_f32 v124, s95, v124\n"
"v_mul_f32 v125, s95, v125\n"
"v_mul_f32 v126, s95, v126\n"
"v_mul_f32 v127, s95, v127\n"
"v_cvt_pk_bf16_f32 v92, v92, v93\n"
"v_cvt_pk_bf16_f32 v93, v94, v95\n"
"v_cvt_pk_bf16_f32 v94, v124, v125\n"
"v_cvt_pk_bf16_f32 v95, v126, v127\n"
"v_mfma_f32_16x16x32_bf16 v[124:127], v[220:223], a[12:15], 0\n"
"s_sub_u32 s31, 32, s99\n"
"s_waitcnt lgkmcnt(6)\n"
"v_mfma_f32_16x16x32_bf16 v[32:35], v[224:227], a[16:19], v[32:35]\n"
"ds_read_b64_tr_b16 v[220:221], v244 offset:51208\n"
"ds_read_b64_tr_b16 v[222:223], v244 offset:51464\n"
"v_mfma_f32_16x16x32_bf16 v[36:39], v[224:227], a[20:23], v[36:39]\n"
"s_add_u32 s40, s64, s31\n"
"s_addc_u32 s41, s65, 0\n"
"v_mfma_f32_16x16x32_bf16 v[40:43], v[224:227], a[24:27], v[40:43]\n"
"s_sub_u32 s42, s66, s31\n"
"v_mfma_f32_16x16x32_bf16 v[44:47], v[224:227], a[28:31], v[44:47]\n"
"v_mfma_f32_16x16x32_bf16 v[96:99], v[228:231], a[16:19], v[96:99]\n"
"s_cmp_lt_u32 s90, 6\n"
"s_cselect_b32 s66, s66, 0\n"
"v_mfma_f32_16x16x32_bf16 v[100:103], v[228:231], a[20:23], v[100:103]\n"
"v_mul_f32 v128, s95, v128\n"
"v_mul_f32 v129, s95, v129\n"
"v_mfma_f32_16x16x32_bf16 v[104:107], v[228:231], a[24:27], v[104:107]\n"
"v_mul_f32 v130, s95, v130\n"
"v_mul_f32 v131, s95, v131\n"
"v_mfma_f32_16x16x32_bf16 v[108:111], v[228:231], a[28:31], v[108:111]\n"
"v_mul_f32 v160, s95, v160\n"
"v_mul_f32 v161, s95, v161\n"
"v_mfma_f32_16x16x32_bf16 v[48:51], v[232:235], a[16:19], v[48:51]\n"
"v_mul_f32 v162, s95, v162\n"
"v_mul_f32 v163, s95, v163\n"
"v_mfma_f32_16x16x32_bf16 v[52:55], v[232:235], a[20:23], v[52:55]\n"
"v_cvt_pk_bf16_f32 v128, v128, v129\n"
"v_cvt_pk_bf16_f32 v129, v130, v131\n"
"v_mfma_f32_16x16x32_bf16 v[56:59], v[232:235], a[24:27], v[56:59]\n"
"v_cvt_pk_bf16_f32 v130, v160, v161\n"
"v_cvt_pk_bf16_f32 v131, v162, v163\n"
"v_mfma_f32_16x16x32_bf16 v[60:63], v[232:235], a[28:31], v[60:63]\n"
"v_mul_f32 v132, s95, v132\n"
"v_mul_f32 v133, s95, v133\n"
"v_mfma_f32_16x16x32_bf16 v[112:115], v[236:239], a[16:19], v[112:115]\n"
"v_mul_f32 v134, s95, v134\n"
"v_mul_f32 v135, s95, v135\n"
"v_mfma_f32_16x16x32_bf16 v[116:119], v[236:239], a[20:23], v[116:119]\n"
"v_mul_f32 v164, s95, v164\n"
"v_mul_f32 v165, s95, v165\n"
"v_mfma_f32_16x16x32_bf16 v[120:123], v[236:239], a[24:27], v[120:123]\n"
"v_mul_f32 v166, s95, v166\n"
"v_mfma_f32_16x16x32_bf16 v[124:127], v[236:239], a[28:31], v[124:127]\n"
"s_waitcnt lgkmcnt(0)\n"
"v_mfma_f32_16x16x32_bf16 v[32:35], v[208:211], a[32:35], v[32:35]\n"
"v_mul_f32 v167, s95, v167\n"
"v_cvt_pk_bf16_f32 v132, v132, v133\n"
"v_mfma_f32_16x16x32_bf16 v[36:39], v[208:211], a[36:39], v[36:39]\n"
"s_waitcnt vmcnt(12)\n"
"s_barrier\n"
"s_add_u32 m0, s72, 0x1cd80\n"
"v_mfma_f32_16x16x32_bf16 v[40:43], v[208:211], a[40:43], v[40:43]\n"
"buffer_load_dwordx4 v199, s[44:47], s86 offen offset:128 lds\n"
"v_mfma_f32_16x16x32_bf16 v[44:47], v[208:211], a[44:47], v[44:47]\n"
"ds_read_b64_tr_b16 v[224:225], v244 offset:59136\n"
"ds_read_b64_tr_b16 v[226:227], v244 offset:59392\n"
"v_mfma_f32_16x16x32_bf16 v[96:99], v[212:215], a[32:35], v[96:99]\n"
"v_cvt_pk_bf16_f32 v133, v134, v135\n"
"s_add_u32 m0, s72, 0x1d580\n"
"v_mfma_f32_16x16x32_bf16 v[100:103], v[212:215], a[36:39], v[100:103]\n"
"buffer_load_dwordx4 v202, s[44:47], s86 offen offset:128 lds\n"
"v_mfma_f32_16x16x32_bf16 v[104:107], v[212:215], a[40:43], v[104:107]\n"
"ds_read_b64_tr_b16 v[228:229], v244 offset:59144\n"
"ds_read_b64_tr_b16 v[230:231], v244 offset:59400\n"
"v_mfma_f32_16x16x32_bf16 v[108:111], v[212:215], a[44:47], v[108:111]\n"
"v_cvt_pk_bf16_f32 v134, v164, v165\n"
"s_add_u32 m0, s72, 0x1db80\n"
"v_mfma_f32_16x16x32_bf16 v[48:51], v[216:219], a[32:35], v[48:51]\n"
"buffer_load_dwordx4 v199, s[44:47], s86 offen offset:640 lds\n"
"v_mfma_f32_16x16x32_bf16 v[52:55], v[216:219], a[36:39], v[52:55]\n"
"ds_read_b64_tr_b16 v[232:233], v244 offset:59648\n"
"ds_read_b64_tr_b16 v[234:235], v244 offset:59904\n"
"ds_read_b64_tr_b16 v[236:237], v244 offset:59656\n"
"ds_read_b64_tr_b16 v[238:239], v244 offset:59912\n"
"v_mfma_f32_16x16x32_bf16 v[56:59], v[216:219], a[40:43], v[56:59]\n"
"v_cvt_pk_bf16_f32 v135, v166, v167\n"
"s_add_u32 m0, s72, 0x1e380\n"
"v_mfma_f32_16x16x32_bf16 v[60:63], v[216:219], a[44:47], v[60:63]\n"
"buffer_load_dwordx4 v202, s[44:47], s86 offen offset:640 lds\n"
"v_mfma_f32_16x16x32_bf16 v[112:115], v[220:223], a[32:35], v[112:115]\n"
"ds_read_b64_tr_b16 v[208:209], v245 offset:0\n"
"ds_read_b64_tr_b16 v[210:211], v245 offset:256\n"
"v_mfma_f32_16x16x32_bf16 v[116:119], v[220:223], a[36:39], v[116:119]\n"
"ds_read_b64_tr_b16 v[212:213], v245 offset:8\n"
"ds_read_b64_tr_b16 v[214:215], v245 offset:264\n"
"v_mfma_f32_16x16x32_bf16 v[120:123], v[220:223], a[40:43], v[120:123]\n"
"ds_read_b64_tr_b16 v[216:217], v245 offset:512\n"
"ds_read_b64_tr_b16 v[218:219], v245 offset:768\n"
"v_mfma_f32_16x16x32_bf16 v[124:127], v[220:223], a[44:47], v[124:127]\n"
"v_mul_f32 v136, s95, v136\n"
"s_waitcnt lgkmcnt(6)\n"
"v_mfma_f32_16x16x32_bf16 v[32:35], v[224:227], a[48:51], v[32:35]\n"
"s_waitcnt vmcnt(12)\n"
"s_barrier\n"
"s_add_u32 m0, s72, 0x1ee80\n"
"v_mfma_f32_16x16x32_bf16 v[36:39], v[224:227], a[52:55], v[36:39]\n"
"buffer_load_dwordx4 v200, s[44:47], s86 offen offset:128 lds\n"
"v_mfma_f32_16x16x32_bf16 v[40:43], v[224:227], a[56:59], v[40:43]\n"
"ds_read_b64_tr_b16 v[220:221], v245 offset:520\n"
"ds_read_b64_tr_b16 v[222:223], v245 offset:776\n"
"v_mfma_f32_16x16x32_bf16 v[44:47], v[224:227], a[60:63], v[44:47]\n"
"v_mul_f32 v137, s95, v137\n"
"s_add_u32 m0, s72, 0x1f680\n"
"v_mfma_f32_16x16x32_bf16 v[96:99], v[228:231], a[48:51], v[96:99]\n"
"buffer_load_dwordx4 v203, s[44:47], s86 offen offset:128 lds\n"
"v_mfma_f32_16x16x32_bf16 v[100:103], v[228:231], a[52:55], v[100:103]\n"
"v_mul_f32 v138, s95, v138\n"
"v_mul_f32 v139, s95, v139\n"
"v_mfma_f32_16x16x32_bf16 v[104:107], v[228:231], a[56:59], v[104:107]\n"
"v_mul_f32 v168, s95, v168\n"
"s_add_u32 m0, s72, 0x1fc80\n"
"v_mfma_f32_16x16x32_bf16 v[108:111], v[228:231], a[60:63], v[108:111]\n"
"buffer_load_dwordx4 v200, s[44:47], s86 offen offset:640 lds\n"
"v_mfma_f32_16x16x32_bf16 v[48:51], v[232:235], a[48:51], v[48:51]\n"
"v_mul_f32 v169, s95, v169\n"
"v_mul_f32 v170, s95, v170\n"
"v_mfma_f32_16x16x32_bf16 v[52:55], v[232:235], a[52:55], v[52:55]\n"
"v_mul_f32 v171, s95, v171\n"
"s_add_u32 m0, s72, 0x20480\n"
"v_mfma_f32_16x16x32_bf16 v[56:59], v[232:235], a[56:59], v[56:59]\n"
"buffer_load_dwordx4 v203, s[44:47], s86 offen offset:640 lds\n"
"v_mfma_f32_16x16x32_bf16 v[60:63], v[232:235], a[60:63], v[60:63]\n"
"v_cvt_pk_bf16_f32 v136, v136, v137\n"
"v_cvt_pk_bf16_f32 v137, v138, v139\n"
"v_mfma_f32_16x16x32_bf16 v[112:115], v[236:239], a[48:51], v[112:115]\n"
"ds_read_b64_tr_b16 v[224:225], v245 offset:16896\n"
"ds_read_b64_tr_b16 v[226:227], v245 offset:17152\n"
"v_mfma_f32_16x16x32_bf16 v[116:119], v[236:239], a[52:55], v[116:119]\n"
"ds_read_b64_tr_b16 v[228:229], v245 offset:16904\n"
"ds_read_b64_tr_b16 v[230:231], v245 offset:17160\n"
"v_mfma_f32_16x16x32_bf16 v[120:123], v[236:239], a[56:59], v[120:123]\n"
"ds_read_b64_tr_b16 v[232:233], v245 offset:17408\n"
"ds_read_b64_tr_b16 v[234:235], v245 offset:17664\n"
"v_mfma_f32_16x16x32_bf16 v[124:127], v[236:239], a[60:63], v[124:127]\n"
"v_cvt_pk_bf16_f32 v138, v168, v169\n"
"s_waitcnt lgkmcnt(6)\n"
"v_mfma_f32_16x16x32_bf16 v[32:35], v[208:211], a[64:67], v[32:35]\n"
"ds_read_b64_tr_b16 v[236:237], v245 offset:17416\n"
"ds_read_b64_tr_b16 v[238:239], v245 offset:17672\n"
"v_mfma_f32_16x16x32_bf16 v[36:39], v[208:211], a[68:71], v[36:39]\n"
"v_cvt_pk_bf16_f32 v139, v170, v171\n"
"v_mul_f32 v140, s95, v140\n"
"v_mfma_f32_16x16x32_bf16 v[40:43], v[208:211], a[72:75], v[40:43]\n"
"s_waitcnt vmcnt(12)\n"
"s_barrier\n"
"s_add_u32 m0, s72, 0x20f80\n"
"v_mfma_f32_16x16x32_bf16 v[44:47], v[208:211], a[76:79], v[44:47]\n"
"buffer_load_dwordx4 v199, s[44:47], s87 offen offset:128 lds\n"
"v_mfma_f32_16x16x32_bf16 v[96:99], v[212:215], a[64:67], v[96:99]\n"
"v_mul_f32 v141, s95, v141\n"
"v_mul_f32 v142, s95, v142\n"
"v_mfma_f32_16x16x32_bf16 v[100:103], v[212:215], a[68:71], v[100:103]\n"
"v_mul_f32 v143, s95, v143\n"
"s_add_u32 m0, s72, 0x21780\n"
"v_mfma_f32_16x16x32_bf16 v[104:107], v[212:215], a[72:75], v[104:107]\n"
"buffer_load_dwordx4 v202, s[44:47], s87 offen offset:128 lds\n"
"v_mfma_f32_16x16x32_bf16 v[108:111], v[212:215], a[76:79], v[108:111]\n"
"v_mul_f32 v172, s95, v172\n"
"v_mul_f32 v173, s95, v173\n"
"v_mfma_f32_16x16x32_bf16 v[48:51], v[216:219], a[64:67], v[48:51]\n"
"v_mul_f32 v174, s95, v174\n"
"s_add_u32 m0, s72, 0x21d80\n"
"v_mfma_f32_16x16x32_bf16 v[52:55], v[216:219], a[68:71], v[52:55]\n"
"buffer_load_dwordx4 v199, s[44:47], s87 offen offset:640 lds\n"
"v_mfma_f32_16x16x32_bf16 v[56:59], v[216:219], a[72:75], v[56:59]\n"
"v_mul_f32 v175, s95, v175\n"
"v_cvt_pk_bf16_f32 v140, v140, v141\n"
"v_mfma_f32_16x16x32_bf16 v[60:63], v[216:219], a[76:79], v[60:63]\n"
"v_cvt_pk_bf16_f32 v141, v142, v143\n"
"s_add_u32 m0, s72, 0x22580\n"
"v_mfma_f32_16x16x32_bf16 v[112:115], v[220:223], a[64:67], v[112:115]\n"
"buffer_load_dwordx4 v202, s[44:47], s87 offen offset:640 lds\n"
"v_mfma_f32_16x16x32_bf16 v[116:119], v[220:223], a[68:71], v[116:119]\n"
"ds_read_b64_tr_b16 v[208:209], v245 offset:8448\n"
"ds_read_b64_tr_b16 v[210:211], v245 offset:8704\n"
"v_mfma_f32_16x16x32_bf16 v[120:123], v[220:223], a[72:75], v[120:123]\n"
"v_cvt_pk_bf16_f32 v142, v172, v173\n"
"v_mfma_f32_16x16x32_bf16 v[124:127], v[220:223], a[76:79], v[124:127]\n"
"s_waitcnt lgkmcnt(2)\n"
"v_mfma_f32_16x16x32_bf16 v[32:35], v[224:227], a[80:83], v[32:35]\n"
"ds_read_b64_tr_b16 v[212:213], v245 offset:8456\n"
"ds_read_b64_tr_b16 v[214:215], v245 offset:8712\n"
"v_mfma_f32_16x16x32_bf16 v[36:39], v[224:227], a[84:87], v[36:39]\n"
"s_waitcnt vmcnt(12)\n"
"s_barrier\n"
"s_add_u32 m0, s72, 0x25180\n"
"v_mfma_f32_16x16x32_bf16 v[40:43], v[224:227], a[88:91], v[40:43]\n"
"buffer_load_dwordx4 v200, s[44:47], s87 offen offset:128 lds\n"
"v_mfma_f32_16x16x32_bf16 v[44:47], v[224:227], a[92:95], v[44:47]\n"
"ds_read_b64_tr_b16 v[216:217], v245 offset:8960\n"
"ds_read_b64_tr_b16 v[218:219], v245 offset:9216\n"
"v_mfma_f32_16x16x32_bf16 v[96:99], v[228:231], a[80:83], v[96:99]\n"
"v_cvt_pk_bf16_f32 v143, v174, v175\n"
"s_add_u32 m0, s72, 0x25980\n"
"v_mfma_f32_16x16x32_bf16 v[100:103], v[228:231], a[84:87], v[100:103]\n"
"buffer_load_dwordx4 v203, s[44:47], s87 offen offset:128 lds\n"
"v_mfma_f32_16x16x32_bf16 v[104:107], v[228:231], a[88:91], v[104:107]\n"
"ds_read_b64_tr_b16 v[220:221], v245 offset:8968\n"
"ds_read_b64_tr_b16 v[222:223], v245 offset:9224\n"
"v_mfma_f32_16x16x32_bf16 v[108:111], v[228:231], a[92:95], v[108:111]\n"
"v_mul_f32 v144, s95, v144\n"
"s_add_u32 m0, s72, 0x25f80\n"
"v_mfma_f32_16x16x32_bf16 v[48:51], v[232:235], a[80:83], v[48:51]\n"
"buffer_load_dwordx4 v200, s[44:47], s87 offen offset:640 lds\n"
"v_mfma_f32_16x16x32_bf16 v[52:55], v[232:235], a[84:87], v[52:55]\n"
"v_mul_f32 v145, s95, v145\n"
"v_mul_f32 v146, s95, v146\n"
"v_mfma_f32_16x16x32_bf16 v[56:59], v[232:235], a[88:91], v[56:59]\n"
"v_mul_f32 v147, s95, v147\n"
"s_add_u32 m0, s72, 0x26780\n"
"v_mfma_f32_16x16x32_bf16 v[60:63], v[232:235], a[92:95], v[60:63]\n"
"buffer_load_dwordx4 v203, s[44:47], s87 offen offset:640 lds\n"
"v_mfma_f32_16x16x32_bf16 v[112:115], v[236:239], a[80:83], v[112:115]\n"
"ds_read_b64_tr_b16 v[224:225], v244 offset:33792\n"
"ds_read_b64_tr_b16 v[226:227], v244 offset:34048\n"
"v_mfma_f32_16x16x32_bf16 v[116:119], v[236:239], a[84:87], v[116:119]\n"
"ds_read_b64_tr_b16 v[228:229], v244 offset:33800\n"
"ds_read_b64_tr_b16 v[230:231], v244 offset:34056\n"
"v_mfma_f32_16x16x32_bf16 v[120:123], v[236:239], a[88:91], v[120:123]\n"
"ds_read_b64_tr_b16 v[232:233], v244 offset:34304\n"
"ds_read_b64_tr_b16 v[234:235], v244 offset:34560\n"
"v_mfma_f32_16x16x32_bf16 v[124:127], v[236:239], a[92:95], v[124:127]\n"
"v_mul_f32 v176, s95, v176\n"
"s_waitcnt lgkmcnt(6)\n"
"v_mfma_f32_16x16x32_bf16 v[32:35], v[208:211], a[96:99], v[32:35]\n"
"s_barrier\n"
"s_add_u32 m0, s72, 0x23080\n"
"v_mfma_f32_16x16x32_bf16 v[36:39], v[208:211], a[100:103], v[36:39]\n"
"buffer_load_dwordx4 v199, s[44:47], s88 offen offset:128 lds\n"
"v_mfma_f32_16x16x32_bf16 v[40:43], v[208:211], a[104:107], v[40:43]\n"
"ds_read_b64_tr_b16 v[236:237], v244 offset:34312\n"
"ds_read_b64_tr_b16 v[238:239], v244 offset:34568\n"
"v_mfma_f32_16x16x32_bf16 v[44:47], v[208:211], a[108:111], v[44:47]\n"
"v_mul_f32 v177, s95, v177\n"
"s_add_u32 m0, s72, 0x23880\n"
"v_mfma_f32_16x16x32_bf16 v[96:99], v[212:215], a[96:99], v[96:99]\n"
"buffer_load_dwordx4 v202, s[44:47], s88 offen offset:128 lds\n"
"v_mfma_f32_16x16x32_bf16 v[100:103], v[212:215], a[100:103], v[100:103]\n"
"v_mul_f32 v178, s95, v178\n"
"v_mul_f32 v179, s95, v179\n"
"v_mfma_f32_16x16x32_bf16 v[104:107], v[212:215], a[104:107], v[104:107]\n"
"v_cvt_pk_bf16_f32 v144, v144, v145\n"
"s_add_u32 m0, s72, 0x23e80\n"
"v_mfma_f32_16x16x32_bf16 v[108:111], v[212:215], a[108:111], v[108:111]\n"
"buffer_load_dwordx4 v199, s[44:47], s88 offen offset:640 lds\n"
"v_mfma_f32_16x16x32_bf16 v[48:51], v[216:219], a[96:99], v[48:51]\n"
"v_cvt_pk_bf16_f32 v145, v146, v147\n"
"v_cvt_pk_bf16_f32 v146, v176, v177\n"
"v_mfma_f32_16x16x32_bf16 v[52:55], v[216:219], a[100:103], v[52:55]\n"
"v_cvt_pk_bf16_f32 v147, v178, v179\n"
"s_add_u32 m0, s72, 0x24680\n"
"v_mfma_f32_16x16x32_bf16 v[56:59], v[216:219], a[104:107], v[56:59]\n"
"buffer_load_dwordx4 v202, s[44:47], s88 offen offset:640 lds\n"
"v_mfma_f32_16x16x32_bf16 v[60:63], v[216:219], a[108:111], v[60:63]\n"
"v_mul_f32 v148, s95, v148\n"
"v_mul_f32 v149, s95, v149\n"
"v_mfma_f32_16x16x32_bf16 v[112:115], v[220:223], a[96:99], v[112:115]\n"
"v_mul_f32 v150, s95, v150\n"
"v_mul_f32 v151, s95, v151\n"
"v_mfma_f32_16x16x32_bf16 v[116:119], v[220:223], a[100:103], v[116:119]\n"
"v_mul_f32 v180, s95, v180\n"
"v_mul_f32 v181, s95, v181\n"
"v_mfma_f32_16x16x32_bf16 v[120:123], v[220:223], a[104:107], v[120:123]\n"
"v_mul_f32 v182, s95, v182\n"
"v_mul_f32 v183, s95, v183\n"
"v_mfma_f32_16x16x32_bf16 v[124:127], v[220:223], a[108:111], v[124:127]\n"
"v_cvt_pk_bf16_f32 v148, v148, v149\n"
"s_waitcnt lgkmcnt(0)\n"
"v_mfma_f32_16x16x32_bf16 v[32:35], v[224:227], a[112:115], v[32:35]\n"
"v_cvt_pk_bf16_f32 v149, v150, v151\n"
"v_cvt_pk_bf16_f32 v150, v180, v181\n"
"v_mfma_f32_16x16x32_bf16 v[36:39], v[224:227], a[116:119], v[36:39]\n"
"v_cvt_pk_bf16_f32 v151, v182, v183\n"
"v_mul_f32 v152, s95, v152\n"
"v_mfma_f32_16x16x32_bf16 v[40:43], v[224:227], a[120:123], v[40:43]\n"
"v_mul_f32 v153, s95, v153\n"
"v_mfma_f32_16x16x32_bf16 v[44:47], v[224:227], a[124:127], v[44:47]\n"
"v_mfma_f32_16x16x32_bf16 v[96:99], v[228:231], a[112:115], v[96:99]\n"
"s_waitcnt vmcnt(16)\n"
"s_barrier\n"
"ds_read_b64_tr_b16 v[208:209], v244 offset:50688\n"
"ds_read_b64_tr_b16 v[210:211], v244 offset:50944\n"
"v_mfma_f32_16x16x32_bf16 v[100:103], v[228:231], a[116:119], v[100:103]\n"
"ds_read_b64_tr_b16 v[212:213], v244 offset:50696\n"
"ds_read_b64_tr_b16 v[214:215], v244 offset:50952\n"
"v_mfma_f32_16x16x32_bf16 v[104:107], v[228:231], a[120:123], v[104:107]\n"
"ds_read_b64_tr_b16 v[216:217], v244 offset:51200\n"
"ds_read_b64_tr_b16 v[218:219], v244 offset:51456\n"
"v_mfma_f32_16x16x32_bf16 v[108:111], v[228:231], a[124:127], v[108:111]\n"
"ds_read_b64_tr_b16 v[220:221], v244 offset:51208\n"
"ds_read_b64_tr_b16 v[222:223], v244 offset:51464\n"
"v_mfma_f32_16x16x32_bf16 v[48:51], v[232:235], a[112:115], v[48:51]\n"
"v_mul_f32 v154, s95, v154\n"
"v_mul_f32 v155, s95, v155\n"
"v_mfma_f32_16x16x32_bf16 v[52:55], v[232:235], a[116:119], v[52:55]\n"
"v_mul_f32 v184, s95, v184\n"
"v_mul_f32 v185, s95, v185\n"
"v_mfma_f32_16x16x32_bf16 v[56:59], v[232:235], a[120:123], v[56:59]\n"
"v_mul_f32 v186, s95, v186\n"
"v_mul_f32 v187, s95, v187\n"
"v_mfma_f32_16x16x32_bf16 v[60:63], v[232:235], a[124:127], v[60:63]\n"
"v_cvt_pk_bf16_f32 v152, v152, v153\n"
"v_cvt_pk_bf16_f32 v153, v154, v155\n"
"v_mfma_f32_16x16x32_bf16 v[112:115], v[236:239], a[112:115], v[112:115]\n"
"v_cvt_pk_bf16_f32 v154, v184, v185\n"
"v_cvt_pk_bf16_f32 v155, v186, v187\n"
"v_mfma_f32_16x16x32_bf16 v[116:119], v[236:239], a[116:119], v[116:119]\n"
"v_mul_f32 v156, s95, v156\n"
"v_mul_f32 v157, s95, v157\n"
"v_mfma_f32_16x16x32_bf16 v[120:123], v[236:239], a[120:123], v[120:123]\n"
"v_mul_f32 v158, s95, v158\n"
"v_mfma_f32_16x16x32_bf16 v[124:127], v[236:239], a[124:127], v[124:127]\n"
"s_waitcnt lgkmcnt(0)\n"
"v_mfma_f32_16x16x32_bf16 v[32:35], v[208:211], a[128:131], v[32:35]\n"
"v_mul_f32 v159, s95, v159\n"
"v_mul_f32 v188, s95, v188\n"
"v_mfma_f32_16x16x32_bf16 v[36:39], v[208:211], a[132:135], v[36:39]\n"
"s_waitcnt vmcnt(12)\n"
"s_barrier\n"
"s_add_u32 m0, s72, 0x1cd80\n"
"v_mfma_f32_16x16x32_bf16 v[40:43], v[208:211], a[136:139], v[40:43]\n"
"buffer_load_dwordx4 v200, s[44:47], s88 offen offset:128 lds\n"
"v_mfma_f32_16x16x32_bf16 v[44:47], v[208:211], a[140:143], v[44:47]\n"
"ds_read_b64_tr_b16 v[224:225], v244 offset:59136\n"
"ds_read_b64_tr_b16 v[226:227], v244 offset:59392\n"
"v_mfma_f32_16x16x32_bf16 v[96:99], v[212:215], a[128:131], v[96:99]\n"
"v_mul_f32 v189, s95, v189\n"
"s_add_u32 m0, s72, 0x1d580\n"
"v_mfma_f32_16x16x32_bf16 v[100:103], v[212:215], a[132:135], v[100:103]\n"
"buffer_load_dwordx4 v203, s[44:47], s88 offen offset:128 lds\n"
"v_mfma_f32_16x16x32_bf16 v[104:107], v[212:215], a[136:139], v[104:107]\n"
"ds_read_b64_tr_b16 v[228:229], v244 offset:59144\n"
"ds_read_b64_tr_b16 v[230:231], v244 offset:59400\n"
"v_mfma_f32_16x16x32_bf16 v[108:111], v[212:215], a[140:143], v[108:111]\n"
"v_mul_f32 v190, s95, v190\n"
"s_add_u32 m0, s72, 0x1db80\n"
"v_mfma_f32_16x16x32_bf16 v[48:51], v[216:219], a[128:131], v[48:51]\n"
"buffer_load_dwordx4 v200, s[44:47], s88 offen offset:640 lds\n"
"v_mfma_f32_16x16x32_bf16 v[52:55], v[216:219], a[132:135], v[52:55]\n"
"ds_read_b64_tr_b16 v[232:233], v244 offset:59648\n"
"ds_read_b64_tr_b16 v[234:235], v244 offset:59904\n"
"ds_read_b64_tr_b16 v[236:237], v244 offset:59656\n"
"ds_read_b64_tr_b16 v[238:239], v244 offset:59912\n"
"v_mfma_f32_16x16x32_bf16 v[56:59], v[216:219], a[136:139], v[56:59]\n"
"v_mul_f32 v191, s95, v191\n"
"s_add_u32 m0, s72, 0x1e380\n"
"v_mfma_f32_16x16x32_bf16 v[60:63], v[216:219], a[140:143], v[60:63]\n"
"buffer_load_dwordx4 v203, s[44:47], s88 offen offset:640 lds\n"
"v_mfma_f32_16x16x32_bf16 v[112:115], v[220:223], a[128:131], v[112:115]\n"
"v_cvt_pk_bf16_f32 v156, v156, v157\n"
"v_cvt_pk_bf16_f32 v157, v158, v159\n"
"v_mfma_f32_16x16x32_bf16 v[116:119], v[220:223], a[132:135], v[116:119]\n"
"v_cvt_pk_bf16_f32 v158, v188, v189\n"
"v_cvt_pk_bf16_f32 v159, v190, v191\n"
"v_mfma_f32_16x16x32_bf16 v[120:123], v[220:223], a[136:139], v[120:123]\n"
"v_mfma_f32_16x16x32_bf16 v[124:127], v[220:223], a[140:143], v[124:127]\n"
"s_waitcnt lgkmcnt(0)\n"
"v_mfma_f32_16x16x32_bf16 v[32:35], v[224:227], a[144:147], v[32:35]\n"
"v_mfma_f32_16x16x32_bf16 v[36:39], v[224:227], a[148:151], v[36:39]\n"
"v_mfma_f32_16x16x32_bf16 v[40:43], v[224:227], a[152:155], v[40:43]\n"
"v_mfma_f32_16x16x32_bf16 v[44:47], v[224:227], a[156:159], v[44:47]\n"
"v_mfma_f32_16x16x32_bf16 v[96:99], v[228:231], a[144:147], v[96:99]\n"
"s_waitcnt vmcnt(12)\n"
"s_barrier\n"
"ds_read_b64_tr_b16 v[208:209], v245 offset:0\n"
"ds_read_b64_tr_b16 v[210:211], v245 offset:256\n"
"v_mfma_f32_16x16x32_bf16 v[100:103], v[228:231], a[148:151], v[100:103]\n"
"ds_read_b64_tr_b16 v[212:213], v245 offset:8\n"
"ds_read_b64_tr_b16 v[214:215], v245 offset:264\n"
"v_mfma_f32_16x16x32_bf16 v[104:107], v[228:231], a[152:155], v[104:107]\n"
"v_mfma_f32_16x16x32_bf16 v[108:111], v[228:231], a[156:159], v[108:111]\n"
"v_mfma_f32_16x16x32_bf16 v[48:51], v[232:235], a[144:147], v[48:51]\n"
"ds_read_b64_tr_b16 v[216:217], v245 offset:512\n"
"ds_read_b64_tr_b16 v[218:219], v245 offset:768\n"
"v_mfma_f32_16x16x32_bf16 v[52:55], v[232:235], a[148:151], v[52:55]\n"
"ds_read_b64_tr_b16 v[220:221], v245 offset:520\n"
"ds_read_b64_tr_b16 v[222:223], v245 offset:776\n"
"v_mfma_f32_16x16x32_bf16 v[56:59], v[232:235], a[152:155], v[56:59]\n"
"v_mfma_f32_16x16x32_bf16 v[60:63], v[232:235], a[156:159], v[60:63]\n"
"v_mfma_f32_16x16x32_bf16 v[112:115], v[236:239], a[144:147], v[112:115]\n"
"v_mfma_f32_16x16x32_bf16 v[116:119], v[236:239], a[148:151], v[116:119]\n"
"v_mfma_f32_16x16x32_bf16 v[120:123], v[236:239], a[152:155], v[120:123]\n"
"v_mfma_f32_16x16x32_bf16 v[124:127], v[236:239], a[156:159], v[124:127]\n"
"s_waitcnt lgkmcnt(0)\n"
"v_mfma_f32_16x16x32_bf16 v[32:35], v[208:211], a[160:163], v[32:35]\n"
"v_mfma_f32_16x16x32_bf16 v[36:39], v[208:211], a[164:167], v[36:39]\n"
"v_mfma_f32_16x16x32_bf16 v[40:43], v[208:211], a[168:171], v[40:43]\n"
"v_mfma_f32_16x16x32_bf16 v[44:47], v[208:211], a[172:175], v[44:47]\n"
"v_mfma_f32_16x16x32_bf16 v[96:99], v[212:215], a[160:163], v[96:99]\n"
"s_waitcnt vmcnt(8)\n"
"s_barrier\n"
"ds_read_b64_tr_b16 v[224:225], v245 offset:16896\n"
"ds_read_b64_tr_b16 v[226:227], v245 offset:17152\n"
"v_mfma_f32_16x16x32_bf16 v[100:103], v[212:215], a[164:167], v[100:103]\n"
"ds_read_b64_tr_b16 v[228:229], v245 offset:16904\n"
"ds_read_b64_tr_b16 v[230:231], v245 offset:17160\n"
"v_mfma_f32_16x16x32_bf16 v[104:107], v[212:215], a[168:171], v[104:107]\n"
"ds_read_b64_tr_b16 v[232:233], v245 offset:17408\n"
"ds_read_b64_tr_b16 v[234:235], v245 offset:17664\n"
"v_mfma_f32_16x16x32_bf16 v[108:111], v[212:215], a[172:175], v[108:111]\n"
"ds_read_b64_tr_b16 v[236:237], v245 offset:17416\n"
"ds_read_b64_tr_b16 v[238:239], v245 offset:17672\n"
"v_mfma_f32_16x16x32_bf16 v[48:51], v[216:219], a[160:163], v[48:51]\n"
"v_mfma_f32_16x16x32_bf16 v[52:55], v[216:219], a[164:167], v[52:55]\n"
"v_mfma_f32_16x16x32_bf16 v[56:59], v[216:219], a[168:171], v[56:59]\n"
"v_mfma_f32_16x16x32_bf16 v[60:63], v[216:219], a[172:175], v[60:63]\n"
"v_mfma_f32_16x16x32_bf16 v[112:115], v[220:223], a[160:163], v[112:115]\n"
"v_mfma_f32_16x16x32_bf16 v[116:119], v[220:223], a[164:167], v[116:119]\n"
"v_mfma_f32_16x16x32_bf16 v[120:123], v[220:223], a[168:171], v[120:123]\n"
"v_mfma_f32_16x16x32_bf16 v[124:127], v[220:223], a[172:175], v[124:127]\n"
"s_waitcnt lgkmcnt(0)\n"
"v_mfma_f32_16x16x32_bf16 v[32:35], v[224:227], a[176:179], v[32:35]\n"
"v_mfma_f32_16x16x32_bf16 v[36:39], v[224:227], a[180:183], v[36:39]\n"
"v_mfma_f32_16x16x32_bf16 v[40:43], v[224:227], a[184:187], v[40:43]\n"
"v_mfma_f32_16x16x32_bf16 v[44:47], v[224:227], a[188:191], v[44:47]\n"
"v_mfma_f32_16x16x32_bf16 v[96:99], v[228:231], a[176:179], v[96:99]\n"
"s_waitcnt vmcnt(4)\n"
"s_barrier\n"
"ds_read_b64_tr_b16 v[208:209], v245 offset:8448\n"
"ds_read_b64_tr_b16 v[210:211], v245 offset:8704\n"
"v_mfma_f32_16x16x32_bf16 v[100:103], v[228:231], a[180:183], v[100:103]\n"
"ds_read_b64_tr_b16 v[212:213], v245 offset:8456\n"
"ds_read_b64_tr_b16 v[214:215], v245 offset:8712\n"
"v_mfma_f32_16x16x32_bf16 v[104:107], v[228:231], a[184:187], v[104:107]\n"
"ds_read_b64_tr_b16 v[216:217], v245 offset:8960\n"
"ds_read_b64_tr_b16 v[218:219], v245 offset:9216\n"
"v_mfma_f32_16x16x32_bf16 v[108:111], v[228:231], a[188:191], v[108:111]\n"
"ds_read_b64_tr_b16 v[220:221], v245 offset:8968\n"
"ds_read_b64_tr_b16 v[222:223], v245 offset:9224\n"
"v_mfma_f32_16x16x32_bf16 v[48:51], v[232:235], a[176:179], v[48:51]\n"
"v_mfma_f32_16x16x32_bf16 v[52:55], v[232:235], a[180:183], v[52:55]\n"
"v_mfma_f32_16x16x32_bf16 v[56:59], v[232:235], a[184:187], v[56:59]\n"
"v_mfma_f32_16x16x32_bf16 v[60:63], v[232:235], a[188:191], v[60:63]\n"
"v_mfma_f32_16x16x32_bf16 v[112:115], v[236:239], a[176:179], v[112:115]\n"
"v_mfma_f32_16x16x32_bf16 v[116:119], v[236:239], a[180:183], v[116:119]\n"
"v_mfma_f32_16x16x32_bf16 v[120:123], v[236:239], a[184:187], v[120:123]\n"
"v_mfma_f32_16x16x32_bf16 v[124:127], v[236:239], a[188:191], v[124:127]\n"
"s_waitcnt lgkmcnt(0)\n"
"v_mfma_f32_16x16x32_bf16 v[32:35], v[208:211], a[192:195], v[32:35]\n"
"v_mfma_f32_16x16x32_bf16 v[36:39], v[208:211], a[196:199], v[36:39]\n"
"v_mfma_f32_16x16x32_bf16 v[40:43], v[208:211], a[200:203], v[40:43]\n"
"v_mfma_f32_16x16x32_bf16 v[44:47], v[208:211], a[204:207], v[44:47]\n"
"v_mfma_f32_16x16x32_bf16 v[96:99], v[212:215], a[192:195], v[96:99]\n"
"s_waitcnt vmcnt(0)\n"
"s_barrier\n"
"ds_read_b64_tr_b16 v[224:225], v244 offset:50688\n"
"ds_read_b64_tr_b16 v[226:227], v244 offset:50944\n"
"v_mfma_f32_16x16x32_bf16 v[100:103], v[212:215], a[196:199], v[100:103]\n"
"ds_read_b64_tr_b16 v[228:229], v244 offset:50696\n"
"ds_read_b64_tr_b16 v[230:231], v244 offset:50952\n"
"v_mfma_f32_16x16x32_bf16 v[104:107], v[212:215], a[200:203], v[104:107]\n"
"ds_read_b64_tr_b16 v[232:233], v244 offset:51200\n"
"ds_read_b64_tr_b16 v[234:235], v244 offset:51456\n"
"v_mfma_f32_16x16x32_bf16 v[108:111], v[212:215], a[204:207], v[108:111]\n"
"ds_read_b64_tr_b16 v[236:237], v244 offset:51208\n"
"ds_read_b64_tr_b16 v[238:239], v244 offset:51464\n"
"v_mfma_f32_16x16x32_bf16 v[48:51], v[216:219], a[192:195], v[48:51]\n"
"v_mfma_f32_16x16x32_bf16 v[52:55], v[216:219], a[196:199], v[52:55]\n"
"v_mfma_f32_16x16x32_bf16 v[56:59], v[216:219], a[200:203], v[56:59]\n"
"v_mfma_f32_16x16x32_bf16 v[60:63], v[216:219], a[204:207], v[60:63]\n"
"v_mfma_f32_16x16x32_bf16 v[112:115], v[220:223], a[192:195], v[112:115]\n"
"ds_read_b128 v[208:211], v240 offset:0\n"
"ds_read_b128 v[212:215], v240 offset:8448\n"
"v_mfma_f32_16x16x32_bf16 v[116:119], v[220:223], a[196:199], v[116:119]\n"
"ds_read_b128 v[216:219], v240 offset:64\n"
"v_mfma_f32_16x16x32_bf16 v[120:123], v[220:223], a[200:203], v[120:123]\n"
"v_mfma_f32_16x16x32_bf16 v[124:127], v[220:223], a[204:207], v[124:127]\n"
"ds_read_b128 v[220:223], v240 offset:8512\n"
"s_waitcnt lgkmcnt(4)\n"
"v_mfma_f32_16x16x32_bf16 v[32:35], v[224:227], a[208:211], v[32:35]\n"
"v_mfma_f32_16x16x32_bf16 v[36:39], v[224:227], a[212:215], v[36:39]\n"
"v_mfma_f32_16x16x32_bf16 v[40:43], v[224:227], a[216:219], v[40:43]\n"
"v_mfma_f32_16x16x32_bf16 v[44:47], v[224:227], a[220:223], v[44:47]\n"
"v_mfma_f32_16x16x32_bf16 v[96:99], v[228:231], a[208:211], v[96:99]\n"
"v_mfma_f32_16x16x32_bf16 v[100:103], v[228:231], a[212:215], v[100:103]\n"
"v_mfma_f32_16x16x32_bf16 v[104:107], v[228:231], a[216:219], v[104:107]\n"
"v_mfma_f32_16x16x32_bf16 v[108:111], v[228:231], a[220:223], v[108:111]\n"
"v_mfma_f32_16x16x32_bf16 v[48:51], v[232:235], a[208:211], v[48:51]\n"
"v_mul_f32 v32, s95, v32\n"
"v_mul_f32 v33, s95, v33\n"
"v_mfma_f32_16x16x32_bf16 v[52:55], v[232:235], a[212:215], v[52:55]\n"
"v_mul_f32 v34, s95, v34\n"
"v_mul_f32 v35, s95, v35\n"
"v_mfma_f32_16x16x32_bf16 v[56:59], v[232:235], a[216:219], v[56:59]\n"
"v_mul_f32 v96, s95, v96\n"
"v_mul_f32 v97, s95, v97\n"
"v_mfma_f32_16x16x32_bf16 v[60:63], v[232:235], a[220:223], v[60:63]\n"
"v_mul_f32 v98, s95, v98\n"
"v_mul_f32 v99, s95, v99\n"
"v_mfma_f32_16x16x32_bf16 v[112:115], v[236:239], a[208:211], v[112:115]\n"
"ds_read_b128 v[224:227], v240 offset:128\n"
"ds_read_b128 v[228:231], v240 offset:8576\n"
"v_mfma_f32_16x16x32_bf16 v[116:119], v[236:239], a[212:215], v[116:119]\n"
"ds_read_b128 v[232:235], v240 offset:192\n"
"v_cvt_pk_bf16_f32 v32, v32, v33\n"
"v_mfma_f32_16x16x32_bf16 v[120:123], v[236:239], a[216:219], v[120:123]\n"
"v_cvt_pk_bf16_f32 v33, v34, v35\n"
"v_cvt_pk_bf16_f32 v34, v96, v97\n"
"v_mfma_f32_16x16x32_bf16 v[124:127], v[236:239], a[220:223], v[124:127]\n"
"ds_read_b128 v[236:239], v240 offset:8640\n"
"L_join_%=:\n"
"s_waitcnt lgkmcnt(4)\n"
"v_mfma_f32_16x16x32_bf16 v[160:163], v[208:211], v[0:3], 0\n"
"v_cvt_pk_bf16_f32 v35, v98, v99\n"
"v_mul_f32 v36, s95, v36\n"
"v_mfma_f32_16x16x32_bf16 v[164:167], v[208:211], v[4:7], 0\n"
"v_mul_f32 v37, s95, v37\n"
"v_mul_f32 v38, s95, v38\n"
"v_mfma_f32_16x16x32_bf16 v[168:171], v[208:211], v[8:11], 0\n"
"v_mul_f32 v39, s95, v39\n"
"v_mul_f32 v100, s95, v100\n"
"v_mfma_f32_16x16x32_bf16 v[172:175], v[208:211], v[12:15], 0\n"
"v_mul_f32 v101, s95, v101\n"
"v_mul_f32 v102, s95, v102\n"
"v_mfma_f32_16x16x32_bf16 v[176:179], v[212:215], v[0:3], 0\n"
"v_mul_f32 v103, s95, v103\n"
"v_cvt_pk_bf16_f32 v36, v36, v37\n"
"v_mfma_f32_16x16x32_bf16 v[180:183], v[212:215], v[4:7], 0\n"
"v_cvt_pk_bf16_f32 v37, v38, v39\n"
"v_cvt_pk_bf16_f32 v38, v100, v101\n"
"v_mfma_f32_16x16x32_bf16 v[184:187], v[212:215], v[8:11], 0\n"
"buffer_load_dword v249, v252, s[68:71], s30 offen sc1\n"
"v_mfma_f32_16x16x32_bf16 v[188:191], v[212:215], v[12:15], 0\n"
"v_cvt_pk_bf16_f32 v39, v102, v103\n"
"v_mul_f32 v40, s95, v40\n"
"v_mfma_f32_16x16x32_bf16 v[160:163], v[216:219], v[16:19], v[160:163]\n"
"v_mul_f32 v41, s95, v41\n"
"v_mul_f32 v42, s95, v42\n"
"v_mfma_f32_16x16x32_bf16 v[164:167], v[216:219], v[20:23], v[164:167]\n"
"v_mul_f32 v43, s95, v43\n"
"v_mul_f32 v104, s95, v104\n"
"v_mfma_f32_16x16x32_bf16 v[168:171], v[216:219], v[24:27], v[168:171]\n"
"buffer_store_dword v253, v252, s[68:71], s98 offen sc1\n"
"v_mfma_f32_16x16x32_bf16 v[172:175], v[216:219], v[28:31], v[172:175]\n"
"v_mul_f32 v105, s95, v105\n"
"v_mul_f32 v106, s95, v106\n"
"v_mfma_f32_16x16x32_bf16 v[176:179], v[220:223], v[16:19], v[176:179]\n"
"ds_read_b128 v[208:211], v240 offset:256\n"
"ds_read_b128 v[212:215], v240 offset:8704\n"
"v_mfma_f32_16x16x32_bf16 v[180:183], v[220:223], v[20:23], v[180:183]\n"
"ds_read_b128 v[216:219], v240 offset:320\n"
"v_mul_f32 v107, s95, v107\n"
"v_mfma_f32_16x16x32_bf16 v[184:187], v[220:223], v[24:27], v[184:187]\n"
"v_cvt_pk_bf16_f32 v40, v40, v41\n"
"v_cvt_pk_bf16_f32 v41, v42, v43\n"
"v_mfma_f32_16x16x32_bf16 v[188:191], v[220:223], v[28:31], v[188:191]\n"
"ds_read_b128 v[220:223], v240 offset:8768\n"
"s_waitcnt lgkmcnt(4)\n"
"v_mfma_f32_16x16x32_bf16 v[160:163], v[224:227], v[64:67], v[160:163]\n"
"v_cvt_pk_bf16_f32 v42, v104, v105\n"
"v_cvt_pk_bf16_f32 v43, v106, v107\n"
"v_mfma_f32_16x16x32_bf16 v[164:167], v[224:227], v[68:71], v[164:167]\n"
"v_mul_f32 v44, s95, v44\n"
"v_mul_f32 v45, s95, v45\n"
"v_mfma_f32_16x16x32_bf16 v[168:171], v[224:227], v[72:75], v[168:171]\n"
"v_mul_f32 v46, s95, v46\n"
"v_mul_f32 v47, s95, v47\n"
"v_mfma_f32_16x16x32_bf16 v[172:175], v[224:227], v[76:79], v[172:175]\n"
"v_mul_f32 v108, s95, v108\n"
"v_mul_f32 v109, s95, v109\n"
"v_mfma_f32_16x16x32_bf16 v[176:179], v[228:231], v[64:67], v[176:179]\n"
"v_mul_f32 v110, s95, v110\n"
"v_mul_f32 v111, s95, v111\n"
"v_mfma_f32_16x16x32_bf16 v[180:183], v[228:231], v[68:71], v[180:183]\n"
"v_cvt_pk_bf16_f32 v44, v44, v45\n"
"v_cvt_pk_bf16_f32 v45, v46, v47\n"
"v_mfma_f32_16x16x32_bf16 v[184:187], v[228:231], v[72:75], v[184:187]\n"
"v_cvt_pk_bf16_f32 v46, v108, v109\n"
"v_cvt_pk_bf16_f32 v47, v110, v111\n"
"v_mfma_f32_16x16x32_bf16 v[188:191], v[228:231], v[76:79], v[188:191]\n"
"v_mul_f32 v48, s95, v48\n"
"v_mul_f32 v49, s95, v49\n"
"v_mfma_f32_16x16x32_bf16 v[160:163], v[232:235], v[80:83], v[160:163]\n"
"v_mul_f32 v50, s95, v50\n"
"v_mul_f32 v51, s95, v51\n"
"v_mfma_f32_16x16x32_bf16 v[164:167], v[232:235], v[84:87], v[164:167]\n"
"v_mul_f32 v112, s95, v112\n"
"v_mul_f32 v113, s95, v113\n"
"v_mfma_f32_16x16x32_bf16 v[168:171], v[232:235], v[88:91], v[168:171]\n"
"v_mul_f32 v114, s95, v114\n"
"v_mul_f32 v115, s95, v115\n"
"v_mfma_f32_16x16x32_bf16 v[172:175], v[232:235], v[92:95], v[172:175]\n"
"v_cvt_pk_bf16_f32 v48, v48, v49\n"
"v_cvt_pk_bf16_f32 v49, v50, v51\n"
"v_mfma_f32_16x16x32_bf16 v[176:179], v[236:239], v[80:83], v[176:179]\n"
"ds_read_b128 v[224:227], v240 offset:384\n"
"ds_read_b128 v[228:231], v240 offset:8832\n"
"v_mfma_f32_16x16x32_bf16 v[180:183], v[236:239], v[84:87], v[180:183]\n"
"ds_read_b128 v[232:235], v240 offset:448\n"
"v_cvt_pk_bf16_f32 v50, v112, v113\n"
"v_mfma_f32_16x16x32_bf16 v[184:187], v[236:239], v[88:91], v[184:187]\n"
"v_cvt_pk_bf16_f32 v51, v114, v115\n"
"v_mul_f32 v52, s95, v52\n"
"v_mfma_f32_16x16x32_bf16 v[188:191], v[236:239], v[92:95], v[188:191]\n"
"ds_read_b128 v[236:239], v240 offset:8896\n"
"s_waitcnt lgkmcnt(4)\n"
"v_mfma_f32_16x16x32_bf16 v[160:163], v[208:211], v[128:131], v[160:163]\n"
"v_mul_f32 v53, s95, v53\n"
"v_mul_f32 v54, s95, v54\n"
"v_mfma_f32_16x16x32_bf16 v[164:167], v[208:211], v[132:135], v[164:167]\n"
"v_mul_f32 v55, s95, v55\n"
"v_mul_f32 v116, s95, v116\n"
"v_mfma_f32_16x16x32_bf16 v[168:171], v[208:211], v[136:139], v[168:171]\n"
"v_mul_f32 v117, s95, v117\n"
"v_mul_f32 v118, s95, v118\n"
"v_mfma_f32_16x16x32_bf16 v[172:175], v[208:211], v[140:143], v[172:175]\n"
"v_mul_f32 v119, s95, v119\n"
"v_cvt_pk_bf16_f32 v52, v52, v53\n"
"v_mfma_f32_16x16x32_bf16 v[176:179], v[212:215], v[128:131], v[176:179]\n"
"s_bfe_u32 s10, %7, 0x40005\n"
"s_lshl_b32 s10, s10, 2\n"
"s_add_u32 s10, s10, s89\n"
"s_mul_i32 s10, s10, 7168\n"
"v_mfma_f32_16x16x32_bf16 v[180:183], v[212:215], v[132:135], v[180:183]\n"
"v_cvt_pk_bf16_f32 v53, v54, v55\n"
"v_cvt_pk_bf16_f32 v54, v116, v117\n"
"v_mfma_f32_16x16x32_bf16 v[184:187], v[212:215], v[136:139], v[184:187]\n"
"v_cvt_pk_bf16_f32 v55, v118, v119\n"
"v_mul_f32 v56, s95, v56\n"
"v_mfma_f32_16x16x32_bf16 v[188:191], v[212:215], v[140:143], v[188:191]\n"
"v_mul_f32 v57, s95, v57\n"
"v_mul_f32 v58, s95, v58\n"
"v_mfma_f32_16x16x32_bf16 v[160:163], v[216:219], v[144:147], v[160:163]\n"
"v_mul_f32 v59, s95, v59\n"
"v_mul_f32 v120, s95, v120\n"
"v_mfma_f32_16x16x32_bf16 v[164:167], v[216:219], v[148:151], v[164:167]\n"
"v_mul_f32 v121, s95, v121\n"
"v_mul_f32 v122, s95, v122\n"
"v_mfma_f32_16x16x32_bf16 v[168:171], v[216:219], v[152:155], v[168:171]\n"
"v_mul_f32 v123, s95, v123\n"
"v_cvt_pk_bf16_f32 v56, v56, v57\n"
"v_mfma_f32_16x16x32_bf16 v[172:175], v[216:219], v[156:159], v[172:175]\n"
"v_cvt_pk_bf16_f32 v57, v58, v59\n"
"v_cvt_pk_bf16_f32 v58, v120, v121\n"
"v_mfma_f32_16x16x32_bf16 v[176:179], v[220:223], v[144:147], v[176:179]\n"
"v_cvt_pk_bf16_f32 v59, v122, v123\n"
"v_mul_f32 v60, s95, v60\n"
"v_mfma_f32_16x16x32_bf16 v[180:183], v[220:223], v[148:151], v[180:183]\n"
"v_mul_f32 v61, s95, v61\n"
"v_mul_f32 v62, s95, v62\n"
"v_mfma_f32_16x16x32_bf16 v[184:187], v[220:223], v[152:155], v[184:187]\n"
"v_mul_f32 v63, s95, v63\n"
"v_mul_f32 v124, s95, v124\n"
"v_mfma_f32_16x16x32_bf16 v[188:191], v[220:223], v[156:159], v[188:191]\n"
"s_waitcnt vmcnt(34) lgkmcnt(0)\n"
"s_barrier\n"
"v_mfma_f32_16x16x32_bf16 v[160:163], v[224:227], v[32:35], v[160:163]\n"
"ds_read_b128 v[208:211], v240 offset:16896\n"
"ds_read_b128 v[212:215], v240 offset:25344\n"
"v_mfma_f32_16x16x32_bf16 v[164:167], v[224:227], v[36:39], v[164:167]\n"
"s_add_u32 m0, s74, 0x18c00\n"
"s_mov_b32 s77, 0x8000\n"
"v_mfma_f32_16x16x32_bf16 v[168:171], v[224:227], v[40:43], v[168:171]\n"
"buffer_load_dwordx4 v243, s[52:55], s77 offen offset:0 nt lds\n"
"v_mfma_f32_16x16x32_bf16 v[172:175], v[224:227], v[44:47], v[172:175]\n"
"ds_read_b128 v[216:219], v240 offset:16960\n"
"ds_read_b128 v[220:223], v240 offset:25408\n"
"v_mfma_f32_16x16x32_bf16 v[176:179], v[228:231], v[32:35], v[176:179]\n"
"v_mul_f32 v125, s95, v125\n"
"s_add_u32 m0, s74, 0x19c00\n"
"v_mfma_f32_16x16x32_bf16 v[180:183], v[228:231], v[36:39], v[180:183]\n"
"buffer_load_dwordx4 v243, s[52:55], s77 offen offset:128 nt lds\n"
"v_mfma_f32_16x16x32_bf16 v[184:187], v[228:231], v[40:43], v[184:187]\n"
"v_mul_f32 v126, s95, v126\n"
"v_mul_f32 v127, s95, v127\n"
"v_mfma_f32_16x16x32_bf16 v[188:191], v[228:231], v[44:47], v[188:191]\n"
"v_cvt_pk_bf16_f32 v60, v60, v61\n"
"s_add_u32 m0, s74, 0x1ac00\n"
"v_mfma_f32_16x16x32_bf16 v[160:163], v[232:235], v[48:51], v[160:163]\n"
"buffer_load_dwordx4 v243, s[52:55], s77 offen offset:256 nt lds\n"
"v_mfma_f32_16x16x32_bf16 v[164:167], v[232:235], v[52:55], v[164:167]\n"
"v_cvt_pk_bf16_f32 v61, v62, v63\n"
"v_cvt_pk_bf16_f32 v62, v124, v125\n"
"v_mfma_f32_16x16x32_bf16 v[168:171], v[232:235], v[56:59], v[168:171]\n"
"v_cvt_pk_bf16_f32 v63, v126, v127\n"
"s_add_u32 m0, s74, 0x1bc00\n"
"s_nop 0\n"
"v_mfma_f32_16x16x32_bf16 v[172:175], v[232:235], v[60:63], v[172:175]\n"
"buffer_load_dwordx4 v243, s[52:55], s77 offen offset:384 nt lds\n"
"v_mfma_f32_16x16x32_bf16 v[176:179], v[236:239], v[48:51], v[176:179]\n"
"s_add_u32 s77, s77, 0x4000\n"
"v_mbcnt_lo_u32_b32 v255, -1, 0\n"
"v_mfma_f32_16x16x32_bf16 v[180:183], v[236:239], v[52:55], v[180:183]\n"
"ds_read_b128 v[224:227], v240 offset:17024\n"
"ds_read_b128 v[228:231], v240 offset:25472\n"
"v_mfma_f32_16x16x32_bf16 v[184:187], v[236:239], v[56:59], v[184:187]\n"
"ds_read_b128 v[232:235], v240 offset:17088\n"
"v_mbcnt_hi_u32_b32 v255, -1, v255\n"
"v_mfma_f32_16x16x32_bf16 v[188:191], v[236:239], v[60:63], v[188:191]\n"
"ds_read_b128 v[236:239], v240 offset:25536\n"
"v_and_b32 v255, 7, v255\n"
"v_mul_u32_u24 v255, 0x380, v255\n"
"v_add_u32 v255, s10, v255\n"
"v_max3_f32 v96, v160, v161, v162\n"
"v_max3_f32 v100, v164, v165, v166\n"
"v_max3_f32 v104, v168, v169, v170\n"
"v_max3_f32 v108, v172, v173, v174\n"
"v_max3_f32 v96, v96, v163, v176\n"
"v_max3_f32 v100, v100, v167, v180\n"
"v_max3_f32 v104, v104, v171, v184\n"
"v_max3_f32 v108, v108, v175, v188\n"
"v_max3_f32 v96, v96, v177, v178\n"
"v_max3_f32 v100, v100, v181, v182\n"
"v_max3_f32 v104, v104, v185, v186\n"
"v_max3_f32 v108, v108, v189, v190\n"
"v_max_f32 v96, v96, v179\n"
"v_max_f32 v100, v100, v183\n"
"v_max_f32 v104, v104, v187\n"
"v_max_f32 v108, v108, v191\n"
"v_mov_b32 v97, v96\n"
"v_mov_b32 v101, v100\n"
"v_mov_b32 v105, v104\n"
"v_mov_b32 v109, v108\n"
"v_permlane32_swap_b32 v96, v97\n"
"v_permlane32_swap_b32 v100, v101\n"
"v_permlane32_swap_b32 v104, v105\n"
"v_permlane32_swap_b32 v108, v109\n"
"v_max_f32 v96, v96, v97\n"
"v_max_f32 v100, v100, v101\n"
"v_max_f32 v104, v104, v105\n"
"v_max_f32 v108, v108, v109\n"
"v_mov_b32 v97, v96\n"
"v_mov_b32 v101, v100\n"
"v_mov_b32 v105, v104\n"
"v_mov_b32 v109, v108\n"
"v_permlane16_swap_b32 v96, v97\n"
"v_permlane16_swap_b32 v100, v101\n"
"v_permlane16_swap_b32 v104, v105\n"
"v_permlane16_swap_b32 v108, v109\n"
"v_max_f32 v96, v96, v97\n"
"v_max_f32 v100, v100, v101\n"
"v_max_f32 v104, v104, v105\n"
"v_max_f32 v108, v108, v109\n"
"v_sub_f32 v192, 0, v96\n"
"v_sub_f32 v196, 0, v100\n"
"v_sub_f32 v200, 0, v104\n"
"v_sub_f32 v204, 0, v108\n"
"v_sub_f32 v193, 0, v96\n"
"v_sub_f32 v197, 0, v100\n"
"v_sub_f32 v201, 0, v104\n"
"v_sub_f32 v205, 0, v108\n"
"v_sub_f32 v194, 0, v96\n"
"v_sub_f32 v198, 0, v100\n"
"v_sub_f32 v202, 0, v104\n"
"v_sub_f32 v206, 0, v108\n"
"v_sub_f32 v195, 0, v96\n"
"v_sub_f32 v199, 0, v100\n"
"v_sub_f32 v203, 0, v104\n"
"v_sub_f32 v207, 0, v108\n"
"v_mov_b32 v244, 0\n"
"s_waitcnt lgkmcnt(4)\n"
"v_mfma_f32_16x16x32_bf16 v[96:99], v[208:211], v[0:3], v[192:195]\n"
"v_mov_b32 v245, 0\n"
"v_mov_b32 v246, 0\n"
"v_mfma_f32_16x16x32_bf16 v[100:103], v[208:211], v[4:7], v[196:199]\n"
"v_mov_b32 v247, 0\n"
"v_add_f32 v160, v160, v192\n"
"v_mfma_f32_16x16x32_bf16 v[104:107], v[208:211], v[8:11], v[200:203]\n"
"v_exp_f32 v160, v160\n"
"v_mfma_f32_16x16x32_bf16 v[108:111], v[208:211], v[12:15], v[204:207]\n"
"v_add_f32 v161, v161, v192\n"
"v_add_f32 v162, v162, v192\n"
"v_mfma_f32_16x16x32_bf16 v[112:115], v[212:215], v[0:3], v[192:195]\n"
"v_exp_f32 v161, v161\n"
"v_mfma_f32_16x16x32_bf16 v[116:119], v[212:215], v[4:7], v[196:199]\n"
"v_add_f32 v163, v163, v192\n"
"s_add_u32 m0, s73, 0x0\n"
"v_mfma_f32_16x16x32_bf16 v[120:123], v[212:215], v[8:11], v[200:203]\n"
"buffer_load_dwordx4 v242, s[48:51], s76 offen offset:0 nt lds\n"
"v_mfma_f32_16x16x32_bf16 v[124:127], v[212:215], v[12:15], v[204:207]\n"
"v_exp_f32 v162, v162\n"
"v_mfma_f32_16x16x32_bf16 v[96:99], v[216:219], v[16:19], v[96:99]\n"
"v_exp_f32 v163, v163\n"
"v_mfma_f32_16x16x32_bf16 v[100:103], v[216:219], v[20:23], v[100:103]\n"
"v_add_f32 v176, v176, v192\n"
"v_add_f32 v177, v177, v192\n"
"v_mfma_f32_16x16x32_bf16 v[104:107], v[216:219], v[24:27], v[104:107]\n"
"v_exp_f32 v176, v176\n"
"v_mfma_f32_16x16x32_bf16 v[108:111], v[216:219], v[28:31], v[108:111]\n"
"v_exp_f32 v177, v177\n"
"v_mfma_f32_16x16x32_bf16 v[112:115], v[220:223], v[16:19], v[112:115]\n"
"ds_read_b128 v[208:211], v240 offset:17152\n"
"ds_read_b128 v[212:215], v240 offset:25600\n"
"v_mfma_f32_16x16x32_bf16 v[116:119], v[220:223], v[20:23], v[116:119]\n"
"ds_read_b128 v[216:219], v240 offset:17216\n"
"v_add_f32 v178, v178, v192\n"
"v_mfma_f32_16x16x32_bf16 v[120:123], v[220:223], v[24:27], v[120:123]\n"
"v_exp_f32 v178, v178\n"
"v_mfma_f32_16x16x32_bf16 v[124:127], v[220:223], v[28:31], v[124:127]\n"
"ds_read_b128 v[220:223], v240 offset:25664\n"
"s_waitcnt lgkmcnt(4)\n"
"v_mfma_f32_16x16x32_bf16 v[96:99], v[224:227], v[64:67], v[96:99]\n"
"v_add_f32 v179, v179, v192\n"
"v_add_f32 v244, v244, v160\n"
"v_mfma_f32_16x16x32_bf16 v[100:103], v[224:227], v[68:71], v[100:103]\n"
"v_add_f32 v244, v244, v161\n"
"s_add_u32 m0, s73, 0x220\n"
"v_mfma_f32_16x16x32_bf16 v[104:107], v[224:227], v[72:75], v[104:107]\n"
"buffer_load_dwordx4 v242, s[48:51], s76 offen offset:512 nt lds\n"
"v_mfma_f32_16x16x32_bf16 v[108:111], v[224:227], v[76:79], v[108:111]\n"
"v_exp_f32 v179, v179\n"
"v_mfma_f32_16x16x32_bf16 v[112:115], v[228:231], v[64:67], v[112:115]\n"
"v_add_f32 v244, v244, v162\n"
"v_add_f32 v244, v244, v163\n"
"v_mfma_f32_16x16x32_bf16 v[116:119], v[228:231], v[68:71], v[116:119]\n"
"v_add_f32 v244, v244, v176\n"
"v_add_f32 v244, v244, v177\n"
"v_mfma_f32_16x16x32_bf16 v[120:123], v[228:231], v[72:75], v[120:123]\n"
"v_add_f32 v244, v244, v178\n"
"v_add_f32 v244, v244, v179\n"
"v_mfma_f32_16x16x32_bf16 v[124:127], v[228:231], v[76:79], v[124:127]\n"
"v_cvt_pk_bf16_f32 v160, v160, v161\n"
"v_cvt_pk_bf16_f32 v161, v162, v163\n"
"v_mfma_f32_16x16x32_bf16 v[96:99], v[232:235], v[80:83], v[96:99]\n"
"v_cvt_pk_bf16_f32 v162, v176, v177\n"
"v_cvt_pk_bf16_f32 v163, v178, v179\n"
"v_mfma_f32_16x16x32_bf16 v[100:103], v[232:235], v[84:87], v[100:103]\n"
"v_add_f32 v164, v164, v196\n"
"v_add_f32 v165, v165, v196\n"
"v_mfma_f32_16x16x32_bf16 v[104:107], v[232:235], v[88:91], v[104:107]\n"
"v_exp_f32 v164, v164\n"
"v_mfma_f32_16x16x32_bf16 v[108:111], v[232:235], v[92:95], v[108:111]\n"
"v_exp_f32 v165, v165\n"
"v_mfma_f32_16x16x32_bf16 v[112:115], v[236:239], v[80:83], v[112:115]\n"
"ds_read_b128 v[224:227], v240 offset:17280\n"
"ds_read_b128 v[228:231], v240 offset:25728\n"
"v_mfma_f32_16x16x32_bf16 v[116:119], v[236:239], v[84:87], v[116:119]\n"
"ds_read_b128 v[232:235], v240 offset:17344\n"
"s_add_u32 m0, s73, 0x440\n"
"v_mfma_f32_16x16x32_bf16 v[120:123], v[236:239], v[88:91], v[120:123]\n"
"buffer_load_dwordx4 v242, s[48:51], s76 offen offset:1024 nt lds\n"
"v_mfma_f32_16x16x32_bf16 v[124:127], v[236:239], v[92:95], v[124:127]\n"
"ds_read_b128 v[236:239], v240 offset:25792\n"
"s_waitcnt lgkmcnt(4)\n"
"v_mfma_f32_16x16x32_bf16 v[96:99], v[208:211], v[128:131], v[96:99]\n"
"v_add_f32 v166, v166, v196\n"
"v_add_f32 v167, v167, v196\n"
"v_mfma_f32_16x16x32_bf16 v[100:103], v[208:211], v[132:135], v[100:103]\n"
"v_exp_f32 v166, v166\n"
"v_mfma_f32_16x16x32_bf16 v[104:107], v[208:211], v[136:139], v[104:107]\n"
"v_exp_f32 v167, v167\n"
"v_mfma_f32_16x16x32_bf16 v[108:111], v[208:211], v[140:143], v[108:111]\n"
"v_add_f32 v180, v180, v196\n"
"v_add_f32 v181, v181, v196\n"
"v_mfma_f32_16x16x32_bf16 v[112:115], v[212:215], v[128:131], v[112:115]\n"
"v_exp_f32 v180, v180\n"
"v_mfma_f32_16x16x32_bf16 v[116:119], v[212:215], v[132:135], v[116:119]\n"
"v_exp_f32 v181, v181\n"
"v_mfma_f32_16x16x32_bf16 v[120:123], v[212:215], v[136:139], v[120:123]\n"
"v_add_f32 v182, v182, v196\n"
"v_add_f32 v183, v183, v196\n"
"v_mfma_f32_16x16x32_bf16 v[124:127], v[212:215], v[140:143], v[124:127]\n"
"v_exp_f32 v182, v182\n"
"v_mfma_f32_16x16x32_bf16 v[96:99], v[216:219], v[144:147], v[96:99]\n"
"v_exp_f32 v183, v183\n"
"v_mfma_f32_16x16x32_bf16 v[100:103], v[216:219], v[148:151], v[100:103]\n"
"v_add_f32 v245, v245, v164\n"
"s_add_u32 m0, s73, 0x660\n"
"v_mfma_f32_16x16x32_bf16 v[104:107], v[216:219], v[152:155], v[104:107]\n"
"buffer_load_dwordx4 v242, s[48:51], s76 offen offset:1536 nt lds\n"
"v_mfma_f32_16x16x32_bf16 v[108:111], v[216:219], v[156:159], v[108:111]\n"
"s_add_u32 s76, s76, 0x4000\n"
"v_add_f32 v245, v245, v165\n"
"v_mfma_f32_16x16x32_bf16 v[112:115], v[220:223], v[144:147], v[112:115]\n"
"v_add_f32 v245, v245, v166\n"
"v_add_f32 v245, v245, v167\n"
"v_mfma_f32_16x16x32_bf16 v[116:119], v[220:223], v[148:151], v[116:119]\n"
"v_add_f32 v245, v245, v180\n"
"v_add_f32 v245, v245, v181\n"
"v_mfma_f32_16x16x32_bf16 v[120:123], v[220:223], v[152:155], v[120:123]\n"
"v_add_f32 v245, v245, v182\n"
"v_add_f32 v245, v245, v183\n"
"v_mfma_f32_16x16x32_bf16 v[124:127], v[220:223], v[156:159], v[124:127]\n"
"s_waitcnt vmcnt(18) lgkmcnt(0)\n"
"s_barrier\n"
"v_mfma_f32_16x16x32_bf16 v[96:99], v[224:227], v[32:35], v[96:99]\n"
"ds_read_b64_tr_b16 v[208:209], v241 offset:0\n"
"ds_read_b64_tr_b16 v[210:211], v241 offset:256\n"
"v_mfma_f32_16x16x32_bf16 v[100:103], v[224:227], v[36:39], v[100:103]\n"
"ds_read_b64_tr_b16 v[212:213], v241 offset:32\n"
"ds_read_b64_tr_b16 v[214:215], v241 offset:288\n"
"v_mfma_f32_16x16x32_bf16 v[104:107], v[224:227], v[40:43], v[104:107]\n"
"ds_read_b64_tr_b16 v[216:217], v241 offset:512\n"
"ds_read_b64_tr_b16 v[218:219], v241 offset:768\n"
"v_mfma_f32_16x16x32_bf16 v[108:111], v[224:227], v[44:47], v[108:111]\n"
"ds_read_b64_tr_b16 v[220:221], v241 offset:544\n"
"ds_read_b64_tr_b16 v[222:223], v241 offset:800\n"
"v_mfma_f32_16x16x32_bf16 v[112:115], v[228:231], v[32:35], v[112:115]\n"
"v_cvt_pk_bf16_f32 v164, v164, v165\n"
"v_cvt_pk_bf16_f32 v165, v166, v167\n"
"v_mfma_f32_16x16x32_bf16 v[116:119], v[228:231], v[36:39], v[116:119]\n"
"v_cvt_pk_bf16_f32 v166, v180, v181\n"
"v_cvt_pk_bf16_f32 v167, v182, v183\n"
"v_mfma_f32_16x16x32_bf16 v[120:123], v[228:231], v[40:43], v[120:123]\n"
"v_add_f32 v168, v168, v200\n"
"v_add_f32 v169, v169, v200\n"
"v_mfma_f32_16x16x32_bf16 v[124:127], v[228:231], v[44:47], v[124:127]\n"
"v_exp_f32 v168, v168\n"
"v_mfma_f32_16x16x32_bf16 v[96:99], v[232:235], v[48:51], v[96:99]\n"
"v_exp_f32 v169, v169\n"
"v_mfma_f32_16x16x32_bf16 v[100:103], v[232:235], v[52:55], v[100:103]\n"
"v_add_f32 v170, v170, v200\n"
"v_add_f32 v171, v171, v200\n"
"v_mfma_f32_16x16x32_bf16 v[104:107], v[232:235], v[56:59], v[104:107]\n"
"v_exp_f32 v170, v170\n"
"v_mfma_f32_16x16x32_bf16 v[108:111], v[232:235], v[60:63], v[108:111]\n"
"v_exp_f32 v171, v171\n"
"v_mfma_f32_16x16x32_bf16 v[112:115], v[236:239], v[48:51], v[112:115]\n"
"ds_read_b64_tr_b16 v[224:225], v241 offset:4224\n"
"ds_read_b64_tr_b16 v[226:227], v241 offset:4480\n"
"v_mfma_f32_16x16x32_bf16 v[116:119], v[236:239], v[52:55], v[116:119]\n"
"ds_read_b64_tr_b16 v[228:229], v241 offset:4256\n"
"ds_read_b64_tr_b16 v[230:231], v241 offset:4512\n"
"v_mfma_f32_16x16x32_bf16 v[120:123], v[236:239], v[56:59], v[120:123]\n"
"ds_read_b64_tr_b16 v[232:233], v241 offset:4736\n"
"ds_read_b64_tr_b16 v[234:235], v241 offset:4992\n"
"v_mfma_f32_16x16x32_bf16 v[124:127], v[236:239], v[60:63], v[124:127]\n"
"v_add_f32 v184, v184, v200\n"
"v_add_f32 v185, v185, v200\n"
"v_add_f32 v186, v186, v200\n"
"v_add_f32 v187, v187, v200\n"
"v_add_f32 v246, v246, v168\n"
"v_add_f32 v246, v246, v169\n"
"v_add_f32 v246, v246, v170\n"
"v_add_f32 v246, v246, v171\n"
"v_exp_f32 v184, v184\n"
"v_exp_f32 v185, v185\n"
"v_add_f32 v246, v246, v184\n"
"v_add_f32 v246, v246, v185\n"
"v_exp_f32 v186, v186\n"
"v_exp_f32 v187, v187\n"
"v_add_f32 v246, v246, v186\n"
"v_add_f32 v246, v246, v187\n"
"v_cvt_pk_bf16_f32 v168, v168, v169\n"
"v_cvt_pk_bf16_f32 v169, v170, v171\n"
"v_cvt_pk_bf16_f32 v170, v184, v185\n"
"v_cvt_pk_bf16_f32 v171, v186, v187\n"
"v_add_f32 v172, v172, v204\n"
"v_add_f32 v173, v173, v204\n"
"v_add_f32 v174, v174, v204\n"
"v_add_f32 v175, v175, v204\n"
"v_add_f32 v188, v188, v204\n"
"v_add_f32 v189, v189, v204\n"
"v_add_f32 v190, v190, v204\n"
"v_add_f32 v191, v191, v204\n"
"v_exp_f32 v172, v172\n"
"v_exp_f32 v173, v173\n"
"v_add_f32 v247, v247, v172\n"
"v_add_f32 v247, v247, v173\n"
"v_exp_f32 v174, v174\n"
"v_exp_f32 v175, v175\n"
"v_add_f32 v247, v247, v174\n"
"v_add_f32 v247, v247, v175\n"
"v_exp_f32 v188, v188\n"
"v_exp_f32 v189, v189\n"
"v_add_f32 v247, v247, v188\n"
"v_add_f32 v247, v247, v189\n"
"v_exp_f32 v190, v190\n"
"v_exp_f32 v191, v191\n"
"v_add_f32 v247, v247, v190\n"
"v_add_f32 v247, v247, v191\n"
"v_cvt_pk_bf16_f32 v172, v172, v173\n"
"v_cvt_pk_bf16_f32 v173, v174, v175\n"
"v_cvt_pk_bf16_f32 v174, v188, v189\n"
"v_cvt_pk_bf16_f32 v175, v190, v191\n"
"s_waitcnt lgkmcnt(6)\n"
"v_mfma_f32_16x16x32_bf16 a[0:3], v[208:211], v[160:163], 0\n"
"ds_read_b64_tr_b16 v[236:237], v241 offset:4768\n"
"ds_read_b64_tr_b16 v[238:239], v241 offset:5024\n"
"v_mfma_f32_16x16x32_bf16 a[4:7], v[208:211], v[164:167], 0\n"
"v_max3_f32 v248, v96, v97, v98\n"
"v_max3_f32 v248, v248, v99, v100\n"
"v_mfma_f32_16x16x32_bf16 a[8:11], v[208:211], v[168:171], 0\n"
"v_max3_f32 v248, v248, v101, v102\n"
"v_max3_f32 v248, v248, v103, v104\n"
"v_mfma_f32_16x16x32_bf16 a[12:15], v[208:211], v[172:175], 0\n"
"v_max3_f32 v248, v248, v105, v106\n"
"v_max3_f32 v248, v248, v107, v108\n"
"v_mfma_f32_16x16x32_bf16 a[16:19], v[212:215], v[160:163], 0\n"
"v_max3_f32 v248, v248, v109, v110\n"
"v_max3_f32 v248, v248, v111, v112\n"
"v_mfma_f32_16x16x32_bf16 a[20:23], v[212:215], v[164:167], 0\n"
"v_max3_f32 v248, v248, v113, v114\n"
"s_add_u32 m0, s74, 0x1ce00\n"
"v_mfma_f32_16x16x32_bf16 a[24:27], v[212:215], v[168:171], 0\n"
"buffer_load_dwordx4 v243, s[52:55], s77 offen offset:0 nt lds\n"
"v_mfma_f32_16x16x32_bf16 a[28:31], v[212:215], v[172:175], 0\n"
"v_max3_f32 v248, v248, v115, v116\n"
"v_max3_f32 v248, v248, v117, v118\n"
"v_mfma_f32_16x16x32_bf16 a[32:35], v[216:219], v[160:163], 0\n"
"v_max3_f32 v248, v248, v119, v120\n"
"v_max3_f32 v248, v248, v121, v122\n"
"v_mfma_f32_16x16x32_bf16 a[36:39], v[216:219], v[164:167], 0\n"
"v_max3_f32 v248, v248, v123, v124\n"
"v_max3_f32 v248, v248, v125, v126\n"
"v_mfma_f32_16x16x32_bf16 a[40:43], v[216:219], v[168:171], 0\n"
"v_max_f32 v248, v248, v127\n"
"v_cmp_lt_f32 vcc, 0x41000000, v248\n"
"v_mfma_f32_16x16x32_bf16 a[44:47], v[216:219], v[172:175], 0\n"
"v_mfma_f32_16x16x32_bf16 a[48:51], v[220:223], v[160:163], 0\n"
"ds_read_b64_tr_b16 v[208:209], v241 offset:8448\n"
"ds_read_b64_tr_b16 v[210:211], v241 offset:8704\n"
"v_mfma_f32_16x16x32_bf16 a[52:55], v[220:223], v[164:167], 0\n"
"ds_read_b64_tr_b16 v[212:213], v241 offset:8480\n"
"ds_read_b64_tr_b16 v[214:215], v241 offset:8736\n"
"v_mfma_f32_16x16x32_bf16 a[56:59], v[220:223], v[168:171], 0\n"
"ds_read_b64_tr_b16 v[216:217], v241 offset:8960\n"
"ds_read_b64_tr_b16 v[218:219], v241 offset:9216\n"
"v_mfma_f32_16x16x32_bf16 a[60:63], v[220:223], v[172:175], 0\n"
"s_cbranch_vccnz L_tB0_%=\n"
"L_bB0_%=:\n"
"s_waitcnt lgkmcnt(6)\n"
"v_mfma_f32_16x16x32_bf16 a[64:67], v[224:227], v[160:163], 0\n"
"ds_read_b64_tr_b16 v[220:221], v241 offset:8992\n"
"ds_read_b64_tr_b16 v[222:223], v241 offset:9248\n"
"v_mfma_f32_16x16x32_bf16 a[68:71], v[224:227], v[164:167], 0\n"
"v_exp_f32 v96, v96\n"
"v_mfma_f32_16x16x32_bf16 a[72:75], v[224:227], v[168:171], 0\n"
"v_exp_f32 v97, v97\n"
"v_mfma_f32_16x16x32_bf16 a[76:79], v[224:227], v[172:175], 0\n"
"v_exp_f32 v98, v98\n"
"v_mfma_f32_16x16x32_bf16 a[80:83], v[228:231], v[160:163], 0\n"
"v_exp_f32 v99, v99\n"
"v_mfma_f32_16x16x32_bf16 a[84:87], v[228:231], v[164:167], 0\n"
"v_add_f32 v244, v244, v96\n"
"s_add_u32 m0, s74, 0x1de00\n"
"v_mfma_f32_16x16x32_bf16 a[88:91], v[228:231], v[168:171], 0\n"
"buffer_load_dwordx4 v243, s[52:55], s77 offen offset:128 nt lds\n"
"v_mfma_f32_16x16x32_bf16 a[92:95], v[228:231], v[172:175], 0\n"
"v_exp_f32 v112, v112\n"
"v_mfma_f32_16x16x32_bf16 a[96:99], v[232:235], v[160:163], 0\n"
"v_exp_f32 v113, v113\n"
"v_mfma_f32_16x16x32_bf16 a[100:103], v[232:235], v[164:167], 0\n"
"v_exp_f32 v114, v114\n"
"v_mfma_f32_16x16x32_bf16 a[104:107], v[232:235], v[168:171], 0\n"
"v_exp_f32 v115, v115\n"
"v_mfma_f32_16x16x32_bf16 a[108:111], v[232:235], v[172:175], 0\n"
"v_exp_f32 v100, v100\n"
"v_mfma_f32_16x16x32_bf16 a[112:115], v[236:239], v[160:163], 0\n"
"ds_read_b64_tr_b16 v[224:225], v241 offset:12672\n"
"ds_read_b64_tr_b16 v[226:227], v241 offset:12928\n"
"v_mfma_f32_16x16x32_bf16 a[116:119], v[236:239], v[164:167], 0\n"
"ds_read_b64_tr_b16 v[228:229], v241 offset:12704\n"
"ds_read_b64_tr_b16 v[230:231], v241 offset:12960\n"
"v_mfma_f32_16x16x32_bf16 a[120:123], v[236:239], v[168:171], 0\n"
"ds_read_b64_tr_b16 v[232:233], v241 offset:13184\n"
"ds_read_b64_tr_b16 v[234:235], v241 offset:13440\n"
"v_mfma_f32_16x16x32_bf16 a[124:127], v[236:239], v[172:175], 0\n"
"v_add_f32 v244, v244, v97\n"
"s_waitcnt lgkmcnt(6)\n"
"v_mfma_f32_16x16x32_bf16 a[128:131], v[208:211], v[160:163], 0\n"
"ds_read_b64_tr_b16 v[236:237], v241 offset:13216\n"
"ds_read_b64_tr_b16 v[238:239], v241 offset:13472\n"
"v_mfma_f32_16x16x32_bf16 a[132:135], v[208:211], v[164:167], 0\n"
"v_exp_f32 v101, v101\n"
"v_mfma_f32_16x16x32_bf16 a[136:139], v[208:211], v[168:171], 0\n"
"v_exp_f32 v102, v102\n"
"v_mfma_f32_16x16x32_bf16 a[140:143], v[208:211], v[172:175], 0\n"
"v_exp_f32 v103, v103\n"
"v_mfma_f32_16x16x32_bf16 a[144:147], v[212:215], v[160:163], 0\n"
"v_exp_f32 v116, v116\n"
"v_mfma_f32_16x16x32_bf16 a[148:151], v[212:215], v[164:167], 0\n"
"v_add_f32 v244, v244, v98\n"
"s_add_u32 m0, s74, 0x1ee00\n"
"v_mfma_f32_16x16x32_bf16 a[152:155], v[212:215], v[168:171], 0\n"
"buffer_load_dwordx4 v243, s[52:55], s77 offen offset:256 nt lds\n"
"v_mfma_f32_16x16x32_bf16 a[156:159], v[212:215], v[172:175], 0\n"
"v_exp_f32 v117, v117\n"
"v_mfma_f32_16x16x32_bf16 a[160:163], v[216:219], v[160:163], 0\n"
"v_exp_f32 v118, v118\n"
"v_mfma_f32_16x16x32_bf16 a[164:167], v[216:219], v[164:167], 0\n"
"v_exp_f32 v119, v119\n"
"v_mfma_f32_16x16x32_bf16 a[168:171], v[216:219], v[168:171], 0\n"
"v_exp_f32 v104, v104\n"
"v_mfma_f32_16x16x32_bf16 a[172:175], v[216:219], v[172:175], 0\n"
"v_exp_f32 v105, v105\n"
"v_mfma_f32_16x16x32_bf16 a[176:179], v[220:223], v[160:163], 0\n"
"ds_read_b128 v[208:211], v240 offset:33792\n"
"ds_read_b128 v[212:215], v240 offset:42240\n"
"v_mfma_f32_16x16x32_bf16 a[180:183], v[220:223], v[164:167], 0\n"
"ds_read_b128 v[216:219], v240 offset:33856\n"
"v_add_f32 v244, v244, v99\n"
"v_mfma_f32_16x16x32_bf16 a[184:187], v[220:223], v[168:171], 0\n"
"v_exp_f32 v106, v106\n"
"v_mfma_f32_16x16x32_bf16 a[188:191], v[220:223], v[172:175], 0\n"
"ds_read_b128 v[220:223], v240 offset:42304\n"
"s_waitcnt lgkmcnt(4)\n"
"v_mfma_f32_16x16x32_bf16 a[192:195], v[224:227], v[160:163], 0\n"
"v_exp_f32 v107, v107\n"
"v_mfma_f32_16x16x32_bf16 a[196:199], v[224:227], v[164:167], 0\n"
"v_exp_f32 v120, v120\n"
"v_mfma_f32_16x16x32_bf16 a[200:203], v[224:227], v[168:171], 0\n"
"v_exp_f32 v121, v121\n"
"v_mfma_f32_16x16x32_bf16 a[204:207], v[224:227], v[172:175], 0\n"
"v_exp_f32 v122, v122\n"
"v_mfma_f32_16x16x32_bf16 a[208:211], v[228:231], v[160:163], 0\n"
"v_exp_f32 v123, v123\n"
"v_mfma_f32_16x16x32_bf16 a[212:215], v[228:231], v[164:167], 0\n"
"v_add_f32 v244, v244, v112\n"
"s_add_u32 m0, s74, 0x1fe00\n"
"v_mfma_f32_16x16x32_bf16 a[216:219], v[228:231], v[168:171], 0\n"
"buffer_load_dwordx4 v243, s[52:55], s77 offen offset:384 nt lds\n"
"v_mfma_f32_16x16x32_bf16 a[220:223], v[228:231], v[172:175], 0\n"
"s_add_u32 s77, s77, 0x4000\n"
"v_add_f32 v244, v244, v113\n"
"v_mfma_f32_16x16x32_bf16 a[224:227], v[232:235], v[160:163], 0\n"
"v_add_f32 v244, v244, v114\n"
"v_add_f32 v244, v244, v115\n"
"v_mfma_f32_16x16x32_bf16 a[228:231], v[232:235], v[164:167], 0\n"
"v_cvt_pk_bf16_f32 v96, v96, v97\n"
"v_cvt_pk_bf16_f32 v97, v98, v99\n"
"v_mfma_f32_16x16x32_bf16 a[232:235], v[232:235], v[168:171], 0\n"
"v_exp_f32 v108, v108\n"
"v_mfma_f32_16x16x32_bf16 a[236:239], v[232:235], v[172:175], 0\n"
"v_exp_f32 v109, v109\n"
"v_mfma_f32_16x16x32_bf16 a[240:243], v[236:239], v[160:163], 0\n"
"ds_read_b128 v[224:227], v240 offset:33920\n"
"ds_read_b128 v[228:231], v240 offset:42368\n"
"v_mfma_f32_16x16x32_bf16 a[244:247], v[236:239], v[164:167], 0\n"
"ds_read_b128 v[232:235], v240 offset:33984\n"
"v_cvt_pk_bf16_f32 v98, v112, v113\n"
"v_mfma_f32_16x16x32_bf16 a[248:251], v[236:239], v[168:171], 0\n"
"v_exp_f32 v110, v110\n"
"v_mfma_f32_16x16x32_bf16 a[252:255], v[236:239], v[172:175], 0\n"
"s_cbranch_vccnz L_tC0_%=\n"
"L_bC0_%=:\n"
"s_waitcnt lgkmcnt(3)\n"
"v_mfma_f32_16x16x32_bf16 v[160:163], v[208:211], v[0:3], v[192:195]\n"
"ds_read_b128 v[236:239], v240 offset:42432\n"
"v_cvt_pk_bf16_f32 v99, v114, v115\n"
"v_mfma_f32_16x16x32_bf16 v[164:167], v[208:211], v[4:7], v[196:199]\n"
"v_exp_f32 v111, v111\n"
"v_mfma_f32_16x16x32_bf16 v[168:171], v[208:211], v[8:11], v[200:203]\n"
"v_add_f32 v245, v245, v100\n"
"v_add_f32 v245, v245, v101\n"
"v_mfma_f32_16x16x32_bf16 v[172:175], v[208:211], v[12:15], v[204:207]\n"
"v_exp_f32 v124, v124\n"
"v_mfma_f32_16x16x32_bf16 v[176:179], v[212:215], v[0:3], v[192:195]\n"
"v_exp_f32 v125, v125\n"
"v_mfma_f32_16x16x32_bf16 v[180:183], v[212:215], v[4:7], v[196:199]\n"
"v_add_f32 v245, v245, v102\n"
"s_add_u32 m0, s73, 0x4200\n"
"v_mfma_f32_16x16x32_bf16 v[184:187], v[212:215], v[8:11], v[200:203]\n"
"buffer_load_dwordx4 v242, s[48:51], s76 offen offset:0 nt lds\n"
"v_mfma_f32_16x16x32_bf16 v[188:191], v[212:215], v[12:15], v[204:207]\n"
"v_exp_f32 v126, v126\n"
"v_mfma_f32_16x16x32_bf16 v[160:163], v[216:219], v[16:19], v[160:163]\n"
"v_add_f32 v245, v245, v103\n"
"v_add_f32 v245, v245, v116\n"
"v_mfma_f32_16x16x32_bf16 v[164:167], v[216:219], v[20:23], v[164:167]\n"
"v_exp_f32 v127, v127\n"
"v_mfma_f32_16x16x32_bf16 v[168:171], v[216:219], v[24:27], v[168:171]\n"
"v_add_f32 v245, v245, v117\n"
"v_add_f32 v245, v245, v118\n"
"v_mfma_f32_16x16x32_bf16 v[172:175], v[216:219], v[28:31], v[172:175]\n"
"v_add_f32 v245, v245, v119\n"
"v_cvt_pk_bf16_f32 v100, v100, v101\n"
"v_mfma_f32_16x16x32_bf16 v[176:179], v[220:223], v[16:19], v[176:179]\n"
"ds_read_b128 v[208:211], v240 offset:34048\n"
"ds_read_b128 v[212:215], v240 offset:42496\n"
"v_mfma_f32_16x16x32_bf16 v[180:183], v[220:223], v[20:23], v[180:183]\n"
"ds_read_b128 v[216:219], v240 offset:34112\n"
"v_cvt_pk_bf16_f32 v101, v102, v103\n"
"v_mfma_f32_16x16x32_bf16 v[184:187], v[220:223], v[24:27], v[184:187]\n"
"v_cvt_pk_bf16_f32 v102, v116, v117\n"
"v_cvt_pk_bf16_f32 v103, v118, v119\n"
"v_mfma_f32_16x16x32_bf16 v[188:191], v[220:223], v[28:31], v[188:191]\n"
"ds_read_b128 v[220:223], v240 offset:42560\n"
"s_waitcnt lgkmcnt(4)\n"
"v_mfma_f32_16x16x32_bf16 v[160:163], v[224:227], v[64:67], v[160:163]\n"
"v_add_f32 v246, v246, v104\n"
"v_add_f32 v246, v246, v105\n"
"v_mfma_f32_16x16x32_bf16 v[164:167], v[224:227], v[68:71], v[164:167]\n"
"v_add_f32 v246, v246, v106\n"
"v_add_f32 v246, v246, v107\n"
"v_mfma_f32_16x16x32_bf16 v[168:171], v[224:227], v[72:75], v[168:171]\n"
"v_add_f32 v246, v246, v120\n"
"v_add_f32 v246, v246, v121\n"
"v_mfma_f32_16x16x32_bf16 v[172:175], v[224:227], v[76:79], v[172:175]\n"
"v_add_f32 v246, v246, v122\n"
"v_add_f32 v246, v246, v123\n"
"v_mfma_f32_16x16x32_bf16 v[176:179], v[228:231], v[64:67], v[176:179]\n"
"v_cvt_pk_bf16_f32 v104, v104, v105\n"
"v_cvt_pk_bf16_f32 v105, v106, v107\n"
"v_mfma_f32_16x16x32_bf16 v[180:183], v[228:231], v[68:71], v[180:183]\n"
"v_cvt_pk_bf16_f32 v106, v120, v121\n"
"s_add_u32 m0, s73, 0x4420\n"
"v_mfma_f32_16x16x32_bf16 v[184:187], v[228:231], v[72:75], v[184:187]\n"
"buffer_load_dwordx4 v242, s[48:51], s76 offen offset:512 nt lds\n"
"v_mfma_f32_16x16x32_bf16 v[188:191], v[228:231], v[76:79], v[188:191]\n"
"v_cvt_pk_bf16_f32 v107, v122, v123\n"
"v_add_f32 v247, v247, v108\n"
"v_mfma_f32_16x16x32_bf16 v[160:163], v[232:235], v[80:83], v[160:163]\n"
"v_add_f32 v247, v247, v109\n"
"v_add_f32 v247, v247, v110\n"
"v_mfma_f32_16x16x32_bf16 v[164:167], v[232:235], v[84:87], v[164:167]\n"
"v_add_f32 v247, v247, v111\n"
"v_add_f32 v247, v247, v124\n"
"v_mfma_f32_16x16x32_bf16 v[168:171], v[232:235], v[88:91], v[168:171]\n"
"v_add_f32 v247, v247, v125\n"
"v_add_f32 v247, v247, v126\n"
"v_mfma_f32_16x16x32_bf16 v[172:175], v[232:235], v[92:95], v[172:175]\n"
"v_add_f32 v247, v247, v127\n"
"v_cvt_pk_bf16_f32 v108, v108, v109\n"
"v_mfma_f32_16x16x32_bf16 v[176:179], v[236:239], v[80:83], v[176:179]\n"
"ds_read_b128 v[224:227], v240 offset:34176\n"
"ds_read_b128 v[228:231], v240 offset:42624\n"
"v_mfma_f32_16x16x32_bf16 v[180:183], v[236:239], v[84:87], v[180:183]\n"
"ds_read_b128 v[232:235], v240 offset:34240\n"
"v_cvt_pk_bf16_f32 v109, v110, v111\n"
"v_mfma_f32_16x16x32_bf16 v[184:187], v[236:239], v[88:91], v[184:187]\n"
"v_cvt_pk_bf16_f32 v110, v124, v125\n"
"v_cvt_pk_bf16_f32 v111, v126, v127\n"
"v_mfma_f32_16x16x32_bf16 v[188:191], v[236:239], v[92:95], v[188:191]\n"
"ds_read_b128 v[236:239], v240 offset:42688\n"
"s_waitcnt lgkmcnt(4)\n"
"v_mfma_f32_16x16x32_bf16 v[160:163], v[208:211], v[128:131], v[160:163]\n"
"s_bfe_u32 s97, s17, 0x40018\n"
"s_lshl_b32 s97, s97, 14\n"
"v_mfma_f32_16x16x32_bf16 v[164:167], v[208:211], v[132:135], v[164:167]\n"
"s_add_u32 s97, s97, s96\n"
"s_add_u32 s97, s97, 0xc0000\n"
"v_mfma_f32_16x16x32_bf16 v[168:171], v[208:211], v[136:139], v[168:171]\n"
"s_add_u32 s32, s32, s96\n"
"s_addc_u32 s33, s33, 0\n"
"v_mfma_f32_16x16x32_bf16 v[172:175], v[208:211], v[140:143], v[172:175]\n"
"s_bfe_u32 s82, s16, 0x40000\n"
"s_lshl_b32 s82, s82, 7\n"
"v_mfma_f32_16x16x32_bf16 v[176:179], v[212:215], v[128:131], v[176:179]\n"
"s_bfe_u32 s83, s16, 0x40004\n"
"s_lshl_b32 s83, s83, 7\n"
"v_mfma_f32_16x16x32_bf16 v[180:183], v[212:215], v[132:135], v[180:183]\n"
"s_bfe_u32 s84, s16, 0x40008\n"
"s_add_u32 m0, s73, 0x4640\n"
"v_mfma_f32_16x16x32_bf16 v[184:187], v[212:215], v[136:139], v[184:187]\n"
"buffer_load_dwordx4 v242, s[48:51], s76 offen offset:1024 nt lds\n"
"v_mfma_f32_16x16x32_bf16 v[188:191], v[212:215], v[140:143], v[188:191]\n"
"s_lshl_b32 s84, s84, 7\n"
"s_bfe_u32 s85, s16, 0x4000c\n"
"v_mfma_f32_16x16x32_bf16 v[160:163], v[216:219], v[144:147], v[160:163]\n"
"s_lshl_b32 s85, s85, 7\n"
"s_bfe_u32 s86, s16, 0x40010\n"
"v_mfma_f32_16x16x32_bf16 v[164:167], v[216:219], v[148:151], v[164:167]\n"
"s_lshl_b32 s86, s86, 7\n"
"s_bfe_u32 s87, s16, 0x40014\n"
"v_mfma_f32_16x16x32_bf16 v[168:171], v[216:219], v[152:155], v[168:171]\n"
"s_lshl_b32 s87, s87, 7\n"
"s_bfe_u32 s88, s16, 0x40018\n"
"v_mfma_f32_16x16x32_bf16 v[172:175], v[216:219], v[156:159], v[172:175]\n"
"s_lshl_b32 s88, s88, 7\n"
"v_mfma_f32_16x16x32_bf16 v[176:179], v[220:223], v[144:147], v[176:179]\n"
"v_mfma_f32_16x16x32_bf16 v[180:183], v[220:223], v[148:151], v[180:183]\n"
"v_mfma_f32_16x16x32_bf16 v[184:187], v[220:223], v[152:155], v[184:187]\n"
"v_mfma_f32_16x16x32_bf16 v[188:191], v[220:223], v[156:159], v[188:191]\n"
"s_waitcnt vmcnt(17) lgkmcnt(0)\n"
"s_barrier\n"
"v_mfma_f32_16x16x32_bf16 v[160:163], v[224:227], v[32:35], v[160:163]\n"
"ds_read_b64_tr_b16 v[208:209], v241 offset:16896\n"
"ds_read_b64_tr_b16 v[210:211], v241 offset:17152\n"
"v_mfma_f32_16x16x32_bf16 v[164:167], v[224:227], v[36:39], v[164:167]\n"
"ds_read_b64_tr_b16 v[212:213], v241 offset:16928\n"
"ds_read_b64_tr_b16 v[214:215], v241 offset:17184\n"
"v_mfma_f32_16x16x32_bf16 v[168:171], v[224:227], v[40:43], v[168:171]\n"
"ds_read_b64_tr_b16 v[216:217], v241 offset:17408\n"
"ds_read_b64_tr_b16 v[218:219], v241 offset:17664\n"
"v_mfma_f32_16x16x32_bf16 v[172:175], v[224:227], v[44:47], v[172:175]\n"
"ds_read_b64_tr_b16 v[220:221], v241 offset:17440\n"
"ds_read_b64_tr_b16 v[222:223], v241 offset:17696\n"
"v_mfma_f32_16x16x32_bf16 v[176:179], v[228:231], v[32:35], v[176:179]\n"
"v_mfma_f32_16x16x32_bf16 v[180:183], v[228:231], v[36:39], v[180:183]\n"
"s_add_u32 m0, s73, 0x4860\n"
"v_mfma_f32_16x16x32_bf16 v[184:187], v[228:231], v[40:43], v[184:187]\n"
"buffer_load_dwordx4 v242, s[48:51], s76 offen offset:1536 nt lds\n"
"v_mfma_f32_16x16x32_bf16 v[188:191], v[228:231], v[44:47], v[188:191]\n"
"s_add_u32 s76, s76, 0x4000\n"
"v_mfma_f32_16x16x32_bf16 v[160:163], v[232:235], v[48:51], v[160:163]\n"
"s_waitcnt vmcnt(17)\n"
"v_readlane_b32 s28, v249, s90\n"
"v_mfma_f32_16x16x32_bf16 v[164:167], v[232:235], v[52:55], v[164:167]\n"
"v_mfma_f32_16x16x32_bf16 v[168:171], v[232:235], v[56:59], v[168:171]\n"
"v_mfma_f32_16x16x32_bf16 v[172:175], v[232:235], v[60:63], v[172:175]\n"
"v_mfma_f32_16x16x32_bf16 v[176:179], v[236:239], v[48:51], v[176:179]\n"
"ds_read_b64_tr_b16 v[224:225], v241 offset:21120\n"
"ds_read_b64_tr_b16 v[226:227], v241 offset:21376\n"
"v_mfma_f32_16x16x32_bf16 v[180:183], v[236:239], v[52:55], v[180:183]\n"
"ds_read_b64_tr_b16 v[228:229], v241 offset:21152\n"
"ds_read_b64_tr_b16 v[230:231], v241 offset:21408\n"
"v_mfma_f32_16x16x32_bf16 v[184:187], v[236:239], v[56:59], v[184:187]\n"
"ds_read_b64_tr_b16 v[232:233], v241 offset:21632\n"
"ds_read_b64_tr_b16 v[234:235], v241 offset:21888\n"
"v_mfma_f32_16x16x32_bf16 v[188:191], v[236:239], v[60:63], v[188:191]\n"
"s_mov_b32 s78, 3\n"
"L_loop_%=:\n"
"s_waitcnt lgkmcnt(6)\n"
"v_mfma_f32_16x16x32_bf16 a[0:3], v[208:211], v[96:99], a[0:3]\n"
"ds_read_b64_tr_b16 v[236:237], v241 offset:21664\n"
"ds_read_b64_tr_b16 v[238:239], v241 offset:21920\n"
"v_mfma_f32_16x16x32_bf16 a[4:7], v[208:211], v[100:103], a[4:7]\n"
"v_max3_f32 v248, v160, v161, v162\n"
"v_max3_f32 v248, v248, v163, v164\n"
"v_mfma_f32_16x16x32_bf16 a[8:11], v[208:211], v[104:107], a[8:11]\n"
"v_max3_f32 v248, v248, v165, v166\n"
"v_max3_f32 v248, v248, v167, v168\n"
"v_mfma_f32_16x16x32_bf16 a[12:15], v[208:211], v[108:111], a[12:15]\n"
"v_max3_f32 v248, v248, v169, v170\n"
"v_max3_f32 v248, v248, v171, v172\n"
"v_mfma_f32_16x16x32_bf16 a[16:19], v[212:215], v[96:99], a[16:19]\n"
"v_max3_f32 v248, v248, v173, v174\n"
"v_max3_f32 v248, v248, v175, v176\n"
"v_mfma_f32_16x16x32_bf16 a[20:23], v[212:215], v[100:103], a[20:23]\n"
"v_max3_f32 v248, v248, v177, v178\n"
"s_add_u32 m0, s74, 0x10800\n"
"v_mfma_f32_16x16x32_bf16 a[24:27], v[212:215], v[104:107], a[24:27]\n"
"buffer_load_dwordx4 v243, s[52:55], s77 offen offset:0 nt lds\n"
"v_mfma_f32_16x16x32_bf16 a[28:31], v[212:215], v[108:111], a[28:31]\n"
"v_max3_f32 v248, v248, v179, v180\n"
"v_max3_f32 v248, v248, v181, v182\n"
"v_mfma_f32_16x16x32_bf16 a[32:35], v[216:219], v[96:99], a[32:35]\n"
"v_max3_f32 v248, v248, v183, v184\n"
"v_max3_f32 v248, v248, v185, v186\n"
"v_mfma_f32_16x16x32_bf16 a[36:39], v[216:219], v[100:103], a[36:39]\n"
"v_max3_f32 v248, v248, v187, v188\n"
"v_max3_f32 v248, v248, v189, v190\n"
"v_mfma_f32_16x16x32_bf16 a[40:43], v[216:219], v[104:107], a[40:43]\n"
"v_max_f32 v248, v248, v191\n"
"v_cmp_lt_f32 vcc, 0x41000000, v248\n"
"v_mfma_f32_16x16x32_bf16 a[44:47], v[216:219], v[108:111], a[44:47]\n"
"v_mfma_f32_16x16x32_bf16 a[48:51], v[220:223], v[96:99], a[48:51]\n"
"ds_read_b64_tr_b16 v[208:209], v241 offset:25344\n"
"ds_read_b64_tr_b16 v[210:211], v241 offset:25600\n"
"v_mfma_f32_16x16x32_bf16 a[52:55], v[220:223], v[100:103], a[52:55]\n"
"ds_read_b64_tr_b16 v[212:213], v241 offset:25376\n"
"ds_read_b64_tr_b16 v[214:215], v241 offset:25632\n"
"v_mfma_f32_16x16x32_bf16 a[56:59], v[220:223], v[104:107], a[56:59]\n"
"ds_read_b64_tr_b16 v[216:217], v241 offset:25856\n"
"ds_read_b64_tr_b16 v[218:219], v241 offset:26112\n"
"v_mfma_f32_16x16x32_bf16 a[60:63], v[220:223], v[108:111], a[60:63]\n"
"s_cbranch_vccnz L_tBl0_%=\n"
"L_bBl0_%=:\n"
"s_waitcnt lgkmcnt(6)\n"
"v_mfma_f32_16x16x32_bf16 a[64:67], v[224:227], v[96:99], a[64:67]\n"
"ds_read_b64_tr_b16 v[220:221], v241 offset:25888\n"
"ds_read_b64_tr_b16 v[222:223], v241 offset:26144\n"
"v_mfma_f32_16x16x32_bf16 a[68:71], v[224:227], v[100:103], a[68:71]\n"
"v_exp_f32 v160, v160\n"
"v_mfma_f32_16x16x32_bf16 a[72:75], v[224:227], v[104:107], a[72:75]\n"
"v_exp_f32 v161, v161\n"
"v_mfma_f32_16x16x32_bf16 a[76:79], v[224:227], v[108:111], a[76:79]\n"
"v_exp_f32 v162, v162\n"
"v_mfma_f32_16x16x32_bf16 a[80:83], v[228:231], v[96:99], a[80:83]\n"
"v_exp_f32 v163, v163\n"
"v_mfma_f32_16x16x32_bf16 a[84:87], v[228:231], v[100:103], a[84:87]\n"
"v_add_f32 v244, v244, v160\n"
"s_add_u32 m0, s74, 0x11800\n"
"v_mfma_f32_16x16x32_bf16 a[88:91], v[228:231], v[104:107], a[88:91]\n"
"buffer_load_dwordx4 v243, s[52:55], s77 offen offset:128 nt lds\n"
"v_mfma_f32_16x16x32_bf16 a[92:95], v[228:231], v[108:111], a[92:95]\n"
"v_exp_f32 v176, v176\n"
"v_mfma_f32_16x16x32_bf16 a[96:99], v[232:235], v[96:99], a[96:99]\n"
"v_exp_f32 v177, v177\n"
"v_mfma_f32_16x16x32_bf16 a[100:103], v[232:235], v[100:103], a[100:103]\n"
"v_exp_f32 v178, v178\n"
"v_mfma_f32_16x16x32_bf16 a[104:107], v[232:235], v[104:107], a[104:107]\n"
"v_exp_f32 v179, v179\n"
"v_mfma_f32_16x16x32_bf16 a[108:111], v[232:235], v[108:111], a[108:111]\n"
"v_exp_f32 v164, v164\n"
"v_mfma_f32_16x16x32_bf16 a[112:115], v[236:239], v[96:99], a[112:115]\n"
"ds_read_b64_tr_b16 v[224:225], v241 offset:29568\n"
"ds_read_b64_tr_b16 v[226:227], v241 offset:29824\n"
"v_mfma_f32_16x16x32_bf16 a[116:119], v[236:239], v[100:103], a[116:119]\n"
"ds_read_b64_tr_b16 v[228:229], v241 offset:29600\n"
"ds_read_b64_tr_b16 v[230:231], v241 offset:29856\n"
"v_mfma_f32_16x16x32_bf16 a[120:123], v[236:239], v[104:107], a[120:123]\n"
"ds_read_b64_tr_b16 v[232:233], v241 offset:30080\n"
"ds_read_b64_tr_b16 v[234:235], v241 offset:30336\n"
"v_mfma_f32_16x16x32_bf16 a[124:127], v[236:239], v[108:111], a[124:127]\n"
"v_add_f32 v244, v244, v161\n"
"s_waitcnt lgkmcnt(6)\n"
"v_mfma_f32_16x16x32_bf16 a[128:131], v[208:211], v[96:99], a[128:131]\n"
"ds_read_b64_tr_b16 v[236:237], v241 offset:30112\n"
"ds_read_b64_tr_b16 v[238:239], v241 offset:30368\n"
"v_mfma_f32_16x16x32_bf16 a[132:135], v[208:211], v[100:103], a[132:135]\n"
"v_exp_f32 v165, v165\n"
"v_mfma_f32_16x16x32_bf16 a[136:139], v[208:211], v[104:107], a[136:139]\n"
"v_exp_f32 v166, v166\n"
"v_mfma_f32_16x16x32_bf16 a[140:143], v[208:211], v[108:111], a[140:143]\n"
"v_exp_f32 v167, v167\n"
"v_mfma_f32_16x16x32_bf16 a[144:147], v[212:215], v[96:99], a[144:147]\n"
"v_exp_f32 v180, v180\n"
"v_mfma_f32_16x16x32_bf16 a[148:151], v[212:215], v[100:103], a[148:151]\n"
"v_add_f32 v244, v244, v162\n"
"s_add_u32 m0, s74, 0x12800\n"
"v_mfma_f32_16x16x32_bf16 a[152:155], v[212:215], v[104:107], a[152:155]\n"
"buffer_load_dwordx4 v243, s[52:55], s77 offen offset:256 nt lds\n"
"v_mfma_f32_16x16x32_bf16 a[156:159], v[212:215], v[108:111], a[156:159]\n"
"v_exp_f32 v181, v181\n"
"v_mfma_f32_16x16x32_bf16 a[160:163], v[216:219], v[96:99], a[160:163]\n"
"v_exp_f32 v182, v182\n"
"v_mfma_f32_16x16x32_bf16 a[164:167], v[216:219], v[100:103], a[164:167]\n"
"v_exp_f32 v183, v183\n"
"v_mfma_f32_16x16x32_bf16 a[168:171], v[216:219], v[104:107], a[168:171]\n"
"v_exp_f32 v168, v168\n"
"v_mfma_f32_16x16x32_bf16 a[172:175], v[216:219], v[108:111], a[172:175]\n"
"v_exp_f32 v169, v169\n"
"v_mfma_f32_16x16x32_bf16 a[176:179], v[220:223], v[96:99], a[176:179]\n"
"ds_read_b128 v[208:211], v240 offset:50688\n"
"ds_read_b128 v[212:215], v240 offset:59136\n"
"v_mfma_f32_16x16x32_bf16 a[180:183], v[220:223], v[100:103], a[180:183]\n"
"ds_read_b128 v[216:219], v240 offset:50752\n"
"v_add_f32 v244, v244, v163\n"
"v_mfma_f32_16x16x32_bf16 a[184:187], v[220:223], v[104:107], a[184:187]\n"
"v_exp_f32 v170, v170\n"
"v_mfma_f32_16x16x32_bf16 a[188:191], v[220:223], v[108:111], a[188:191]\n"
"ds_read_b128 v[220:223], v240 offset:59200\n"
"s_waitcnt lgkmcnt(4)\n"
"v_mfma_f32_16x16x32_bf16 a[192:195], v[224:227], v[96:99], a[192:195]\n"
"v_exp_f32 v171, v171\n"
"v_mfma_f32_16x16x32_bf16 a[196:199], v[224:227], v[100:103], a[196:199]\n"
"v_exp_f32 v184, v184\n"
"v_mfma_f32_16x16x32_bf16 a[200:203], v[224:227], v[104:107], a[200:203]\n"
"v_exp_f32 v185, v185\n"
"v_mfma_f32_16x16x32_bf16 a[204:207], v[224:227], v[108:111], a[204:207]\n"
"v_exp_f32 v186, v186\n"
"v_mfma_f32_16x16x32_bf16 a[208:211], v[228:231], v[96:99], a[208:211]\n"
"v_exp_f32 v187, v187\n"
"v_mfma_f32_16x16x32_bf16 a[212:215], v[228:231], v[100:103], a[212:215]\n"
"v_add_f32 v244, v244, v176\n"
"s_add_u32 m0, s74, 0x13800\n"
"v_mfma_f32_16x16x32_bf16 a[216:219], v[228:231], v[104:107], a[216:219]\n"
"buffer_load_dwordx4 v243, s[52:55], s77 offen offset:384 nt lds\n"
"v_mfma_f32_16x16x32_bf16 a[220:223], v[228:231], v[108:111], a[220:223]\n"
"s_add_u32 s77, s77, 0x4000\n"
"v_add_f32 v244, v244, v177\n"
"v_mfma_f32_16x16x32_bf16 a[224:227], v[232:235], v[96:99], a[224:227]\n"
"v_add_f32 v244, v244, v178\n"
"v_add_f32 v244, v244, v179\n"
"v_mfma_f32_16x16x32_bf16 a[228:231], v[232:235], v[100:103], a[228:231]\n"
"v_cvt_pk_bf16_f32 v160, v160, v161\n"
"v_cvt_pk_bf16_f32 v161, v162, v163\n"
"v_mfma_f32_16x16x32_bf16 a[232:235], v[232:235], v[104:107], a[232:235]\n"
"v_exp_f32 v172, v172\n"
"v_mfma_f32_16x16x32_bf16 a[236:239], v[232:235], v[108:111], a[236:239]\n"
"v_exp_f32 v173, v173\n"
"v_mfma_f32_16x16x32_bf16 a[240:243], v[236:239], v[96:99], a[240:243]\n"
"ds_read_b128 v[224:227], v240 offset:50816\n"
"ds_read_b128 v[228:231], v240 offset:59264\n"
"v_mfma_f32_16x16x32_bf16 a[244:247], v[236:239], v[100:103], a[244:247]\n"
"ds_read_b128 v[232:235], v240 offset:50880\n"
"v_cvt_pk_bf16_f32 v162, v176, v177\n"
"v_mfma_f32_16x16x32_bf16 a[248:251], v[236:239], v[104:107], a[248:251]\n"
"v_exp_f32 v174, v174\n"
"v_mfma_f32_16x16x32_bf16 a[252:255], v[236:239], v[108:111], a[252:255]\n"
"s_cbranch_vccnz L_tCl0_%=\n"
"L_bCl0_%=:\n"
"s_waitcnt lgkmcnt(3)\n"
"v_mfma_f32_16x16x32_bf16 v[96:99], v[208:211], v[0:3], v[192:195]\n"
"ds_read_b128 v[236:239], v240 offset:59328\n"
"v_cvt_pk_bf16_f32 v163, v178, v179\n"
"v_mfma_f32_16x16x32_bf16 v[100:103], v[208:211], v[4:7], v[196:199]\n"
"v_exp_f32 v175, v175\n"
"v_mfma_f32_16x16x32_bf16 v[104:107], v[208:211], v[8:11], v[200:203]\n"
"v_add_f32 v245, v245, v164\n"
"v_add_f32 v245, v245, v165\n"
"v_mfma_f32_16x16x32_bf16 v[108:111], v[208:211], v[12:15], v[204:207]\n"
"v_exp_f32 v188, v188\n"
"v_mfma_f32_16x16x32_bf16 v[112:115], v[212:215], v[0:3], v[192:195]\n"
"v_exp_f32 v189, v189\n"
"v_mfma_f32_16x16x32_bf16 v[116:119], v[212:215], v[4:7], v[196:199]\n"
"v_add_f32 v245, v245, v166\n"
"s_add_u32 m0, s73, 0x8400\n"
"v_mfma_f32_16x16x32_bf16 v[120:123], v[212:215], v[8:11], v[200:203]\n"
"buffer_load_dwordx4 v242, s[48:51], s76 offen offset:0 nt lds\n"
"v_mfma_f32_16x16x32_bf16 v[124:127], v[212:215], v[12:15], v[204:207]\n"
"v_exp_f32 v190, v190\n"
"v_mfma_f32_16x16x32_bf16 v[96:99], v[216:219], v[16:19], v[96:99]\n"
"v_add_f32 v245, v245, v167\n"
"v_add_f32 v245, v245, v180\n"
"v_mfma_f32_16x16x32_bf16 v[100:103], v[216:219], v[20:23], v[100:103]\n"
"v_exp_f32 v191, v191\n"
"v_mfma_f32_16x16x32_bf16 v[104:107], v[216:219], v[24:27], v[104:107]\n"
"v_add_f32 v245, v245, v181\n"
"v_add_f32 v245, v245, v182\n"
"v_mfma_f32_16x16x32_bf16 v[108:111], v[216:219], v[28:31], v[108:111]\n"
"v_add_f32 v245, v245, v183\n"
"v_cvt_pk_bf16_f32 v164, v164, v165\n"
"v_mfma_f32_16x16x32_bf16 v[112:115], v[220:223], v[16:19], v[112:115]\n"
"ds_read_b128 v[208:211], v240 offset:50944\n"
"ds_read_b128 v[212:215], v240 offset:59392\n"
"v_mfma_f32_16x16x32_bf16 v[116:119], v[220:223], v[20:23], v[116:119]\n"
"ds_read_b128 v[216:219], v240 offset:51008\n"
"v_cvt_pk_bf16_f32 v165, v166, v167\n"
"v_mfma_f32_16x16x32_bf16 v[120:123], v[220:223], v[24:27], v[120:123]\n"
"v_cvt_pk_bf16_f32 v166, v180, v181\n"
"v_cvt_pk_bf16_f32 v167, v182, v183\n"
"v_mfma_f32_16x16x32_bf16 v[124:127], v[220:223], v[28:31], v[124:127]\n"
"ds_read_b128 v[220:223], v240 offset:59456\n"
"s_waitcnt lgkmcnt(4)\n"
"v_mfma_f32_16x16x32_bf16 v[96:99], v[224:227], v[64:67], v[96:99]\n"
"v_add_f32 v246, v246, v168\n"
"v_add_f32 v246, v246, v169\n"
"v_mfma_f32_16x16x32_bf16 v[100:103], v[224:227], v[68:71], v[100:103]\n"
"v_add_f32 v246, v246, v170\n"
"v_add_f32 v246, v246, v171\n"
"v_mfma_f32_16x16x32_bf16 v[104:107], v[224:227], v[72:75], v[104:107]\n"
"v_add_f32 v246, v246, v184\n"
"v_add_f32 v246, v246, v185\n"
"v_mfma_f32_16x16x32_bf16 v[108:111], v[224:227], v[76:79], v[108:111]\n"
"v_add_f32 v246, v246, v186\n"
"v_add_f32 v246, v246, v187\n"
"v_mfma_f32_16x16x32_bf16 v[112:115], v[228:231], v[64:67], v[112:115]\n"
"v_cvt_pk_bf16_f32 v168, v168, v169\n"
"v_cvt_pk_bf16_f32 v169, v170, v171\n"
"v_mfma_f32_16x16x32_bf16 v[116:119], v[228:231], v[68:71], v[116:119]\n"
"v_cvt_pk_bf16_f32 v170, v184, v185\n"
"s_add_u32 m0, s73, 0x8620\n"
"v_mfma_f32_16x16x32_bf16 v[120:123], v[228:231], v[72:75], v[120:123]\n"
"buffer_load_dwordx4 v242, s[48:51], s76 offen offset:512 nt lds\n"
"v_mfma_f32_16x16x32_bf16 v[124:127], v[228:231], v[76:79], v[124:127]\n"
"v_cvt_pk_bf16_f32 v171, v186, v187\n"
"v_add_f32 v247, v247, v172\n"
"v_mfma_f32_16x16x32_bf16 v[96:99], v[232:235], v[80:83], v[96:99]\n"
"v_add_f32 v247, v247, v173\n"
"v_add_f32 v247, v247, v174\n"
"v_mfma_f32_16x16x32_bf16 v[100:103], v[232:235], v[84:87], v[100:103]\n"
"v_add_f32 v247, v247, v175\n"
"v_add_f32 v247, v247, v188\n"
"v_mfma_f32_16x16x32_bf16 v[104:107], v[232:235], v[88:91], v[104:107]\n"
"v_add_f32 v247, v247, v189\n"
"v_add_f32 v247, v247, v190\n"
"v_mfma_f32_16x16x32_bf16 v[108:111], v[232:235], v[92:95], v[108:111]\n"
"v_add_f32 v247, v247, v191\n"
"v_cvt_pk_bf16_f32 v172, v172, v173\n"
"v_mfma_f32_16x16x32_bf16 v[112:115], v[236:239], v[80:83], v[112:115]\n"
"ds_read_b128 v[224:227], v240 offset:51072\n"
"ds_read_b128 v[228:231], v240 offset:59520\n"
"v_mfma_f32_16x16x32_bf16 v[116:119], v[236:239], v[84:87], v[116:119]\n"
"ds_read_b128 v[232:235], v240 offset:51136\n"
"v_cvt_pk_bf16_f32 v173, v174, v175\n"
"v_mfma_f32_16x16x32_bf16 v[120:123], v[236:239], v[88:91], v[120:123]\n"
"v_cvt_pk_bf16_f32 v174, v188, v189\n"
"v_cvt_pk_bf16_f32 v175, v190, v191\n"
"v_mfma_f32_16x16x32_bf16 v[124:127], v[236:239], v[92:95], v[124:127]\n"
"ds_read_b128 v[236:239], v240 offset:59584\n"
"s_waitcnt lgkmcnt(4)\n"
"v_mfma_f32_16x16x32_bf16 v[96:99], v[208:211], v[128:131], v[96:99]\n"
"v_mfma_f32_16x16x32_bf16 v[100:103], v[208:211], v[132:135], v[100:103]\n"
"v_mfma_f32_16x16x32_bf16 v[104:107], v[208:211], v[136:139], v[104:107]\n"
"v_mfma_f32_16x16x32_bf16 v[108:111], v[208:211], v[140:143], v[108:111]\n"
"v_mfma_f32_16x16x32_bf16 v[112:115], v[212:215], v[128:131], v[112:115]\n"
"v_mfma_f32_16x16x32_bf16 v[116:119], v[212:215], v[132:135], v[116:119]\n"
"s_add_u32 m0, s73, 0x8840\n"
"v_mfma_f32_16x16x32_bf16 v[120:123], v[212:215], v[136:139], v[120:123]\n"
"buffer_load_dwordx4 v242, s[48:51], s76 offen offset:1024 nt lds\n"
"v_mfma_f32_16x16x32_bf16 v[124:127], v[212:215], v[140:143], v[124:127]\n"
"v_mfma_f32_16x16x32_bf16 v[96:99], v[216:219], v[144:147], v[96:99]\n"
"v_mfma_f32_16x16x32_bf16 v[100:103], v[216:219], v[148:151], v[100:103]\n"
"v_mfma_f32_16x16x32_bf16 v[104:107], v[216:219], v[152:155], v[104:107]\n"
"v_mfma_f32_16x16x32_bf16 v[108:111], v[216:219], v[156:159], v[108:111]\n"
"v_mfma_f32_16x16x32_bf16 v[112:115], v[220:223], v[144:147], v[112:115]\n"
"v_mfma_f32_16x16x32_bf16 v[116:119], v[220:223], v[148:151], v[116:119]\n"
"v_mfma_f32_16x16x32_bf16 v[120:123], v[220:223], v[152:155], v[120:123]\n"
"v_mfma_f32_16x16x32_bf16 v[124:127], v[220:223], v[156:159], v[124:127]\n"
"s_waitcnt vmcnt(15) lgkmcnt(0)\n"
"s_barrier\n"
"v_mfma_f32_16x16x32_bf16 v[96:99], v[224:227], v[32:35], v[96:99]\n"
"ds_read_b64_tr_b16 v[208:209], v241 offset:33792\n"
"ds_read_b64_tr_b16 v[210:211], v241 offset:34048\n"
"v_mfma_f32_16x16x32_bf16 v[100:103], v[224:227], v[36:39], v[100:103]\n"
"ds_read_b64_tr_b16 v[212:213], v241 offset:33824\n"
"ds_read_b64_tr_b16 v[214:215], v241 offset:34080\n"
"v_mfma_f32_16x16x32_bf16 v[104:107], v[224:227], v[40:43], v[104:107]\n"
"ds_read_b64_tr_b16 v[216:217], v241 offset:34304\n"
"ds_read_b64_tr_b16 v[218:219], v241 offset:34560\n"
"v_mfma_f32_16x16x32_bf16 v[108:111], v[224:227], v[44:47], v[108:111]\n"
"ds_read_b64_tr_b16 v[220:221], v241 offset:34336\n"
"ds_read_b64_tr_b16 v[222:223], v241 offset:34592\n"
"v_mfma_f32_16x16x32_bf16 v[112:115], v[228:231], v[32:35], v[112:115]\n"
"v_mfma_f32_16x16x32_bf16 v[116:119], v[228:231], v[36:39], v[116:119]\n"
"s_add_u32 m0, s73, 0x8a60\n"
"v_mfma_f32_16x16x32_bf16 v[120:123], v[228:231], v[40:43], v[120:123]\n"
"buffer_load_dwordx4 v242, s[48:51], s76 offen offset:1536 nt lds\n"
"v_mfma_f32_16x16x32_bf16 v[124:127], v[228:231], v[44:47], v[124:127]\n"
"s_add_u32 s76, s76, 0x4000\n"
"v_mfma_f32_16x16x32_bf16 v[96:99], v[232:235], v[48:51], v[96:99]\n"
"v_mfma_f32_16x16x32_bf16 v[100:103], v[232:235], v[52:55], v[100:103]\n"
"v_mfma_f32_16x16x32_bf16 v[104:107], v[232:235], v[56:59], v[104:107]\n"
"s_mov_b32 m0, 0x27300\n"
"v_mfma_f32_16x16x32_bf16 v[108:111], v[232:235], v[60:63], v[108:111]\n"
"global_load_lds_dword v251, s[8:9]\n"
"v_mfma_f32_16x16x32_bf16 v[112:115], v[236:239], v[48:51], v[112:115]\n"
"ds_read_b64_tr_b16 v[224:225], v241 offset:38016\n"
"ds_read_b64_tr_b16 v[226:227], v241 offset:38272\n"
"v_mfma_f32_16x16x32_bf16 v[116:119], v[236:239], v[52:55], v[116:119]\n"
"ds_read_b64_tr_b16 v[228:229], v241 offset:38048\n"
"ds_read_b64_tr_b16 v[230:231], v241 offset:38304\n"
"v_mfma_f32_16x16x32_bf16 v[120:123], v[236:239], v[56:59], v[120:123]\n"
"ds_read_b64_tr_b16 v[232:233], v241 offset:38528\n"
"ds_read_b64_tr_b16 v[234:235], v241 offset:38784\n"
"v_mfma_f32_16x16x32_bf16 v[124:127], v[236:239], v[60:63], v[124:127]\n"
"s_waitcnt lgkmcnt(6)\n"
"v_mfma_f32_16x16x32_bf16 a[0:3], v[208:211], v[160:163], a[0:3]\n"
"ds_read_b64_tr_b16 v[236:237], v241 offset:38560\n"
"ds_read_b64_tr_b16 v[238:239], v241 offset:38816\n"
"v_mfma_f32_16x16x32_bf16 a[4:7], v[208:211], v[164:167], a[4:7]\n"
"v_max3_f32 v248, v96, v97, v98\n"
"v_max3_f32 v248, v248, v99, v100\n"
"v_mfma_f32_16x16x32_bf16 a[8:11], v[208:211], v[168:171], a[8:11]\n"
"v_max3_f32 v248, v248, v101, v102\n"
"v_max3_f32 v248, v248, v103, v104\n"
"v_mfma_f32_16x16x32_bf16 a[12:15], v[208:211], v[172:175], a[12:15]\n"
"v_max3_f32 v248, v248, v105, v106\n"
"v_max3_f32 v248, v248, v107, v108\n"
"v_mfma_f32_16x16x32_bf16 a[16:19], v[212:215], v[160:163], a[16:19]\n"
"v_max3_f32 v248, v248, v109, v110\n"
"v_max3_f32 v248, v248, v111, v112\n"
"v_mfma_f32_16x16x32_bf16 a[20:23], v[212:215], v[164:167], a[20:23]\n"
"v_max3_f32 v248, v248, v113, v114\n"
"s_add_u32 m0, s74, 0x14a00\n"
"v_mfma_f32_16x16x32_bf16 a[24:27], v[212:215], v[168:171], a[24:27]\n"
"buffer_load_dwordx4 v243, s[52:55], s77 offen offset:0 nt lds\n"
"v_mfma_f32_16x16x32_bf16 a[28:31], v[212:215], v[172:175], a[28:31]\n"
"v_max3_f32 v248, v248, v115, v116\n"
"v_max3_f32 v248, v248, v117, v118\n"
"v_mfma_f32_16x16x32_bf16 a[32:35], v[216:219], v[160:163], a[32:35]\n"
"v_max3_f32 v248, v248, v119, v120\n"
"v_max3_f32 v248, v248, v121, v122\n"
"v_mfma_f32_16x16x32_bf16 a[36:39], v[216:219], v[164:167], a[36:39]\n"
"v_max3_f32 v248, v248, v123, v124\n"
"v_max3_f32 v248, v248, v125, v126\n"
"v_mfma_f32_16x16x32_bf16 a[40:43], v[216:219], v[168:171], a[40:43]\n"
"v_max_f32 v248, v248, v127\n"
"v_cmp_lt_f32 vcc, 0x41000000, v248\n"
"v_mfma_f32_16x16x32_bf16 a[44:47], v[216:219], v[172:175], a[44:47]\n"
"v_mfma_f32_16x16x32_bf16 a[48:51], v[220:223], v[160:163], a[48:51]\n"
"ds_read_b64_tr_b16 v[208:209], v241 offset:42240\n"
"ds_read_b64_tr_b16 v[210:211], v241 offset:42496\n"
"v_mfma_f32_16x16x32_bf16 a[52:55], v[220:223], v[164:167], a[52:55]\n"
"ds_read_b64_tr_b16 v[212:213], v241 offset:42272\n"
"ds_read_b64_tr_b16 v[214:215], v241 offset:42528\n"
"v_mfma_f32_16x16x32_bf16 a[56:59], v[220:223], v[168:171], a[56:59]\n"
"ds_read_b64_tr_b16 v[216:217], v241 offset:42752\n"
"ds_read_b64_tr_b16 v[218:219], v241 offset:43008\n"
"v_mfma_f32_16x16x32_bf16 a[60:63], v[220:223], v[172:175], a[60:63]\n"
"s_cbranch_vccnz L_tBl1_%=\n"
"L_bBl1_%=:\n"
"s_waitcnt lgkmcnt(6)\n"
"v_mfma_f32_16x16x32_bf16 a[64:67], v[224:227], v[160:163], a[64:67]\n"
"ds_read_b64_tr_b16 v[220:221], v241 offset:42784\n"
"ds_read_b64_tr_b16 v[222:223], v241 offset:43040\n"
"v_mfma_f32_16x16x32_bf16 a[68:71], v[224:227], v[164:167], a[68:71]\n"
"v_exp_f32 v96, v96\n"
"v_mfma_f32_16x16x32_bf16 a[72:75], v[224:227], v[168:171], a[72:75]\n"
"v_exp_f32 v97, v97\n"
"v_mfma_f32_16x16x32_bf16 a[76:79], v[224:227], v[172:175], a[76:79]\n"
"v_exp_f32 v98, v98\n"
"v_mfma_f32_16x16x32_bf16 a[80:83], v[228:231], v[160:163], a[80:83]\n"
"v_exp_f32 v99, v99\n"
"v_mfma_f32_16x16x32_bf16 a[84:87], v[228:231], v[164:167], a[84:87]\n"
"v_add_f32 v244, v244, v96\n"
"s_add_u32 m0, s74, 0x15a00\n"
"v_mfma_f32_16x16x32_bf16 a[88:91], v[228:231], v[168:171], a[88:91]\n"
"buffer_load_dwordx4 v243, s[52:55], s77 offen offset:128 nt lds\n"
"v_mfma_f32_16x16x32_bf16 a[92:95], v[228:231], v[172:175], a[92:95]\n"
"v_exp_f32 v112, v112\n"
"v_mfma_f32_16x16x32_bf16 a[96:99], v[232:235], v[160:163], a[96:99]\n"
"v_exp_f32 v113, v113\n"
"v_mfma_f32_16x16x32_bf16 a[100:103], v[232:235], v[164:167], a[100:103]\n"
"v_exp_f32 v114, v114\n"
"v_mfma_f32_16x16x32_bf16 a[104:107], v[232:235], v[168:171], a[104:107]\n"
"v_exp_f32 v115, v115\n"
"v_mfma_f32_16x16x32_bf16 a[108:111], v[232:235], v[172:175], a[108:111]\n"
"v_exp_f32 v100, v100\n"
"v_mfma_f32_16x16x32_bf16 a[112:115], v[236:239], v[160:163], a[112:115]\n"
"ds_read_b64_tr_b16 v[224:225], v241 offset:46464\n"
"ds_read_b64_tr_b16 v[226:227], v241 offset:46720\n"
"v_mfma_f32_16x16x32_bf16 a[116:119], v[236:239], v[164:167], a[116:119]\n"
"ds_read_b64_tr_b16 v[228:229], v241 offset:46496\n"
"ds_read_b64_tr_b16 v[230:231], v241 offset:46752\n"
"v_mfma_f32_16x16x32_bf16 a[120:123], v[236:239], v[168:171], a[120:123]\n"
"ds_read_b64_tr_b16 v[232:233], v241 offset:46976\n"
"ds_read_b64_tr_b16 v[234:235], v241 offset:47232\n"
"v_mfma_f32_16x16x32_bf16 a[124:127], v[236:239], v[172:175], a[124:127]\n"
"v_add_f32 v244, v244, v97\n"
"s_waitcnt lgkmcnt(6)\n"
"v_mfma_f32_16x16x32_bf16 a[128:131], v[208:211], v[160:163], a[128:131]\n"
"ds_read_b64_tr_b16 v[236:237], v241 offset:47008\n"
"ds_read_b64_tr_b16 v[238:239], v241 offset:47264\n"
"v_mfma_f32_16x16x32_bf16 a[132:135], v[208:211], v[164:167], a[132:135]\n"
"v_exp_f32 v101, v101\n"
"v_mfma_f32_16x16x32_bf16 a[136:139], v[208:211], v[168:171], a[136:139]\n"
"v_exp_f32 v102, v102\n"
"v_mfma_f32_16x16x32_bf16 a[140:143], v[208:211], v[172:175], a[140:143]\n"
"v_exp_f32 v103, v103\n"
"v_mfma_f32_16x16x32_bf16 a[144:147], v[212:215], v[160:163], a[144:147]\n"
"v_exp_f32 v116, v116\n"
"v_mfma_f32_16x16x32_bf16 a[148:151], v[212:215], v[164:167], a[148:151]\n"
"v_add_f32 v244, v244, v98\n"
"s_add_u32 m0, s74, 0x16a00\n"
"v_mfma_f32_16x16x32_bf16 a[152:155], v[212:215], v[168:171], a[152:155]\n"
"buffer_load_dwordx4 v243, s[52:55], s77 offen offset:256 nt lds\n"
"v_mfma_f32_16x16x32_bf16 a[156:159], v[212:215], v[172:175], a[156:159]\n"
"v_exp_f32 v117, v117\n"
"v_mfma_f32_16x16x32_bf16 a[160:163], v[216:219], v[160:163], a[160:163]\n"
"v_exp_f32 v118, v118\n"
"v_mfma_f32_16x16x32_bf16 a[164:167], v[216:219], v[164:167], a[164:167]\n"
"v_exp_f32 v119, v119\n"
"v_mfma_f32_16x16x32_bf16 a[168:171], v[216:219], v[168:171], a[168:171]\n"
"v_exp_f32 v104, v104\n"
"v_mfma_f32_16x16x32_bf16 a[172:175], v[216:219], v[172:175], a[172:175]\n"
"v_exp_f32 v105, v105\n"
"v_mfma_f32_16x16x32_bf16 a[176:179], v[220:223], v[160:163], a[176:179]\n"
"ds_read_b128 v[208:211], v240 offset:0\n"
"ds_read_b128 v[212:215], v240 offset:8448\n"
"v_mfma_f32_16x16x32_bf16 a[180:183], v[220:223], v[164:167], a[180:183]\n"
"ds_read_b128 v[216:219], v240 offset:64\n"
"v_add_f32 v244, v244, v99\n"
"v_mfma_f32_16x16x32_bf16 a[184:187], v[220:223], v[168:171], a[184:187]\n"
"v_exp_f32 v106, v106\n"
"v_mfma_f32_16x16x32_bf16 a[188:191], v[220:223], v[172:175], a[188:191]\n"
"ds_read_b128 v[220:223], v240 offset:8512\n"
"s_waitcnt lgkmcnt(4)\n"
"v_mfma_f32_16x16x32_bf16 a[192:195], v[224:227], v[160:163], a[192:195]\n"
"v_exp_f32 v107, v107\n"
"v_mfma_f32_16x16x32_bf16 a[196:199], v[224:227], v[164:167], a[196:199]\n"
"v_exp_f32 v120, v120\n"
"v_mfma_f32_16x16x32_bf16 a[200:203], v[224:227], v[168:171], a[200:203]\n"
"v_exp_f32 v121, v121\n"
"v_mfma_f32_16x16x32_bf16 a[204:207], v[224:227], v[172:175], a[204:207]\n"
"v_exp_f32 v122, v122\n"
"v_mfma_f32_16x16x32_bf16 a[208:211], v[228:231], v[160:163], a[208:211]\n"
"v_exp_f32 v123, v123\n"
"v_mfma_f32_16x16x32_bf16 a[212:215], v[228:231], v[164:167], a[212:215]\n"
"v_add_f32 v244, v244, v112\n"
"s_add_u32 m0, s74, 0x17a00\n"
"v_mfma_f32_16x16x32_bf16 a[216:219], v[228:231], v[168:171], a[216:219]\n"
"buffer_load_dwordx4 v243, s[52:55], s77 offen offset:384 nt lds\n"
"v_mfma_f32_16x16x32_bf16 a[220:223], v[228:231], v[172:175], a[220:223]\n"
"s_add_u32 s77, s77, 0x4000\n"
"v_add_f32 v244, v244, v113\n"
"v_mfma_f32_16x16x32_bf16 a[224:227], v[232:235], v[160:163], a[224:227]\n"
"v_add_f32 v244, v244, v114\n"
"v_add_f32 v244, v244, v115\n"
"v_mfma_f32_16x16x32_bf16 a[228:231], v[232:235], v[164:167], a[228:231]\n"
"v_cvt_pk_bf16_f32 v96, v96, v97\n"
"v_cvt_pk_bf16_f32 v97, v98, v99\n"
"v_mfma_f32_16x16x32_bf16 a[232:235], v[232:235], v[168:171], a[232:235]\n"
"v_exp_f32 v108, v108\n"
"v_mfma_f32_16x16x32_bf16 a[236:239], v[232:235], v[172:175], a[236:239]\n"
"v_exp_f32 v109, v109\n"
"v_mfma_f32_16x16x32_bf16 a[240:243], v[236:239], v[160:163], a[240:243]\n"
"ds_read_b128 v[224:227], v240 offset:128\n"
"ds_read_b128 v[228:231], v240 offset:8576\n"
"v_mfma_f32_16x16x32_bf16 a[244:247], v[236:239], v[164:167], a[244:247]\n"
"ds_read_b128 v[232:235], v240 offset:192\n"
"v_cvt_pk_bf16_f32 v98, v112, v113\n"
"v_mfma_f32_16x16x32_bf16 a[248:251], v[236:239], v[168:171], a[248:251]\n"
"v_exp_f32 v110, v110\n"
"v_mfma_f32_16x16x32_bf16 a[252:255], v[236:239], v[172:175], a[252:255]\n"
"s_cbranch_vccnz L_tCl1_%=\n"
"L_bCl1_%=:\n"
"s_waitcnt lgkmcnt(3)\n"
"v_mfma_f32_16x16x32_bf16 v[160:163], v[208:211], v[0:3], v[192:195]\n"
"ds_read_b128 v[236:239], v240 offset:8640\n"
"v_cvt_pk_bf16_f32 v99, v114, v115\n"
"v_mfma_f32_16x16x32_bf16 v[164:167], v[208:211], v[4:7], v[196:199]\n"
"v_exp_f32 v111, v111\n"
"v_mfma_f32_16x16x32_bf16 v[168:171], v[208:211], v[8:11], v[200:203]\n"
"v_add_f32 v245, v245, v100\n"
"v_add_f32 v245, v245, v101\n"
"v_mfma_f32_16x16x32_bf16 v[172:175], v[208:211], v[12:15], v[204:207]\n"
"v_exp_f32 v124, v124\n"
"v_mfma_f32_16x16x32_bf16 v[176:179], v[212:215], v[0:3], v[192:195]\n"
"v_exp_f32 v125, v125\n"
"v_mfma_f32_16x16x32_bf16 v[180:183], v[212:215], v[4:7], v[196:199]\n"
"v_add_f32 v245, v245, v102\n"
"s_add_u32 m0, s73, 0xc600\n"
"v_mfma_f32_16x16x32_bf16 v[184:187], v[212:215], v[8:11], v[200:203]\n"
"buffer_load_dwordx4 v242, s[48:51], s76 offen offset:0 nt lds\n"
"v_mfma_f32_16x16x32_bf16 v[188:191], v[212:215], v[12:15], v[204:207]\n"
"v_exp_f32 v126, v126\n"
"v_mfma_f32_16x16x32_bf16 v[160:163], v[216:219], v[16:19], v[160:163]\n"
"v_add_f32 v245, v245, v103\n"
"v_add_f32 v245, v245, v116\n"
"v_mfma_f32_16x16x32_bf16 v[164:167], v[216:219], v[20:23], v[164:167]\n"
"v_exp_f32 v127, v127\n"
"v_mfma_f32_16x16x32_bf16 v[168:171], v[216:219], v[24:27], v[168:171]\n"
"v_add_f32 v245, v245, v117\n"
"v_add_f32 v245, v245, v118\n"
"v_mfma_f32_16x16x32_bf16 v[172:175], v[216:219], v[28:31], v[172:175]\n"
"v_add_f32 v245, v245, v119\n"
"v_cvt_pk_bf16_f32 v100, v100, v101\n"
"v_mfma_f32_16x16x32_bf16 v[176:179], v[220:223], v[16:19], v[176:179]\n"
"ds_read_b128 v[208:211], v240 offset:256\n"
"ds_read_b128 v[212:215], v240 offset:8704\n"
"v_mfma_f32_16x16x32_bf16 v[180:183], v[220:223], v[20:23], v[180:183]\n"
"ds_read_b128 v[216:219], v240 offset:320\n"
"v_cvt_pk_bf16_f32 v101, v102, v103\n"
"v_mfma_f32_16x16x32_bf16 v[184:187], v[220:223], v[24:27], v[184:187]\n"
"v_cvt_pk_bf16_f32 v102, v116, v117\n"
"v_cvt_pk_bf16_f32 v103, v118, v119\n"
"v_mfma_f32_16x16x32_bf16 v[188:191], v[220:223], v[28:31], v[188:191]\n"
"ds_read_b128 v[220:223], v240 offset:8768\n"
"s_waitcnt lgkmcnt(4)\n"
"v_mfma_f32_16x16x32_bf16 v[160:163], v[224:227], v[64:67], v[160:163]\n"
"v_add_f32 v246, v246, v104\n"
"v_add_f32 v246, v246, v105\n"
"v_mfma_f32_16x16x32_bf16 v[164:167], v[224:227], v[68:71], v[164:167]\n"
"v_add_f32 v246, v246, v106\n"
"v_add_f32 v246, v246, v107\n"
"v_mfma_f32_16x16x32_bf16 v[168:171], v[224:227], v[72:75], v[168:171]\n"
"v_add_f32 v246, v246, v120\n"
"v_add_f32 v246, v246, v121\n"
"v_mfma_f32_16x16x32_bf16 v[172:175], v[224:227], v[76:79], v[172:175]\n"
"v_add_f32 v246, v246, v122\n"
"v_add_f32 v246, v246, v123\n"
"v_mfma_f32_16x16x32_bf16 v[176:179], v[228:231], v[64:67], v[176:179]\n"
"v_cvt_pk_bf16_f32 v104, v104, v105\n"
"v_cvt_pk_bf16_f32 v105, v106, v107\n"
"v_mfma_f32_16x16x32_bf16 v[180:183], v[228:231], v[68:71], v[180:183]\n"
"v_cvt_pk_bf16_f32 v106, v120, v121\n"
"s_add_u32 m0, s73, 0xc820\n"
"v_mfma_f32_16x16x32_bf16 v[184:187], v[228:231], v[72:75], v[184:187]\n"
"buffer_load_dwordx4 v242, s[48:51], s76 offen offset:512 nt lds\n"
"v_mfma_f32_16x16x32_bf16 v[188:191], v[228:231], v[76:79], v[188:191]\n"
"v_cvt_pk_bf16_f32 v107, v122, v123\n"
"v_add_f32 v247, v247, v108\n"
"v_mfma_f32_16x16x32_bf16 v[160:163], v[232:235], v[80:83], v[160:163]\n"
"v_add_f32 v247, v247, v109\n"
"v_add_f32 v247, v247, v110\n"
"v_mfma_f32_16x16x32_bf16 v[164:167], v[232:235], v[84:87], v[164:167]\n"
"v_add_f32 v247, v247, v111\n"
"v_add_f32 v247, v247, v124\n"
"v_mfma_f32_16x16x32_bf16 v[168:171], v[232:235], v[88:91], v[168:171]\n"
"v_add_f32 v247, v247, v125\n"
"v_add_f32 v247, v247, v126\n"
"v_mfma_f32_16x16x32_bf16 v[172:175], v[232:235], v[92:95], v[172:175]\n"
"v_add_f32 v247, v247, v127\n"
"v_cvt_pk_bf16_f32 v108, v108, v109\n"
"v_mfma_f32_16x16x32_bf16 v[176:179], v[236:239], v[80:83], v[176:179]\n"
"ds_read_b128 v[224:227], v240 offset:384\n"
"ds_read_b128 v[228:231], v240 offset:8832\n"
"v_mfma_f32_16x16x32_bf16 v[180:183], v[236:239], v[84:87], v[180:183]\n"
"ds_read_b128 v[232:235], v240 offset:448\n"
"v_cvt_pk_bf16_f32 v109, v110, v111\n"
"v_mfma_f32_16x16x32_bf16 v[184:187], v[236:239], v[88:91], v[184:187]\n"
"v_cvt_pk_bf16_f32 v110, v124, v125\n"
"v_cvt_pk_bf16_f32 v111, v126, v127\n"
"v_mfma_f32_16x16x32_bf16 v[188:191], v[236:239], v[92:95], v[188:191]\n"
"ds_read_b128 v[236:239], v240 offset:8896\n"
"s_waitcnt lgkmcnt(4)\n"
"v_mfma_f32_16x16x32_bf16 v[160:163], v[208:211], v[128:131], v[160:163]\n"
"v_mfma_f32_16x16x32_bf16 v[164:167], v[208:211], v[132:135], v[164:167]\n"
"v_mfma_f32_16x16x32_bf16 v[168:171], v[208:211], v[136:139], v[168:171]\n"
"v_mfma_f32_16x16x32_bf16 v[172:175], v[208:211], v[140:143], v[172:175]\n"
"v_mfma_f32_16x16x32_bf16 v[176:179], v[212:215], v[128:131], v[176:179]\n"
"v_mfma_f32_16x16x32_bf16 v[180:183], v[212:215], v[132:135], v[180:183]\n"
"s_add_u32 m0, s73, 0xca40\n"
"v_mfma_f32_16x16x32_bf16 v[184:187], v[212:215], v[136:139], v[184:187]\n"
"buffer_load_dwordx4 v242, s[48:51], s76 offen offset:1024 nt lds\n"
"v_mfma_f32_16x16x32_bf16 v[188:191], v[212:215], v[140:143], v[188:191]\n"
"v_mfma_f32_16x16x32_bf16 v[160:163], v[216:219], v[144:147], v[160:163]\n"
"v_mfma_f32_16x16x32_bf16 v[164:167], v[216:219], v[148:151], v[164:167]\n"
"v_mfma_f32_16x16x32_bf16 v[168:171], v[216:219], v[152:155], v[168:171]\n"
"v_mfma_f32_16x16x32_bf16 v[172:175], v[216:219], v[156:159], v[172:175]\n"
"v_mfma_f32_16x16x32_bf16 v[176:179], v[220:223], v[144:147], v[176:179]\n"
"v_mfma_f32_16x16x32_bf16 v[180:183], v[220:223], v[148:151], v[180:183]\n"
"v_mfma_f32_16x16x32_bf16 v[184:187], v[220:223], v[152:155], v[184:187]\n"
"v_mfma_f32_16x16x32_bf16 v[188:191], v[220:223], v[156:159], v[188:191]\n"
"s_waitcnt vmcnt(16) lgkmcnt(0)\n"
"s_barrier\n"
"v_mfma_f32_16x16x32_bf16 v[160:163], v[224:227], v[32:35], v[160:163]\n"
"ds_read_b64_tr_b16 v[208:209], v241 offset:50688\n"
"ds_read_b64_tr_b16 v[210:211], v241 offset:50944\n"
"v_mfma_f32_16x16x32_bf16 v[164:167], v[224:227], v[36:39], v[164:167]\n"
"ds_read_b64_tr_b16 v[212:213], v241 offset:50720\n"
"ds_read_b64_tr_b16 v[214:215], v241 offset:50976\n"
"v_mfma_f32_16x16x32_bf16 v[168:171], v[224:227], v[40:43], v[168:171]\n"
"ds_read_b64_tr_b16 v[216:217], v241 offset:51200\n"
"ds_read_b64_tr_b16 v[218:219], v241 offset:51456\n"
"v_mfma_f32_16x16x32_bf16 v[172:175], v[224:227], v[44:47], v[172:175]\n"
"ds_read_b64_tr_b16 v[220:221], v241 offset:51232\n"
"ds_read_b64_tr_b16 v[222:223], v241 offset:51488\n"
"v_mfma_f32_16x16x32_bf16 v[176:179], v[228:231], v[32:35], v[176:179]\n"
"v_mfma_f32_16x16x32_bf16 v[180:183], v[228:231], v[36:39], v[180:183]\n"
"s_add_u32 m0, s73, 0xcc60\n"
"v_mfma_f32_16x16x32_bf16 v[184:187], v[228:231], v[40:43], v[184:187]\n"
"buffer_load_dwordx4 v242, s[48:51], s76 offen offset:1536 nt lds\n"
"v_mfma_f32_16x16x32_bf16 v[188:191], v[228:231], v[44:47], v[188:191]\n"
"s_add_u32 s76, s76, 0x4000\n"
"v_mfma_f32_16x16x32_bf16 v[160:163], v[232:235], v[48:51], v[160:163]\n"
"v_mfma_f32_16x16x32_bf16 v[164:167], v[232:235], v[52:55], v[164:167]\n"
"v_mfma_f32_16x16x32_bf16 v[168:171], v[232:235], v[56:59], v[168:171]\n"
"v_mfma_f32_16x16x32_bf16 v[172:175], v[232:235], v[60:63], v[172:175]\n"
"v_mfma_f32_16x16x32_bf16 v[176:179], v[236:239], v[48:51], v[176:179]\n"
"ds_read_b64_tr_b16 v[224:225], v241 offset:54912\n"
"ds_read_b64_tr_b16 v[226:227], v241 offset:55168\n"
"v_mfma_f32_16x16x32_bf16 v[180:183], v[236:239], v[52:55], v[180:183]\n"
"ds_read_b64_tr_b16 v[228:229], v241 offset:54944\n"
"ds_read_b64_tr_b16 v[230:231], v241 offset:55200\n"
"v_mfma_f32_16x16x32_bf16 v[184:187], v[236:239], v[56:59], v[184:187]\n"
"ds_read_b64_tr_b16 v[232:233], v241 offset:55424\n"
"ds_read_b64_tr_b16 v[234:235], v241 offset:55680\n"
"v_mfma_f32_16x16x32_bf16 v[188:191], v[236:239], v[60:63], v[188:191]\n"
"s_waitcnt lgkmcnt(6)\n"
"v_mfma_f32_16x16x32_bf16 a[0:3], v[208:211], v[96:99], a[0:3]\n"
"ds_read_b64_tr_b16 v[236:237], v241 offset:55456\n"
"ds_read_b64_tr_b16 v[238:239], v241 offset:55712\n"
"v_mfma_f32_16x16x32_bf16 a[4:7], v[208:211], v[100:103], a[4:7]\n"
"v_max3_f32 v248, v160, v161, v162\n"
"v_max3_f32 v248, v248, v163, v164\n"
"v_mfma_f32_16x16x32_bf16 a[8:11], v[208:211], v[104:107], a[8:11]\n"
"v_max3_f32 v248, v248, v165, v166\n"
"v_max3_f32 v248, v248, v167, v168\n"
"v_mfma_f32_16x16x32_bf16 a[12:15], v[208:211], v[108:111], a[12:15]\n"
"v_max3_f32 v248, v248, v169, v170\n"
"v_max3_f32 v248, v248, v171, v172\n"
"v_mfma_f32_16x16x32_bf16 a[16:19], v[212:215], v[96:99], a[16:19]\n"
"v_max3_f32 v248, v248, v173, v174\n"
"v_max3_f32 v248, v248, v175, v176\n"
"v_mfma_f32_16x16x32_bf16 a[20:23], v[212:215], v[100:103], a[20:23]\n"
"v_max3_f32 v248, v248, v177, v178\n"
"s_add_u32 m0, s74, 0x18c00\n"
"v_mfma_f32_16x16x32_bf16 a[24:27], v[212:215], v[104:107], a[24:27]\n"
"buffer_load_dwordx4 v243, s[52:55], s77 offen offset:0 nt lds\n"
"v_mfma_f32_16x16x32_bf16 a[28:31], v[212:215], v[108:111], a[28:31]\n"
"v_max3_f32 v248, v248, v179, v180\n"
"v_max3_f32 v248, v248, v181, v182\n"
"v_mfma_f32_16x16x32_bf16 a[32:35], v[216:219], v[96:99], a[32:35]\n"
"v_max3_f32 v248, v248, v183, v184\n"
"v_max3_f32 v248, v248, v185, v186\n"
"v_mfma_f32_16x16x32_bf16 a[36:39], v[216:219], v[100:103], a[36:39]\n"
"v_max3_f32 v248, v248, v187, v188\n"
"v_max3_f32 v248, v248, v189, v190\n"
"v_mfma_f32_16x16x32_bf16 a[40:43], v[216:219], v[104:107], a[40:43]\n"
"v_max_f32 v248, v248, v191\n"
"v_cmp_lt_f32 vcc, 0x41000000, v248\n"
"v_mfma_f32_16x16x32_bf16 a[44:47], v[216:219], v[108:111], a[44:47]\n"
"v_mfma_f32_16x16x32_bf16 a[48:51], v[220:223], v[96:99], a[48:51]\n"
"ds_read_b64_tr_b16 v[208:209], v241 offset:59136\n"
"ds_read_b64_tr_b16 v[210:211], v241 offset:59392\n"
"v_mfma_f32_16x16x32_bf16 a[52:55], v[220:223], v[100:103], a[52:55]\n"
"ds_read_b64_tr_b16 v[212:213], v241 offset:59168\n"
"ds_read_b64_tr_b16 v[214:215], v241 offset:59424\n"
"v_mfma_f32_16x16x32_bf16 a[56:59], v[220:223], v[104:107], a[56:59]\n"
"ds_read_b64_tr_b16 v[216:217], v241 offset:59648\n"
"ds_read_b64_tr_b16 v[218:219], v241 offset:59904\n"
"v_mfma_f32_16x16x32_bf16 a[60:63], v[220:223], v[108:111], a[60:63]\n"
"s_cbranch_vccnz L_tBl2_%=\n"
"L_bBl2_%=:\n"
"s_waitcnt lgkmcnt(6)\n"
"v_mfma_f32_16x16x32_bf16 a[64:67], v[224:227], v[96:99], a[64:67]\n"
"ds_read_b64_tr_b16 v[220:221], v241 offset:59680\n"
"ds_read_b64_tr_b16 v[222:223], v241 offset:59936\n"
"v_mfma_f32_16x16x32_bf16 a[68:71], v[224:227], v[100:103], a[68:71]\n"
"v_exp_f32 v160, v160\n"
"v_mfma_f32_16x16x32_bf16 a[72:75], v[224:227], v[104:107], a[72:75]\n"
"v_exp_f32 v161, v161\n"
"v_mfma_f32_16x16x32_bf16 a[76:79], v[224:227], v[108:111], a[76:79]\n"
"v_exp_f32 v162, v162\n"
"v_mfma_f32_16x16x32_bf16 a[80:83], v[228:231], v[96:99], a[80:83]\n"
"v_exp_f32 v163, v163\n"
"v_mfma_f32_16x16x32_bf16 a[84:87], v[228:231], v[100:103], a[84:87]\n"
"v_add_f32 v244, v244, v160\n"
"s_add_u32 m0, s74, 0x19c00\n"
"v_mfma_f32_16x16x32_bf16 a[88:91], v[228:231], v[104:107], a[88:91]\n"
"buffer_load_dwordx4 v243, s[52:55], s77 offen offset:128 nt lds\n"
"v_mfma_f32_16x16x32_bf16 a[92:95], v[228:231], v[108:111], a[92:95]\n"
"v_exp_f32 v176, v176\n"
"v_mfma_f32_16x16x32_bf16 a[96:99], v[232:235], v[96:99], a[96:99]\n"
"v_exp_f32 v177, v177\n"
"v_mfma_f32_16x16x32_bf16 a[100:103], v[232:235], v[100:103], a[100:103]\n"
"v_exp_f32 v178, v178\n"
"v_mfma_f32_16x16x32_bf16 a[104:107], v[232:235], v[104:107], a[104:107]\n"
"v_exp_f32 v179, v179\n"
"v_mfma_f32_16x16x32_bf16 a[108:111], v[232:235], v[108:111], a[108:111]\n"
"v_exp_f32 v164, v164\n"
"v_mfma_f32_16x16x32_bf16 a[112:115], v[236:239], v[96:99], a[112:115]\n"
"ds_read_b64_tr_b16 v[224:225], v241 offset:63360\n"
"ds_read_b64_tr_b16 v[226:227], v241 offset:63616\n"
"v_mfma_f32_16x16x32_bf16 a[116:119], v[236:239], v[100:103], a[116:119]\n"
"ds_read_b64_tr_b16 v[228:229], v241 offset:63392\n"
"ds_read_b64_tr_b16 v[230:231], v241 offset:63648\n"
"v_mfma_f32_16x16x32_bf16 a[120:123], v[236:239], v[104:107], a[120:123]\n"
"ds_read_b64_tr_b16 v[232:233], v241 offset:63872\n"
"ds_read_b64_tr_b16 v[234:235], v241 offset:64128\n"
"v_mfma_f32_16x16x32_bf16 a[124:127], v[236:239], v[108:111], a[124:127]\n"
"v_add_f32 v244, v244, v161\n"
"s_waitcnt lgkmcnt(6)\n"
"v_mfma_f32_16x16x32_bf16 a[128:131], v[208:211], v[96:99], a[128:131]\n"
"ds_read_b64_tr_b16 v[236:237], v241 offset:63904\n"
"ds_read_b64_tr_b16 v[238:239], v241 offset:64160\n"
"v_mfma_f32_16x16x32_bf16 a[132:135], v[208:211], v[100:103], a[132:135]\n"
"v_exp_f32 v165, v165\n"
"v_mfma_f32_16x16x32_bf16 a[136:139], v[208:211], v[104:107], a[136:139]\n"
"v_exp_f32 v166, v166\n"
"v_mfma_f32_16x16x32_bf16 a[140:143], v[208:211], v[108:111], a[140:143]\n"
"v_exp_f32 v167, v167\n"
"v_mfma_f32_16x16x32_bf16 a[144:147], v[212:215], v[96:99], a[144:147]\n"
"v_exp_f32 v180, v180\n"
"v_mfma_f32_16x16x32_bf16 a[148:151], v[212:215], v[100:103], a[148:151]\n"
"v_add_f32 v244, v244, v162\n"
"s_add_u32 m0, s74, 0x1ac00\n"
"v_mfma_f32_16x16x32_bf16 a[152:155], v[212:215], v[104:107], a[152:155]\n"
"buffer_load_dwordx4 v243, s[52:55], s77 offen offset:256 nt lds\n"
"v_mfma_f32_16x16x32_bf16 a[156:159], v[212:215], v[108:111], a[156:159]\n"
"v_exp_f32 v181, v181\n"
"v_mfma_f32_16x16x32_bf16 a[160:163], v[216:219], v[96:99], a[160:163]\n"
"v_exp_f32 v182, v182\n"
"v_mfma_f32_16x16x32_bf16 a[164:167], v[216:219], v[100:103], a[164:167]\n"
"v_exp_f32 v183, v183\n"
"v_mfma_f32_16x16x32_bf16 a[168:171], v[216:219], v[104:107], a[168:171]\n"
"v_exp_f32 v168, v168\n"
"v_mfma_f32_16x16x32_bf16 a[172:175], v[216:219], v[108:111], a[172:175]\n"
"v_exp_f32 v169, v169\n"
"v_mfma_f32_16x16x32_bf16 a[176:179], v[220:223], v[96:99], a[176:179]\n"
"ds_read_b128 v[208:211], v240 offset:16896\n"
"ds_read_b128 v[212:215], v240 offset:25344\n"
"v_mfma_f32_16x16x32_bf16 a[180:183], v[220:223], v[100:103], a[180:183]\n"
"ds_read_b128 v[216:219], v240 offset:16960\n"
"v_add_f32 v244, v244, v163\n"
"v_mfma_f32_16x16x32_bf16 a[184:187], v[220:223], v[104:107], a[184:187]\n"
"v_exp_f32 v170, v170\n"
"v_mfma_f32_16x16x32_bf16 a[188:191], v[220:223], v[108:111], a[188:191]\n"
"ds_read_b128 v[220:223], v240 offset:25408\n"
"s_waitcnt lgkmcnt(4)\n"
"v_mfma_f32_16x16x32_bf16 a[192:195], v[224:227], v[96:99], a[192:195]\n"
"v_exp_f32 v171, v171\n"
"v_mfma_f32_16x16x32_bf16 a[196:199], v[224:227], v[100:103], a[196:199]\n"
"v_exp_f32 v184, v184\n"
"v_mfma_f32_16x16x32_bf16 a[200:203], v[224:227], v[104:107], a[200:203]\n"
"v_exp_f32 v185, v185\n"
"v_mfma_f32_16x16x32_bf16 a[204:207], v[224:227], v[108:111], a[204:207]\n"
"v_exp_f32 v186, v186\n"
"v_mfma_f32_16x16x32_bf16 a[208:211], v[228:231], v[96:99], a[208:211]\n"
"v_exp_f32 v187, v187\n"
"v_mfma_f32_16x16x32_bf16 a[212:215], v[228:231], v[100:103], a[212:215]\n"
"v_add_f32 v244, v244, v176\n"
"s_add_u32 m0, s74, 0x1bc00\n"
"v_mfma_f32_16x16x32_bf16 a[216:219], v[228:231], v[104:107], a[216:219]\n"
"buffer_load_dwordx4 v243, s[52:55], s77 offen offset:384 nt lds\n"
"v_mfma_f32_16x16x32_bf16 a[220:223], v[228:231], v[108:111], a[220:223]\n"
"s_add_u32 s77, s77, 0x4000\n"
"v_add_f32 v244, v244, v177\n"
"v_mfma_f32_16x16x32_bf16 a[224:227], v[232:235], v[96:99], a[224:227]\n"
"v_add_f32 v244, v244, v178\n"
"v_add_f32 v244, v244, v179\n"
"v_mfma_f32_16x16x32_bf16 a[228:231], v[232:235], v[100:103], a[228:231]\n"
"v_cvt_pk_bf16_f32 v160, v160, v161\n"
"v_cvt_pk_bf16_f32 v161, v162, v163\n"
"v_mfma_f32_16x16x32_bf16 a[232:235], v[232:235], v[104:107], a[232:235]\n"
"v_exp_f32 v172, v172\n"
"v_mfma_f32_16x16x32_bf16 a[236:239], v[232:235], v[108:111], a[236:239]\n"
"v_exp_f32 v173, v173\n"
"v_mfma_f32_16x16x32_bf16 a[240:243], v[236:239], v[96:99], a[240:243]\n"
"ds_read_b128 v[224:227], v240 offset:17024\n"
"ds_read_b128 v[228:231], v240 offset:25472\n"
"v_mfma_f32_16x16x32_bf16 a[244:247], v[236:239], v[100:103], a[244:247]\n"
"ds_read_b128 v[232:235], v240 offset:17088\n"
"v_cvt_pk_bf16_f32 v162, v176, v177\n"
"v_mfma_f32_16x16x32_bf16 a[248:251], v[236:239], v[104:107], a[248:251]\n"
"v_exp_f32 v174, v174\n"
"v_mfma_f32_16x16x32_bf16 a[252:255], v[236:239], v[108:111], a[252:255]\n"
"s_cbranch_vccnz L_tCl2_%=\n"
"L_bCl2_%=:\n"
"s_waitcnt lgkmcnt(3)\n"
"v_mfma_f32_16x16x32_bf16 v[96:99], v[208:211], v[0:3], v[192:195]\n"
"ds_read_b128 v[236:239], v240 offset:25536\n"
"v_cvt_pk_bf16_f32 v163, v178, v179\n"
"v_mfma_f32_16x16x32_bf16 v[100:103], v[208:211], v[4:7], v[196:199]\n"
"v_exp_f32 v175, v175\n"
"v_mfma_f32_16x16x32_bf16 v[104:107], v[208:211], v[8:11], v[200:203]\n"
"v_add_f32 v245, v245, v164\n"
"v_add_f32 v245, v245, v165\n"
"v_mfma_f32_16x16x32_bf16 v[108:111], v[208:211], v[12:15], v[204:207]\n"
"v_exp_f32 v188, v188\n"
"v_mfma_f32_16x16x32_bf16 v[112:115], v[212:215], v[0:3], v[192:195]\n"
"v_exp_f32 v189, v189\n"
"v_mfma_f32_16x16x32_bf16 v[116:119], v[212:215], v[4:7], v[196:199]\n"
"v_add_f32 v245, v245, v166\n"
"s_add_u32 m0, s73, 0x0\n"
"v_mfma_f32_16x16x32_bf16 v[120:123], v[212:215], v[8:11], v[200:203]\n"
"buffer_load_dwordx4 v242, s[48:51], s76 offen offset:0 nt lds\n"
"v_mfma_f32_16x16x32_bf16 v[124:127], v[212:215], v[12:15], v[204:207]\n"
"v_exp_f32 v190, v190\n"
"v_mfma_f32_16x16x32_bf16 v[96:99], v[216:219], v[16:19], v[96:99]\n"
"v_add_f32 v245, v245, v167\n"
"v_add_f32 v245, v245, v180\n"
"v_mfma_f32_16x16x32_bf16 v[100:103], v[216:219], v[20:23], v[100:103]\n"
"v_exp_f32 v191, v191\n"
"v_mfma_f32_16x16x32_bf16 v[104:107], v[216:219], v[24:27], v[104:107]\n"
"v_add_f32 v245, v245, v181\n"
"v_add_f32 v245, v245, v182\n"
"v_mfma_f32_16x16x32_bf16 v[108:111], v[216:219], v[28:31], v[108:111]\n"
"v_add_f32 v245, v245, v183\n"
"v_cvt_pk_bf16_f32 v164, v164, v165\n"
"v_mfma_f32_16x16x32_bf16 v[112:115], v[220:223], v[16:19], v[112:115]\n"
"ds_read_b128 v[208:211], v240 offset:17152\n"
"ds_read_b128 v[212:215], v240 offset:25600\n"
"v_mfma_f32_16x16x32_bf16 v[116:119], v[220:223], v[20:23], v[116:119]\n"
"ds_read_b128 v[216:219], v240 offset:17216\n"
"v_cvt_pk_bf16_f32 v165, v166, v167\n"
"v_mfma_f32_16x16x32_bf16 v[120:123], v[220:223], v[24:27], v[120:123]\n"
"v_cvt_pk_bf16_f32 v166, v180, v181\n"
"v_cvt_pk_bf16_f32 v167, v182, v183\n"
"v_mfma_f32_16x16x32_bf16 v[124:127], v[220:223], v[28:31], v[124:127]\n"
"ds_read_b128 v[220:223], v240 offset:25664\n"
"s_waitcnt lgkmcnt(4)\n"
"v_mfma_f32_16x16x32_bf16 v[96:99], v[224:227], v[64:67], v[96:99]\n"
"v_add_f32 v246, v246, v168\n"
"v_add_f32 v246, v246, v169\n"
"v_mfma_f32_16x16x32_bf16 v[100:103], v[224:227], v[68:71], v[100:103]\n"
"v_add_f32 v246, v246, v170\n"
"v_add_f32 v246, v246, v171\n"
"v_mfma_f32_16x16x32_bf16 v[104:107], v[224:227], v[72:75], v[104:107]\n"
"v_add_f32 v246, v246, v184\n"
"v_add_f32 v246, v246, v185\n"
"v_mfma_f32_16x16x32_bf16 v[108:111], v[224:227], v[76:79], v[108:111]\n"
"v_add_f32 v246, v246, v186\n"
"v_add_f32 v246, v246, v187\n"
"v_mfma_f32_16x16x32_bf16 v[112:115], v[228:231], v[64:67], v[112:115]\n"
"v_cvt_pk_bf16_f32 v168, v168, v169\n"
"v_cvt_pk_bf16_f32 v169, v170, v171\n"
"v_mfma_f32_16x16x32_bf16 v[116:119], v[228:231], v[68:71], v[116:119]\n"
"v_cvt_pk_bf16_f32 v170, v184, v185\n"
"s_add_u32 m0, s73, 0x220\n"
"v_mfma_f32_16x16x32_bf16 v[120:123], v[228:231], v[72:75], v[120:123]\n"
"buffer_load_dwordx4 v242, s[48:51], s76 offen offset:512 nt lds\n"
"v_mfma_f32_16x16x32_bf16 v[124:127], v[228:231], v[76:79], v[124:127]\n"
"v_cvt_pk_bf16_f32 v171, v186, v187\n"
"v_add_f32 v247, v247, v172\n"
"v_mfma_f32_16x16x32_bf16 v[96:99], v[232:235], v[80:83], v[96:99]\n"
"v_add_f32 v247, v247, v173\n"
"v_add_f32 v247, v247, v174\n"
"v_mfma_f32_16x16x32_bf16 v[100:103], v[232:235], v[84:87], v[100:103]\n"
"v_add_f32 v247, v247, v175\n"
"v_add_f32 v247, v247, v188\n"
"v_mfma_f32_16x16x32_bf16 v[104:107], v[232:235], v[88:91], v[104:107]\n"
"v_add_f32 v247, v247, v189\n"
"v_add_f32 v247, v247, v190\n"
"v_mfma_f32_16x16x32_bf16 v[108:111], v[232:235], v[92:95], v[108:111]\n"
"v_add_f32 v247, v247, v191\n"
"v_cvt_pk_bf16_f32 v172, v172, v173\n"
"v_mfma_f32_16x16x32_bf16 v[112:115], v[236:239], v[80:83], v[112:115]\n"
"ds_read_b128 v[224:227], v240 offset:17280\n"
"ds_read_b128 v[228:231], v240 offset:25728\n"
"v_mfma_f32_16x16x32_bf16 v[116:119], v[236:239], v[84:87], v[116:119]\n"
"ds_read_b128 v[232:235], v240 offset:17344\n"
"v_cvt_pk_bf16_f32 v173, v174, v175\n"
"v_mfma_f32_16x16x32_bf16 v[120:123], v[236:239], v[88:91], v[120:123]\n"
"v_cvt_pk_bf16_f32 v174, v188, v189\n"
"v_cvt_pk_bf16_f32 v175, v190, v191\n"
"v_mfma_f32_16x16x32_bf16 v[124:127], v[236:239], v[92:95], v[124:127]\n"
"ds_read_b128 v[236:239], v240 offset:25792\n"
"s_waitcnt lgkmcnt(4)\n"
"v_mfma_f32_16x16x32_bf16 v[96:99], v[208:211], v[128:131], v[96:99]\n"
"v_mfma_f32_16x16x32_bf16 v[100:103], v[208:211], v[132:135], v[100:103]\n"
"v_mfma_f32_16x16x32_bf16 v[104:107], v[208:211], v[136:139], v[104:107]\n"
"v_mfma_f32_16x16x32_bf16 v[108:111], v[208:211], v[140:143], v[108:111]\n"
"v_mfma_f32_16x16x32_bf16 v[112:115], v[212:215], v[128:131], v[112:115]\n"
"v_mfma_f32_16x16x32_bf16 v[116:119], v[212:215], v[132:135], v[116:119]\n"
"s_add_u32 m0, s73, 0x440\n"
"v_mfma_f32_16x16x32_bf16 v[120:123], v[212:215], v[136:139], v[120:123]\n"
"buffer_load_dwordx4 v242, s[48:51], s76 offen offset:1024 nt lds\n"
"v_mfma_f32_16x16x32_bf16 v[124:127], v[212:215], v[140:143], v[124:127]\n"
"v_mfma_f32_16x16x32_bf16 v[96:99], v[216:219], v[144:147], v[96:99]\n"
"v_mfma_f32_16x16x32_bf16 v[100:103], v[216:219], v[148:151], v[100:103]\n"
"v_mfma_f32_16x16x32_bf16 v[104:107], v[216:219], v[152:155], v[104:107]\n"
"v_mfma_f32_16x16x32_bf16 v[108:111], v[216:219], v[156:159], v[108:111]\n"
"v_mfma_f32_16x16x32_bf16 v[112:115], v[220:223], v[144:147], v[112:115]\n"
"v_mfma_f32_16x16x32_bf16 v[116:119], v[220:223], v[148:151], v[116:119]\n"
"v_mfma_f32_16x16x32_bf16 v[120:123], v[220:223], v[152:155], v[120:123]\n"
"v_mfma_f32_16x16x32_bf16 v[124:127], v[220:223], v[156:159], v[124:127]\n"
"s_waitcnt vmcnt(16) lgkmcnt(0)\n"
"s_barrier\n"
"v_mfma_f32_16x16x32_bf16 v[96:99], v[224:227], v[32:35], v[96:99]\n"
"ds_read_b64_tr_b16 v[208:209], v241 offset:0\n"
"ds_read_b64_tr_b16 v[210:211], v241 offset:256\n"
"v_mfma_f32_16x16x32_bf16 v[100:103], v[224:227], v[36:39], v[100:103]\n"
"ds_read_b64_tr_b16 v[212:213], v241 offset:32\n"
"ds_read_b64_tr_b16 v[214:215], v241 offset:288\n"
"v_mfma_f32_16x16x32_bf16 v[104:107], v[224:227], v[40:43], v[104:107]\n"
"ds_read_b64_tr_b16 v[216:217], v241 offset:512\n"
"ds_read_b64_tr_b16 v[218:219], v241 offset:768\n"
"v_mfma_f32_16x16x32_bf16 v[108:111], v[224:227], v[44:47], v[108:111]\n"
"ds_read_b64_tr_b16 v[220:221], v241 offset:544\n"
"ds_read_b64_tr_b16 v[222:223], v241 offset:800\n"
"v_mfma_f32_16x16x32_bf16 v[112:115], v[228:231], v[32:35], v[112:115]\n"
"v_mfma_f32_16x16x32_bf16 v[116:119], v[228:231], v[36:39], v[116:119]\n"
"s_add_u32 m0, s73, 0x660\n"
"v_mfma_f32_16x16x32_bf16 v[120:123], v[228:231], v[40:43], v[120:123]\n"
"buffer_load_dwordx4 v242, s[48:51], s76 offen offset:1536 nt lds\n"
"v_mfma_f32_16x16x32_bf16 v[124:127], v[228:231], v[44:47], v[124:127]\n"
"s_add_u32 s76, s76, 0x4000\n"
"v_mfma_f32_16x16x32_bf16 v[96:99], v[232:235], v[48:51], v[96:99]\n"
"v_mfma_f32_16x16x32_bf16 v[100:103], v[232:235], v[52:55], v[100:103]\n"
"v_mfma_f32_16x16x32_bf16 v[104:107], v[232:235], v[56:59], v[104:107]\n"
"v_mfma_f32_16x16x32_bf16 v[108:111], v[232:235], v[60:63], v[108:111]\n"
"v_mfma_f32_16x16x32_bf16 v[112:115], v[236:239], v[48:51], v[112:115]\n"
"ds_read_b64_tr_b16 v[224:225], v241 offset:4224\n"
"ds_read_b64_tr_b16 v[226:227], v241 offset:4480\n"
"v_mfma_f32_16x16x32_bf16 v[116:119], v[236:239], v[52:55], v[116:119]\n"
"ds_read_b64_tr_b16 v[228:229], v241 offset:4256\n"
"ds_read_b64_tr_b16 v[230:231], v241 offset:4512\n"
"v_mfma_f32_16x16x32_bf16 v[120:123], v[236:239], v[56:59], v[120:123]\n"
"ds_read_b64_tr_b16 v[232:233], v241 offset:4736\n"
"ds_read_b64_tr_b16 v[234:235], v241 offset:4992\n"
"v_mfma_f32_16x16x32_bf16 v[124:127], v[236:239], v[60:63], v[124:127]\n"
"s_waitcnt lgkmcnt(6)\n"
"v_mfma_f32_16x16x32_bf16 a[0:3], v[208:211], v[160:163], a[0:3]\n"
"ds_read_b64_tr_b16 v[236:237], v241 offset:4768\n"
"ds_read_b64_tr_b16 v[238:239], v241 offset:5024\n"
"v_mfma_f32_16x16x32_bf16 a[4:7], v[208:211], v[164:167], a[4:7]\n"
"v_max3_f32 v248, v96, v97, v98\n"
"v_max3_f32 v248, v248, v99, v100\n"
"v_mfma_f32_16x16x32_bf16 a[8:11], v[208:211], v[168:171], a[8:11]\n"
"v_max3_f32 v248, v248, v101, v102\n"
"v_max3_f32 v248, v248, v103, v104\n"
"v_mfma_f32_16x16x32_bf16 a[12:15], v[208:211], v[172:175], a[12:15]\n"
"v_max3_f32 v248, v248, v105, v106\n"
"v_max3_f32 v248, v248, v107, v108\n"
"v_mfma_f32_16x16x32_bf16 a[16:19], v[212:215], v[160:163], a[16:19]\n"
"v_max3_f32 v248, v248, v109, v110\n"
"v_max3_f32 v248, v248, v111, v112\n"
"v_mfma_f32_16x16x32_bf16 a[20:23], v[212:215], v[164:167], a[20:23]\n"
"v_max3_f32 v248, v248, v113, v114\n"
"s_add_u32 m0, s74, 0x1ce00\n"
"v_mfma_f32_16x16x32_bf16 a[24:27], v[212:215], v[168:171], a[24:27]\n"
"buffer_load_dwordx4 v243, s[52:55], s77 offen offset:0 nt lds\n"
"v_mfma_f32_16x16x32_bf16 a[28:31], v[212:215], v[172:175], a[28:31]\n"
"v_max3_f32 v248, v248, v115, v116\n"
"v_max3_f32 v248, v248, v117, v118\n"
"v_mfma_f32_16x16x32_bf16 a[32:35], v[216:219], v[160:163], a[32:35]\n"
"v_max3_f32 v248, v248, v119, v120\n"
"v_max3_f32 v248, v248, v121, v122\n"
"v_mfma_f32_16x16x32_bf16 a[36:39], v[216:219], v[164:167], a[36:39]\n"
"v_max3_f32 v248, v248, v123, v124\n"
"v_max3_f32 v248, v248, v125, v126\n"
"v_mfma_f32_16x16x32_bf16 a[40:43], v[216:219], v[168:171], a[40:43]\n"
"v_max_f32 v248, v248, v127\n"
"v_cmp_lt_f32 vcc, 0x41000000, v248\n"
"v_mfma_f32_16x16x32_bf16 a[44:47], v[216:219], v[172:175], a[44:47]\n"
"v_mfma_f32_16x16x32_bf16 a[48:51], v[220:223], v[160:163], a[48:51]\n"
"ds_read_b64_tr_b16 v[208:209], v241 offset:8448\n"
"ds_read_b64_tr_b16 v[210:211], v241 offset:8704\n"
"v_mfma_f32_16x16x32_bf16 a[52:55], v[220:223], v[164:167], a[52:55]\n"
"ds_read_b64_tr_b16 v[212:213], v241 offset:8480\n"
"ds_read_b64_tr_b16 v[214:215], v241 offset:8736\n"
"v_mfma_f32_16x16x32_bf16 a[56:59], v[220:223], v[168:171], a[56:59]\n"
"ds_read_b64_tr_b16 v[216:217], v241 offset:8960\n"
"ds_read_b64_tr_b16 v[218:219], v241 offset:9216\n"
"v_mfma_f32_16x16x32_bf16 a[60:63], v[220:223], v[172:175], a[60:63]\n"
"s_cbranch_vccnz L_tBl3_%=\n"
"L_bBl3_%=:\n"
"s_waitcnt lgkmcnt(6)\n"
"v_mfma_f32_16x16x32_bf16 a[64:67], v[224:227], v[160:163], a[64:67]\n"
"ds_read_b64_tr_b16 v[220:221], v241 offset:8992\n"
"ds_read_b64_tr_b16 v[222:223], v241 offset:9248\n"
"v_mfma_f32_16x16x32_bf16 a[68:71], v[224:227], v[164:167], a[68:71]\n"
"v_exp_f32 v96, v96\n"
"v_mfma_f32_16x16x32_bf16 a[72:75], v[224:227], v[168:171], a[72:75]\n"
"v_exp_f32 v97, v97\n"
"v_mfma_f32_16x16x32_bf16 a[76:79], v[224:227], v[172:175], a[76:79]\n"
"v_exp_f32 v98, v98\n"
"v_mfma_f32_16x16x32_bf16 a[80:83], v[228:231], v[160:163], a[80:83]\n"
"v_exp_f32 v99, v99\n"
"v_mfma_f32_16x16x32_bf16 a[84:87], v[228:231], v[164:167], a[84:87]\n"
"v_add_f32 v244, v244, v96\n"
"s_add_u32 m0, s74, 0x1de00\n"
"v_mfma_f32_16x16x32_bf16 a[88:91], v[228:231], v[168:171], a[88:91]\n"
"buffer_load_dwordx4 v243, s[52:55], s77 offen offset:128 nt lds\n"
"v_mfma_f32_16x16x32_bf16 a[92:95], v[228:231], v[172:175], a[92:95]\n"
"v_exp_f32 v112, v112\n"
"v_mfma_f32_16x16x32_bf16 a[96:99], v[232:235], v[160:163], a[96:99]\n"
"v_exp_f32 v113, v113\n"
"v_mfma_f32_16x16x32_bf16 a[100:103], v[232:235], v[164:167], a[100:103]\n"
"v_exp_f32 v114, v114\n"
"v_mfma_f32_16x16x32_bf16 a[104:107], v[232:235], v[168:171], a[104:107]\n"
"v_exp_f32 v115, v115\n"
"v_mfma_f32_16x16x32_bf16 a[108:111], v[232:235], v[172:175], a[108:111]\n"
"v_exp_f32 v100, v100\n"
"v_mfma_f32_16x16x32_bf16 a[112:115], v[236:239], v[160:163], a[112:115]\n"
"ds_read_b64_tr_b16 v[224:225], v241 offset:12672\n"
"ds_read_b64_tr_b16 v[226:227], v241 offset:12928\n"
"v_mfma_f32_16x16x32_bf16 a[116:119], v[236:239], v[164:167], a[116:119]\n"
"ds_read_b64_tr_b16 v[228:229], v241 offset:12704\n"
"ds_read_b64_tr_b16 v[230:231], v241 offset:12960\n"
"v_mfma_f32_16x16x32_bf16 a[120:123], v[236:239], v[168:171], a[120:123]\n"
"ds_read_b64_tr_b16 v[232:233], v241 offset:13184\n"
"ds_read_b64_tr_b16 v[234:235], v241 offset:13440\n"
"v_mfma_f32_16x16x32_bf16 a[124:127], v[236:239], v[172:175], a[124:127]\n"
"v_add_f32 v244, v244, v97\n"
"s_waitcnt lgkmcnt(6)\n"
"v_mfma_f32_16x16x32_bf16 a[128:131], v[208:211], v[160:163], a[128:131]\n"
"ds_read_b64_tr_b16 v[236:237], v241 offset:13216\n"
"ds_read_b64_tr_b16 v[238:239], v241 offset:13472\n"
"v_mfma_f32_16x16x32_bf16 a[132:135], v[208:211], v[164:167], a[132:135]\n"
"v_exp_f32 v101, v101\n"
"v_mfma_f32_16x16x32_bf16 a[136:139], v[208:211], v[168:171], a[136:139]\n"
"v_exp_f32 v102, v102\n"
"v_mfma_f32_16x16x32_bf16 a[140:143], v[208:211], v[172:175], a[140:143]\n"
"v_exp_f32 v103, v103\n"
"v_mfma_f32_16x16x32_bf16 a[144:147], v[212:215], v[160:163], a[144:147]\n"
"v_exp_f32 v116, v116\n"
"v_mfma_f32_16x16x32_bf16 a[148:151], v[212:215], v[164:167], a[148:151]\n"
"v_add_f32 v244, v244, v98\n"
"s_add_u32 m0, s74, 0x1ee00\n"
"v_mfma_f32_16x16x32_bf16 a[152:155], v[212:215], v[168:171], a[152:155]\n"
"buffer_load_dwordx4 v243, s[52:55], s77 offen offset:256 nt lds\n"
"v_mfma_f32_16x16x32_bf16 a[156:159], v[212:215], v[172:175], a[156:159]\n"
"v_exp_f32 v117, v117\n"
"v_mfma_f32_16x16x32_bf16 a[160:163], v[216:219], v[160:163], a[160:163]\n"
"v_exp_f32 v118, v118\n"
"v_mfma_f32_16x16x32_bf16 a[164:167], v[216:219], v[164:167], a[164:167]\n"
"v_exp_f32 v119, v119\n"
"v_mfma_f32_16x16x32_bf16 a[168:171], v[216:219], v[168:171], a[168:171]\n"
"v_exp_f32 v104, v104\n"
"v_mfma_f32_16x16x32_bf16 a[172:175], v[216:219], v[172:175], a[172:175]\n"
"v_exp_f32 v105, v105\n"
"v_mfma_f32_16x16x32_bf16 a[176:179], v[220:223], v[160:163], a[176:179]\n"
"ds_read_b128 v[208:211], v240 offset:33792\n"
"ds_read_b128 v[212:215], v240 offset:42240\n"
"v_mfma_f32_16x16x32_bf16 a[180:183], v[220:223], v[164:167], a[180:183]\n"
"ds_read_b128 v[216:219], v240 offset:33856\n"
"v_add_f32 v244, v244, v99\n"
"v_mfma_f32_16x16x32_bf16 a[184:187], v[220:223], v[168:171], a[184:187]\n"
"v_exp_f32 v106, v106\n"
"v_mfma_f32_16x16x32_bf16 a[188:191], v[220:223], v[172:175], a[188:191]\n"
"ds_read_b128 v[220:223], v240 offset:42304\n"
"s_waitcnt lgkmcnt(4)\n"
"v_mfma_f32_16x16x32_bf16 a[192:195], v[224:227], v[160:163], a[192:195]\n"
"v_exp_f32 v107, v107\n"
"v_mfma_f32_16x16x32_bf16 a[196:199], v[224:227], v[164:167], a[196:199]\n"
"v_exp_f32 v120, v120\n"
"v_mfma_f32_16x16x32_bf16 a[200:203], v[224:227], v[168:171], a[200:203]\n"
"v_exp_f32 v121, v121\n"
"v_mfma_f32_16x16x32_bf16 a[204:207], v[224:227], v[172:175], a[204:207]\n"
"v_exp_f32 v122, v122\n"
"v_mfma_f32_16x16x32_bf16 a[208:211], v[228:231], v[160:163], a[208:211]\n"
"v_exp_f32 v123, v123\n"
"v_mfma_f32_16x16x32_bf16 a[212:215], v[228:231], v[164:167], a[212:215]\n"
"v_add_f32 v244, v244, v112\n"
"s_add_u32 m0, s74, 0x1fe00\n"
"v_mfma_f32_16x16x32_bf16 a[216:219], v[228:231], v[168:171], a[216:219]\n"
"buffer_load_dwordx4 v243, s[52:55], s77 offen offset:384 nt lds\n"
"v_mfma_f32_16x16x32_bf16 a[220:223], v[228:231], v[172:175], a[220:223]\n"
"s_add_u32 s77, s77, 0x4000\n"
"v_add_f32 v244, v244, v113\n"
"v_mfma_f32_16x16x32_bf16 a[224:227], v[232:235], v[160:163], a[224:227]\n"
"v_add_f32 v244, v244, v114\n"
"v_add_f32 v244, v244, v115\n"
"v_mfma_f32_16x16x32_bf16 a[228:231], v[232:235], v[164:167], a[228:231]\n"
"v_cvt_pk_bf16_f32 v96, v96, v97\n"
"v_cvt_pk_bf16_f32 v97, v98, v99\n"
"v_mfma_f32_16x16x32_bf16 a[232:235], v[232:235], v[168:171], a[232:235]\n"
"v_exp_f32 v108, v108\n"
"v_mfma_f32_16x16x32_bf16 a[236:239], v[232:235], v[172:175], a[236:239]\n"
"v_exp_f32 v109, v109\n"
"v_mfma_f32_16x16x32_bf16 a[240:243], v[236:239], v[160:163], a[240:243]\n"
"ds_read_b128 v[224:227], v240 offset:33920\n"
"ds_read_b128 v[228:231], v240 offset:42368\n"
"v_mfma_f32_16x16x32_bf16 a[244:247], v[236:239], v[164:167], a[244:247]\n"
"ds_read_b128 v[232:235], v240 offset:33984\n"
"v_cvt_pk_bf16_f32 v98, v112, v113\n"
"v_mfma_f32_16x16x32_bf16 a[248:251], v[236:239], v[168:171], a[248:251]\n"
"v_exp_f32 v110, v110\n"
"v_mfma_f32_16x16x32_bf16 a[252:255], v[236:239], v[172:175], a[252:255]\n"
"s_cbranch_vccnz L_tCl3_%=\n"
"L_bCl3_%=:\n"
"s_waitcnt lgkmcnt(3)\n"
"v_mfma_f32_16x16x32_bf16 v[160:163], v[208:211], v[0:3], v[192:195]\n"
"ds_read_b128 v[236:239], v240 offset:42432\n"
"v_cvt_pk_bf16_f32 v99, v114, v115\n"
"v_mfma_f32_16x16x32_bf16 v[164:167], v[208:211], v[4:7], v[196:199]\n"
"v_exp_f32 v111, v111\n"
"v_mfma_f32_16x16x32_bf16 v[168:171], v[208:211], v[8:11], v[200:203]\n"
"v_add_f32 v245, v245, v100\n"
"v_add_f32 v245, v245, v101\n"
"v_mfma_f32_16x16x32_bf16 v[172:175], v[208:211], v[12:15], v[204:207]\n"
"v_exp_f32 v124, v124\n"
"v_mfma_f32_16x16x32_bf16 v[176:179], v[212:215], v[0:3], v[192:195]\n"
"v_exp_f32 v125, v125\n"
"v_mfma_f32_16x16x32_bf16 v[180:183], v[212:215], v[4:7], v[196:199]\n"
"v_add_f32 v245, v245, v102\n"
"s_add_u32 m0, s73, 0x4200\n"
"v_mfma_f32_16x16x32_bf16 v[184:187], v[212:215], v[8:11], v[200:203]\n"
"buffer_load_dwordx4 v242, s[48:51], s76 offen offset:0 nt lds\n"
"v_mfma_f32_16x16x32_bf16 v[188:191], v[212:215], v[12:15], v[204:207]\n"
"v_exp_f32 v126, v126\n"
"v_mfma_f32_16x16x32_bf16 v[160:163], v[216:219], v[16:19], v[160:163]\n"
"v_add_f32 v245, v245, v103\n"
"v_add_f32 v245, v245, v116\n"
"v_mfma_f32_16x16x32_bf16 v[164:167], v[216:219], v[20:23], v[164:167]\n"
"v_exp_f32 v127, v127\n"
"v_mfma_f32_16x16x32_bf16 v[168:171], v[216:219], v[24:27], v[168:171]\n"
"v_add_f32 v245, v245, v117\n"
"v_add_f32 v245, v245, v118\n"
"v_mfma_f32_16x16x32_bf16 v[172:175], v[216:219], v[28:31], v[172:175]\n"
"v_add_f32 v245, v245, v119\n"
"v_cvt_pk_bf16_f32 v100, v100, v101\n"
"v_mfma_f32_16x16x32_bf16 v[176:179], v[220:223], v[16:19], v[176:179]\n"
"ds_read_b128 v[208:211], v240 offset:34048\n"
"ds_read_b128 v[212:215], v240 offset:42496\n"
"v_mfma_f32_16x16x32_bf16 v[180:183], v[220:223], v[20:23], v[180:183]\n"
"ds_read_b128 v[216:219], v240 offset:34112\n"
"v_cvt_pk_bf16_f32 v101, v102, v103\n"
"v_mfma_f32_16x16x32_bf16 v[184:187], v[220:223], v[24:27], v[184:187]\n"
"v_cvt_pk_bf16_f32 v102, v116, v117\n"
"v_cvt_pk_bf16_f32 v103, v118, v119\n"
"v_mfma_f32_16x16x32_bf16 v[188:191], v[220:223], v[28:31], v[188:191]\n"
"ds_read_b128 v[220:223], v240 offset:42560\n"
"s_waitcnt lgkmcnt(4)\n"
"v_mfma_f32_16x16x32_bf16 v[160:163], v[224:227], v[64:67], v[160:163]\n"
"v_add_f32 v246, v246, v104\n"
"v_add_f32 v246, v246, v105\n"
"v_mfma_f32_16x16x32_bf16 v[164:167], v[224:227], v[68:71], v[164:167]\n"
"v_add_f32 v246, v246, v106\n"
"v_add_f32 v246, v246, v107\n"
"v_mfma_f32_16x16x32_bf16 v[168:171], v[224:227], v[72:75], v[168:171]\n"
"v_add_f32 v246, v246, v120\n"
"v_add_f32 v246, v246, v121\n"
"v_mfma_f32_16x16x32_bf16 v[172:175], v[224:227], v[76:79], v[172:175]\n"
"v_add_f32 v246, v246, v122\n"
"v_add_f32 v246, v246, v123\n"
"v_mfma_f32_16x16x32_bf16 v[176:179], v[228:231], v[64:67], v[176:179]\n"
"v_cvt_pk_bf16_f32 v104, v104, v105\n"
"v_cvt_pk_bf16_f32 v105, v106, v107\n"
"v_mfma_f32_16x16x32_bf16 v[180:183], v[228:231], v[68:71], v[180:183]\n"
"v_cvt_pk_bf16_f32 v106, v120, v121\n"
"s_add_u32 m0, s73, 0x4420\n"
"v_mfma_f32_16x16x32_bf16 v[184:187], v[228:231], v[72:75], v[184:187]\n"
"buffer_load_dwordx4 v242, s[48:51], s76 offen offset:512 nt lds\n"
"v_mfma_f32_16x16x32_bf16 v[188:191], v[228:231], v[76:79], v[188:191]\n"
"v_cvt_pk_bf16_f32 v107, v122, v123\n"
"v_add_f32 v247, v247, v108\n"
"v_mfma_f32_16x16x32_bf16 v[160:163], v[232:235], v[80:83], v[160:163]\n"
"v_add_f32 v247, v247, v109\n"
"v_add_f32 v247, v247, v110\n"
"v_mfma_f32_16x16x32_bf16 v[164:167], v[232:235], v[84:87], v[164:167]\n"
"v_add_f32 v247, v247, v111\n"
"v_add_f32 v247, v247, v124\n"
"v_mfma_f32_16x16x32_bf16 v[168:171], v[232:235], v[88:91], v[168:171]\n"
"v_add_f32 v247, v247, v125\n"
"v_add_f32 v247, v247, v126\n"
"v_mfma_f32_16x16x32_bf16 v[172:175], v[232:235], v[92:95], v[172:175]\n"
"v_add_f32 v247, v247, v127\n"
"v_cvt_pk_bf16_f32 v108, v108, v109\n"
"v_mfma_f32_16x16x32_bf16 v[176:179], v[236:239], v[80:83], v[176:179]\n"
"ds_read_b128 v[224:227], v240 offset:34176\n"
"ds_read_b128 v[228:231], v240 offset:42624\n"
"v_mfma_f32_16x16x32_bf16 v[180:183], v[236:239], v[84:87], v[180:183]\n"
"ds_read_b128 v[232:235], v240 offset:34240\n"
"v_cvt_pk_bf16_f32 v109, v110, v111\n"
"v_mfma_f32_16x16x32_bf16 v[184:187], v[236:239], v[88:91], v[184:187]\n"
"v_cvt_pk_bf16_f32 v110, v124, v125\n"
"v_cvt_pk_bf16_f32 v111, v126, v127\n"
"v_mfma_f32_16x16x32_bf16 v[188:191], v[236:239], v[92:95], v[188:191]\n"
"ds_read_b128 v[236:239], v240 offset:42688\n"
"s_waitcnt lgkmcnt(4)\n"
"v_mfma_f32_16x16x32_bf16 v[160:163], v[208:211], v[128:131], v[160:163]\n"
"v_mfma_f32_16x16x32_bf16 v[164:167], v[208:211], v[132:135], v[164:167]\n"
"v_mfma_f32_16x16x32_bf16 v[168:171], v[208:211], v[136:139], v[168:171]\n"
"v_mfma_f32_16x16x32_bf16 v[172:175], v[208:211], v[140:143], v[172:175]\n"
"v_mfma_f32_16x16x32_bf16 v[176:179], v[212:215], v[128:131], v[176:179]\n"
"v_mfma_f32_16x16x32_bf16 v[180:183], v[212:215], v[132:135], v[180:183]\n"
"s_add_u32 m0, s73, 0x4640\n"
"v_mfma_f32_16x16x32_bf16 v[184:187], v[212:215], v[136:139], v[184:187]\n"
"buffer_load_dwordx4 v242, s[48:51], s76 offen offset:1024 nt lds\n"
"v_mfma_f32_16x16x32_bf16 v[188:191], v[212:215], v[140:143], v[188:191]\n"
"v_mfma_f32_16x16x32_bf16 v[160:163], v[216:219], v[144:147], v[160:163]\n"
"v_mfma_f32_16x16x32_bf16 v[164:167], v[216:219], v[148:151], v[164:167]\n"
"v_mfma_f32_16x16x32_bf16 v[168:171], v[216:219], v[152:155], v[168:171]\n"
"v_mfma_f32_16x16x32_bf16 v[172:175], v[216:219], v[156:159], v[172:175]\n"
"v_mfma_f32_16x16x32_bf16 v[176:179], v[220:223], v[144:147], v[176:179]\n"
"v_mfma_f32_16x16x32_bf16 v[180:183], v[220:223], v[148:151], v[180:183]\n"
"v_mfma_f32_16x16x32_bf16 v[184:187], v[220:223], v[152:155], v[184:187]\n"
"v_mfma_f32_16x16x32_bf16 v[188:191], v[220:223], v[156:159], v[188:191]\n"
"s_waitcnt vmcnt(15) lgkmcnt(0)\n"
"s_barrier\n"
"v_mfma_f32_16x16x32_bf16 v[160:163], v[224:227], v[32:35], v[160:163]\n"
"ds_read_b64_tr_b16 v[208:209], v241 offset:16896\n"
"ds_read_b64_tr_b16 v[210:211], v241 offset:17152\n"
"v_mfma_f32_16x16x32_bf16 v[164:167], v[224:227], v[36:39], v[164:167]\n"
"ds_read_b64_tr_b16 v[212:213], v241 offset:16928\n"
"ds_read_b64_tr_b16 v[214:215], v241 offset:17184\n"
"v_mfma_f32_16x16x32_bf16 v[168:171], v[224:227], v[40:43], v[168:171]\n"
"ds_read_b64_tr_b16 v[216:217], v241 offset:17408\n"
"ds_read_b64_tr_b16 v[218:219], v241 offset:17664\n"
"v_mfma_f32_16x16x32_bf16 v[172:175], v[224:227], v[44:47], v[172:175]\n"
"ds_read_b64_tr_b16 v[220:221], v241 offset:17440\n"
"ds_read_b64_tr_b16 v[222:223], v241 offset:17696\n"
"v_mfma_f32_16x16x32_bf16 v[176:179], v[228:231], v[32:35], v[176:179]\n"
"v_mfma_f32_16x16x32_bf16 v[180:183], v[228:231], v[36:39], v[180:183]\n"
"s_add_u32 m0, s73, 0x4860\n"
"v_mfma_f32_16x16x32_bf16 v[184:187], v[228:231], v[40:43], v[184:187]\n"
"buffer_load_dwordx4 v242, s[48:51], s76 offen offset:1536 nt lds\n"
"v_mfma_f32_16x16x32_bf16 v[188:191], v[228:231], v[44:47], v[188:191]\n"
"s_add_u32 s76, s76, 0x4000\n"
"v_mfma_f32_16x16x32_bf16 v[160:163], v[232:235], v[48:51], v[160:163]\n"
"v_mfma_f32_16x16x32_bf16 v[164:167], v[232:235], v[52:55], v[164:167]\n"
"v_mfma_f32_16x16x32_bf16 v[168:171], v[232:235], v[56:59], v[168:171]\n"
"v_mfma_f32_16x16x32_bf16 v[172:175], v[232:235], v[60:63], v[172:175]\n"
"v_mfma_f32_16x16x32_bf16 v[176:179], v[236:239], v[48:51], v[176:179]\n"
"ds_read_b64_tr_b16 v[224:225], v241 offset:21120\n"
"ds_read_b64_tr_b16 v[226:227], v241 offset:21376\n"
"v_mfma_f32_16x16x32_bf16 v[180:183], v[236:239], v[52:55], v[180:183]\n"
"ds_read_b64_tr_b16 v[228:229], v241 offset:21152\n"
"ds_read_b64_tr_b16 v[230:231], v241 offset:21408\n"
"v_mfma_f32_16x16x32_bf16 v[184:187], v[236:239], v[56:59], v[184:187]\n"
"ds_read_b64_tr_b16 v[232:233], v241 offset:21632\n"
"ds_read_b64_tr_b16 v[234:235], v241 offset:21888\n"
"v_mfma_f32_16x16x32_bf16 v[188:191], v[236:239], v[60:63], v[188:191]\n"
"s_sub_u32 s78, s78, 1\n"
"s_cmp_lg_u32 s78, 0\n"
"s_cbranch_scc1 L_loop_%=\n"
"L_u13_%=:\n"
"v_lshrrev_b32 v251, 9, v243\n"
"v_mul_u32_u24 v251, 0x380, v251\n"
"v_and_b32 v242, 127, v243\n"
"v_add_u32 v242, v242, v251\n"
"v_add_u32 v243, 0x38000, v242\n"
"v_mbcnt_lo_u32_b32 v251, -1, 0\n"
"v_mbcnt_hi_u32_b32 v251, -1, v251\n"
"s_waitcnt lgkmcnt(6)\n"
"v_mfma_f32_16x16x32_bf16 a[0:3], v[208:211], v[96:99], a[0:3]\n"
"ds_read_b64_tr_b16 v[236:237], v241 offset:21664\n"
"ds_read_b64_tr_b16 v[238:239], v241 offset:21920\n"
"v_mfma_f32_16x16x32_bf16 a[4:7], v[208:211], v[100:103], a[4:7]\n"
"v_max3_f32 v248, v160, v161, v162\n"
"s_mov_b32 m0, 0x27300\n"
"v_mfma_f32_16x16x32_bf16 a[8:11], v[208:211], v[104:107], a[8:11]\n"
"buffer_load_dword v255, s[56:59], s88 offen lds\n"
"v_mfma_f32_16x16x32_bf16 a[12:15], v[208:211], v[108:111], a[12:15]\n"
"v_max3_f32 v248, v248, v163, v164\n"
"v_max3_f32 v248, v248, v165, v166\n"
"v_mfma_f32_16x16x32_bf16 a[16:19], v[212:215], v[96:99], a[16:19]\n"
"s_add_u32 m0, s74, 0x21000\n"
"s_add_u32 s75, s82, 0x0\n"
"v_mfma_f32_16x16x32_bf16 a[20:23], v[212:215], v[100:103], a[20:23]\n"
"buffer_load_dwordx4 v242, s[56:59], s75 offen lds\n"
"v_mfma_f32_16x16x32_bf16 a[24:27], v[212:215], v[104:107], a[24:27]\n"
"v_max3_f32 v248, v248, v167, v168\n"
"v_max3_f32 v248, v248, v169, v170\n"
"v_mfma_f32_16x16x32_bf16 a[28:31], v[212:215], v[108:111], a[28:31]\n"
"v_max3_f32 v248, v248, v171, v172\n"
"v_max3_f32 v248, v248, v173, v174\n"
"v_mfma_f32_16x16x32_bf16 a[32:35], v[216:219], v[96:99], a[32:35]\n"
"v_max3_f32 v248, v248, v175, v176\n"
"v_max3_f32 v248, v248, v177, v178\n"
"v_mfma_f32_16x16x32_bf16 a[36:39], v[216:219], v[100:103], a[36:39]\n"
"v_max3_f32 v248, v248, v179, v180\n"
"v_max3_f32 v248, v248, v181, v182\n"
"v_mfma_f32_16x16x32_bf16 a[40:43], v[216:219], v[104:107], a[40:43]\n"
"v_max3_f32 v248, v248, v183, v184\n"
"v_max3_f32 v248, v248, v185, v186\n"
"v_mfma_f32_16x16x32_bf16 a[44:47], v[216:219], v[108:111], a[44:47]\n"
"v_max3_f32 v248, v248, v187, v188\n"
"v_max3_f32 v248, v248, v189, v190\n"
"v_mfma_f32_16x16x32_bf16 a[48:51], v[220:223], v[96:99], a[48:51]\n"
"v_max_f32 v248, v248, v191\n"
"s_add_u32 m0, s74, 0x22080\n"
"v_mfma_f32_16x16x32_bf16 a[52:55], v[220:223], v[100:103], a[52:55]\n"
"buffer_load_dwordx4 v243, s[56:59], s75 offen lds\n"
"v_mfma_f32_16x16x32_bf16 a[56:59], v[220:223], v[104:107], a[56:59]\n"
"ds_read_b64_tr_b16 v[208:209], v241 offset:25344\n"
"ds_read_b64_tr_b16 v[210:211], v241 offset:25600\n"
"v_mfma_f32_16x16x32_bf16 a[60:63], v[220:223], v[108:111], a[60:63]\n"
"v_cmp_lt_f32 vcc, 0x41000000, v248\n"
"s_waitcnt lgkmcnt(2)\n"
"v_mfma_f32_16x16x32_bf16 a[64:67], v[224:227], v[96:99], a[64:67]\n"
"ds_read_b64_tr_b16 v[212:213], v241 offset:25376\n"
"ds_read_b64_tr_b16 v[214:215], v241 offset:25632\n"
"v_mfma_f32_16x16x32_bf16 a[68:71], v[224:227], v[100:103], a[68:71]\n"
"ds_read_b64_tr_b16 v[216:217], v241 offset:25856\n"
"ds_read_b64_tr_b16 v[218:219], v241 offset:26112\n"
"v_mfma_f32_16x16x32_bf16 a[72:75], v[224:227], v[104:107], a[72:75]\n"
"ds_read_b64_tr_b16 v[220:221], v241 offset:25888\n"
"ds_read_b64_tr_b16 v[222:223], v241 offset:26144\n"
"v_mfma_f32_16x16x32_bf16 a[76:79], v[224:227], v[108:111], a[76:79]\n"
"s_cbranch_vccnz L_tB13_%=\n"
"L_bB13_%=:\n"
"v_mfma_f32_16x16x32_bf16 a[80:83], v[228:231], v[96:99], a[80:83]\n"
"s_add_u32 m0, s74, 0x23100\n"
"s_add_u32 s75, s83, 0x0\n"
"v_mfma_f32_16x16x32_bf16 a[84:87], v[228:231], v[100:103], a[84:87]\n"
"buffer_load_dwordx4 v242, s[56:59], s75 offen lds\n"
"v_mfma_f32_16x16x32_bf16 a[88:91], v[228:231], v[104:107], a[88:91]\n"
"v_exp_f32 v160, v160\n"
"v_mfma_f32_16x16x32_bf16 a[92:95], v[228:231], v[108:111], a[92:95]\n"
"v_exp_f32 v161, v161\n"
"v_mfma_f32_16x16x32_bf16 a[96:99], v[232:235], v[96:99], a[96:99]\n"
"v_exp_f32 v162, v162\n"
"v_mfma_f32_16x16x32_bf16 a[100:103], v[232:235], v[100:103], a[100:103]\n"
"v_exp_f32 v163, v163\n"
"v_mfma_f32_16x16x32_bf16 a[104:107], v[232:235], v[104:107], a[104:107]\n"
"v_exp_f32 v176, v176\n"
"v_mfma_f32_16x16x32_bf16 a[108:111], v[232:235], v[108:111], a[108:111]\n"
"v_exp_f32 v177, v177\n"
"v_mfma_f32_16x16x32_bf16 a[112:115], v[236:239], v[96:99], a[112:115]\n"
"v_add_f32 v244, v244, v160\n"
"s_add_u32 m0, s74, 0x24180\n"
"v_mfma_f32_16x16x32_bf16 a[116:119], v[236:239], v[100:103], a[116:119]\n"
"buffer_load_dwordx4 v243, s[56:59], s75 offen lds\n"
"v_mfma_f32_16x16x32_bf16 a[120:123], v[236:239], v[104:107], a[120:123]\n"
"ds_read_b64_tr_b16 v[224:225], v241 offset:29568\n"
"ds_read_b64_tr_b16 v[226:227], v241 offset:29824\n"
"v_mfma_f32_16x16x32_bf16 a[124:127], v[236:239], v[108:111], a[124:127]\n"
"v_add_f32 v244, v244, v161\n"
"s_waitcnt lgkmcnt(2)\n"
"v_mfma_f32_16x16x32_bf16 a[128:131], v[208:211], v[96:99], a[128:131]\n"
"ds_read_b64_tr_b16 v[228:229], v241 offset:29600\n"
"ds_read_b64_tr_b16 v[230:231], v241 offset:29856\n"
"v_mfma_f32_16x16x32_bf16 a[132:135], v[208:211], v[100:103], a[132:135]\n"
"ds_read_b64_tr_b16 v[232:233], v241 offset:30080\n"
"ds_read_b64_tr_b16 v[234:235], v241 offset:30336\n"
"v_mfma_f32_16x16x32_bf16 a[136:139], v[208:211], v[104:107], a[136:139]\n"
"ds_read_b64_tr_b16 v[236:237], v241 offset:30112\n"
"ds_read_b64_tr_b16 v[238:239], v241 offset:30368\n"
"v_mfma_f32_16x16x32_bf16 a[140:143], v[208:211], v[108:111], a[140:143]\n"
"v_exp_f32 v178, v178\n"
"v_mfma_f32_16x16x32_bf16 a[144:147], v[212:215], v[96:99], a[144:147]\n"
"s_add_u32 m0, s74, 0x25200\n"
"s_add_u32 s75, s82, 0x7000\n"
"v_mfma_f32_16x16x32_bf16 a[148:151], v[212:215], v[100:103], a[148:151]\n"
"buffer_load_dwordx4 v242, s[56:59], s75 offen lds\n"
"v_mfma_f32_16x16x32_bf16 a[152:155], v[212:215], v[104:107], a[152:155]\n"
"v_exp_f32 v179, v179\n"
"v_mfma_f32_16x16x32_bf16 a[156:159], v[212:215], v[108:111], a[156:159]\n"
"v_exp_f32 v164, v164\n"
"v_mfma_f32_16x16x32_bf16 a[160:163], v[216:219], v[96:99], a[160:163]\n"
"v_exp_f32 v165, v165\n"
"v_mfma_f32_16x16x32_bf16 a[164:167], v[216:219], v[100:103], a[164:167]\n"
"v_exp_f32 v166, v166\n"
"v_mfma_f32_16x16x32_bf16 a[168:171], v[216:219], v[104:107], a[168:171]\n"
"v_exp_f32 v167, v167\n"
"v_mfma_f32_16x16x32_bf16 a[172:175], v[216:219], v[108:111], a[172:175]\n"
"v_exp_f32 v180, v180\n"
"v_mfma_f32_16x16x32_bf16 a[176:179], v[220:223], v[96:99], a[176:179]\n"
"ds_read_b128 v[208:211], v240 offset:50688\n"
"s_add_u32 m0, s74, 0x26280\n"
"v_mfma_f32_16x16x32_bf16 a[180:183], v[220:223], v[100:103], a[180:183]\n"
"buffer_load_dwordx4 v243, s[56:59], s75 offen lds\n"
"v_mfma_f32_16x16x32_bf16 a[184:187], v[220:223], v[104:107], a[184:187]\n"
"ds_read_b128 v[212:215], v240 offset:59136\n"
"ds_read_b128 v[216:219], v240 offset:50752\n"
"v_mfma_f32_16x16x32_bf16 a[188:191], v[220:223], v[108:111], a[188:191]\n"
"ds_read_b128 v[220:223], v240 offset:59200\n"
"s_waitcnt lgkmcnt(4)\n"
"v_mfma_f32_16x16x32_bf16 a[192:195], v[224:227], v[96:99], a[192:195]\n"
"v_exp_f32 v181, v181\n"
"v_mfma_f32_16x16x32_bf16 a[196:199], v[224:227], v[100:103], a[196:199]\n"
"v_exp_f32 v182, v182\n"
"v_mfma_f32_16x16x32_bf16 a[200:203], v[224:227], v[104:107], a[200:203]\n"
"v_exp_f32 v183, v183\n"
"v_mfma_f32_16x16x32_bf16 a[204:207], v[224:227], v[108:111], a[204:207]\n"
"v_exp_f32 v168, v168\n"
"v_mfma_f32_16x16x32_bf16 a[208:211], v[228:231], v[96:99], a[208:211]\n"
"s_add_u32 m0, s74, 0x8400\n"
"s_add_u32 s75, s83, 0x7000\n"
"v_mfma_f32_16x16x32_bf16 a[212:215], v[228:231], v[100:103], a[212:215]\n"
"buffer_load_dwordx4 v242, s[56:59], s75 offen lds\n"
"v_mfma_f32_16x16x32_bf16 a[216:219], v[228:231], v[104:107], a[216:219]\n"
"v_exp_f32 v169, v169\n"
"v_mfma_f32_16x16x32_bf16 a[220:223], v[228:231], v[108:111], a[220:223]\n"
"v_exp_f32 v170, v170\n"
"v_mfma_f32_16x16x32_bf16 a[224:227], v[232:235], v[96:99], a[224:227]\n"
"v_exp_f32 v171, v171\n"
"v_mfma_f32_16x16x32_bf16 a[228:231], v[232:235], v[100:103], a[228:231]\n"
"v_exp_f32 v184, v184\n"
"v_mfma_f32_16x16x32_bf16 a[232:235], v[232:235], v[104:107], a[232:235]\n"
"v_exp_f32 v185, v185\n"
"v_mfma_f32_16x16x32_bf16 a[236:239], v[232:235], v[108:111], a[236:239]\n"
"v_add_f32 v244, v244, v162\n"
"v_add_f32 v244, v244, v163\n"
"v_mfma_f32_16x16x32_bf16 a[240:243], v[236:239], v[96:99], a[240:243]\n"
"ds_read_b128 v[224:227], v240 offset:50816\n"
"s_add_u32 m0, s74, 0x9480\n"
"v_mfma_f32_16x16x32_bf16 a[244:247], v[236:239], v[100:103], a[244:247]\n"
"buffer_load_dwordx4 v243, s[56:59], s75 offen lds\n"
"v_mfma_f32_16x16x32_bf16 a[248:251], v[236:239], v[104:107], a[248:251]\n"
"ds_read_b128 v[228:231], v240 offset:59264\n"
"ds_read_b128 v[232:235], v240 offset:50880\n"
"v_mfma_f32_16x16x32_bf16 a[252:255], v[236:239], v[108:111], a[252:255]\n"
"s_cbranch_vccnz L_tC13_%=\n"
"L_bC13_%=:\n"
"s_waitcnt lgkmcnt(3)\n"
"v_mfma_f32_16x16x32_bf16 v[96:99], v[208:211], v[0:3], v[192:195]\n"
"ds_read_b128 v[236:239], v240 offset:59328\n"
"v_add_f32 v244, v244, v176\n"
"v_mfma_f32_16x16x32_bf16 v[100:103], v[208:211], v[4:7], v[196:199]\n"
"v_exp_f32 v186, v186\n"
"v_mfma_f32_16x16x32_bf16 v[104:107], v[208:211], v[8:11], v[200:203]\n"
"v_exp_f32 v187, v187\n"
"v_mfma_f32_16x16x32_bf16 v[108:111], v[208:211], v[12:15], v[204:207]\n"
"v_add_f32 v244, v244, v177\n"
"v_add_f32 v244, v244, v178\n"
"v_mfma_f32_16x16x32_bf16 v[112:115], v[212:215], v[0:3], v[192:195]\n"
"s_add_u32 m0, s74, 0xa500\n"
"s_add_u32 s75, s82, 0xe000\n"
"v_mfma_f32_16x16x32_bf16 v[116:119], v[212:215], v[4:7], v[196:199]\n"
"buffer_load_dwordx4 v242, s[56:59], s75 offen lds\n"
"v_mfma_f32_16x16x32_bf16 v[120:123], v[212:215], v[8:11], v[200:203]\n"
"v_add_f32 v244, v244, v179\n"
"v_cvt_pk_bf16_f32 v160, v160, v161\n"
"v_mfma_f32_16x16x32_bf16 v[124:127], v[212:215], v[12:15], v[204:207]\n"
"v_exp_f32 v172, v172\n"
"v_mfma_f32_16x16x32_bf16 v[96:99], v[216:219], v[16:19], v[96:99]\n"
"v_cvt_pk_bf16_f32 v161, v162, v163\n"
"v_cvt_pk_bf16_f32 v162, v176, v177\n"
"v_mfma_f32_16x16x32_bf16 v[100:103], v[216:219], v[20:23], v[100:103]\n"
"v_exp_f32 v173, v173\n"
"v_mfma_f32_16x16x32_bf16 v[104:107], v[216:219], v[24:27], v[104:107]\n"
"v_exp_f32 v174, v174\n"
"v_mfma_f32_16x16x32_bf16 v[108:111], v[216:219], v[28:31], v[108:111]\n"
"v_cvt_pk_bf16_f32 v163, v178, v179\n"
"v_add_f32 v245, v245, v164\n"
"v_mfma_f32_16x16x32_bf16 v[112:115], v[220:223], v[16:19], v[112:115]\n"
"ds_read_b128 v[208:211], v240 offset:50944\n"
"s_add_u32 m0, s74, 0xb580\n"
"v_mfma_f32_16x16x32_bf16 v[116:119], v[220:223], v[20:23], v[116:119]\n"
"buffer_load_dwordx4 v243, s[56:59], s75 offen lds\n"
"v_mfma_f32_16x16x32_bf16 v[120:123], v[220:223], v[24:27], v[120:123]\n"
"ds_read_b128 v[212:215], v240 offset:59392\n"
"ds_read_b128 v[216:219], v240 offset:51008\n"
"v_mfma_f32_16x16x32_bf16 v[124:127], v[220:223], v[28:31], v[124:127]\n"
"ds_read_b128 v[220:223], v240 offset:59456\n"
"s_waitcnt lgkmcnt(4)\n"
"v_mfma_f32_16x16x32_bf16 v[96:99], v[224:227], v[64:67], v[96:99]\n"
"v_exp_f32 v175, v175\n"
"v_mfma_f32_16x16x32_bf16 v[100:103], v[224:227], v[68:71], v[100:103]\n"
"v_exp_f32 v188, v188\n"
"v_mfma_f32_16x16x32_bf16 v[104:107], v[224:227], v[72:75], v[104:107]\n"
"v_add_f32 v245, v245, v165\n"
"v_add_f32 v245, v245, v166\n"
"v_mfma_f32_16x16x32_bf16 v[108:111], v[224:227], v[76:79], v[108:111]\n"
"v_exp_f32 v189, v189\n"
"v_mfma_f32_16x16x32_bf16 v[112:115], v[228:231], v[64:67], v[112:115]\n"
"v_exp_f32 v190, v190\n"
"v_mfma_f32_16x16x32_bf16 v[116:119], v[228:231], v[68:71], v[116:119]\n"
"v_add_f32 v245, v245, v167\n"
"v_add_f32 v245, v245, v180\n"
"v_mfma_f32_16x16x32_bf16 v[120:123], v[228:231], v[72:75], v[120:123]\n"
"v_exp_f32 v191, v191\n"
"v_mfma_f32_16x16x32_bf16 v[124:127], v[228:231], v[76:79], v[124:127]\n"
"v_add_f32 v245, v245, v181\n"
"v_add_f32 v245, v245, v182\n"
"v_mfma_f32_16x16x32_bf16 v[96:99], v[232:235], v[80:83], v[96:99]\n"
"v_add_f32 v245, v245, v183\n"
"v_cvt_pk_bf16_f32 v164, v164, v165\n"
"v_mfma_f32_16x16x32_bf16 v[100:103], v[232:235], v[84:87], v[100:103]\n"
"v_cvt_pk_bf16_f32 v165, v166, v167\n"
"v_cvt_pk_bf16_f32 v166, v180, v181\n"
"v_mfma_f32_16x16x32_bf16 v[104:107], v[232:235], v[88:91], v[104:107]\n"
"v_cvt_pk_bf16_f32 v167, v182, v183\n"
"v_add_f32 v246, v246, v168\n"
"v_mfma_f32_16x16x32_bf16 v[108:111], v[232:235], v[92:95], v[108:111]\n"
"v_add_f32 v246, v246, v169\n"
"v_add_f32 v246, v246, v170\n"
"v_mfma_f32_16x16x32_bf16 v[112:115], v[236:239], v[80:83], v[112:115]\n"
"ds_read_b128 v[224:227], v240 offset:51072\n"
"ds_read_b128 v[228:231], v240 offset:59520\n"
"v_mfma_f32_16x16x32_bf16 v[116:119], v[236:239], v[84:87], v[116:119]\n"
"ds_read_b128 v[232:235], v240 offset:51136\n"
"v_add_f32 v246, v246, v171\n"
"v_mfma_f32_16x16x32_bf16 v[120:123], v[236:239], v[88:91], v[120:123]\n"
"v_add_f32 v246, v246, v184\n"
"v_add_f32 v246, v246, v185\n"
"v_mfma_f32_16x16x32_bf16 v[124:127], v[236:239], v[92:95], v[124:127]\n"
"ds_read_b128 v[236:239], v240 offset:59584\n"
"s_waitcnt lgkmcnt(4)\n"
"v_mfma_f32_16x16x32_bf16 v[96:99], v[208:211], v[128:131], v[96:99]\n"
"v_add_f32 v246, v246, v186\n"
"v_add_f32 v246, v246, v187\n"
"v_mfma_f32_16x16x32_bf16 v[100:103], v[208:211], v[132:135], v[100:103]\n"
"v_cvt_pk_bf16_f32 v168, v168, v169\n"
"v_cvt_pk_bf16_f32 v169, v170, v171\n"
"v_mfma_f32_16x16x32_bf16 v[104:107], v[208:211], v[136:139], v[104:107]\n"
"v_cvt_pk_bf16_f32 v170, v184, v185\n"
"v_cvt_pk_bf16_f32 v171, v186, v187\n"
"v_mfma_f32_16x16x32_bf16 v[108:111], v[208:211], v[140:143], v[108:111]\n"
"v_add_f32 v247, v247, v172\n"
"v_add_f32 v247, v247, v173\n"
"v_mfma_f32_16x16x32_bf16 v[112:115], v[212:215], v[128:131], v[112:115]\n"
"v_add_f32 v247, v247, v174\n"
"v_add_f32 v247, v247, v175\n"
"v_mfma_f32_16x16x32_bf16 v[116:119], v[212:215], v[132:135], v[116:119]\n"
"v_add_f32 v247, v247, v188\n"
"v_add_f32 v247, v247, v189\n"
"v_mfma_f32_16x16x32_bf16 v[120:123], v[212:215], v[136:139], v[120:123]\n"
"v_add_f32 v247, v247, v190\n"
"v_add_f32 v247, v247, v191\n"
"v_mfma_f32_16x16x32_bf16 v[124:127], v[212:215], v[140:143], v[124:127]\n"
"v_cvt_pk_bf16_f32 v172, v172, v173\n"
"v_cvt_pk_bf16_f32 v173, v174, v175\n"
"v_mfma_f32_16x16x32_bf16 v[96:99], v[216:219], v[144:147], v[96:99]\n"
"v_cvt_pk_bf16_f32 v174, v188, v189\n"
"v_cvt_pk_bf16_f32 v175, v190, v191\n"
"v_mfma_f32_16x16x32_bf16 v[100:103], v[216:219], v[148:151], v[100:103]\n"
"v_mfma_f32_16x16x32_bf16 v[104:107], v[216:219], v[152:155], v[104:107]\n"
"v_mfma_f32_16x16x32_bf16 v[108:111], v[216:219], v[156:159], v[108:111]\n"
"v_mfma_f32_16x16x32_bf16 v[112:115], v[220:223], v[144:147], v[112:115]\n"
"v_mfma_f32_16x16x32_bf16 v[116:119], v[220:223], v[148:151], v[116:119]\n"
"v_mfma_f32_16x16x32_bf16 v[120:123], v[220:223], v[152:155], v[120:123]\n"
"v_mfma_f32_16x16x32_bf16 v[124:127], v[220:223], v[156:159], v[124:127]\n"
"s_waitcnt vmcnt(11) lgkmcnt(0)\n"
"s_barrier\n"
"v_mfma_f32_16x16x32_bf16 v[96:99], v[224:227], v[32:35], v[96:99]\n"
"ds_read_b64_tr_b16 v[208:209], v241 offset:33792\n"
"ds_read_b64_tr_b16 v[210:211], v241 offset:34048\n"
"v_mfma_f32_16x16x32_bf16 v[100:103], v[224:227], v[36:39], v[100:103]\n"
"ds_read_b64_tr_b16 v[212:213], v241 offset:33824\n"
"ds_read_b64_tr_b16 v[214:215], v241 offset:34080\n"
"v_mfma_f32_16x16x32_bf16 v[104:107], v[224:227], v[40:43], v[104:107]\n"
"ds_read_b64_tr_b16 v[216:217], v241 offset:34304\n"
"ds_read_b64_tr_b16 v[218:219], v241 offset:34560\n"
"v_mfma_f32_16x16x32_bf16 v[108:111], v[224:227], v[44:47], v[108:111]\n"
"ds_read_b64_tr_b16 v[220:221], v241 offset:34336\n"
"ds_read_b64_tr_b16 v[222:223], v241 offset:34592\n"
"v_mfma_f32_16x16x32_bf16 v[112:115], v[228:231], v[32:35], v[112:115]\n"
"s_add_u32 m0, s74, 0x0\n"
"s_add_u32 s75, s83, 0xe000\n"
"v_mfma_f32_16x16x32_bf16 v[116:119], v[228:231], v[36:39], v[116:119]\n"
"buffer_load_dwordx4 v242, s[56:59], s75 offen lds\n"
"v_mfma_f32_16x16x32_bf16 v[120:123], v[228:231], v[40:43], v[120:123]\n"
"v_mfma_f32_16x16x32_bf16 v[124:127], v[228:231], v[44:47], v[124:127]\n"
"v_mfma_f32_16x16x32_bf16 v[96:99], v[232:235], v[48:51], v[96:99]\n"
"v_mfma_f32_16x16x32_bf16 v[100:103], v[232:235], v[52:55], v[100:103]\n"
"v_mfma_f32_16x16x32_bf16 v[104:107], v[232:235], v[56:59], v[104:107]\n"
"v_mfma_f32_16x16x32_bf16 v[108:111], v[232:235], v[60:63], v[108:111]\n"
"v_mfma_f32_16x16x32_bf16 v[112:115], v[236:239], v[48:51], v[112:115]\n"
"s_add_u32 m0, s74, 0x1080\n"
"v_mfma_f32_16x16x32_bf16 v[116:119], v[236:239], v[52:55], v[116:119]\n"
"buffer_load_dwordx4 v243, s[56:59], s75 offen lds\n"
"v_mfma_f32_16x16x32_bf16 v[120:123], v[236:239], v[56:59], v[120:123]\n"
"ds_read_b64_tr_b16 v[224:225], v241 offset:38016\n"
"ds_read_b64_tr_b16 v[226:227], v241 offset:38272\n"
"v_mfma_f32_16x16x32_bf16 v[124:127], v[236:239], v[60:63], v[124:127]\n"
"s_waitcnt lgkmcnt(2)\n"
"v_mfma_f32_16x16x32_bf16 a[0:3], v[208:211], v[160:163], a[0:3]\n"
"ds_read_b64_tr_b16 v[228:229], v241 offset:38048\n"
"ds_read_b64_tr_b16 v[230:231], v241 offset:38304\n"
"v_mfma_f32_16x16x32_bf16 a[4:7], v[208:211], v[164:167], a[4:7]\n"
"ds_read_b64_tr_b16 v[232:233], v241 offset:38528\n"
"ds_read_b64_tr_b16 v[234:235], v241 offset:38784\n"
"v_mfma_f32_16x16x32_bf16 a[8:11], v[208:211], v[168:171], a[8:11]\n"
"ds_read_b64_tr_b16 v[236:237], v241 offset:38560\n"
"ds_read_b64_tr_b16 v[238:239], v241 offset:38816\n"
"v_mfma_f32_16x16x32_bf16 a[12:15], v[208:211], v[172:175], a[12:15]\n"
"v_max3_f32 v248, v96, v97, v98\n"
"v_max3_f32 v248, v248, v99, v100\n"
"v_mfma_f32_16x16x32_bf16 a[16:19], v[212:215], v[160:163], a[16:19]\n"
"s_add_u32 m0, s74, 0x2100\n"
"s_add_u32 s75, s82, 0x15000\n"
"v_mfma_f32_16x16x32_bf16 a[20:23], v[212:215], v[164:167], a[20:23]\n"
"buffer_load_dwordx4 v242, s[56:59], s75 offen lds\n"
"v_mfma_f32_16x16x32_bf16 a[24:27], v[212:215], v[168:171], a[24:27]\n"
"v_max3_f32 v248, v248, v101, v102\n"
"v_max3_f32 v248, v248, v103, v104\n"
"v_mfma_f32_16x16x32_bf16 a[28:31], v[212:215], v[172:175], a[28:31]\n"
"v_max3_f32 v248, v248, v105, v106\n"
"v_max3_f32 v248, v248, v107, v108\n"
"v_mfma_f32_16x16x32_bf16 a[32:35], v[216:219], v[160:163], a[32:35]\n"
"v_max3_f32 v248, v248, v109, v110\n"
"v_max3_f32 v248, v248, v111, v112\n"
"v_mfma_f32_16x16x32_bf16 a[36:39], v[216:219], v[164:167], a[36:39]\n"
"v_max3_f32 v248, v248, v113, v114\n"
"v_max3_f32 v248, v248, v115, v116\n"
"v_mfma_f32_16x16x32_bf16 a[40:43], v[216:219], v[168:171], a[40:43]\n"
"v_max3_f32 v248, v248, v117, v118\n"
"v_max3_f32 v248, v248, v119, v120\n"
"v_mfma_f32_16x16x32_bf16 a[44:47], v[216:219], v[172:175], a[44:47]\n"
"v_max3_f32 v248, v248, v121, v122\n"
"v_max3_f32 v248, v248, v123, v124\n"
"v_mfma_f32_16x16x32_bf16 a[48:51], v[220:223], v[160:163], a[48:51]\n"
"v_max3_f32 v248, v248, v125, v126\n"
"s_add_u32 m0, s74, 0x3180\n"
"v_mfma_f32_16x16x32_bf16 a[52:55], v[220:223], v[164:167], a[52:55]\n"
"buffer_load_dwordx4 v243, s[56:59], s75 offen lds\n"
"v_mfma_f32_16x16x32_bf16 a[56:59], v[220:223], v[168:171], a[56:59]\n"
"ds_read_b64_tr_b16 v[208:209], v241 offset:42240\n"
"ds_read_b64_tr_b16 v[210:211], v241 offset:42496\n"
"v_mfma_f32_16x16x32_bf16 a[60:63], v[220:223], v[172:175], a[60:63]\n"
"v_max_f32 v248, v248, v127\n"
"s_waitcnt lgkmcnt(2)\n"
"v_mfma_f32_16x16x32_bf16 a[64:67], v[224:227], v[160:163], a[64:67]\n"
"ds_read_b64_tr_b16 v[212:213], v241 offset:42272\n"
"ds_read_b64_tr_b16 v[214:215], v241 offset:42528\n"
"v_mfma_f32_16x16x32_bf16 a[68:71], v[224:227], v[164:167], a[68:71]\n"
"ds_read_b64_tr_b16 v[216:217], v241 offset:42752\n"
"ds_read_b64_tr_b16 v[218:219], v241 offset:43008\n"
"v_mfma_f32_16x16x32_bf16 a[72:75], v[224:227], v[168:171], a[72:75]\n"
"ds_read_b64_tr_b16 v[220:221], v241 offset:42784\n"
"ds_read_b64_tr_b16 v[222:223], v241 offset:43040\n"
"v_mfma_f32_16x16x32_bf16 a[76:79], v[224:227], v[172:175], a[76:79]\n"
"v_cmp_lt_f32 vcc, 0x41000000, v248\n"
"v_mfma_f32_16x16x32_bf16 a[80:83], v[228:231], v[160:163], a[80:83]\n"
"s_add_u32 m0, s74, 0x4200\n"
"s_add_u32 s75, s83, 0x15000\n"
"v_mfma_f32_16x16x32_bf16 a[84:87], v[228:231], v[164:167], a[84:87]\n"
"buffer_load_dwordx4 v242, s[56:59], s75 offen lds\n"
"v_mfma_f32_16x16x32_bf16 a[88:91], v[228:231], v[168:171], a[88:91]\n"
"s_cbranch_vccnz L_tB14_%=\n"
"L_bB14_%=:\n"
"v_mfma_f32_16x16x32_bf16 a[92:95], v[228:231], v[172:175], a[92:95]\n"
"v_exp_f32 v96, v96\n"
"v_mfma_f32_16x16x32_bf16 a[96:99], v[232:235], v[160:163], a[96:99]\n"
"v_exp_f32 v97, v97\n"
"v_mfma_f32_16x16x32_bf16 a[100:103], v[232:235], v[164:167], a[100:103]\n"
"v_exp_f32 v98, v98\n"
"v_mfma_f32_16x16x32_bf16 a[104:107], v[232:235], v[168:171], a[104:107]\n"
"v_exp_f32 v99, v99\n"
"v_mfma_f32_16x16x32_bf16 a[108:111], v[232:235], v[172:175], a[108:111]\n"
"v_exp_f32 v112, v112\n"
"v_mfma_f32_16x16x32_bf16 a[112:115], v[236:239], v[160:163], a[112:115]\n"
"v_add_f32 v244, v244, v96\n"
"s_add_u32 m0, s74, 0x5280\n"
"v_mfma_f32_16x16x32_bf16 a[116:119], v[236:239], v[164:167], a[116:119]\n"
"buffer_load_dwordx4 v243, s[56:59], s75 offen lds\n"
"v_mfma_f32_16x16x32_bf16 a[120:123], v[236:239], v[168:171], a[120:123]\n"
"ds_read_b64_tr_b16 v[224:225], v241 offset:46464\n"
"ds_read_b64_tr_b16 v[226:227], v241 offset:46720\n"
"v_mfma_f32_16x16x32_bf16 a[124:127], v[236:239], v[172:175], a[124:127]\n"
"v_add_f32 v244, v244, v97\n"
"s_waitcnt lgkmcnt(2)\n"
"v_mfma_f32_16x16x32_bf16 a[128:131], v[208:211], v[160:163], a[128:131]\n"
"ds_read_b64_tr_b16 v[228:229], v241 offset:46496\n"
"ds_read_b64_tr_b16 v[230:231], v241 offset:46752\n"
"v_mfma_f32_16x16x32_bf16 a[132:135], v[208:211], v[164:167], a[132:135]\n"
"ds_read_b64_tr_b16 v[232:233], v241 offset:46976\n"
"ds_read_b64_tr_b16 v[234:235], v241 offset:47232\n"
"v_mfma_f32_16x16x32_bf16 a[136:139], v[208:211], v[168:171], a[136:139]\n"
"ds_read_b64_tr_b16 v[236:237], v241 offset:47008\n"
"ds_read_b64_tr_b16 v[238:239], v241 offset:47264\n"
"v_mfma_f32_16x16x32_bf16 a[140:143], v[208:211], v[172:175], a[140:143]\n"
"v_exp_f32 v113, v113\n"
"v_mfma_f32_16x16x32_bf16 a[144:147], v[212:215], v[160:163], a[144:147]\n"
"s_add_u32 m0, s74, 0x6300\n"
"s_add_u32 s75, s82, 0x1c000\n"
"v_mfma_f32_16x16x32_bf16 a[148:151], v[212:215], v[164:167], a[148:151]\n"
"buffer_load_dwordx4 v242, s[56:59], s75 offen lds\n"
"v_mfma_f32_16x16x32_bf16 a[152:155], v[212:215], v[168:171], a[152:155]\n"
"v_exp_f32 v114, v114\n"
"v_mfma_f32_16x16x32_bf16 a[156:159], v[212:215], v[172:175], a[156:159]\n"
"v_exp_f32 v115, v115\n"
"v_mfma_f32_16x16x32_bf16 a[160:163], v[216:219], v[160:163], a[160:163]\n"
"v_exp_f32 v100, v100\n"
"v_mfma_f32_16x16x32_bf16 a[164:167], v[216:219], v[164:167], a[164:167]\n"
"v_exp_f32 v101, v101\n"
"v_mfma_f32_16x16x32_bf16 a[168:171], v[216:219], v[168:171], a[168:171]\n"
"v_exp_f32 v102, v102\n"
"v_mfma_f32_16x16x32_bf16 a[172:175], v[216:219], v[172:175], a[172:175]\n"
"v_exp_f32 v103, v103\n"
"v_mfma_f32_16x16x32_bf16 a[176:179], v[220:223], v[160:163], a[176:179]\n"
"v_add_f32 v244, v244, v98\n"
"s_add_u32 m0, s74, 0x7380\n"
"v_mfma_f32_16x16x32_bf16 a[180:183], v[220:223], v[164:167], a[180:183]\n"
"buffer_load_dwordx4 v243, s[56:59], s75 offen lds\n"
"v_mfma_f32_16x16x32_bf16 a[184:187], v[220:223], v[168:171], a[184:187]\n"
"v_exp_f32 v116, v116\n"
"v_mfma_f32_16x16x32_bf16 a[188:191], v[220:223], v[172:175], a[188:191]\n"
"v_add_f32 v244, v244, v99\n"
"s_waitcnt lgkmcnt(0)\n"
"v_mfma_f32_16x16x32_bf16 a[192:195], v[224:227], v[160:163], a[192:195]\n"
"v_exp_f32 v117, v117\n"
"v_mfma_f32_16x16x32_bf16 a[196:199], v[224:227], v[164:167], a[196:199]\n"
"v_exp_f32 v118, v118\n"
"v_mfma_f32_16x16x32_bf16 a[200:203], v[224:227], v[168:171], a[200:203]\n"
"v_exp_f32 v119, v119\n"
"v_mfma_f32_16x16x32_bf16 a[204:207], v[224:227], v[172:175], a[204:207]\n"
"v_exp_f32 v104, v104\n"
"v_mfma_f32_16x16x32_bf16 a[208:211], v[228:231], v[160:163], a[208:211]\n"
"s_add_u32 m0, s74, 0xc600\n"
"s_add_u32 s75, s83, 0x1c000\n"
"v_mfma_f32_16x16x32_bf16 a[212:215], v[228:231], v[164:167], a[212:215]\n"
"buffer_load_dwordx4 v242, s[56:59], s75 offen lds\n"
"v_mfma_f32_16x16x32_bf16 a[216:219], v[228:231], v[168:171], a[216:219]\n"
"s_waitcnt vmcnt(9)\n"
"s_barrier\n"
"ds_read_b64_tr_b16 v[208:209], v241 offset:50688\n"
"ds_read_b64_tr_b16 v[210:211], v241 offset:50944\n"
"v_mfma_f32_16x16x32_bf16 a[220:223], v[228:231], v[172:175], a[220:223]\n"
"ds_read_b64_tr_b16 v[212:213], v241 offset:50720\n"
"ds_read_b64_tr_b16 v[214:215], v241 offset:50976\n"
"v_mfma_f32_16x16x32_bf16 a[224:227], v[232:235], v[160:163], a[224:227]\n"
"ds_read_b64_tr_b16 v[216:217], v241 offset:51200\n"
"ds_read_b64_tr_b16 v[218:219], v241 offset:51456\n"
"v_mfma_f32_16x16x32_bf16 a[228:231], v[232:235], v[164:167], a[228:231]\n"
"ds_read_b64_tr_b16 v[220:221], v241 offset:51232\n"
"ds_read_b64_tr_b16 v[222:223], v241 offset:51488\n"
"v_mfma_f32_16x16x32_bf16 a[232:235], v[232:235], v[168:171], a[232:235]\n"
"v_exp_f32 v105, v105\n"
"v_mfma_f32_16x16x32_bf16 a[236:239], v[232:235], v[172:175], a[236:239]\n"
"v_exp_f32 v106, v106\n"
"v_mfma_f32_16x16x32_bf16 a[240:243], v[236:239], v[160:163], a[240:243]\n"
"v_add_f32 v244, v244, v112\n"
"s_add_u32 m0, s74, 0xd680\n"
"v_mfma_f32_16x16x32_bf16 a[244:247], v[236:239], v[164:167], a[244:247]\n"
"buffer_load_dwordx4 v243, s[56:59], s75 offen lds\n"
"v_mfma_f32_16x16x32_bf16 a[248:251], v[236:239], v[168:171], a[248:251]\n"
"ds_read_b64_tr_b16 v[224:225], v241 offset:54912\n"
"ds_read_b64_tr_b16 v[226:227], v241 offset:55168\n"
"v_mfma_f32_16x16x32_bf16 a[252:255], v[236:239], v[172:175], a[252:255]\n"
"s_cbranch_vccnz L_tC14_%=\n"
"L_bC14_%=:\n"
"v_add_f32 v244, v244, v113\n"
"v_add_f32 v244, v244, v114\n"
"v_add_f32 v244, v244, v115\n"
"v_cvt_pk_bf16_f32 v96, v96, v97\n"
"v_cvt_pk_bf16_f32 v97, v98, v99\n"
"v_cvt_pk_bf16_f32 v98, v112, v113\n"
"v_cvt_pk_bf16_f32 v99, v114, v115\n"
"v_add_f32 v245, v245, v100\n"
"v_add_f32 v245, v245, v101\n"
"v_add_f32 v245, v245, v102\n"
"v_add_f32 v245, v245, v103\n"
"v_add_f32 v245, v245, v116\n"
"v_add_f32 v245, v245, v117\n"
"v_add_f32 v245, v245, v118\n"
"v_add_f32 v245, v245, v119\n"
"v_cvt_pk_bf16_f32 v100, v100, v101\n"
"v_cvt_pk_bf16_f32 v101, v102, v103\n"
"v_cvt_pk_bf16_f32 v102, v116, v117\n"
"v_cvt_pk_bf16_f32 v103, v118, v119\n"
"v_add_f32 v246, v246, v104\n"
"v_add_f32 v246, v246, v105\n"
"v_add_f32 v246, v246, v106\n"
"v_exp_f32 v107, v107\n"
"v_exp_f32 v120, v120\n"
"v_add_f32 v246, v246, v107\n"
"v_add_f32 v246, v246, v120\n"
"v_exp_f32 v121, v121\n"
"v_exp_f32 v122, v122\n"
"v_add_f32 v246, v246, v121\n"
"v_add_f32 v246, v246, v122\n"
"v_exp_f32 v123, v123\n"
"v_cvt_pk_bf16_f32 v104, v104, v105\n"
"v_add_f32 v246, v246, v123\n"
"v_cvt_pk_bf16_f32 v105, v106, v107\n"
"v_cvt_pk_bf16_f32 v106, v120, v121\n"
"v_cvt_pk_bf16_f32 v107, v122, v123\n"
"v_exp_f32 v108, v108\n"
"v_exp_f32 v109, v109\n"
"v_add_f32 v247, v247, v108\n"
"v_add_f32 v247, v247, v109\n"
"v_exp_f32 v110, v110\n"
"v_exp_f32 v111, v111\n"
"v_add_f32 v247, v247, v110\n"
"v_add_f32 v247, v247, v111\n"
"v_exp_f32 v124, v124\n"
"v_exp_f32 v125, v125\n"
"v_add_f32 v247, v247, v124\n"
"v_add_f32 v247, v247, v125\n"
"v_exp_f32 v126, v126\n"
"v_exp_f32 v127, v127\n"
"v_add_f32 v247, v247, v126\n"
"v_add_f32 v247, v247, v127\n"
"v_cvt_pk_bf16_f32 v108, v108, v109\n"
"v_cvt_pk_bf16_f32 v109, v110, v111\n"
"v_cvt_pk_bf16_f32 v110, v124, v125\n"
"v_cvt_pk_bf16_f32 v111, v126, v127\n"
"v_lshlrev_b32 v248, 4, v251\n"
"v_mov_b32 v252, -1\n"
"v_mov_b32 v253, -1\n"
"v_mov_b32 v254, -1\n"
"v_mov_b32 v255, -1\n"
"s_and_b32 s79, s89, 1\n"
"s_mul_i32 s94, s79, 4736\n"
"s_sub_u32 s94, s94, 0x10800\n"
"v_add_u32 v200, s94, v241\n"
"s_mul_i32 s94, s79, 3712\n"
"s_add_u32 s94, s94, 0xf780\n"
"v_subrev_u32 v203, s94, v241\n"
"v_add_u32 v201, 0x10000, v200\n"
"v_add_u32 v202, 0x20000, v200\n"
"v_add_u32 v204, 0x10000, v203\n"
"v_add_u32 v205, 0x20000, v203\n"
"s_add_u32 s79, s96, 0x10800\n"
"v_lshlrev_b32 v207, 4, v251\n"
"v_add_u32 v206, s79, v207\n"
"s_xor_b32 s79, s96, 0x1000\n"
"s_add_u32 s79, s79, 0x10800\n"
"v_add_u32 v207, s79, v207\n"
"s_waitcnt lgkmcnt(2)\n"
"v_mfma_f32_16x16x32_bf16 a[0:3], v[208:211], v[96:99], a[0:3]\n"
"ds_read_b64_tr_b16 v[228:229], v241 offset:54944\n"
"ds_read_b64_tr_b16 v[230:231], v241 offset:55200\n"
"v_mfma_f32_16x16x32_bf16 a[4:7], v[208:211], v[100:103], a[4:7]\n"
"ds_read_b64_tr_b16 v[232:233], v241 offset:55424\n"
"ds_read_b64_tr_b16 v[234:235], v241 offset:55680\n"
"v_mfma_f32_16x16x32_bf16 a[8:11], v[208:211], v[104:107], a[8:11]\n"
"ds_read_b64_tr_b16 v[236:237], v241 offset:55456\n"
"ds_read_b64_tr_b16 v[238:239], v241 offset:55712\n"
"v_mfma_f32_16x16x32_bf16 a[12:15], v[208:211], v[108:111], a[12:15]\n"
"v_mov_b32 v192, v244\n"
"v_mov_b32 v193, v245\n"
"v_mfma_f32_16x16x32_bf16 a[16:19], v[212:215], v[96:99], a[16:19]\n"
"buffer_store_dwordx4 v[252:255], v248, s[36:39], s97 offen offset:0 sc1\n"
"s_add_u32 m0, s74, 0xe700\n"
"s_add_u32 s75, s82, 0x23000\n"
"v_mfma_f32_16x16x32_bf16 a[20:23], v[212:215], v[100:103], a[20:23]\n"
"buffer_load_dwordx4 v242, s[56:59], s75 offen lds\n"
"v_mfma_f32_16x16x32_bf16 a[24:27], v[212:215], v[104:107], a[24:27]\n"
"v_mov_b32 v194, v246\n"
"v_mov_b32 v195, v247\n"
"v_mfma_f32_16x16x32_bf16 a[28:31], v[212:215], v[108:111], a[28:31]\n"
"v_permlane32_swap_b32 v244, v192\n"
"v_permlane32_swap_b32 v245, v193\n"
"v_mfma_f32_16x16x32_bf16 a[32:35], v[216:219], v[96:99], a[32:35]\n"
"v_permlane32_swap_b32 v246, v194\n"
"v_permlane32_swap_b32 v247, v195\n"
"v_mfma_f32_16x16x32_bf16 a[36:39], v[216:219], v[100:103], a[36:39]\n"
"buffer_store_dwordx4 v[252:255], v248, s[36:39], s97 offen offset:1024 sc1\n"
"v_mfma_f32_16x16x32_bf16 a[40:43], v[216:219], v[104:107], a[40:43]\n"
"v_add_f32 v244, v244, v192\n"
"v_add_f32 v245, v245, v193\n"
"v_mfma_f32_16x16x32_bf16 a[44:47], v[216:219], v[108:111], a[44:47]\n"
"v_add_f32 v246, v246, v194\n"
"v_add_f32 v247, v247, v195\n"
"v_mfma_f32_16x16x32_bf16 a[48:51], v[220:223], v[96:99], a[48:51]\n"
"v_mov_b32 v192, v244\n"
"s_add_u32 m0, s74, 0xf780\n"
"v_mfma_f32_16x16x32_bf16 a[52:55], v[220:223], v[100:103], a[52:55]\n"
"buffer_load_dwordx4 v243, s[56:59], s75 offen lds\n"
"v_mfma_f32_16x16x32_bf16 a[56:59], v[220:223], v[104:107], a[56:59]\n"
"buffer_store_dwordx4 v[252:255], v248, s[36:39], s97 offen offset:2048 sc1\n"
"v_mfma_f32_16x16x32_bf16 a[60:63], v[220:223], v[108:111], a[60:63]\n"
"v_mov_b32 v193, v245\n"
"s_waitcnt lgkmcnt(0)\n"
"v_mfma_f32_16x16x32_bf16 a[64:67], v[224:227], v[96:99], a[64:67]\n"
"ds_read_b64_tr_b16 v[208:209], v241 offset:59136\n"
"ds_read_b64_tr_b16 v[210:211], v241 offset:59392\n"
"v_mfma_f32_16x16x32_bf16 a[68:71], v[224:227], v[100:103], a[68:71]\n"
"ds_read_b64_tr_b16 v[212:213], v241 offset:59168\n"
"ds_read_b64_tr_b16 v[214:215], v241 offset:59424\n"
"v_mfma_f32_16x16x32_bf16 a[72:75], v[224:227], v[104:107], a[72:75]\n"
"ds_read_b64_tr_b16 v[216:217], v241 offset:59648\n"
"ds_read_b64_tr_b16 v[218:219], v241 offset:59904\n"
"v_mfma_f32_16x16x32_bf16 a[76:79], v[224:227], v[108:111], a[76:79]\n"
"buffer_store_dwordx4 v[252:255], v248, s[36:39], s97 offen offset:3072 sc1\n"
"v_mfma_f32_16x16x32_bf16 a[80:83], v[228:231], v[96:99], a[80:83]\n"
"ds_read_b64_tr_b16 v[220:221], v241 offset:59680\n"
"ds_read_b64_tr_b16 v[222:223], v241 offset:59936\n"
"v_mfma_f32_16x16x32_bf16 a[84:87], v[228:231], v[100:103], a[84:87]\n"
"v_mov_b32 v194, v246\n"
"v_mov_b32 v195, v247\n"
"v_mfma_f32_16x16x32_bf16 a[88:91], v[228:231], v[104:107], a[88:91]\n"
"v_permlane16_swap_b32 v244, v192\n"
"v_permlane16_swap_b32 v245, v193\n"
"v_mfma_f32_16x16x32_bf16 a[92:95], v[228:231], v[108:111], a[92:95]\n"
"v_permlane16_swap_b32 v246, v194\n"
"v_permlane16_swap_b32 v247, v195\n"
"v_mfma_f32_16x16x32_bf16 a[96:99], v[232:235], v[96:99], a[96:99]\n"
"v_add_f32 v244, v244, v192\n"
"v_add_f32 v245, v245, v193\n"
"v_mfma_f32_16x16x32_bf16 a[100:103], v[232:235], v[100:103], a[100:103]\n"
"v_rcp_f32 v244, v244\n"
"v_mfma_f32_16x16x32_bf16 a[104:107], v[232:235], v[104:107], a[104:107]\n"
"v_rcp_f32 v245, v245\n"
"v_mfma_f32_16x16x32_bf16 a[108:111], v[232:235], v[108:111], a[108:111]\n"
"v_add_f32 v246, v246, v194\n"
"v_add_f32 v247, v247, v195\n"
"v_mfma_f32_16x16x32_bf16 a[112:115], v[236:239], v[96:99], a[112:115]\n"
"ds_read_b64_tr_b16 v[224:225], v241 offset:63360\n"
"ds_read_b64_tr_b16 v[226:227], v241 offset:63616\n"
"v_mfma_f32_16x16x32_bf16 a[116:119], v[236:239], v[100:103], a[116:119]\n"
"ds_read_b64_tr_b16 v[228:229], v241 offset:63392\n"
"ds_read_b64_tr_b16 v[230:231], v241 offset:63648\n"
"v_mfma_f32_16x16x32_bf16 a[120:123], v[236:239], v[104:107], a[120:123]\n"
"ds_read_b64_tr_b16 v[232:233], v241 offset:63872\n"
"ds_read_b64_tr_b16 v[234:235], v241 offset:64128\n"
"v_mfma_f32_16x16x32_bf16 a[124:127], v[236:239], v[108:111], a[124:127]\n"
"v_accvgpr_read_b32 v192, a0\n"
"s_waitcnt lgkmcnt(6)\n"
"v_mfma_f32_16x16x32_bf16 a[128:131], v[208:211], v[96:99], a[128:131]\n"
"ds_read_b64_tr_b16 v[236:237], v241 offset:63904\n"
"ds_read_b64_tr_b16 v[238:239], v241 offset:64160\n"
"v_mfma_f32_16x16x32_bf16 a[132:135], v[208:211], v[100:103], a[132:135]\n"
"v_rcp_f32 v246, v246\n"
"v_mfma_f32_16x16x32_bf16 a[136:139], v[208:211], v[104:107], a[136:139]\n"
"v_rcp_f32 v247, v247\n"
"v_mfma_f32_16x16x32_bf16 a[140:143], v[208:211], v[108:111], a[140:143]\n"
"v_accvgpr_read_b32 v193, a1\n"
"v_accvgpr_read_b32 v194, a2\n"
"v_mfma_f32_16x16x32_bf16 a[144:147], v[212:215], v[96:99], a[144:147]\n"
"v_accvgpr_read_b32 v195, a3\n"
"v_mul_f32 v192, v192, v244\n"
"v_mfma_f32_16x16x32_bf16 a[148:151], v[212:215], v[100:103], a[148:151]\n"
"v_mul_f32 v193, v193, v244\n"
"v_mul_f32 v194, v194, v244\n"
"v_mfma_f32_16x16x32_bf16 a[152:155], v[212:215], v[104:107], a[152:155]\n"
"v_mul_f32 v195, v195, v244\n"
"v_accvgpr_read_b32 v196, a16\n"
"v_mfma_f32_16x16x32_bf16 a[156:159], v[212:215], v[108:111], a[156:159]\n"
"v_accvgpr_read_b32 v197, a17\n"
"v_accvgpr_read_b32 v198, a18\n"
"v_mfma_f32_16x16x32_bf16 a[160:163], v[216:219], v[96:99], a[160:163]\n"
"v_accvgpr_read_b32 v199, a19\n"
"v_mul_f32 v196, v196, v244\n"
"v_mfma_f32_16x16x32_bf16 a[164:167], v[216:219], v[100:103], a[164:167]\n"
"v_mul_f32 v197, v197, v244\n"
"v_mul_f32 v198, v198, v244\n"
"v_mfma_f32_16x16x32_bf16 a[168:171], v[216:219], v[104:107], a[168:171]\n"
"v_mul_f32 v199, v199, v244\n"
"v_cvt_pk_bf16_f32 v0, v192, v193\n"
"v_mfma_f32_16x16x32_bf16 a[172:175], v[216:219], v[108:111], a[172:175]\n"
"v_cvt_pk_bf16_f32 v1, v194, v195\n"
"v_cvt_pk_bf16_f32 v2, v196, v197\n"
"v_mfma_f32_16x16x32_bf16 a[176:179], v[220:223], v[96:99], a[176:179]\n"
"ds_read_b64_tr_b16 v[208:209], v202 offset:4096\n"
"ds_read_b64_tr_b16 v[210:211], v202 offset:4352\n"
"v_mfma_f32_16x16x32_bf16 a[180:183], v[220:223], v[100:103], a[180:183]\n"
"ds_read_b64_tr_b16 v[212:213], v202 offset:4128\n"
"ds_read_b64_tr_b16 v[214:215], v202 offset:4384\n"
"v_mfma_f32_16x16x32_bf16 a[184:187], v[220:223], v[104:107], a[184:187]\n"
"ds_read_b64_tr_b16 v[216:217], v202 offset:12544\n"
"ds_read_b64_tr_b16 v[218:219], v202 offset:12800\n"
"v_mfma_f32_16x16x32_bf16 a[188:191], v[220:223], v[108:111], a[188:191]\n"
"v_cvt_pk_bf16_f32 v3, v198, v199\n"
"s_waitcnt lgkmcnt(6)\n"
"v_mfma_f32_16x16x32_bf16 a[192:195], v[224:227], v[96:99], a[192:195]\n"
"ds_read_b64_tr_b16 v[220:221], v202 offset:12576\n"
"ds_read_b64_tr_b16 v[222:223], v202 offset:12832\n"
"v_mfma_f32_16x16x32_bf16 a[196:199], v[224:227], v[100:103], a[196:199]\n"
"ds_write_b128 v206, v[0:3] offset:0\n"
"v_mfma_f32_16x16x32_bf16 a[200:203], v[224:227], v[104:107], a[200:203]\n"
"v_accvgpr_read_b32 v192, a4\n"
"v_accvgpr_read_b32 v193, a5\n"
"v_mfma_f32_16x16x32_bf16 a[204:207], v[224:227], v[108:111], a[204:207]\n"
"v_accvgpr_read_b32 v194, a6\n"
"v_accvgpr_read_b32 v195, a7\n"
"v_mfma_f32_16x16x32_bf16 a[208:211], v[228:231], v[96:99], a[208:211]\n"
"v_mul_f32 v192, v192, v245\n"
"v_mul_f32 v193, v193, v245\n"
"v_mfma_f32_16x16x32_bf16 a[212:215], v[228:231], v[100:103], a[212:215]\n"
"v_mul_f32 v194, v194, v245\n"
"v_mul_f32 v195, v195, v245\n"
"v_mfma_f32_16x16x32_bf16 a[216:219], v[228:231], v[104:107], a[216:219]\n"
"v_accvgpr_read_b32 v196, a20\n"
"v_accvgpr_read_b32 v197, a21\n"
"v_mfma_f32_16x16x32_bf16 a[220:223], v[228:231], v[108:111], a[220:223]\n"
"v_accvgpr_read_b32 v198, a22\n"
"v_accvgpr_read_b32 v199, a23\n"
"v_mfma_f32_16x16x32_bf16 a[224:227], v[232:235], v[96:99], a[224:227]\n"
"v_mul_f32 v196, v196, v245\n"
"v_mul_f32 v197, v197, v245\n"
"v_mfma_f32_16x16x32_bf16 a[228:231], v[232:235], v[100:103], a[228:231]\n"
"v_mul_f32 v198, v198, v245\n"
"v_mul_f32 v199, v199, v245\n"
"v_mfma_f32_16x16x32_bf16 a[232:235], v[232:235], v[104:107], a[232:235]\n"
"v_cvt_pk_bf16_f32 v4, v192, v193\n"
"v_cvt_pk_bf16_f32 v5, v194, v195\n"
"v_mfma_f32_16x16x32_bf16 a[236:239], v[232:235], v[108:111], a[236:239]\n"
"v_cvt_pk_bf16_f32 v6, v196, v197\n"
"v_cvt_pk_bf16_f32 v7, v198, v199\n"
"v_mfma_f32_16x16x32_bf16 a[240:243], v[236:239], v[96:99], a[240:243]\n"
"ds_read_b64_tr_b16 v[224:225], v202 offset:20992\n"
"ds_read_b64_tr_b16 v[226:227], v202 offset:21248\n"
"v_mfma_f32_16x16x32_bf16 a[244:247], v[236:239], v[100:103], a[244:247]\n"
"ds_read_b64_tr_b16 v[228:229], v202 offset:21024\n"
"ds_read_b64_tr_b16 v[230:231], v202 offset:21280\n"
"v_mfma_f32_16x16x32_bf16 a[248:251], v[236:239], v[104:107], a[248:251]\n"
"ds_read_b64_tr_b16 v[232:233], v200 offset:33792\n"
"ds_read_b64_tr_b16 v[234:235], v200 offset:34048\n"
"v_mfma_f32_16x16x32_bf16 a[252:255], v[236:239], v[108:111], a[252:255]\n"
"v_accvgpr_read_b32 v192, a8\n"
"s_waitcnt lgkmcnt(7)\n"
"v_mfma_f32_16x16x32_bf16 v[160:163], v[208:211], v[0:3], 0\n"
"ds_read_b64_tr_b16 v[236:237], v200 offset:33824\n"
"ds_read_b64_tr_b16 v[238:239], v200 offset:34080\n"
"v_mfma_f32_16x16x32_bf16 v[164:167], v[208:211], v[4:7], 0\n"
"ds_write_b128 v206, v[4:7] offset:1024\n"
"v_accvgpr_read_b32 v193, a9\n"
"v_accvgpr_read_b32 v194, a10\n"
"v_accvgpr_read_b32 v195, a11\n"
"v_mul_f32 v192, v192, v246\n"
"v_mul_f32 v193, v193, v246\n"
"v_mul_f32 v194, v194, v246\n"
"v_mul_f32 v195, v195, v246\n"
"v_accvgpr_read_b32 v196, a24\n"
"v_accvgpr_read_b32 v197, a25\n"
"v_accvgpr_read_b32 v198, a26\n"
"v_accvgpr_read_b32 v199, a27\n"
"v_mul_f32 v196, v196, v246\n"
"v_mul_f32 v197, v197, v246\n"
"v_mul_f32 v198, v198, v246\n"
"v_mul_f32 v199, v199, v246\n"
"v_cvt_pk_bf16_f32 v8, v192, v193\n"
"v_cvt_pk_bf16_f32 v9, v194, v195\n"
"v_cvt_pk_bf16_f32 v10, v196, v197\n"
"v_cvt_pk_bf16_f32 v11, v198, v199\n"
"s_nop 1\n"
"v_mfma_f32_16x16x32_bf16 v[168:171], v[208:211], v[8:11], 0\n"
"ds_write_b128 v206, v[8:11] offset:2048\n"
"v_accvgpr_read_b32 v192, a12\n"
"v_accvgpr_read_b32 v193, a13\n"
"v_accvgpr_read_b32 v194, a14\n"
"v_accvgpr_read_b32 v195, a15\n"
"v_mul_f32 v192, v192, v247\n"
"v_mul_f32 v193, v193, v247\n"
"v_mul_f32 v194, v194, v247\n"
"v_mul_f32 v195, v195, v247\n"
"v_accvgpr_read_b32 v196, a28\n"
"v_accvgpr_read_b32 v197, a29\n"
"v_accvgpr_read_b32 v198, a30\n"
"v_accvgpr_read_b32 v199, a31\n"
"v_mul_f32 v196, v196, v247\n"
"v_mul_f32 v197, v197, v247\n"
"v_mul_f32 v198, v198, v247\n"
"v_mul_f32 v199, v199, v247\n"
"v_cvt_pk_bf16_f32 v12, v192, v193\n"
"v_cvt_pk_bf16_f32 v13, v194, v195\n"
"v_cvt_pk_bf16_f32 v14, v196, v197\n"
"v_cvt_pk_bf16_f32 v15, v198, v199\n"
"s_nop 1\n"
"v_mfma_f32_16x16x32_bf16 v[172:175], v[208:211], v[12:15], 0\n"
"ds_write_b128 v206, v[12:15] offset:3072\n"
"v_mfma_f32_16x16x32_bf16 v[176:179], v[212:215], v[0:3], 0\n"
"v_accvgpr_read_b32 v192, a32\n"
"v_accvgpr_read_b32 v193, a33\n"
"v_mfma_f32_16x16x32_bf16 v[180:183], v[212:215], v[4:7], 0\n"
"v_accvgpr_read_b32 v194, a34\n"
"v_accvgpr_read_b32 v195, a35\n"
"v_mfma_f32_16x16x32_bf16 v[184:187], v[212:215], v[8:11], 0\n"
"v_mul_f32 v192, v192, v244\n"
"v_mul_f32 v193, v193, v244\n"
"v_mfma_f32_16x16x32_bf16 v[188:191], v[212:215], v[12:15], 0\n"
"v_mul_f32 v194, v194, v244\n"
"v_mul_f32 v195, v195, v244\n"
"v_mfma_f32_16x16x32_bf16 v[96:99], v[216:219], v[0:3], 0\n"
"v_accvgpr_read_b32 v196, a48\n"
"v_accvgpr_read_b32 v197, a49\n"
"v_mfma_f32_16x16x32_bf16 v[100:103], v[216:219], v[4:7], 0\n"
"v_accvgpr_read_b32 v198, a50\n"
"v_accvgpr_read_b32 v199, a51\n"
"v_mfma_f32_16x16x32_bf16 v[104:107], v[216:219], v[8:11], 0\n"
"v_mul_f32 v196, v196, v244\n"
"v_mul_f32 v197, v197, v244\n"
"v_mfma_f32_16x16x32_bf16 v[108:111], v[216:219], v[12:15], 0\n"
"v_mul_f32 v198, v198, v244\n"
"v_mul_f32 v199, v199, v244\n"
"v_mfma_f32_16x16x32_bf16 v[112:115], v[220:223], v[0:3], 0\n"
"ds_read_b64_tr_b16 v[208:209], v200 offset:42240\n"
"ds_read_b64_tr_b16 v[210:211], v200 offset:42496\n"
"v_mfma_f32_16x16x32_bf16 v[116:119], v[220:223], v[4:7], 0\n"
"v_cvt_pk_bf16_f32 v16, v192, v193\n"
"v_cvt_pk_bf16_f32 v17, v194, v195\n"
"v_mfma_f32_16x16x32_bf16 v[120:123], v[220:223], v[8:11], 0\n"
"v_cvt_pk_bf16_f32 v18, v196, v197\n"
"v_cvt_pk_bf16_f32 v19, v198, v199\n"
"v_mfma_f32_16x16x32_bf16 v[124:127], v[220:223], v[12:15], 0\n"
"v_accvgpr_read_b32 v192, a36\n"
"s_waitcnt lgkmcnt(5)\n"
"v_mfma_f32_16x16x32_bf16 v[160:163], v[224:227], v[16:19], v[160:163]\n"
"ds_read_b64_tr_b16 v[212:213], v200 offset:42272\n"
"ds_read_b64_tr_b16 v[214:215], v200 offset:42528\n"
"v_accvgpr_read_b32 v193, a37\n"
"v_accvgpr_read_b32 v194, a38\n"
"v_accvgpr_read_b32 v195, a39\n"
"v_mul_f32 v192, v192, v245\n"
"v_mul_f32 v193, v193, v245\n"
"v_mul_f32 v194, v194, v245\n"
"v_mul_f32 v195, v195, v245\n"
"v_accvgpr_read_b32 v196, a52\n"
"v_accvgpr_read_b32 v197, a53\n"
"v_accvgpr_read_b32 v198, a54\n"
"v_accvgpr_read_b32 v199, a55\n"
"v_mul_f32 v196, v196, v245\n"
"v_mul_f32 v197, v197, v245\n"
"v_mul_f32 v198, v198, v245\n"
"v_mul_f32 v199, v199, v245\n"
"v_cvt_pk_bf16_f32 v20, v192, v193\n"
"v_cvt_pk_bf16_f32 v21, v194, v195\n"
"v_cvt_pk_bf16_f32 v22, v196, v197\n"
"v_cvt_pk_bf16_f32 v23, v198, v199\n"
"s_nop 1\n"
"v_mfma_f32_16x16x32_bf16 v[164:167], v[224:227], v[20:23], v[164:167]\n"
"ds_write_b128 v206, v[16:19] offset:16896\n"
"v_accvgpr_read_b32 v192, a40\n"
"v_accvgpr_read_b32 v193, a41\n"
"v_accvgpr_read_b32 v194, a42\n"
"v_accvgpr_read_b32 v195, a43\n"
"v_mul_f32 v192, v192, v246\n"
"v_mul_f32 v193, v193, v246\n"
"v_mul_f32 v194, v194, v246\n"
"v_mul_f32 v195, v195, v246\n"
"v_accvgpr_read_b32 v196, a56\n"
"v_accvgpr_read_b32 v197, a57\n"
"v_accvgpr_read_b32 v198, a58\n"
"v_accvgpr_read_b32 v199, a59\n"
"v_mul_f32 v196, v196, v246\n"
"v_mul_f32 v197, v197, v246\n"
"v_mul_f32 v198, v198, v246\n"
"v_mul_f32 v199, v199, v246\n"
"v_cvt_pk_bf16_f32 v24, v192, v193\n"
"v_cvt_pk_bf16_f32 v25, v194, v195\n"
"v_cvt_pk_bf16_f32 v26, v196, v197\n"
"v_cvt_pk_bf16_f32 v27, v198, v199\n"
"s_nop 1\n"
"v_mfma_f32_16x16x32_bf16 v[168:171], v[224:227], v[24:27], v[168:171]\n"
"ds_write_b128 v206, v[20:23] offset:17920\n"
"v_accvgpr_read_b32 v192, a44\n"
"v_accvgpr_read_b32 v193, a45\n"
"v_accvgpr_read_b32 v194, a46\n"
"v_accvgpr_read_b32 v195, a47\n"
"v_mul_f32 v192, v192, v247\n"
"v_mul_f32 v193, v193, v247\n"
"v_mul_f32 v194, v194, v247\n"
"v_mul_f32 v195, v195, v247\n"
"v_accvgpr_read_b32 v196, a60\n"
"v_accvgpr_read_b32 v197, a61\n"
"v_accvgpr_read_b32 v198, a62\n"
"v_accvgpr_read_b32 v199, a63\n"
"v_mul_f32 v196, v196, v247\n"
"v_mul_f32 v197, v197, v247\n"
"v_mul_f32 v198, v198, v247\n"
"v_mul_f32 v199, v199, v247\n"
"v_cvt_pk_bf16_f32 v28, v192, v193\n"
"v_cvt_pk_bf16_f32 v29, v194, v195\n"
"v_cvt_pk_bf16_f32 v30, v196, v197\n"
"v_cvt_pk_bf16_f32 v31, v198, v199\n"
"s_nop 1\n"
"v_mfma_f32_16x16x32_bf16 v[172:175], v[224:227], v[28:31], v[172:175]\n"
"ds_write_b128 v206, v[24:27] offset:18944\n"
"v_mfma_f32_16x16x32_bf16 v[176:179], v[228:231], v[16:19], v[176:179]\n"
"s_waitcnt vmcnt(8) lgkmcnt(7)\n"
"s_barrier\n"
"ds_read_b64_tr_b16 v[216:217], v200 offset:0\n"
"ds_read_b64_tr_b16 v[218:219], v200 offset:256\n"
"v_mfma_f32_16x16x32_bf16 v[180:183], v[228:231], v[20:23], v[180:183]\n"
"s_add_u32 m0, s74, 0x1ce00\n"
"s_add_u32 s75, s83, 0x23000\n"
"v_mfma_f32_16x16x32_bf16 v[184:187], v[228:231], v[24:27], v[184:187]\n"
"buffer_load_dwordx4 v242, s[56:59], s75 offen lds\n"
"v_mfma_f32_16x16x32_bf16 v[188:191], v[228:231], v[28:31], v[188:191]\n"
"ds_read_b64_tr_b16 v[220:221], v200 offset:32\n"
"ds_read_b64_tr_b16 v[222:223], v200 offset:288\n"
"v_mfma_f32_16x16x32_bf16 v[96:99], v[232:235], v[16:19], v[96:99]\n"
"v_accvgpr_read_b32 v192, a64\n"
"s_add_u32 m0, s74, 0x1de80\n"
"v_mfma_f32_16x16x32_bf16 v[100:103], v[232:235], v[20:23], v[100:103]\n"
"buffer_load_dwordx4 v243, s[56:59], s75 offen lds\n"
"v_mfma_f32_16x16x32_bf16 v[104:107], v[232:235], v[24:27], v[104:107]\n"
"ds_write_b128 v206, v[28:31] offset:19968\n"
"v_mfma_f32_16x16x32_bf16 v[108:111], v[232:235], v[28:31], v[108:111]\n"
"s_add_u32 m0, s74, 0x1ef00\n"
"s_add_u32 s75, s82, 0x2a000\n"
"v_mfma_f32_16x16x32_bf16 v[112:115], v[236:239], v[16:19], v[112:115]\n"
"buffer_load_dwordx4 v242, s[56:59], s75 offen lds\n"
"v_mfma_f32_16x16x32_bf16 v[116:119], v[236:239], v[20:23], v[116:119]\n"
"ds_read_b128 a[0:3], v207 offset:0\n"
"ds_read_b128 a[4:7], v207 offset:1024\n"
"v_mfma_f32_16x16x32_bf16 v[120:123], v[236:239], v[24:27], v[120:123]\n"
"ds_read_b128 a[8:11], v207 offset:2048\n"
"s_add_u32 m0, s74, 0x1ff80\n"
"v_mfma_f32_16x16x32_bf16 v[124:127], v[236:239], v[28:31], v[124:127]\n"
"buffer_load_dwordx4 v243, s[56:59], s75 offen lds\n"
"v_accvgpr_read_b32 v193, a65\n"
"v_accvgpr_read_b32 v194, a66\n"
"v_accvgpr_read_b32 v195, a67\n"
"v_mul_f32 v192, v192, v244\n"
"v_mul_f32 v193, v193, v244\n"
"v_mul_f32 v194, v194, v244\n"
"v_mul_f32 v195, v195, v244\n"
"v_accvgpr_read_b32 v196, a80\n"
"v_accvgpr_read_b32 v197, a81\n"
"v_accvgpr_read_b32 v198, a82\n"
"v_accvgpr_read_b32 v199, a83\n"
"v_mul_f32 v196, v196, v244\n"
"v_mul_f32 v197, v197, v244\n"
"v_mul_f32 v198, v198, v244\n"
"v_mul_f32 v199, v199, v244\n"
"v_cvt_pk_bf16_f32 v64, v192, v193\n"
"v_cvt_pk_bf16_f32 v65, v194, v195\n"
"v_cvt_pk_bf16_f32 v66, v196, v197\n"
"v_cvt_pk_bf16_f32 v67, v198, v199\n"
"s_waitcnt lgkmcnt(4)\n"
"s_nop 1\n"
"v_mfma_f32_16x16x32_bf16 v[160:163], v[208:211], v[64:67], v[160:163]\n"
"ds_read_b128 a[12:15], v207 offset:3072\n"
"v_accvgpr_read_b32 v192, a68\n"
"v_accvgpr_read_b32 v193, a69\n"
"v_accvgpr_read_b32 v194, a70\n"
"v_accvgpr_read_b32 v195, a71\n"
"v_mul_f32 v192, v192, v245\n"
"v_mul_f32 v193, v193, v245\n"
"v_mul_f32 v194, v194, v245\n"
"v_mul_f32 v195, v195, v245\n"
"v_accvgpr_read_b32 v196, a84\n"
"v_accvgpr_read_b32 v197, a85\n"
"v_accvgpr_read_b32 v198, a86\n"
"v_accvgpr_read_b32 v199, a87\n"
"v_mul_f32 v196, v196, v245\n"
"v_mul_f32 v197, v197, v245\n"
"v_mul_f32 v198, v198, v245\n"
"v_mul_f32 v199, v199, v245\n"
"v_cvt_pk_bf16_f32 v68, v192, v193\n"
"v_cvt_pk_bf16_f32 v69, v194, v195\n"
"v_cvt_pk_bf16_f32 v70, v196, v197\n"
"v_cvt_pk_bf16_f32 v71, v198, v199\n"
"s_nop 1\n"
"v_mfma_f32_16x16x32_bf16 v[164:167], v[208:211], v[68:71], v[164:167]\n"
"ds_read_b64_tr_b16 v[224:225], v205 offset:4096\n"
"ds_read_b64_tr_b16 v[226:227], v205 offset:4352\n"
"v_accvgpr_read_b32 v192, a72\n"
"v_accvgpr_read_b32 v193, a73\n"
"v_accvgpr_read_b32 v194, a74\n"
"v_accvgpr_read_b32 v195, a75\n"
"v_mul_f32 v192, v192, v246\n"
"v_mul_f32 v193, v193, v246\n"
"v_mul_f32 v194, v194, v246\n"
"v_mul_f32 v195, v195, v246\n"
"v_accvgpr_read_b32 v196, a88\n"
"v_accvgpr_read_b32 v197, a89\n"
"v_accvgpr_read_b32 v198, a90\n"
"v_accvgpr_read_b32 v199, a91\n"
"v_mul_f32 v196, v196, v246\n"
"v_mul_f32 v197, v197, v246\n"
"v_mul_f32 v198, v198, v246\n"
"v_mul_f32 v199, v199, v246\n"
"v_cvt_pk_bf16_f32 v72, v192, v193\n"
"v_cvt_pk_bf16_f32 v73, v194, v195\n"
"v_cvt_pk_bf16_f32 v74, v196, v197\n"
"v_cvt_pk_bf16_f32 v75, v198, v199\n"
"s_nop 1\n"
"v_mfma_f32_16x16x32_bf16 v[168:171], v[208:211], v[72:75], v[168:171]\n"
"ds_read_b64_tr_b16 v[228:229], v205 offset:4128\n"
"ds_read_b64_tr_b16 v[230:231], v205 offset:4384\n"
"v_accvgpr_read_b32 v192, a76\n"
"v_accvgpr_read_b32 v193, a77\n"
"v_accvgpr_read_b32 v194, a78\n"
"v_accvgpr_read_b32 v195, a79\n"
"v_mul_f32 v192, v192, v247\n"
"v_mul_f32 v193, v193, v247\n"
"v_mul_f32 v194, v194, v247\n"
"v_mul_f32 v195, v195, v247\n"
"v_accvgpr_read_b32 v196, a92\n"
"v_accvgpr_read_b32 v197, a93\n"
"v_accvgpr_read_b32 v198, a94\n"
"v_accvgpr_read_b32 v199, a95\n"
"v_mul_f32 v196, v196, v247\n"
"v_mul_f32 v197, v197, v247\n"
"v_mul_f32 v198, v198, v247\n"
"v_mul_f32 v199, v199, v247\n"
"v_cvt_pk_bf16_f32 v76, v192, v193\n"
"v_cvt_pk_bf16_f32 v77, v194, v195\n"
"v_cvt_pk_bf16_f32 v78, v196, v197\n"
"v_cvt_pk_bf16_f32 v79, v198, v199\n"
"s_nop 1\n"
"v_mfma_f32_16x16x32_bf16 v[172:175], v[208:211], v[76:79], v[172:175]\n"
"ds_read_b64_tr_b16 v[232:233], v205 offset:12544\n"
"ds_read_b64_tr_b16 v[234:235], v205 offset:12800\n"
"v_mfma_f32_16x16x32_bf16 v[176:179], v[212:215], v[64:67], v[176:179]\n"
"ds_read_b64_tr_b16 v[236:237], v205 offset:12576\n"
"ds_read_b64_tr_b16 v[238:239], v205 offset:12832\n"
"v_mfma_f32_16x16x32_bf16 v[180:183], v[212:215], v[68:71], v[180:183]\n"
"v_accvgpr_read_b32 v192, a96\n"
"v_accvgpr_read_b32 v193, a97\n"
"v_mfma_f32_16x16x32_bf16 v[184:187], v[212:215], v[72:75], v[184:187]\n"
"v_accvgpr_read_b32 v194, a98\n"
"v_accvgpr_read_b32 v195, a99\n"
"v_mfma_f32_16x16x32_bf16 v[188:191], v[212:215], v[76:79], v[188:191]\n"
"v_mul_f32 v192, v192, v244\n"
"v_mul_f32 v193, v193, v244\n"
"v_mfma_f32_16x16x32_bf16 v[96:99], v[216:219], v[64:67], v[96:99]\n"
"s_add_u32 s28, s28, 1\n"
"v_mov_b32 v252, s28\n"
"v_mfma_f32_16x16x32_bf16 v[100:103], v[216:219], v[68:71], v[100:103]\n"
"s_waitcnt vmcnt(4)\n"
"buffer_store_dword v252, off, s[68:71], s29 sc1\n"
"v_mfma_f32_16x16x32_bf16 v[104:107], v[216:219], v[72:75], v[104:107]\n"
"v_mul_f32 v194, v194, v244\n"
"v_mul_f32 v195, v195, v244\n"
"v_mfma_f32_16x16x32_bf16 v[108:111], v[216:219], v[76:79], v[108:111]\n"
"v_accvgpr_read_b32 v196, a112\n"
"v_accvgpr_read_b32 v197, a113\n"
"v_mfma_f32_16x16x32_bf16 v[112:115], v[220:223], v[64:67], v[112:115]\n"
"ds_read_b64_tr_b16 v[208:209], v200 offset:8448\n"
"ds_read_b64_tr_b16 v[210:211], v200 offset:8704\n"
"v_mfma_f32_16x16x32_bf16 v[116:119], v[220:223], v[68:71], v[116:119]\n"
"v_accvgpr_read_b32 v198, a114\n"
"v_accvgpr_read_b32 v199, a115\n"
"v_mfma_f32_16x16x32_bf16 v[120:123], v[220:223], v[72:75], v[120:123]\n"
"v_mul_f32 v196, v196, v244\n"
"v_mul_f32 v197, v197, v244\n"
"v_mfma_f32_16x16x32_bf16 v[124:127], v[220:223], v[76:79], v[124:127]\n"
"v_mul_f32 v198, v198, v244\n"
"s_waitcnt lgkmcnt(2)\n"
"v_mfma_f32_16x16x32_bf16 v[160:163], v[224:227], a[0:3], v[160:163]\n"
"ds_read_b64_tr_b16 v[212:213], v200 offset:8480\n"
"ds_read_b64_tr_b16 v[214:215], v200 offset:8736\n"
"v_mfma_f32_16x16x32_bf16 v[164:167], v[224:227], a[4:7], v[164:167]\n"
"ds_read_b64_tr_b16 v[216:217], v200 offset:16896\n"
"ds_read_b64_tr_b16 v[218:219], v200 offset:17152\n"
"v_mfma_f32_16x16x32_bf16 v[168:171], v[224:227], a[8:11], v[168:171]\n"
"ds_read_b64_tr_b16 v[220:221], v200 offset:16928\n"
"ds_read_b64_tr_b16 v[222:223], v200 offset:17184\n"
"v_mfma_f32_16x16x32_bf16 v[172:175], v[224:227], a[12:15], v[172:175]\n"
"ds_write_b128 v206, v[64:67] offset:33792\n"
"v_mfma_f32_16x16x32_bf16 v[176:179], v[228:231], a[0:3], v[176:179]\n"
"ds_write_b128 v206, v[68:71] offset:34816\n"
"v_mfma_f32_16x16x32_bf16 v[180:183], v[228:231], a[4:7], v[180:183]\n"
"ds_write_b128 v206, v[72:75] offset:35840\n"
"v_mfma_f32_16x16x32_bf16 v[184:187], v[228:231], a[8:11], v[184:187]\n"
"ds_write_b128 v206, v[76:79] offset:36864\n"
"v_mfma_f32_16x16x32_bf16 v[188:191], v[228:231], a[12:15], v[188:191]\n"
"v_mul_f32 v199, v199, v244\n"
"v_cvt_pk_bf16_f32 v80, v192, v193\n"
"v_mfma_f32_16x16x32_bf16 v[96:99], v[232:235], a[0:3], v[96:99]\n"
"v_cvt_pk_bf16_f32 v81, v194, v195\n"
"v_cvt_pk_bf16_f32 v82, v196, v197\n"
"v_mfma_f32_16x16x32_bf16 v[100:103], v[232:235], a[4:7], v[100:103]\n"
"v_cvt_pk_bf16_f32 v83, v198, v199\n"
"v_accvgpr_read_b32 v192, a100\n"
"v_mfma_f32_16x16x32_bf16 v[104:107], v[232:235], a[8:11], v[104:107]\n"
"v_accvgpr_read_b32 v193, a101\n"
"v_accvgpr_read_b32 v194, a102\n"
"v_mfma_f32_16x16x32_bf16 v[108:111], v[232:235], a[12:15], v[108:111]\n"
"v_accvgpr_read_b32 v195, a103\n"
"v_mul_f32 v192, v192, v245\n"
"v_mfma_f32_16x16x32_bf16 v[112:115], v[236:239], a[0:3], v[112:115]\n"
"v_mul_f32 v193, v193, v245\n"
"v_mul_f32 v194, v194, v245\n"
"v_mfma_f32_16x16x32_bf16 v[116:119], v[236:239], a[4:7], v[116:119]\n"
"v_mul_f32 v195, v195, v245\n"
"v_accvgpr_read_b32 v196, a116\n"
"v_mfma_f32_16x16x32_bf16 v[120:123], v[236:239], a[8:11], v[120:123]\n"
"v_accvgpr_read_b32 v197, a117\n"
"v_accvgpr_read_b32 v198, a118\n"
"v_mfma_f32_16x16x32_bf16 v[124:127], v[236:239], a[12:15], v[124:127]\n"
"v_accvgpr_read_b32 v199, a119\n"
"s_waitcnt lgkmcnt(4)\n"
"v_mfma_f32_16x16x32_bf16 v[160:163], v[208:211], v[80:83], v[160:163]\n"
"v_mul_f32 v196, v196, v245\n"
"v_mul_f32 v197, v197, v245\n"
"v_mul_f32 v198, v198, v245\n"
"v_mul_f32 v199, v199, v245\n"
"v_cvt_pk_bf16_f32 v84, v192, v193\n"
"v_cvt_pk_bf16_f32 v85, v194, v195\n"
"v_cvt_pk_bf16_f32 v86, v196, v197\n"
"v_cvt_pk_bf16_f32 v87, v198, v199\n"
"s_nop 1\n"
"v_mfma_f32_16x16x32_bf16 v[164:167], v[208:211], v[84:87], v[164:167]\n"
"v_accvgpr_read_b32 v192, a104\n"
"v_accvgpr_read_b32 v193, a105\n"
"v_accvgpr_read_b32 v194, a106\n"
"v_accvgpr_read_b32 v195, a107\n"
"v_mul_f32 v192, v192, v246\n"
"v_mul_f32 v193, v193, v246\n"
"v_mul_f32 v194, v194, v246\n"
"v_mul_f32 v195, v195, v246\n"
"v_accvgpr_read_b32 v196, a120\n"
"v_accvgpr_read_b32 v197, a121\n"
"v_accvgpr_read_b32 v198, a122\n"
"v_accvgpr_read_b32 v199, a123\n"
"v_mul_f32 v196, v196, v246\n"
"v_mul_f32 v197, v197, v246\n"
"v_mul_f32 v198, v198, v246\n"
"v_mul_f32 v199, v199, v246\n"
"v_cvt_pk_bf16_f32 v88, v192, v193\n"
"v_cvt_pk_bf16_f32 v89, v194, v195\n"
"v_cvt_pk_bf16_f32 v90, v196, v197\n"
"v_cvt_pk_bf16_f32 v91, v198, v199\n"
"s_nop 1\n"
"v_mfma_f32_16x16x32_bf16 v[168:171], v[208:211], v[88:91], v[168:171]\n"
"v_accvgpr_read_b32 v192, a108\n"
"v_accvgpr_read_b32 v193, a109\n"
"v_accvgpr_read_b32 v194, a110\n"
"v_accvgpr_read_b32 v195, a111\n"
"v_mul_f32 v192, v192, v247\n"
"v_mul_f32 v193, v193, v247\n"
"v_mul_f32 v194, v194, v247\n"
"v_mul_f32 v195, v195, v247\n"
"v_accvgpr_read_b32 v196, a124\n"
"v_accvgpr_read_b32 v197, a125\n"
"v_accvgpr_read_b32 v198, a126\n"
"v_accvgpr_read_b32 v199, a127\n"
"v_mul_f32 v196, v196, v247\n"
"v_mul_f32 v197, v197, v247\n"
"v_mul_f32 v198, v198, v247\n"
"v_mul_f32 v199, v199, v247\n"
"v_cvt_pk_bf16_f32 v92, v192, v193\n"
"v_cvt_pk_bf16_f32 v93, v194, v195\n"
"v_cvt_pk_bf16_f32 v94, v196, v197\n"
"v_cvt_pk_bf16_f32 v95, v198, v199\n"
"s_nop 1\n"
"v_mfma_f32_16x16x32_bf16 v[172:175], v[208:211], v[92:95], v[172:175]\n"
"v_accvgpr_read_b32 v192, a128\n"
"v_accvgpr_read_b32 v193, a129\n"
"v_mfma_f32_16x16x32_bf16 v[176:179], v[212:215], v[80:83], v[176:179]\n"
"s_waitcnt lgkmcnt(0)\n"
"s_barrier\n"
"ds_read_b128 a[16:19], v207 offset:16896\n"
"v_mfma_f32_16x16x32_bf16 v[180:183], v[212:215], v[84:87], v[180:183]\n"
"s_add_u32 m0, s74, 0x21000\n"
"s_add_u32 s75, s83, 0x2a000\n"
"v_mfma_f32_16x16x32_bf16 v[184:187], v[212:215], v[88:91], v[184:187]\n"
"buffer_load_dwordx4 v242, s[56:59], s75 offen lds\n"
"v_mfma_f32_16x16x32_bf16 v[188:191], v[212:215], v[92:95], v[188:191]\n"
"ds_read_b128 a[20:23], v207 offset:17920\n"
"ds_read_b128 a[24:27], v207 offset:18944\n"
"v_mfma_f32_16x16x32_bf16 v[96:99], v[216:219], v[80:83], v[96:99]\n"
"ds_read_b128 a[28:31], v207 offset:19968\n"
"s_add_u32 m0, s74, 0x22080\n"
"v_mfma_f32_16x16x32_bf16 v[100:103], v[216:219], v[84:87], v[100:103]\n"
"buffer_load_dwordx4 v243, s[56:59], s75 offen lds\n"
"ds_read_b64_tr_b16 v[224:225], v205 offset:20992\n"
"ds_read_b64_tr_b16 v[226:227], v205 offset:21248\n"
"ds_read_b64_tr_b16 v[228:229], v205 offset:21024\n"
"ds_read_b64_tr_b16 v[230:231], v205 offset:21280\n"
"ds_read_b64_tr_b16 v[232:233], v203 offset:33792\n"
"ds_read_b64_tr_b16 v[234:235], v203 offset:34048\n"
"ds_read_b64_tr_b16 v[236:237], v203 offset:33824\n"
"ds_read_b64_tr_b16 v[238:239], v203 offset:34080\n"
"v_mfma_f32_16x16x32_bf16 v[104:107], v[216:219], v[88:91], v[104:107]\n"
"ds_write_b128 v206, v[80:83] offset:0\n"
"v_mfma_f32_16x16x32_bf16 v[108:111], v[216:219], v[92:95], v[108:111]\n"
"s_add_u32 m0, s74, 0x23100\n"
"s_add_u32 s75, s82, 0x31000\n"
"v_mfma_f32_16x16x32_bf16 v[112:115], v[220:223], v[80:83], v[112:115]\n"
"buffer_load_dwordx4 v242, s[56:59], s75 offen lds\n"
"v_mfma_f32_16x16x32_bf16 v[116:119], v[220:223], v[84:87], v[116:119]\n"
"ds_read_b64_tr_b16 v[208:209], v200 offset:25344\n"
"ds_read_b64_tr_b16 v[210:211], v200 offset:25600\n"
"v_mfma_f32_16x16x32_bf16 v[120:123], v[220:223], v[88:91], v[120:123]\n"
"v_accvgpr_read_b32 v194, a130\n"
"s_add_u32 m0, s74, 0x24180\n"
"v_mfma_f32_16x16x32_bf16 v[124:127], v[220:223], v[92:95], v[124:127]\n"
"buffer_load_dwordx4 v243, s[56:59], s75 offen lds\n"
"s_waitcnt lgkmcnt(3)\n"
"v_mfma_f32_16x16x32_bf16 v[160:163], v[224:227], a[16:19], v[160:163]\n"
"ds_read_b64_tr_b16 v[212:213], v200 offset:25376\n"
"ds_read_b64_tr_b16 v[214:215], v200 offset:25632\n"
"v_mfma_f32_16x16x32_bf16 v[164:167], v[224:227], a[20:23], v[164:167]\n"
"ds_read_b64_tr_b16 v[216:217], v200 offset:50688\n"
"ds_read_b64_tr_b16 v[218:219], v200 offset:50944\n"
"v_mfma_f32_16x16x32_bf16 v[168:171], v[224:227], a[24:27], v[168:171]\n"
"ds_read_b64_tr_b16 v[220:221], v200 offset:50720\n"
"ds_read_b64_tr_b16 v[222:223], v200 offset:50976\n"
"v_mfma_f32_16x16x32_bf16 v[172:175], v[224:227], a[28:31], v[172:175]\n"
"ds_write_b128 v206, v[84:87] offset:1024\n"
"v_mfma_f32_16x16x32_bf16 v[176:179], v[228:231], a[16:19], v[176:179]\n"
"ds_write_b128 v206, v[88:91] offset:2048\n"
"v_mfma_f32_16x16x32_bf16 v[180:183], v[228:231], a[20:23], v[180:183]\n"
"ds_write_b128 v206, v[92:95] offset:3072\n"
"v_mfma_f32_16x16x32_bf16 v[184:187], v[228:231], a[24:27], v[184:187]\n"
"v_accvgpr_read_b32 v195, a131\n"
"v_mul_f32 v192, v192, v244\n"
"v_mfma_f32_16x16x32_bf16 v[188:191], v[228:231], a[28:31], v[188:191]\n"
"v_mul_f32 v193, v193, v244\n"
"v_mul_f32 v194, v194, v244\n"
"v_mfma_f32_16x16x32_bf16 v[96:99], v[232:235], a[16:19], v[96:99]\n"
"v_mul_f32 v195, v195, v244\n"
"v_accvgpr_read_b32 v196, a144\n"
"v_mfma_f32_16x16x32_bf16 v[100:103], v[232:235], a[20:23], v[100:103]\n"
"v_accvgpr_read_b32 v197, a145\n"
"v_accvgpr_read_b32 v198, a146\n"
"v_mfma_f32_16x16x32_bf16 v[104:107], v[232:235], a[24:27], v[104:107]\n"
"v_accvgpr_read_b32 v199, a147\n"
"v_mul_f32 v196, v196, v244\n"
"v_mfma_f32_16x16x32_bf16 v[108:111], v[232:235], a[28:31], v[108:111]\n"
"v_mul_f32 v197, v197, v244\n"
"v_mul_f32 v198, v198, v244\n"
"v_mfma_f32_16x16x32_bf16 v[112:115], v[236:239], a[16:19], v[112:115]\n"
"ds_read_b128 a[32:35], v207 offset:33792\n"
"ds_read_b128 a[36:39], v207 offset:34816\n"
"v_mfma_f32_16x16x32_bf16 v[116:119], v[236:239], a[20:23], v[116:119]\n"
"ds_read_b128 a[40:43], v207 offset:35840\n"
"v_mul_f32 v199, v199, v244\n"
"v_mfma_f32_16x16x32_bf16 v[120:123], v[236:239], a[24:27], v[120:123]\n"
"v_cvt_pk_bf16_f32 v128, v192, v193\n"
"v_cvt_pk_bf16_f32 v129, v194, v195\n"
"v_mfma_f32_16x16x32_bf16 v[124:127], v[236:239], a[28:31], v[124:127]\n"
"v_cvt_pk_bf16_f32 v130, v196, v197\n"
"v_cvt_pk_bf16_f32 v131, v198, v199\n"
"s_waitcnt lgkmcnt(6)\n"
"s_nop 1\n"
"v_mfma_f32_16x16x32_bf16 v[160:163], v[208:211], v[128:131], v[160:163]\n"
"ds_read_b128 a[44:47], v207 offset:36864\n"
"v_accvgpr_read_b32 v192, a132\n"
"v_accvgpr_read_b32 v193, a133\n"
"v_accvgpr_read_b32 v194, a134\n"
"v_accvgpr_read_b32 v195, a135\n"
"v_mul_f32 v192, v192, v245\n"
"v_mul_f32 v193, v193, v245\n"
"v_mul_f32 v194, v194, v245\n"
"v_mul_f32 v195, v195, v245\n"
"v_accvgpr_read_b32 v196, a148\n"
"v_accvgpr_read_b32 v197, a149\n"
"v_accvgpr_read_b32 v198, a150\n"
"v_accvgpr_read_b32 v199, a151\n"
"v_mul_f32 v196, v196, v245\n"
"v_mul_f32 v197, v197, v245\n"
"v_mul_f32 v198, v198, v245\n"
"v_mul_f32 v199, v199, v245\n"
"v_cvt_pk_bf16_f32 v132, v192, v193\n"
"v_cvt_pk_bf16_f32 v133, v194, v195\n"
"v_cvt_pk_bf16_f32 v134, v196, v197\n"
"v_cvt_pk_bf16_f32 v135, v198, v199\n"
"s_nop 1\n"
"v_mfma_f32_16x16x32_bf16 v[164:167], v[208:211], v[132:135], v[164:167]\n"
"ds_read_b64_tr_b16 v[224:225], v203 offset:42240\n"
"ds_read_b64_tr_b16 v[226:227], v203 offset:42496\n"
"v_accvgpr_read_b32 v192, a136\n"
"v_accvgpr_read_b32 v193, a137\n"
"v_accvgpr_read_b32 v194, a138\n"
"v_accvgpr_read_b32 v195, a139\n"
"v_mul_f32 v192, v192, v246\n"
"v_mul_f32 v193, v193, v246\n"
"v_mul_f32 v194, v194, v246\n"
"v_mul_f32 v195, v195, v246\n"
"v_accvgpr_read_b32 v196, a152\n"
"v_accvgpr_read_b32 v197, a153\n"
"v_accvgpr_read_b32 v198, a154\n"
"v_accvgpr_read_b32 v199, a155\n"
"v_mul_f32 v196, v196, v246\n"
"v_mul_f32 v197, v197, v246\n"
"v_mul_f32 v198, v198, v246\n"
"v_mul_f32 v199, v199, v246\n"
"v_cvt_pk_bf16_f32 v136, v192, v193\n"
"v_cvt_pk_bf16_f32 v137, v194, v195\n"
"v_cvt_pk_bf16_f32 v138, v196, v197\n"
"v_cvt_pk_bf16_f32 v139, v198, v199\n"
"s_nop 1\n"
"v_mfma_f32_16x16x32_bf16 v[168:171], v[208:211], v[136:139], v[168:171]\n"
"ds_read_b64_tr_b16 v[228:229], v203 offset:42272\n"
"ds_read_b64_tr_b16 v[230:231], v203 offset:42528\n"
"v_accvgpr_read_b32 v192, a140\n"
"v_accvgpr_read_b32 v193, a141\n"
"v_accvgpr_read_b32 v194, a142\n"
"v_accvgpr_read_b32 v195, a143\n"
"v_mul_f32 v192, v192, v247\n"
"v_mul_f32 v193, v193, v247\n"
"v_mul_f32 v194, v194, v247\n"
"v_mul_f32 v195, v195, v247\n"
"v_accvgpr_read_b32 v196, a156\n"
"v_accvgpr_read_b32 v197, a157\n"
"v_accvgpr_read_b32 v198, a158\n"
"v_accvgpr_read_b32 v199, a159\n"
"v_mul_f32 v196, v196, v247\n"
"v_mul_f32 v197, v197, v247\n"
"v_mul_f32 v198, v198, v247\n"
"v_mul_f32 v199, v199, v247\n"
"v_cvt_pk_bf16_f32 v140, v192, v193\n"
"v_cvt_pk_bf16_f32 v141, v194, v195\n"
"v_cvt_pk_bf16_f32 v142, v196, v197\n"
"v_cvt_pk_bf16_f32 v143, v198, v199\n"
"s_nop 1\n"
"v_mfma_f32_16x16x32_bf16 v[172:175], v[208:211], v[140:143], v[172:175]\n"
"ds_read_b64_tr_b16 v[232:233], v203 offset:0\n"
"ds_read_b64_tr_b16 v[234:235], v203 offset:256\n"
"v_mfma_f32_16x16x32_bf16 v[176:179], v[212:215], v[128:131], v[176:179]\n"
"ds_read_b64_tr_b16 v[236:237], v203 offset:32\n"
"ds_read_b64_tr_b16 v[238:239], v203 offset:288\n"
"v_mfma_f32_16x16x32_bf16 v[180:183], v[212:215], v[132:135], v[180:183]\n"
"v_accvgpr_read_b32 v192, a160\n"
"v_accvgpr_read_b32 v193, a161\n"
"v_mfma_f32_16x16x32_bf16 v[184:187], v[212:215], v[136:139], v[184:187]\n"
"v_accvgpr_read_b32 v194, a162\n"
"v_accvgpr_read_b32 v195, a163\n"
"v_mfma_f32_16x16x32_bf16 v[188:191], v[212:215], v[140:143], v[188:191]\n"
"v_mul_f32 v192, v192, v244\n"
"v_mul_f32 v193, v193, v244\n"
"v_mfma_f32_16x16x32_bf16 v[96:99], v[216:219], v[128:131], v[96:99]\n"
"v_mul_f32 v194, v194, v244\n"
"v_mul_f32 v195, v195, v244\n"
"v_mfma_f32_16x16x32_bf16 v[100:103], v[216:219], v[132:135], v[100:103]\n"
"v_accvgpr_read_b32 v196, a176\n"
"v_accvgpr_read_b32 v197, a177\n"
"v_mfma_f32_16x16x32_bf16 v[104:107], v[216:219], v[136:139], v[104:107]\n"
"v_accvgpr_read_b32 v198, a178\n"
"v_accvgpr_read_b32 v199, a179\n"
"v_mfma_f32_16x16x32_bf16 v[108:111], v[216:219], v[140:143], v[108:111]\n"
"v_mul_f32 v196, v196, v244\n"
"v_mul_f32 v197, v197, v244\n"
"v_mfma_f32_16x16x32_bf16 v[112:115], v[220:223], v[128:131], v[112:115]\n"
"v_mul_f32 v198, v198, v244\n"
"v_mul_f32 v199, v199, v244\n"
"v_mfma_f32_16x16x32_bf16 v[116:119], v[220:223], v[132:135], v[116:119]\n"
"v_cvt_pk_bf16_f32 v144, v192, v193\n"
"v_cvt_pk_bf16_f32 v145, v194, v195\n"
"v_mfma_f32_16x16x32_bf16 v[120:123], v[220:223], v[136:139], v[120:123]\n"
"v_cvt_pk_bf16_f32 v146, v196, v197\n"
"v_cvt_pk_bf16_f32 v147, v198, v199\n"
"v_mfma_f32_16x16x32_bf16 v[124:127], v[220:223], v[140:143], v[124:127]\n"
"v_accvgpr_read_b32 v192, a164\n"
"s_waitcnt lgkmcnt(0)\n"
"v_mfma_f32_16x16x32_bf16 v[160:163], v[224:227], a[32:35], v[160:163]\n"
"ds_read_b64_tr_b16 v[208:209], v200 offset:59136\n"
"ds_read_b64_tr_b16 v[210:211], v200 offset:59392\n"
"v_mfma_f32_16x16x32_bf16 v[164:167], v[224:227], a[36:39], v[164:167]\n"
"ds_read_b64_tr_b16 v[212:213], v200 offset:59168\n"
"ds_read_b64_tr_b16 v[214:215], v200 offset:59424\n"
"v_mfma_f32_16x16x32_bf16 v[168:171], v[224:227], a[40:43], v[168:171]\n"
"v_accvgpr_read_b32 v193, a165\n"
"v_accvgpr_read_b32 v194, a166\n"
"v_mfma_f32_16x16x32_bf16 v[172:175], v[224:227], a[44:47], v[172:175]\n"
"v_accvgpr_read_b32 v195, a167\n"
"v_mul_f32 v192, v192, v245\n"
"v_mfma_f32_16x16x32_bf16 v[176:179], v[228:231], a[32:35], v[176:179]\n"
"s_waitcnt vmcnt(7)\n"
"s_barrier\n"
"ds_read_b64_tr_b16 v[216:217], v201 offset:52736\n"
"ds_read_b64_tr_b16 v[218:219], v201 offset:52992\n"
"v_mfma_f32_16x16x32_bf16 v[180:183], v[228:231], a[36:39], v[180:183]\n"
"s_add_u32 m0, s74, 0x25200\n"
"s_add_u32 s75, s83, 0x31000\n"
"v_mfma_f32_16x16x32_bf16 v[184:187], v[228:231], a[40:43], v[184:187]\n"
"buffer_load_dwordx4 v242, s[56:59], s75 offen lds\n"
"v_mfma_f32_16x16x32_bf16 v[188:191], v[228:231], a[44:47], v[188:191]\n"
"ds_read_b64_tr_b16 v[220:221], v201 offset:52768\n"
"ds_read_b64_tr_b16 v[222:223], v201 offset:53024\n"
"v_mfma_f32_16x16x32_bf16 v[96:99], v[232:235], a[32:35], v[96:99]\n"
"v_mul_f32 v193, v193, v245\n"
"s_add_u32 m0, s74, 0x26280\n"
"v_mfma_f32_16x16x32_bf16 v[100:103], v[232:235], a[36:39], v[100:103]\n"
"buffer_load_dwordx4 v243, s[56:59], s75 offen lds\n"
"v_mfma_f32_16x16x32_bf16 v[104:107], v[232:235], a[40:43], v[104:107]\n"
"ds_write_b128 v206, v[128:131] offset:16896\n"
"v_mfma_f32_16x16x32_bf16 v[108:111], v[232:235], a[44:47], v[108:111]\n"
"s_add_u32 m0, s74, 0x0\n"
"s_add_u32 s75, s84, 0x0\n"
"v_mfma_f32_16x16x32_bf16 v[112:115], v[236:239], a[32:35], v[112:115]\n"
"buffer_load_dwordx4 v242, s[56:59], s75 offen lds\n"
"v_mfma_f32_16x16x32_bf16 v[116:119], v[236:239], a[36:39], v[116:119]\n"
"ds_read_b128 a[48:51], v207 offset:0\n"
"ds_read_b128 a[52:55], v207 offset:1024\n"
"v_mfma_f32_16x16x32_bf16 v[120:123], v[236:239], a[40:43], v[120:123]\n"
"ds_read_b128 a[56:59], v207 offset:2048\n"
"s_add_u32 m0, s74, 0x1080\n"
"v_mfma_f32_16x16x32_bf16 v[124:127], v[236:239], a[44:47], v[124:127]\n"
"buffer_load_dwordx4 v243, s[56:59], s75 offen lds\n"
"s_waitcnt lgkmcnt(4)\n"
"v_mfma_f32_16x16x32_bf16 v[160:163], v[208:211], v[144:147], v[160:163]\n"
"ds_read_b128 a[60:63], v207 offset:3072\n"
"v_mul_f32 v194, v194, v245\n"
"v_mul_f32 v195, v195, v245\n"
"v_accvgpr_read_b32 v196, a180\n"
"v_accvgpr_read_b32 v197, a181\n"
"v_accvgpr_read_b32 v198, a182\n"
"v_accvgpr_read_b32 v199, a183\n"
"v_mul_f32 v196, v196, v245\n"
"v_mul_f32 v197, v197, v245\n"
"v_mul_f32 v198, v198, v245\n"
"v_mul_f32 v199, v199, v245\n"
"v_cvt_pk_bf16_f32 v148, v192, v193\n"
"v_cvt_pk_bf16_f32 v149, v194, v195\n"
"v_cvt_pk_bf16_f32 v150, v196, v197\n"
"v_cvt_pk_bf16_f32 v151, v198, v199\n"
"s_nop 1\n"
"v_mfma_f32_16x16x32_bf16 v[164:167], v[208:211], v[148:151], v[164:167]\n"
"s_add_u32 m0, s74, 0x8400\n"
"s_add_u32 s75, s84, 0x7000\n"
"v_accvgpr_read_b32 v192, a168\n"
"v_accvgpr_read_b32 v193, a169\n"
"v_accvgpr_read_b32 v194, a170\n"
"v_accvgpr_read_b32 v195, a171\n"
"v_mul_f32 v192, v192, v246\n"
"v_mul_f32 v193, v193, v246\n"
"v_mul_f32 v194, v194, v246\n"
"v_mul_f32 v195, v195, v246\n"
"v_accvgpr_read_b32 v196, a184\n"
"v_accvgpr_read_b32 v197, a185\n"
"v_accvgpr_read_b32 v198, a186\n"
"v_accvgpr_read_b32 v199, a187\n"
"v_mul_f32 v196, v196, v246\n"
"v_mul_f32 v197, v197, v246\n"
"v_mul_f32 v198, v198, v246\n"
"v_mul_f32 v199, v199, v246\n"
"v_cvt_pk_bf16_f32 v152, v192, v193\n"
"v_cvt_pk_bf16_f32 v153, v194, v195\n"
"v_cvt_pk_bf16_f32 v154, v196, v197\n"
"v_cvt_pk_bf16_f32 v155, v198, v199\n"
"s_nop 1\n"
"v_mfma_f32_16x16x32_bf16 v[168:171], v[208:211], v[152:155], v[168:171]\n"
"buffer_load_dwordx4 v242, s[56:59], s75 offen lds\n"
"v_accvgpr_read_b32 v192, a172\n"
"v_accvgpr_read_b32 v193, a173\n"
"v_accvgpr_read_b32 v194, a174\n"
"v_accvgpr_read_b32 v195, a175\n"
"v_mul_f32 v192, v192, v247\n"
"v_mul_f32 v193, v193, v247\n"
"v_mul_f32 v194, v194, v247\n"
"v_mul_f32 v195, v195, v247\n"
"v_accvgpr_read_b32 v196, a188\n"
"v_accvgpr_read_b32 v197, a189\n"
"v_accvgpr_read_b32 v198, a190\n"
"v_accvgpr_read_b32 v199, a191\n"
"v_mul_f32 v196, v196, v247\n"
"v_mul_f32 v197, v197, v247\n"
"v_mul_f32 v198, v198, v247\n"
"v_mul_f32 v199, v199, v247\n"
"v_cvt_pk_bf16_f32 v156, v192, v193\n"
"v_cvt_pk_bf16_f32 v157, v194, v195\n"
"v_cvt_pk_bf16_f32 v158, v196, v197\n"
"v_cvt_pk_bf16_f32 v159, v198, v199\n"
"s_nop 1\n"
"v_mfma_f32_16x16x32_bf16 v[172:175], v[208:211], v[156:159], v[172:175]\n"
"ds_read_b64_tr_b16 v[224:225], v203 offset:8448\n"
"ds_read_b64_tr_b16 v[226:227], v203 offset:8704\n"
"v_mfma_f32_16x16x32_bf16 v[176:179], v[212:215], v[144:147], v[176:179]\n"
"v_accvgpr_read_b32 v192, a192\n"
"s_add_u32 m0, s74, 0x9480\n"
"v_mfma_f32_16x16x32_bf16 v[180:183], v[212:215], v[148:151], v[180:183]\n"
"buffer_load_dwordx4 v243, s[56:59], s75 offen lds\n"
"v_mfma_f32_16x16x32_bf16 v[184:187], v[212:215], v[152:155], v[184:187]\n"
"ds_read_b64_tr_b16 v[228:229], v203 offset:8480\n"
"ds_read_b64_tr_b16 v[230:231], v203 offset:8736\n"
"v_mfma_f32_16x16x32_bf16 v[188:191], v[212:215], v[156:159], v[188:191]\n"
"s_add_u32 m0, s74, 0xa500\n"
"s_add_u32 s75, s84, 0xe000\n"
"v_mfma_f32_16x16x32_bf16 v[96:99], v[216:219], v[144:147], v[96:99]\n"
"buffer_load_dwordx4 v242, s[56:59], s75 offen lds\n"
"v_mfma_f32_16x16x32_bf16 v[100:103], v[216:219], v[148:151], v[100:103]\n"
"ds_read_b64_tr_b16 v[232:233], v203 offset:16896\n"
"ds_read_b64_tr_b16 v[234:235], v203 offset:17152\n"
"ds_read_b64_tr_b16 v[236:237], v203 offset:16928\n"
"ds_read_b64_tr_b16 v[238:239], v203 offset:17184\n"
"v_mfma_f32_16x16x32_bf16 v[104:107], v[216:219], v[152:155], v[104:107]\n"
"v_accvgpr_read_b32 v193, a193\n"
"s_add_u32 m0, s74, 0xb580\n"
"v_mfma_f32_16x16x32_bf16 v[108:111], v[216:219], v[156:159], v[108:111]\n"
"buffer_load_dwordx4 v243, s[56:59], s75 offen lds\n"
"v_mfma_f32_16x16x32_bf16 v[112:115], v[220:223], v[144:147], v[112:115]\n"
"v_accvgpr_read_b32 v194, a194\n"
"v_accvgpr_read_b32 v195, a195\n"
"v_mfma_f32_16x16x32_bf16 v[116:119], v[220:223], v[148:151], v[116:119]\n"
"v_mul_f32 v192, v192, v244\n"
"v_mul_f32 v193, v193, v244\n"
"v_mfma_f32_16x16x32_bf16 v[120:123], v[220:223], v[152:155], v[120:123]\n"
"v_mul_f32 v194, v194, v244\n"
"v_mul_f32 v195, v195, v244\n"
"v_mfma_f32_16x16x32_bf16 v[124:127], v[220:223], v[156:159], v[124:127]\n"
"v_accvgpr_read_b32 v196, a208\n"
"s_waitcnt lgkmcnt(0)\n"
"v_mfma_f32_16x16x32_bf16 v[160:163], v[224:227], a[48:51], v[160:163]\n"
"ds_write_b128 v206, v[132:135] offset:17920\n"
"v_mfma_f32_16x16x32_bf16 v[164:167], v[224:227], a[52:55], v[164:167]\n"
"ds_write_b128 v206, v[136:139] offset:18944\n"
"v_mfma_f32_16x16x32_bf16 v[168:171], v[224:227], a[56:59], v[168:171]\n"
"ds_write_b128 v206, v[140:143] offset:19968\n"
"v_mfma_f32_16x16x32_bf16 v[172:175], v[224:227], a[60:63], v[172:175]\n"
"ds_write_b128 v206, v[144:147] offset:33792\n"
"v_mfma_f32_16x16x32_bf16 v[176:179], v[228:231], a[48:51], v[176:179]\n"
"s_waitcnt vmcnt(13) lgkmcnt(1)\n"
"s_barrier\n"
"ds_read_b64_tr_b16 v[208:209], v201 offset:61184\n"
"ds_read_b64_tr_b16 v[210:211], v201 offset:61440\n"
"v_mfma_f32_16x16x32_bf16 v[180:183], v[228:231], a[52:55], v[180:183]\n"
"ds_read_b64_tr_b16 v[212:213], v201 offset:61216\n"
"ds_read_b64_tr_b16 v[214:215], v201 offset:61472\n"
"v_mfma_f32_16x16x32_bf16 v[184:187], v[228:231], a[56:59], v[184:187]\n"
"s_add_u32 m0, s74, 0x2100\n"
"s_add_u32 s75, s84, 0x15000\n"
"v_mfma_f32_16x16x32_bf16 v[188:191], v[228:231], a[60:63], v[188:191]\n"
"buffer_load_dwordx4 v242, s[56:59], s75 offen lds\n"
"v_mfma_f32_16x16x32_bf16 v[96:99], v[232:235], a[48:51], v[96:99]\n"
"s_waitcnt vmcnt(11)\n"
"s_barrier\n"
"ds_read_b64_tr_b16 v[216:217], v202 offset:4096\n"
"ds_read_b64_tr_b16 v[218:219], v202 offset:4352\n"
"v_mfma_f32_16x16x32_bf16 v[100:103], v[232:235], a[52:55], v[100:103]\n"
"ds_read_b64_tr_b16 v[220:221], v202 offset:4128\n"
"ds_read_b64_tr_b16 v[222:223], v202 offset:4384\n"
"s_add_u32 m0, s74, 0x3180\n"
"v_mfma_f32_16x16x32_bf16 v[104:107], v[232:235], a[56:59], v[104:107]\n"
"buffer_load_dwordx4 v243, s[56:59], s75 offen lds\n"
"v_mfma_f32_16x16x32_bf16 v[108:111], v[232:235], a[60:63], v[108:111]\n"
"ds_write_b128 v206, v[148:151] offset:34816\n"
"v_mfma_f32_16x16x32_bf16 v[112:115], v[236:239], a[48:51], v[112:115]\n"
"s_add_u32 m0, s74, 0x4200\n"
"s_add_u32 s75, s84, 0x1c000\n"
"v_mfma_f32_16x16x32_bf16 v[116:119], v[236:239], a[52:55], v[116:119]\n"
"buffer_load_dwordx4 v242, s[56:59], s75 offen lds\n"
"v_mfma_f32_16x16x32_bf16 v[120:123], v[236:239], a[56:59], v[120:123]\n"
"ds_read_b128 a[64:67], v207 offset:16896\n"
"ds_read_b128 a[68:71], v207 offset:17920\n"
"v_mfma_f32_16x16x32_bf16 v[124:127], v[236:239], a[60:63], v[124:127]\n"
"s_add_u32 m0, s74, 0x5280\n"
"v_accvgpr_read_b32 v197, a209\n"
"v_accvgpr_read_b32 v198, a210\n"
"v_accvgpr_read_b32 v199, a211\n"
"v_mul_f32 v196, v196, v244\n"
"v_mul_f32 v197, v197, v244\n"
"v_mul_f32 v198, v198, v244\n"
"v_mul_f32 v199, v199, v244\n"
"v_cvt_pk_bf16_f32 v32, v192, v193\n"
"v_cvt_pk_bf16_f32 v33, v194, v195\n"
"v_cvt_pk_bf16_f32 v34, v196, v197\n"
"v_cvt_pk_bf16_f32 v35, v198, v199\n"
"s_waitcnt lgkmcnt(3)\n"
"s_nop 1\n"
"v_mfma_f32_16x16x32_bf16 v[160:163], v[208:211], v[32:35], v[160:163]\n"
"buffer_load_dwordx4 v243, s[56:59], s75 offen lds\n"
"v_accvgpr_read_b32 v192, a196\n"
"v_accvgpr_read_b32 v193, a197\n"
"v_accvgpr_read_b32 v194, a198\n"
"v_accvgpr_read_b32 v195, a199\n"
"v_mul_f32 v192, v192, v245\n"
"v_mul_f32 v193, v193, v245\n"
"v_mul_f32 v194, v194, v245\n"
"v_mul_f32 v195, v195, v245\n"
"v_accvgpr_read_b32 v196, a212\n"
"v_accvgpr_read_b32 v197, a213\n"
"v_accvgpr_read_b32 v198, a214\n"
"v_accvgpr_read_b32 v199, a215\n"
"v_mul_f32 v196, v196, v245\n"
"v_mul_f32 v197, v197, v245\n"
"v_mul_f32 v198, v198, v245\n"
"v_mul_f32 v199, v199, v245\n"
"v_cvt_pk_bf16_f32 v36, v192, v193\n"
"v_cvt_pk_bf16_f32 v37, v194, v195\n"
"v_cvt_pk_bf16_f32 v38, v196, v197\n"
"v_cvt_pk_bf16_f32 v39, v198, v199\n"
"s_nop 1\n"
"v_mfma_f32_16x16x32_bf16 v[164:167], v[208:211], v[36:39], v[164:167]\n"
"ds_read_b128 a[72:75], v207 offset:18944\n"
"ds_read_b128 a[76:79], v207 offset:19968\n"
"v_accvgpr_read_b32 v192, a200\n"
"v_accvgpr_read_b32 v193, a201\n"
"v_accvgpr_read_b32 v194, a202\n"
"v_accvgpr_read_b32 v195, a203\n"
"v_mul_f32 v192, v192, v246\n"
"v_mul_f32 v193, v193, v246\n"
"v_mul_f32 v194, v194, v246\n"
"v_mul_f32 v195, v195, v246\n"
"v_accvgpr_read_b32 v196, a216\n"
"v_accvgpr_read_b32 v197, a217\n"
"v_accvgpr_read_b32 v198, a218\n"
"v_accvgpr_read_b32 v199, a219\n"
"v_mul_f32 v196, v196, v246\n"
"v_mul_f32 v197, v197, v246\n"
"v_mul_f32 v198, v198, v246\n"
"v_mul_f32 v199, v199, v246\n"
"v_cvt_pk_bf16_f32 v40, v192, v193\n"
"v_cvt_pk_bf16_f32 v41, v194, v195\n"
"v_cvt_pk_bf16_f32 v42, v196, v197\n"
"v_cvt_pk_bf16_f32 v43, v198, v199\n"
"s_nop 1\n"
"v_mfma_f32_16x16x32_bf16 v[168:171], v[208:211], v[40:43], v[168:171]\n"
"ds_read_b64_tr_b16 v[224:225], v203 offset:25344\n"
"ds_read_b64_tr_b16 v[226:227], v203 offset:25600\n"
"v_accvgpr_read_b32 v192, a204\n"
"v_accvgpr_read_b32 v193, a205\n"
"v_accvgpr_read_b32 v194, a206\n"
"v_accvgpr_read_b32 v195, a207\n"
"v_mul_f32 v192, v192, v247\n"
"v_mul_f32 v193, v193, v247\n"
"v_mul_f32 v194, v194, v247\n"
"v_mul_f32 v195, v195, v247\n"
"v_accvgpr_read_b32 v196, a220\n"
"v_accvgpr_read_b32 v197, a221\n"
"v_accvgpr_read_b32 v198, a222\n"
"v_accvgpr_read_b32 v199, a223\n"
"v_mul_f32 v196, v196, v247\n"
"v_mul_f32 v197, v197, v247\n"
"v_mul_f32 v198, v198, v247\n"
"v_mul_f32 v199, v199, v247\n"
"v_cvt_pk_bf16_f32 v44, v192, v193\n"
"v_cvt_pk_bf16_f32 v45, v194, v195\n"
"v_cvt_pk_bf16_f32 v46, v196, v197\n"
"v_cvt_pk_bf16_f32 v47, v198, v199\n"
"s_nop 1\n"
"v_mfma_f32_16x16x32_bf16 v[172:175], v[208:211], v[44:47], v[172:175]\n"
"ds_read_b64_tr_b16 v[228:229], v203 offset:25376\n"
"ds_read_b64_tr_b16 v[230:231], v203 offset:25632\n"
"v_mfma_f32_16x16x32_bf16 v[176:179], v[212:215], v[32:35], v[176:179]\n"
"ds_read_b64_tr_b16 v[232:233], v203 offset:50688\n"
"ds_read_b64_tr_b16 v[234:235], v203 offset:50944\n"
"v_mfma_f32_16x16x32_bf16 v[180:183], v[212:215], v[36:39], v[180:183]\n"
"ds_read_b64_tr_b16 v[236:237], v203 offset:50720\n"
"ds_read_b64_tr_b16 v[238:239], v203 offset:50976\n"
"v_mfma_f32_16x16x32_bf16 v[184:187], v[212:215], v[40:43], v[184:187]\n"
"v_accvgpr_read_b32 v192, a224\n"
"v_accvgpr_read_b32 v193, a225\n"
"v_mfma_f32_16x16x32_bf16 v[188:191], v[212:215], v[44:47], v[188:191]\n"
"v_accvgpr_read_b32 v194, a226\n"
"v_accvgpr_read_b32 v195, a227\n"
"v_mfma_f32_16x16x32_bf16 v[96:99], v[216:219], v[32:35], v[96:99]\n"
"v_mul_f32 v192, v192, v244\n"
"v_mul_f32 v193, v193, v244\n"
"v_mfma_f32_16x16x32_bf16 v[100:103], v[216:219], v[36:39], v[100:103]\n"
"v_mul_f32 v194, v194, v244\n"
"v_mul_f32 v195, v195, v244\n"
"v_mfma_f32_16x16x32_bf16 v[104:107], v[216:219], v[40:43], v[104:107]\n"
"v_accvgpr_read_b32 v196, a240\n"
"v_accvgpr_read_b32 v197, a241\n"
"v_mfma_f32_16x16x32_bf16 v[108:111], v[216:219], v[44:47], v[108:111]\n"
"v_accvgpr_read_b32 v198, a242\n"
"v_accvgpr_read_b32 v199, a243\n"
"v_mfma_f32_16x16x32_bf16 v[112:115], v[220:223], v[32:35], v[112:115]\n"
"v_mul_f32 v196, v196, v244\n"
"v_mul_f32 v197, v197, v244\n"
"v_mfma_f32_16x16x32_bf16 v[116:119], v[220:223], v[36:39], v[116:119]\n"
"v_mul_f32 v198, v198, v244\n"
"v_mul_f32 v199, v199, v244\n"
"v_mfma_f32_16x16x32_bf16 v[120:123], v[220:223], v[40:43], v[120:123]\n"
"v_cvt_pk_bf16_f32 v48, v192, v193\n"
"v_cvt_pk_bf16_f32 v49, v194, v195\n"
"v_mfma_f32_16x16x32_bf16 v[124:127], v[220:223], v[44:47], v[124:127]\n"
"v_cvt_pk_bf16_f32 v50, v196, v197\n"
"s_waitcnt lgkmcnt(0)\n"
"v_mfma_f32_16x16x32_bf16 v[160:163], v[224:227], a[64:67], v[160:163]\n"
"ds_write_b128 v206, v[152:155] offset:35840\n"
"v_mfma_f32_16x16x32_bf16 v[164:167], v[224:227], a[68:71], v[164:167]\n"
"ds_write_b128 v206, v[156:159] offset:36864\n"
"v_mfma_f32_16x16x32_bf16 v[168:171], v[224:227], a[72:75], v[168:171]\n"
"ds_write_b128 v206, v[32:35] offset:0\n"
"v_mfma_f32_16x16x32_bf16 v[172:175], v[224:227], a[76:79], v[172:175]\n"
"ds_write_b128 v206, v[36:39] offset:1024\n"
"v_mfma_f32_16x16x32_bf16 v[176:179], v[228:231], a[64:67], v[176:179]\n"
"s_waitcnt vmcnt(12) lgkmcnt(2)\n"
"s_barrier\n"
"ds_read_b64_tr_b16 v[208:209], v202 offset:12544\n"
"ds_read_b64_tr_b16 v[210:211], v202 offset:12800\n"
"v_mfma_f32_16x16x32_bf16 v[180:183], v[228:231], a[68:71], v[180:183]\n"
"ds_read_b64_tr_b16 v[212:213], v202 offset:12576\n"
"ds_read_b64_tr_b16 v[214:215], v202 offset:12832\n"
"v_mfma_f32_16x16x32_bf16 v[184:187], v[228:231], a[72:75], v[184:187]\n"
"s_waitcnt vmcnt(10)\n"
"s_barrier\n"
"ds_read_b64_tr_b16 v[216:217], v202 offset:20992\n"
"ds_read_b64_tr_b16 v[218:219], v202 offset:21248\n"
"v_mfma_f32_16x16x32_bf16 v[188:191], v[228:231], a[76:79], v[188:191]\n"
"s_add_u32 m0, s74, 0x6300\n"
"s_add_u32 s75, s84, 0x23000\n"
"v_mfma_f32_16x16x32_bf16 v[96:99], v[232:235], a[64:67], v[96:99]\n"
"buffer_load_dwordx4 v242, s[56:59], s75 offen lds\n"
"v_mfma_f32_16x16x32_bf16 v[100:103], v[232:235], a[68:71], v[100:103]\n"
"ds_read_b64_tr_b16 v[220:221], v202 offset:21024\n"
"ds_read_b64_tr_b16 v[222:223], v202 offset:21280\n"
"v_mfma_f32_16x16x32_bf16 v[104:107], v[232:235], a[72:75], v[104:107]\n"
"v_cvt_pk_bf16_f32 v51, v198, v199\n"
"s_add_u32 m0, s74, 0x7380\n"
"v_mfma_f32_16x16x32_bf16 v[108:111], v[232:235], a[76:79], v[108:111]\n"
"buffer_load_dwordx4 v243, s[56:59], s75 offen lds\n"
"v_mfma_f32_16x16x32_bf16 v[112:115], v[236:239], a[64:67], v[112:115]\n"
"ds_read_b128 a[80:83], v207 offset:33792\n"
"ds_read_b128 a[84:87], v207 offset:34816\n"
"v_mfma_f32_16x16x32_bf16 v[116:119], v[236:239], a[68:71], v[116:119]\n"
"s_add_u32 m0, s74, 0xc600\n"
"s_add_u32 s75, s84, 0x2a000\n"
"v_mfma_f32_16x16x32_bf16 v[120:123], v[236:239], a[72:75], v[120:123]\n"
"buffer_load_dwordx4 v242, s[56:59], s75 offen lds\n"
"v_mfma_f32_16x16x32_bf16 v[124:127], v[236:239], a[76:79], v[124:127]\n"
"ds_read_b128 a[88:91], v207 offset:35840\n"
"s_waitcnt lgkmcnt(3)\n"
"v_mfma_f32_16x16x32_bf16 v[160:163], v[208:211], v[48:51], v[160:163]\n"
"ds_read_b128 a[92:95], v207 offset:36864\n"
"s_add_u32 m0, s74, 0xd680\n"
"v_accvgpr_read_b32 v192, a228\n"
"v_accvgpr_read_b32 v193, a229\n"
"v_accvgpr_read_b32 v194, a230\n"
"v_accvgpr_read_b32 v195, a231\n"
"v_mul_f32 v192, v192, v245\n"
"v_mul_f32 v193, v193, v245\n"
"v_mul_f32 v194, v194, v245\n"
"v_mul_f32 v195, v195, v245\n"
"v_accvgpr_read_b32 v196, a244\n"
"v_accvgpr_read_b32 v197, a245\n"
"v_accvgpr_read_b32 v198, a246\n"
"v_accvgpr_read_b32 v199, a247\n"
"v_mul_f32 v196, v196, v245\n"
"v_mul_f32 v197, v197, v245\n"
"v_mul_f32 v198, v198, v245\n"
"v_mul_f32 v199, v199, v245\n"
"v_cvt_pk_bf16_f32 v52, v192, v193\n"
"v_cvt_pk_bf16_f32 v53, v194, v195\n"
"v_cvt_pk_bf16_f32 v54, v196, v197\n"
"v_cvt_pk_bf16_f32 v55, v198, v199\n"
"s_nop 1\n"
"v_mfma_f32_16x16x32_bf16 v[164:167], v[208:211], v[52:55], v[164:167]\n"
"buffer_load_dwordx4 v243, s[56:59], s75 offen lds\n"
"v_accvgpr_read_b32 v192, a232\n"
"v_accvgpr_read_b32 v193, a233\n"
"v_accvgpr_read_b32 v194, a234\n"
"v_accvgpr_read_b32 v195, a235\n"
"v_mul_f32 v192, v192, v246\n"
"v_mul_f32 v193, v193, v246\n"
"v_mul_f32 v194, v194, v246\n"
"v_mul_f32 v195, v195, v246\n"
"v_accvgpr_read_b32 v196, a248\n"
"v_accvgpr_read_b32 v197, a249\n"
"v_accvgpr_read_b32 v198, a250\n"
"v_accvgpr_read_b32 v199, a251\n"
"v_mul_f32 v196, v196, v246\n"
"v_mul_f32 v197, v197, v246\n"
"v_mul_f32 v198, v198, v246\n"
"v_mul_f32 v199, v199, v246\n"
"v_cvt_pk_bf16_f32 v56, v192, v193\n"
"v_cvt_pk_bf16_f32 v57, v194, v195\n"
"v_cvt_pk_bf16_f32 v58, v196, v197\n"
"v_cvt_pk_bf16_f32 v59, v198, v199\n"
"s_nop 1\n"
"v_mfma_f32_16x16x32_bf16 v[168:171], v[208:211], v[56:59], v[168:171]\n"
"ds_read_b64_tr_b16 v[224:225], v203 offset:59136\n"
"ds_read_b64_tr_b16 v[226:227], v203 offset:59392\n"
"v_accvgpr_read_b32 v192, a236\n"
"v_accvgpr_read_b32 v193, a237\n"
"v_accvgpr_read_b32 v194, a238\n"
"v_accvgpr_read_b32 v195, a239\n"
"v_mul_f32 v192, v192, v247\n"
"v_mul_f32 v193, v193, v247\n"
"v_mul_f32 v194, v194, v247\n"
"v_mul_f32 v195, v195, v247\n"
"v_accvgpr_read_b32 v196, a252\n"
"v_accvgpr_read_b32 v197, a253\n"
"v_accvgpr_read_b32 v198, a254\n"
"v_accvgpr_read_b32 v199, a255\n"
"v_mul_f32 v196, v196, v247\n"
"v_mul_f32 v197, v197, v247\n"
"v_mul_f32 v198, v198, v247\n"
"v_mul_f32 v199, v199, v247\n"
"v_cvt_pk_bf16_f32 v60, v192, v193\n"
"v_cvt_pk_bf16_f32 v61, v194, v195\n"
"v_cvt_pk_bf16_f32 v62, v196, v197\n"
"v_cvt_pk_bf16_f32 v63, v198, v199\n"
"s_nop 1\n"
"v_mfma_f32_16x16x32_bf16 v[172:175], v[208:211], v[60:63], v[172:175]\n"
"ds_read_b64_tr_b16 v[228:229], v203 offset:59168\n"
"ds_read_b64_tr_b16 v[230:231], v203 offset:59424\n"
"v_mfma_f32_16x16x32_bf16 v[176:179], v[212:215], v[48:51], v[176:179]\n"
"v_and_b32 v253, 7, v251\n"
"v_lshlrev_b32 v253, 2, v253\n"
"v_mfma_f32_16x16x32_bf16 v[180:183], v[212:215], v[52:55], v[180:183]\n"
"buffer_load_dword v254, v253, s[68:71], s30 offen sc1\n"
"v_mfma_f32_16x16x32_bf16 v[184:187], v[212:215], v[56:59], v[184:187]\n"
"ds_read_b64_tr_b16 v[232:233], v204 offset:52736\n"
"ds_read_b64_tr_b16 v[234:235], v204 offset:52992\n"
"v_mfma_f32_16x16x32_bf16 v[188:191], v[212:215], v[60:63], v[188:191]\n"
"ds_read_b64_tr_b16 v[236:237], v204 offset:52768\n"
"ds_read_b64_tr_b16 v[238:239], v204 offset:53024\n"
"v_mfma_f32_16x16x32_bf16 v[96:99], v[216:219], v[48:51], v[96:99]\n"
"ds_write_b128 v206, v[40:43] offset:2048\n"
"v_mfma_f32_16x16x32_bf16 v[100:103], v[216:219], v[52:55], v[100:103]\n"
"v_mfma_f32_16x16x32_bf16 v[104:107], v[216:219], v[56:59], v[104:107]\n"
"v_mfma_f32_16x16x32_bf16 v[108:111], v[216:219], v[60:63], v[108:111]\n"
"v_mfma_f32_16x16x32_bf16 v[112:115], v[220:223], v[48:51], v[112:115]\n"
"v_mfma_f32_16x16x32_bf16 v[116:119], v[220:223], v[52:55], v[116:119]\n"
"v_mfma_f32_16x16x32_bf16 v[120:123], v[220:223], v[56:59], v[120:123]\n"
"v_mfma_f32_16x16x32_bf16 v[124:127], v[220:223], v[60:63], v[124:127]\n"
"s_waitcnt lgkmcnt(1)\n"
"v_mfma_f32_16x16x32_bf16 v[160:163], v[224:227], a[80:83], v[160:163]\n"
"ds_write_b128 v206, v[44:47] offset:3072\n"
"v_mfma_f32_16x16x32_bf16 v[164:167], v[224:227], a[84:87], v[164:167]\n"
"ds_write_b128 v206, v[48:51] offset:16896\n"
"v_mfma_f32_16x16x32_bf16 v[168:171], v[224:227], a[88:91], v[168:171]\n"
"ds_write_b128 v206, v[52:55] offset:17920\n"
"v_mfma_f32_16x16x32_bf16 v[172:175], v[224:227], a[92:95], v[172:175]\n"
"ds_write_b128 v206, v[56:59] offset:18944\n"
"ds_write_b128 v206, v[60:63] offset:19968\n"
"v_mfma_f32_16x16x32_bf16 v[176:179], v[228:231], a[80:83], v[176:179]\n"
"s_waitcnt lgkmcnt(0)\n"
"s_barrier\n"
"ds_read_b128 a[96:99], v207 offset:0\n"
"v_mfma_f32_16x16x32_bf16 v[180:183], v[228:231], a[84:87], v[180:183]\n"
"s_add_u32 m0, s74, 0x1ce00\n"
"s_add_u32 s75, s84, 0x31000\n"
"v_mfma_f32_16x16x32_bf16 v[184:187], v[228:231], a[88:91], v[184:187]\n"
"buffer_load_dwordx4 v242, s[56:59], s75 offen lds\n"
"v_mfma_f32_16x16x32_bf16 v[188:191], v[228:231], a[92:95], v[188:191]\n"
"ds_read_b128 a[100:103], v207 offset:1024\n"
"ds_read_b128 a[104:107], v207 offset:2048\n"
"v_mfma_f32_16x16x32_bf16 v[96:99], v[232:235], a[80:83], v[96:99]\n"
"ds_read_b128 a[108:111], v207 offset:3072\n"
"s_add_u32 m0, s74, 0x1de80\n"
"v_mfma_f32_16x16x32_bf16 v[100:103], v[232:235], a[84:87], v[100:103]\n"
"buffer_load_dwordx4 v243, s[56:59], s75 offen lds\n"
"ds_read_b128 a[112:115], v207 offset:16896\n"
"ds_read_b128 a[116:119], v207 offset:17920\n"
"ds_read_b128 a[120:123], v207 offset:18944\n"
"ds_read_b128 a[124:127], v207 offset:19968\n"
"ds_read_b64_tr_b16 v[208:209], v204 offset:61184\n"
"ds_read_b64_tr_b16 v[210:211], v204 offset:61440\n"
"ds_read_b64_tr_b16 v[212:213], v204 offset:61216\n"
"ds_read_b64_tr_b16 v[214:215], v204 offset:61472\n"
"ds_read_b64_tr_b16 v[216:217], v205 offset:12544\n"
"ds_read_b64_tr_b16 v[218:219], v205 offset:12800\n"
"s_waitcnt lgkmcnt(8)\n"
"s_nop 0\n"
"ds_read_b64_tr_b16 v[220:221], v205 offset:12576\n"
"ds_read_b64_tr_b16 v[222:223], v205 offset:12832\n"
"v_mfma_f32_16x16x32_bf16 v[104:107], v[232:235], a[88:91], v[104:107]\n"
"v_mfma_f32_16x16x32_bf16 v[108:111], v[232:235], a[92:95], v[108:111]\n"
"s_add_u32 m0, s74, 0xe700\n"
"s_add_u32 s75, s85, 0x0\n"
"v_mfma_f32_16x16x32_bf16 v[112:115], v[236:239], a[80:83], v[112:115]\n"
"buffer_load_dwordx4 v242, s[56:59], s75 offen lds\n"
"v_mfma_f32_16x16x32_bf16 v[116:119], v[236:239], a[84:87], v[116:119]\n"
"ds_read_b64_tr_b16 v[224:225], v205 offset:4096\n"
"ds_read_b64_tr_b16 v[226:227], v205 offset:4352\n"
"v_mfma_f32_16x16x32_bf16 v[120:123], v[236:239], a[88:91], v[120:123]\n"
"s_add_u32 m0, s74, 0xf780\n"
"v_mfma_f32_16x16x32_bf16 v[124:127], v[236:239], a[92:95], v[124:127]\n"
"buffer_load_dwordx4 v243, s[56:59], s75 offen lds\n"
"s_waitcnt lgkmcnt(2)\n"
"v_mfma_f32_16x16x32_bf16 v[160:163], v[208:211], a[96:99], v[160:163]\n"
"ds_read_b64_tr_b16 v[228:229], v205 offset:4128\n"
"ds_read_b64_tr_b16 v[230:231], v205 offset:4384\n"
"v_mfma_f32_16x16x32_bf16 v[164:167], v[208:211], a[100:103], v[164:167]\n"
"s_add_u32 m0, s74, 0x18c00\n"
"s_add_u32 s75, s85, 0x7000\n"
"v_mfma_f32_16x16x32_bf16 v[168:171], v[208:211], a[104:107], v[168:171]\n"
"buffer_load_dwordx4 v242, s[56:59], s75 offen lds\n"
"v_mfma_f32_16x16x32_bf16 v[172:175], v[208:211], a[108:111], v[172:175]\n"
"ds_read_b64_tr_b16 v[232:233], v205 offset:20992\n"
"ds_read_b64_tr_b16 v[234:235], v205 offset:21248\n"
"v_mfma_f32_16x16x32_bf16 v[176:179], v[212:215], a[96:99], v[176:179]\n"
"s_add_u32 m0, s74, 0x19c80\n"
"v_mfma_f32_16x16x32_bf16 v[180:183], v[212:215], a[100:103], v[180:183]\n"
"buffer_load_dwordx4 v243, s[56:59], s75 offen lds\n"
"v_mfma_f32_16x16x32_bf16 v[184:187], v[212:215], a[104:107], v[184:187]\n"
"ds_read_b64_tr_b16 v[236:237], v205 offset:21024\n"
"ds_read_b64_tr_b16 v[238:239], v205 offset:21280\n"
"v_mfma_f32_16x16x32_bf16 v[188:191], v[212:215], a[108:111], v[188:191]\n"
"s_add_u32 m0, s74, 0x1ad00\n"
"s_add_u32 s75, s85, 0xe000\n"
"v_mfma_f32_16x16x32_bf16 v[160:163], v[216:219], a[112:115], v[160:163]\n"
"buffer_load_dwordx4 v242, s[56:59], s75 offen lds\n"
"v_mfma_f32_16x16x32_bf16 v[164:167], v[216:219], a[116:119], v[164:167]\n"
"v_mfma_f32_16x16x32_bf16 v[168:171], v[216:219], a[120:123], v[168:171]\n"
"s_add_u32 m0, s74, 0x1bd80\n"
"v_mfma_f32_16x16x32_bf16 v[172:175], v[216:219], a[124:127], v[172:175]\n"
"buffer_load_dwordx4 v243, s[56:59], s75 offen lds\n"
"v_mfma_f32_16x16x32_bf16 v[176:179], v[220:223], a[112:115], v[176:179]\n"
"v_mfma_f32_16x16x32_bf16 v[180:183], v[220:223], a[116:119], v[180:183]\n"
"v_mfma_f32_16x16x32_bf16 v[184:187], v[220:223], a[120:123], v[184:187]\n"
"v_mfma_f32_16x16x32_bf16 v[188:191], v[220:223], a[124:127], v[188:191]\n"
"s_waitcnt lgkmcnt(0)\n"
"v_mfma_f32_16x16x32_bf16 v[96:99], v[224:227], a[96:99], v[96:99]\n"
"s_lshl_b32 s31, s82, 10\n"
"s_add_u32 s31, s31, s93\n"
"v_mfma_f32_16x16x32_bf16 v[100:103], v[224:227], a[100:103], v[100:103]\n"
"v_cvt_pk_bf16_f32 v160, v160, v161\n"
"v_cvt_pk_bf16_f32 v161, v162, v163\n"
"v_mfma_f32_16x16x32_bf16 v[104:107], v[224:227], a[104:107], v[104:107]\n"
"v_cvt_pk_bf16_f32 v162, v176, v177\n"
"v_cvt_pk_bf16_f32 v163, v178, v179\n"
"v_mfma_f32_16x16x32_bf16 v[108:111], v[224:227], a[108:111], v[108:111]\n"
"buffer_store_dwordx4 v[160:163], v248, s[60:63], s31 offen offset:0 sc1\n"
"v_mfma_f32_16x16x32_bf16 v[112:115], v[228:231], a[96:99], v[112:115]\n"
"s_waitcnt vmcnt(18)\n"
"s_barrier\n"
"ds_read_b64_tr_b16 v[208:209], v200 offset:0\n"
"ds_read_b64_tr_b16 v[210:211], v200 offset:256\n"
"v_mfma_f32_16x16x32_bf16 v[116:119], v[228:231], a[100:103], v[116:119]\n"
"ds_read_b64_tr_b16 v[212:213], v200 offset:32\n"
"ds_read_b64_tr_b16 v[214:215], v200 offset:288\n"
"v_mfma_f32_16x16x32_bf16 v[120:123], v[228:231], a[104:107], v[120:123]\n"
"s_add_u32 m0, s74, 0x21000\n"
"s_add_u32 s75, s85, 0x15000\n"
"v_mfma_f32_16x16x32_bf16 v[124:127], v[228:231], a[108:111], v[124:127]\n"
"buffer_load_dwordx4 v242, s[56:59], s75 offen lds\n"
"v_mfma_f32_16x16x32_bf16 v[96:99], v[232:235], a[112:115], v[96:99]\n"
"ds_read_b64_tr_b16 v[216:217], v203 offset:0\n"
"ds_read_b64_tr_b16 v[218:219], v203 offset:256\n"
"v_mfma_f32_16x16x32_bf16 v[100:103], v[232:235], a[116:119], v[100:103]\n"
"ds_read_b64_tr_b16 v[220:221], v203 offset:32\n"
"ds_read_b64_tr_b16 v[222:223], v203 offset:288\n"
"s_add_u32 m0, s74, 0x22080\n"
"v_mfma_f32_16x16x32_bf16 v[104:107], v[232:235], a[120:123], v[104:107]\n"
"buffer_load_dwordx4 v243, s[56:59], s75 offen lds\n"
"v_mfma_f32_16x16x32_bf16 v[108:111], v[232:235], a[124:127], v[108:111]\n"
"v_cvt_pk_bf16_f32 v164, v164, v165\n"
"v_cvt_pk_bf16_f32 v165, v166, v167\n"
"v_mfma_f32_16x16x32_bf16 v[112:115], v[236:239], a[112:115], v[112:115]\n"
"s_add_u32 m0, s74, 0x23100\n"
"s_add_u32 s75, s85, 0x1c000\n"
"v_mfma_f32_16x16x32_bf16 v[116:119], v[236:239], a[116:119], v[116:119]\n"
"buffer_load_dwordx4 v242, s[56:59], s75 offen lds\n"
"v_mfma_f32_16x16x32_bf16 v[120:123], v[236:239], a[120:123], v[120:123]\n"
"ds_read_b64_tr_b16 v[224:225], v200 offset:33792\n"
"ds_read_b64_tr_b16 v[226:227], v200 offset:34048\n"
"v_mfma_f32_16x16x32_bf16 v[124:127], v[236:239], a[124:127], v[124:127]\n"
"s_add_u32 m0, s74, 0x24180\n"
"s_waitcnt lgkmcnt(2)\n"
"v_mfma_f32_16x16x32_bf16 v[160:163], v[208:211], v[0:3], 0\n"
"buffer_load_dwordx4 v243, s[56:59], s75 offen lds\n"
"v_cvt_pk_bf16_f32 v166, v180, v181\n"
"v_cvt_pk_bf16_f32 v167, v182, v183\n"
"buffer_store_dwordx4 v[164:167], v248, s[60:63], s31 offen offset:1024 sc1\n"
"s_nop 1\n"
"v_mfma_f32_16x16x32_bf16 v[164:167], v[208:211], v[4:7], 0\n"
"ds_read_b64_tr_b16 v[228:229], v200 offset:33824\n"
"ds_read_b64_tr_b16 v[230:231], v200 offset:34080\n"
"v_cvt_pk_bf16_f32 v168, v168, v169\n"
"v_cvt_pk_bf16_f32 v169, v170, v171\n"
"v_cvt_pk_bf16_f32 v170, v184, v185\n"
"v_cvt_pk_bf16_f32 v171, v186, v187\n"
"buffer_store_dwordx4 v[168:171], v248, s[60:63], s31 offen offset:2048 sc1\n"
"s_nop 1\n"
"v_mfma_f32_16x16x32_bf16 v[168:171], v[208:211], v[8:11], 0\n"
"s_add_u32 m0, s74, 0x25200\n"
"s_add_u32 s75, s85, 0x23000\n"
"v_cvt_pk_bf16_f32 v172, v172, v173\n"
"v_cvt_pk_bf16_f32 v173, v174, v175\n"
"v_cvt_pk_bf16_f32 v174, v188, v189\n"
"v_cvt_pk_bf16_f32 v175, v190, v191\n"
"buffer_store_dwordx4 v[172:175], v248, s[60:63], s31 offen offset:3072 sc1\n"
"s_nop 1\n"
"v_mfma_f32_16x16x32_bf16 v[172:175], v[208:211], v[12:15], 0\n"
"buffer_load_dwordx4 v242, s[56:59], s75 offen lds\n"
"v_mfma_f32_16x16x32_bf16 v[176:179], v[212:215], v[0:3], 0\n"
"ds_read_b64_tr_b16 v[232:233], v203 offset:33792\n"
"ds_read_b64_tr_b16 v[234:235], v203 offset:34048\n"
"v_mfma_f32_16x16x32_bf16 v[180:183], v[212:215], v[4:7], 0\n"
"v_cvt_pk_bf16_f32 v96, v96, v97\n"
"s_add_u32 m0, s74, 0x26280\n"
"v_mfma_f32_16x16x32_bf16 v[184:187], v[212:215], v[8:11], 0\n"
"buffer_load_dwordx4 v243, s[56:59], s75 offen lds\n"
"v_mfma_f32_16x16x32_bf16 v[188:191], v[212:215], v[12:15], 0\n"
"ds_read_b64_tr_b16 v[236:237], v203 offset:33824\n"
"ds_read_b64_tr_b16 v[238:239], v203 offset:34080\n"
"v_mfma_f32_16x16x32_bf16 v[160:163], v[216:219], a[0:3], v[160:163]\n"
"s_add_u32 m0, s74, 0x1ef00\n"
"s_add_u32 s75, s85, 0x2a000\n"
"v_mfma_f32_16x16x32_bf16 v[164:167], v[216:219], a[4:7], v[164:167]\n"
"buffer_load_dwordx4 v242, s[56:59], s75 offen lds\n"
"v_mfma_f32_16x16x32_bf16 v[168:171], v[216:219], a[8:11], v[168:171]\n"
"s_lshl_b32 s31, s83, 10\n"
"s_add_u32 s31, s31, s93\n"
"v_mfma_f32_16x16x32_bf16 v[172:175], v[216:219], a[12:15], v[172:175]\n"
"v_cvt_pk_bf16_f32 v97, v98, v99\n"
"s_add_u32 m0, s74, 0x1ff80\n"
"v_mfma_f32_16x16x32_bf16 v[176:179], v[220:223], a[0:3], v[176:179]\n"
"buffer_load_dwordx4 v243, s[56:59], s75 offen lds\n"
"v_mfma_f32_16x16x32_bf16 v[180:183], v[220:223], a[4:7], v[180:183]\n"
"ds_read_b64_tr_b16 v[208:209], v200 offset:42240\n"
"ds_read_b64_tr_b16 v[210:211], v200 offset:42496\n"
"v_mfma_f32_16x16x32_bf16 v[184:187], v[220:223], a[8:11], v[184:187]\n"
"s_add_u32 m0, s74, 0x10800\n"
"s_add_u32 s75, s85, 0x31000\n"
"v_mfma_f32_16x16x32_bf16 v[188:191], v[220:223], a[12:15], v[188:191]\n"
"buffer_load_dwordx4 v242, s[56:59], s75 offen lds\n"
"s_waitcnt lgkmcnt(2)\n"
"v_mfma_f32_16x16x32_bf16 v[160:163], v[224:227], v[16:19], v[160:163]\n"
"ds_read_b64_tr_b16 v[212:213], v200 offset:42272\n"
"ds_read_b64_tr_b16 v[214:215], v200 offset:42528\n"
"v_mfma_f32_16x16x32_bf16 v[164:167], v[224:227], v[20:23], v[164:167]\n"
"v_cvt_pk_bf16_f32 v98, v112, v113\n"
"s_add_u32 m0, s74, 0x11880\n"
"v_mfma_f32_16x16x32_bf16 v[168:171], v[224:227], v[24:27], v[168:171]\n"
"buffer_load_dwordx4 v243, s[56:59], s75 offen lds\n"
"v_mfma_f32_16x16x32_bf16 v[172:175], v[224:227], v[28:31], v[172:175]\n"
"ds_read_b64_tr_b16 v[216:217], v203 offset:42240\n"
"ds_read_b64_tr_b16 v[218:219], v203 offset:42496\n"
"v_mfma_f32_16x16x32_bf16 v[176:179], v[228:231], v[16:19], v[176:179]\n"
"s_add_u32 m0, s74, 0x12900\n"
"s_add_u32 s75, s86, 0x0\n"
"v_mfma_f32_16x16x32_bf16 v[180:183], v[228:231], v[20:23], v[180:183]\n"
"buffer_load_dwordx4 v242, s[56:59], s75 offen lds\n"
"v_mfma_f32_16x16x32_bf16 v[184:187], v[228:231], v[24:27], v[184:187]\n"
"ds_read_b64_tr_b16 v[220:221], v203 offset:42272\n"
"ds_read_b64_tr_b16 v[222:223], v203 offset:42528\n"
"v_mfma_f32_16x16x32_bf16 v[188:191], v[228:231], v[28:31], v[188:191]\n"
"v_cvt_pk_bf16_f32 v99, v114, v115\n"
"s_add_u32 m0, s74, 0x13980\n"
"v_mfma_f32_16x16x32_bf16 v[160:163], v[232:235], a[16:19], v[160:163]\n"
"buffer_load_dwordx4 v243, s[56:59], s75 offen lds\n"
"v_mfma_f32_16x16x32_bf16 v[164:167], v[232:235], a[20:23], v[164:167]\n"
"buffer_store_dwordx4 v[96:99], v248, s[60:63], s31 offen offset:0 sc1\n"
"v_mfma_f32_16x16x32_bf16 v[168:171], v[232:235], a[24:27], v[168:171]\n"
"s_add_u32 m0, s74, 0x14a00\n"
"s_add_u32 s75, s86, 0x7000\n"
"v_mfma_f32_16x16x32_bf16 v[172:175], v[232:235], a[28:31], v[172:175]\n"
"buffer_load_dwordx4 v242, s[56:59], s75 offen lds\n"
"v_mfma_f32_16x16x32_bf16 v[176:179], v[236:239], a[16:19], v[176:179]\n"
"v_cvt_pk_bf16_f32 v100, v100, v101\n"
"v_cvt_pk_bf16_f32 v101, v102, v103\n"
"v_mfma_f32_16x16x32_bf16 v[180:183], v[236:239], a[20:23], v[180:183]\n"
"v_cvt_pk_bf16_f32 v102, v116, v117\n"
"s_add_u32 m0, s74, 0x15a80\n"
"v_mfma_f32_16x16x32_bf16 v[184:187], v[236:239], a[24:27], v[184:187]\n"
"buffer_load_dwordx4 v243, s[56:59], s75 offen lds\n"
"v_mfma_f32_16x16x32_bf16 v[188:191], v[236:239], a[28:31], v[188:191]\n"
"v_cvt_pk_bf16_f32 v103, v118, v119\n"
"s_waitcnt lgkmcnt(0)\n"
"v_mfma_f32_16x16x32_bf16 v[160:163], v[208:211], v[64:67], v[160:163]\n"
"s_add_u32 m0, s74, 0x16b00\n"
"s_add_u32 s75, s86, 0xe000\n"
"v_mfma_f32_16x16x32_bf16 v[164:167], v[208:211], v[68:71], v[164:167]\n"
"buffer_load_dwordx4 v242, s[56:59], s75 offen lds\n"
"v_mfma_f32_16x16x32_bf16 v[168:171], v[208:211], v[72:75], v[168:171]\n"
"buffer_store_dwordx4 v[100:103], v248, s[60:63], s31 offen offset:1024 sc1\n"
"v_mfma_f32_16x16x32_bf16 v[172:175], v[208:211], v[76:79], v[172:175]\n"
"v_cvt_pk_bf16_f32 v104, v104, v105\n"
"s_add_u32 m0, s74, 0x17b80\n"
"v_mfma_f32_16x16x32_bf16 v[176:179], v[212:215], v[64:67], v[176:179]\n"
"buffer_load_dwordx4 v243, s[56:59], s75 offen lds\n"
"v_mfma_f32_16x16x32_bf16 v[180:183], v[212:215], v[68:71], v[180:183]\n"
"s_waitcnt vmcnt(31)\n"
"s_barrier\n"
"ds_read_b64_tr_b16 v[224:225], v200 offset:8448\n"
"ds_read_b64_tr_b16 v[226:227], v200 offset:8704\n"
"v_mfma_f32_16x16x32_bf16 v[184:187], v[212:215], v[72:75], v[184:187]\n"
"ds_read_b64_tr_b16 v[228:229], v200 offset:8480\n"
"ds_read_b64_tr_b16 v[230:231], v200 offset:8736\n"
"v_mfma_f32_16x16x32_bf16 v[188:191], v[212:215], v[76:79], v[188:191]\n"
"ds_read_b64_tr_b16 v[232:233], v203 offset:8448\n"
"ds_read_b64_tr_b16 v[234:235], v203 offset:8704\n"
"v_mfma_f32_16x16x32_bf16 v[160:163], v[216:219], a[32:35], v[160:163]\n"
"ds_read_b64_tr_b16 v[236:237], v203 offset:8480\n"
"ds_read_b64_tr_b16 v[238:239], v203 offset:8736\n"
"v_mfma_f32_16x16x32_bf16 v[164:167], v[216:219], a[36:39], v[164:167]\n"
"v_cvt_pk_bf16_f32 v105, v106, v107\n"
"v_cvt_pk_bf16_f32 v106, v120, v121\n"
"v_mfma_f32_16x16x32_bf16 v[168:171], v[216:219], a[40:43], v[168:171]\n"
"v_cvt_pk_bf16_f32 v107, v122, v123\n"
"v_cvt_pk_bf16_f32 v108, v108, v109\n"
"v_mfma_f32_16x16x32_bf16 v[172:175], v[216:219], a[44:47], v[172:175]\n"
"buffer_store_dwordx4 v[104:107], v248, s[60:63], s31 offen offset:2048 sc1\n"
"v_mfma_f32_16x16x32_bf16 v[176:179], v[220:223], a[32:35], v[176:179]\n"
"ds_read_b64_tr_b16 v[208:209], v200 offset:16896\n"
"ds_read_b64_tr_b16 v[210:211], v200 offset:17152\n"
"v_mfma_f32_16x16x32_bf16 v[180:183], v[220:223], a[36:39], v[180:183]\n"
"ds_read_b64_tr_b16 v[212:213], v200 offset:16928\n"
"ds_read_b64_tr_b16 v[214:215], v200 offset:17184\n"
"v_mfma_f32_16x16x32_bf16 v[184:187], v[220:223], a[40:43], v[184:187]\n"
"ds_read_b64_tr_b16 v[216:217], v203 offset:16896\n"
"ds_read_b64_tr_b16 v[218:219], v203 offset:17152\n"
"v_mfma_f32_16x16x32_bf16 v[188:191], v[220:223], a[44:47], v[188:191]\n"
"v_cvt_pk_bf16_f32 v109, v110, v111\n"
"s_waitcnt lgkmcnt(6)\n"
"v_mfma_f32_16x16x32_bf16 v[160:163], v[224:227], v[80:83], v[160:163]\n"
"ds_read_b64_tr_b16 v[220:221], v203 offset:16928\n"
"ds_read_b64_tr_b16 v[222:223], v203 offset:17184\n"
"v_mfma_f32_16x16x32_bf16 v[164:167], v[224:227], v[84:87], v[164:167]\n"
"v_cvt_pk_bf16_f32 v110, v124, v125\n"
"v_cvt_pk_bf16_f32 v111, v126, v127\n"
"v_mfma_f32_16x16x32_bf16 v[168:171], v[224:227], v[88:91], v[168:171]\n"
"buffer_store_dwordx4 v[108:111], v248, s[60:63], s31 offen offset:3072 sc1\n"
"v_mfma_f32_16x16x32_bf16 v[172:175], v[224:227], v[92:95], v[172:175]\n"
"v_mfma_f32_16x16x32_bf16 v[176:179], v[228:231], v[80:83], v[176:179]\n"
"v_mfma_f32_16x16x32_bf16 v[180:183], v[228:231], v[84:87], v[180:183]\n"
"v_mfma_f32_16x16x32_bf16 v[184:187], v[228:231], v[88:91], v[184:187]\n"
"v_mfma_f32_16x16x32_bf16 v[188:191], v[228:231], v[92:95], v[188:191]\n"
"v_mfma_f32_16x16x32_bf16 v[160:163], v[232:235], a[48:51], v[160:163]\n"
"v_mfma_f32_16x16x32_bf16 v[164:167], v[232:235], a[52:55], v[164:167]\n"
"v_mfma_f32_16x16x32_bf16 v[168:171], v[232:235], a[56:59], v[168:171]\n"
"v_mfma_f32_16x16x32_bf16 v[172:175], v[232:235], a[60:63], v[172:175]\n"
"v_mfma_f32_16x16x32_bf16 v[176:179], v[236:239], a[48:51], v[176:179]\n"
"ds_read_b64_tr_b16 v[224:225], v200 offset:25344\n"
"ds_read_b64_tr_b16 v[226:227], v200 offset:25600\n"
"v_mfma_f32_16x16x32_bf16 v[180:183], v[236:239], a[52:55], v[180:183]\n"
"ds_read_b64_tr_b16 v[228:229], v200 offset:25376\n"
"ds_read_b64_tr_b16 v[230:231], v200 offset:25632\n"
"v_mfma_f32_16x16x32_bf16 v[184:187], v[236:239], a[56:59], v[184:187]\n"
"ds_read_b64_tr_b16 v[232:233], v203 offset:25344\n"
"ds_read_b64_tr_b16 v[234:235], v203 offset:25600\n"
"v_mfma_f32_16x16x32_bf16 v[188:191], v[236:239], a[60:63], v[188:191]\n"
"s_waitcnt lgkmcnt(6)\n"
"v_mfma_f32_16x16x32_bf16 v[160:163], v[208:211], v[128:131], v[160:163]\n"
"ds_read_b64_tr_b16 v[236:237], v203 offset:25376\n"
"ds_read_b64_tr_b16 v[238:239], v203 offset:25632\n"
"v_mfma_f32_16x16x32_bf16 v[164:167], v[208:211], v[132:135], v[164:167]\n"
"v_mfma_f32_16x16x32_bf16 v[168:171], v[208:211], v[136:139], v[168:171]\n"
"v_mfma_f32_16x16x32_bf16 v[172:175], v[208:211], v[140:143], v[172:175]\n"
"v_mfma_f32_16x16x32_bf16 v[176:179], v[212:215], v[128:131], v[176:179]\n"
"v_mfma_f32_16x16x32_bf16 v[180:183], v[212:215], v[132:135], v[180:183]\n"
"v_mfma_f32_16x16x32_bf16 v[184:187], v[212:215], v[136:139], v[184:187]\n"
"v_mfma_f32_16x16x32_bf16 v[188:191], v[212:215], v[140:143], v[188:191]\n"
"v_mfma_f32_16x16x32_bf16 v[160:163], v[216:219], a[64:67], v[160:163]\n"
"v_mfma_f32_16x16x32_bf16 v[164:167], v[216:219], a[68:71], v[164:167]\n"
"v_mfma_f32_16x16x32_bf16 v[168:171], v[216:219], a[72:75], v[168:171]\n"
"v_mfma_f32_16x16x32_bf16 v[172:175], v[216:219], a[76:79], v[172:175]\n"
"v_mfma_f32_16x16x32_bf16 v[176:179], v[220:223], a[64:67], v[176:179]\n"
"ds_read_b64_tr_b16 v[208:209], v200 offset:50688\n"
"ds_read_b64_tr_b16 v[210:211], v200 offset:50944\n"
"v_mfma_f32_16x16x32_bf16 v[180:183], v[220:223], a[68:71], v[180:183]\n"
"ds_read_b64_tr_b16 v[212:213], v200 offset:50720\n"
"ds_read_b64_tr_b16 v[214:215], v200 offset:50976\n"
"v_mfma_f32_16x16x32_bf16 v[184:187], v[220:223], a[72:75], v[184:187]\n"
"ds_read_b64_tr_b16 v[216:217], v203 offset:50688\n"
"ds_read_b64_tr_b16 v[218:219], v203 offset:50944\n"
"v_mfma_f32_16x16x32_bf16 v[188:191], v[220:223], a[76:79], v[188:191]\n"
"s_waitcnt lgkmcnt(6)\n"
"v_mfma_f32_16x16x32_bf16 v[160:163], v[224:227], v[144:147], v[160:163]\n"
"ds_read_b64_tr_b16 v[220:221], v203 offset:50720\n"
"ds_read_b64_tr_b16 v[222:223], v203 offset:50976\n"
"v_mfma_f32_16x16x32_bf16 v[164:167], v[224:227], v[148:151], v[164:167]\n"
"v_mfma_f32_16x16x32_bf16 v[168:171], v[224:227], v[152:155], v[168:171]\n"
"v_mfma_f32_16x16x32_bf16 v[172:175], v[224:227], v[156:159], v[172:175]\n"
"v_mfma_f32_16x16x32_bf16 v[176:179], v[228:231], v[144:147], v[176:179]\n"
"v_mfma_f32_16x16x32_bf16 v[180:183], v[228:231], v[148:151], v[180:183]\n"
"v_mfma_f32_16x16x32_bf16 v[184:187], v[228:231], v[152:155], v[184:187]\n"
"v_mfma_f32_16x16x32_bf16 v[188:191], v[228:231], v[156:159], v[188:191]\n"
"v_mfma_f32_16x16x32_bf16 v[160:163], v[232:235], a[80:83], v[160:163]\n"
"v_mfma_f32_16x16x32_bf16 v[164:167], v[232:235], a[84:87], v[164:167]\n"
"v_mfma_f32_16x16x32_bf16 v[168:171], v[232:235], a[88:91], v[168:171]\n"
"v_mfma_f32_16x16x32_bf16 v[172:175], v[232:235], a[92:95], v[172:175]\n"
"v_mfma_f32_16x16x32_bf16 v[176:179], v[236:239], a[80:83], v[176:179]\n"
"v_mfma_f32_16x16x32_bf16 v[180:183], v[236:239], a[84:87], v[180:183]\n"
"v_mfma_f32_16x16x32_bf16 v[184:187], v[236:239], a[88:91], v[184:187]\n"
"v_mfma_f32_16x16x32_bf16 v[188:191], v[236:239], a[92:95], v[188:191]\n"
"s_waitcnt lgkmcnt(0)\n"
"v_mfma_f32_16x16x32_bf16 v[160:163], v[208:211], v[32:35], v[160:163]\n"
"v_mfma_f32_16x16x32_bf16 v[164:167], v[208:211], v[36:39], v[164:167]\n"
"v_mfma_f32_16x16x32_bf16 v[168:171], v[208:211], v[40:43], v[168:171]\n"
"v_mfma_f32_16x16x32_bf16 v[172:175], v[208:211], v[44:47], v[172:175]\n"
"v_mfma_f32_16x16x32_bf16 v[176:179], v[212:215], v[32:35], v[176:179]\n"
"s_waitcnt vmcnt(19)\n"
"s_barrier\n"
"ds_read_b64_tr_b16 v[224:225], v201 offset:52736\n"
"ds_read_b64_tr_b16 v[226:227], v201 offset:52992\n"
"v_mfma_f32_16x16x32_bf16 v[180:183], v[212:215], v[36:39], v[180:183]\n"
"ds_read_b64_tr_b16 v[228:229], v201 offset:52768\n"
"ds_read_b64_tr_b16 v[230:231], v201 offset:53024\n"
"v_mfma_f32_16x16x32_bf16 v[184:187], v[212:215], v[40:43], v[184:187]\n"
"ds_read_b64_tr_b16 v[232:233], v204 offset:52736\n"
"ds_read_b64_tr_b16 v[234:235], v204 offset:52992\n"
"v_mfma_f32_16x16x32_bf16 v[188:191], v[212:215], v[44:47], v[188:191]\n"
"ds_read_b64_tr_b16 v[236:237], v204 offset:52768\n"
"ds_read_b64_tr_b16 v[238:239], v204 offset:53024\n"
"v_mfma_f32_16x16x32_bf16 v[160:163], v[216:219], a[96:99], v[160:163]\n"
"v_mfma_f32_16x16x32_bf16 v[164:167], v[216:219], a[100:103], v[164:167]\n"
"v_mfma_f32_16x16x32_bf16 v[168:171], v[216:219], a[104:107], v[168:171]\n"
"v_mfma_f32_16x16x32_bf16 v[172:175], v[216:219], a[108:111], v[172:175]\n"
"v_mfma_f32_16x16x32_bf16 v[176:179], v[220:223], a[96:99], v[176:179]\n"
"ds_read_b64_tr_b16 v[208:209], v200 offset:59136\n"
"ds_read_b64_tr_b16 v[210:211], v200 offset:59392\n"
"v_mfma_f32_16x16x32_bf16 v[180:183], v[220:223], a[100:103], v[180:183]\n"
"ds_read_b64_tr_b16 v[212:213], v200 offset:59168\n"
"ds_read_b64_tr_b16 v[214:215], v200 offset:59424\n"
"v_mfma_f32_16x16x32_bf16 v[184:187], v[220:223], a[104:107], v[184:187]\n"
"ds_read_b64_tr_b16 v[216:217], v203 offset:59136\n"
"ds_read_b64_tr_b16 v[218:219], v203 offset:59392\n"
"v_mfma_f32_16x16x32_bf16 v[188:191], v[220:223], a[108:111], v[188:191]\n"
"v_cmp_eq_u32 vcc, v254, v249\n"
"s_waitcnt lgkmcnt(6)\n"
"v_mfma_f32_16x16x32_bf16 v[160:163], v[224:227], v[48:51], v[160:163]\n"
"ds_read_b64_tr_b16 v[220:221], v203 offset:59168\n"
"ds_read_b64_tr_b16 v[222:223], v203 offset:59424\n"
"v_mfma_f32_16x16x32_bf16 v[164:167], v[224:227], v[52:55], v[164:167]\n"
"s_cbranch_vccnz L_thb_%=\n"
"L_bhb_%=:\n"
"v_mfma_f32_16x16x32_bf16 v[168:171], v[224:227], v[56:59], v[168:171]\n"
"v_mfma_f32_16x16x32_bf16 v[172:175], v[224:227], v[60:63], v[172:175]\n"
"v_mfma_f32_16x16x32_bf16 v[176:179], v[228:231], v[48:51], v[176:179]\n"
"v_mfma_f32_16x16x32_bf16 v[180:183], v[228:231], v[52:55], v[180:183]\n"
"v_mfma_f32_16x16x32_bf16 v[184:187], v[228:231], v[56:59], v[184:187]\n"
"s_lshl_b32 s76, s82, 2\n"
"s_add_u32 s76, s76, s29\n"
"s_add_u32 s76, s76, 0x1000\n"
"v_mfma_f32_16x16x32_bf16 v[188:191], v[228:231], v[60:63], v[188:191]\n"
"s_waitcnt vmcnt(16)\n"
"buffer_store_dword v241, off, s[68:71], s76 sc1\n"
"v_mfma_f32_16x16x32_bf16 v[160:163], v[232:235], a[112:115], v[160:163]\n"
"v_mfma_f32_16x16x32_bf16 v[164:167], v[232:235], a[116:119], v[164:167]\n"
"v_mfma_f32_16x16x32_bf16 v[168:171], v[232:235], a[120:123], v[168:171]\n"
"v_mfma_f32_16x16x32_bf16 v[172:175], v[232:235], a[124:127], v[172:175]\n"
"v_mfma_f32_16x16x32_bf16 v[176:179], v[236:239], a[112:115], v[176:179]\n"
"ds_read_b64_tr_b16 v[224:225], v201 offset:35840\n"
"ds_read_b64_tr_b16 v[226:227], v201 offset:36096\n"
"v_mfma_f32_16x16x32_bf16 v[180:183], v[236:239], a[116:119], v[180:183]\n"
"ds_read_b64_tr_b16 v[228:229], v201 offset:35872\n"
"ds_read_b64_tr_b16 v[230:231], v201 offset:36128\n"
"v_mfma_f32_16x16x32_bf16 v[184:187], v[236:239], a[120:123], v[184:187]\n"
"ds_read_b64_tr_b16 v[232:233], v204 offset:35840\n"
"ds_read_b64_tr_b16 v[234:235], v204 offset:36096\n"
"v_mfma_f32_16x16x32_bf16 v[188:191], v[236:239], a[124:127], v[188:191]\n"
"s_waitcnt lgkmcnt(6)\n"
"v_mfma_f32_16x16x32_bf16 v[96:99], v[208:211], v[0:3], 0\n"
"ds_read_b64_tr_b16 v[236:237], v204 offset:35872\n"
"ds_read_b64_tr_b16 v[238:239], v204 offset:36128\n"
"v_mfma_f32_16x16x32_bf16 v[100:103], v[208:211], v[4:7], 0\n"
"s_lshl_b32 s31, s84, 10\n"
"s_add_u32 s31, s31, s93\n"
"v_mfma_f32_16x16x32_bf16 v[104:107], v[208:211], v[8:11], 0\n"
"v_cvt_pk_bf16_f32 v160, v160, v161\n"
"v_cvt_pk_bf16_f32 v161, v162, v163\n"
"v_mfma_f32_16x16x32_bf16 v[108:111], v[208:211], v[12:15], 0\n"
"v_cvt_pk_bf16_f32 v162, v176, v177\n"
"v_cvt_pk_bf16_f32 v163, v178, v179\n"
"v_mfma_f32_16x16x32_bf16 v[112:115], v[212:215], v[0:3], 0\n"
"buffer_store_dwordx4 v[160:163], v248, s[60:63], s31 offen offset:0 sc1\n"
"v_mfma_f32_16x16x32_bf16 v[116:119], v[212:215], v[4:7], 0\n"
"v_cvt_pk_bf16_f32 v164, v164, v165\n"
"v_cvt_pk_bf16_f32 v165, v166, v167\n"
"v_mfma_f32_16x16x32_bf16 v[120:123], v[212:215], v[8:11], 0\n"
"v_cvt_pk_bf16_f32 v166, v180, v181\n"
"v_cvt_pk_bf16_f32 v167, v182, v183\n"
"v_mfma_f32_16x16x32_bf16 v[124:127], v[212:215], v[12:15], 0\n"
"s_lshl_b32 s76, s83, 2\n"
"s_add_u32 s76, s76, s29\n"
"s_add_u32 s76, s76, 0x1000\n"
"v_mfma_f32_16x16x32_bf16 v[96:99], v[216:219], a[0:3], v[96:99]\n"
"s_waitcnt vmcnt(2)\n"
"buffer_store_dword v241, off, s[68:71], s76 sc1\n"
"v_mfma_f32_16x16x32_bf16 v[100:103], v[216:219], a[4:7], v[100:103]\n"
"buffer_store_dwordx4 v[164:167], v248, s[60:63], s31 offen offset:1024 sc1\n"
"v_mfma_f32_16x16x32_bf16 v[104:107], v[216:219], a[8:11], v[104:107]\n"
"v_cvt_pk_bf16_f32 v168, v168, v169\n"
"v_cvt_pk_bf16_f32 v169, v170, v171\n"
"v_mfma_f32_16x16x32_bf16 v[108:111], v[216:219], a[12:15], v[108:111]\n"
"v_cvt_pk_bf16_f32 v170, v184, v185\n"
"v_cvt_pk_bf16_f32 v171, v186, v187\n"
"v_mfma_f32_16x16x32_bf16 v[112:115], v[220:223], a[0:3], v[112:115]\n"
"ds_read_b64_tr_b16 v[208:209], v201 offset:44288\n"
"ds_read_b64_tr_b16 v[210:211], v201 offset:44544\n"
"v_mfma_f32_16x16x32_bf16 v[116:119], v[220:223], a[4:7], v[116:119]\n"
"ds_read_b64_tr_b16 v[212:213], v201 offset:44320\n"
"ds_read_b64_tr_b16 v[214:215], v201 offset:44576\n"
"v_mfma_f32_16x16x32_bf16 v[120:123], v[220:223], a[8:11], v[120:123]\n"
"ds_read_b64_tr_b16 v[216:217], v204 offset:44288\n"
"ds_read_b64_tr_b16 v[218:219], v204 offset:44544\n"
"v_mfma_f32_16x16x32_bf16 v[124:127], v[220:223], a[12:15], v[124:127]\n"
"v_cvt_pk_bf16_f32 v172, v172, v173\n"
"s_waitcnt lgkmcnt(6)\n"
"v_mfma_f32_16x16x32_bf16 v[96:99], v[224:227], v[16:19], v[96:99]\n"
"ds_read_b64_tr_b16 v[220:221], v204 offset:44320\n"
"ds_read_b64_tr_b16 v[222:223], v204 offset:44576\n"
"v_mfma_f32_16x16x32_bf16 v[100:103], v[224:227], v[20:23], v[100:103]\n"
"buffer_store_dwordx4 v[168:171], v248, s[60:63], s31 offen offset:2048 sc1\n"
"v_mfma_f32_16x16x32_bf16 v[104:107], v[224:227], v[24:27], v[104:107]\n"
"v_cvt_pk_bf16_f32 v173, v174, v175\n"
"v_cvt_pk_bf16_f32 v174, v188, v189\n"
"v_mfma_f32_16x16x32_bf16 v[108:111], v[224:227], v[28:31], v[108:111]\n"
"v_cvt_pk_bf16_f32 v175, v190, v191\n"
"v_mfma_f32_16x16x32_bf16 v[112:115], v[228:231], v[16:19], v[112:115]\n"
"buffer_store_dwordx4 v[172:175], v248, s[60:63], s31 offen offset:3072 sc1\n"
"v_mfma_f32_16x16x32_bf16 v[116:119], v[228:231], v[20:23], v[116:119]\n"
"v_mfma_f32_16x16x32_bf16 v[120:123], v[228:231], v[24:27], v[120:123]\n"
"v_mfma_f32_16x16x32_bf16 v[124:127], v[228:231], v[28:31], v[124:127]\n"
"v_mfma_f32_16x16x32_bf16 v[96:99], v[232:235], a[16:19], v[96:99]\n"
"v_mfma_f32_16x16x32_bf16 v[100:103], v[232:235], a[20:23], v[100:103]\n"
"v_mfma_f32_16x16x32_bf16 v[104:107], v[232:235], a[24:27], v[104:107]\n"
"v_mfma_f32_16x16x32_bf16 v[108:111], v[232:235], a[28:31], v[108:111]\n"
"v_mfma_f32_16x16x32_bf16 v[112:115], v[236:239], a[16:19], v[112:115]\n"
"ds_read_b64_tr_b16 v[224:225], v202 offset:4096\n"
"ds_read_b64_tr_b16 v[226:227], v202 offset:4352\n"
"v_mfma_f32_16x16x32_bf16 v[116:119], v[236:239], a[20:23], v[116:119]\n"
"ds_read_b64_tr_b16 v[228:229], v202 offset:4128\n"
"ds_read_b64_tr_b16 v[230:231], v202 offset:4384\n"
"v_mfma_f32_16x16x32_bf16 v[120:123], v[236:239], a[24:27], v[120:123]\n"
"ds_read_b64_tr_b16 v[232:233], v205 offset:4096\n"
"ds_read_b64_tr_b16 v[234:235], v205 offset:4352\n"
"v_mfma_f32_16x16x32_bf16 v[124:127], v[236:239], a[28:31], v[124:127]\n"
"s_waitcnt lgkmcnt(6)\n"
"v_mfma_f32_16x16x32_bf16 v[96:99], v[208:211], v[64:67], v[96:99]\n"
"ds_read_b64_tr_b16 v[236:237], v205 offset:4128\n"
"ds_read_b64_tr_b16 v[238:239], v205 offset:4384\n"
"v_mfma_f32_16x16x32_bf16 v[100:103], v[208:211], v[68:71], v[100:103]\n"
"v_mfma_f32_16x16x32_bf16 v[104:107], v[208:211], v[72:75], v[104:107]\n"
"v_mfma_f32_16x16x32_bf16 v[108:111], v[208:211], v[76:79], v[108:111]\n"
"v_mfma_f32_16x16x32_bf16 v[112:115], v[212:215], v[64:67], v[112:115]\n"
"v_mfma_f32_16x16x32_bf16 v[116:119], v[212:215], v[68:71], v[116:119]\n"
"v_mfma_f32_16x16x32_bf16 v[120:123], v[212:215], v[72:75], v[120:123]\n"
"v_mfma_f32_16x16x32_bf16 v[124:127], v[212:215], v[76:79], v[124:127]\n"
"v_and_b32 v192, 15, v251\n"
"v_lshrrev_b32 v193, 4, v251\n"
"v_lshlrev_b32 v193, 2, v193\n"
"v_sub_u32 v192, v192, v193\n"
"v_mfma_f32_16x16x32_bf16 v[96:99], v[216:219], a[32:35], v[96:99]\n"
"v_cmp_eq_u32 vcc, 0, v192\n"
"v_mov_b32 v194, 0x3f80\n"
"v_mov_b32 v195, 0x3f800000\n"
"v_cndmask_b32 v193, 0, v194, vcc\n"
"v_cmp_eq_u32 vcc, 1, v192\n"
"s_cmp_eq_u32 s99, 0\n"
"s_cselect_b32 s79, -1, 0\n"
"v_cndmask_b32 v193, v193, v195, vcc\n"
"v_and_b32 v194, s79, v193\n"
"v_xor_b32 v195, v194, v193\n"
"v_accvgpr_write_b32 a248, v194\n"
"v_accvgpr_write_b32 a250, v195\n"
"v_accvgpr_write_b32 a252, v194\n"
"v_mfma_f32_16x16x32_bf16 v[100:103], v[216:219], a[36:39], v[100:103]\n"
"v_cmp_eq_u32 vcc, 2, v192\n"
"v_mov_b32 v194, 0x3f80\n"
"v_mov_b32 v195, 0x3f800000\n"
"v_cndmask_b32 v193, 0, v194, vcc\n"
"v_cmp_eq_u32 vcc, 3, v192\n"
"s_cmp_eq_u32 s99, 0\n"
"s_cselect_b32 s79, -1, 0\n"
"v_cndmask_b32 v193, v193, v195, vcc\n"
"v_and_b32 v194, s79, v193\n"
"v_xor_b32 v195, v194, v193\n"
"v_accvgpr_write_b32 a249, v194\n"
"v_accvgpr_write_b32 a251, v195\n"
"v_accvgpr_write_b32 a253, v194\n"
"v_mfma_f32_16x16x32_bf16 v[104:107], v[216:219], a[40:43], v[104:107]\n"
"s_lshr_b32 s79, s89, 1\n"
"s_lshl_b32 s79, s79, 6\n"
"v_and_b32 v240, 15, v251\n"
"v_add_u32 v240, s79, v240\n"
"v_mul_u32_u24 v240, 0x700, v240\n"
"v_lshrrev_b32 v193, 4, v251\n"
"v_lshl_add_u32 v240, v193, 3, v240\n"
"s_and_b32 s79, s89, 1\n"
"s_lshl_b32 s79, s79, 6\n"
"v_add_u32 v240, s79, v240\n"
"v_mfma_f32_16x16x32_bf16 v[108:111], v[216:219], a[44:47], v[108:111]\n"
"s_cmp_lt_u32 s90, 6\n"
"s_cselect_b32 s92, 0x80, 0\n"
"v_mfma_f32_16x16x32_bf16 v[112:115], v[220:223], a[32:35], v[112:115]\n"
"ds_read_b64_tr_b16 v[208:209], v202 offset:12544\n"
"ds_read_b64_tr_b16 v[210:211], v202 offset:12800\n"
"v_mfma_f32_16x16x32_bf16 v[116:119], v[220:223], a[36:39], v[116:119]\n"
"ds_read_b64_tr_b16 v[212:213], v202 offset:12576\n"
"ds_read_b64_tr_b16 v[214:215], v202 offset:12832\n"
"v_mfma_f32_16x16x32_bf16 v[120:123], v[220:223], a[40:43], v[120:123]\n"
"ds_read_b64_tr_b16 v[216:217], v205 offset:12544\n"
"ds_read_b64_tr_b16 v[218:219], v205 offset:12800\n"
"v_mfma_f32_16x16x32_bf16 v[124:127], v[220:223], a[44:47], v[124:127]\n"
"v_add_u32 v244, s99, v200\n"
"s_waitcnt lgkmcnt(6)\n"
"v_mfma_f32_16x16x32_bf16 v[96:99], v[224:227], v[80:83], v[96:99]\n"
"ds_read_b64_tr_b16 v[220:221], v205 offset:12576\n"
"ds_read_b64_tr_b16 v[222:223], v205 offset:12832\n"
"v_mfma_f32_16x16x32_bf16 v[100:103], v[224:227], v[84:87], v[100:103]\n"
"v_subrev_u32 v245, s99, v200\n"
"v_add_u32 v246, s99, v203\n"
"v_mfma_f32_16x16x32_bf16 v[104:107], v[224:227], v[88:91], v[104:107]\n"
"v_subrev_u32 v247, s99, v203\n"
"s_mov_b32 s97, 0\n"
"v_mfma_f32_16x16x32_bf16 v[108:111], v[224:227], v[92:95], v[108:111]\n"
"s_bfe_u32 s48, s17, 0x40000\n"
"s_bitset1_b32 s97, s48\n"
"v_mfma_f32_16x16x32_bf16 v[112:115], v[228:231], v[80:83], v[112:115]\n"
"s_lshl_b32 s48, s48, 14\n"
"s_bfe_u32 s49, s17, 0x40004\n"
"v_mfma_f32_16x16x32_bf16 v[116:119], v[228:231], v[84:87], v[116:119]\n"
"s_bitset1_b32 s97, s49\n"
"s_lshl_b32 s49, s49, 14\n"
"v_mfma_f32_16x16x32_bf16 v[120:123], v[228:231], v[88:91], v[120:123]\n"
"s_bfe_u32 s50, s17, 0x40008\n"
"s_bitset1_b32 s97, s50\n"
"v_mfma_f32_16x16x32_bf16 v[124:127], v[228:231], v[92:95], v[124:127]\n"
"s_lshl_b32 s50, s50, 14\n"
"s_bfe_u32 s51, s17, 0x4000c\n"
"v_mfma_f32_16x16x32_bf16 v[96:99], v[232:235], a[48:51], v[96:99]\n"
"s_bitset1_b32 s97, s51\n"
"s_lshl_b32 s51, s51, 14\n"
"v_mfma_f32_16x16x32_bf16 v[100:103], v[232:235], a[52:55], v[100:103]\n"
"s_bfe_u32 s52, s17, 0x40010\n"
"s_bitset1_b32 s92, s52\n"
"v_mfma_f32_16x16x32_bf16 v[104:107], v[232:235], a[56:59], v[104:107]\n"
"s_lshl_b32 s52, s52, 14\n"
"s_bfe_u32 s53, s17, 0x40014\n"
"v_mfma_f32_16x16x32_bf16 v[108:111], v[232:235], a[60:63], v[108:111]\n"
"s_bitset1_b32 s92, s53\n"
"s_lshl_b32 s53, s53, 14\n"
"v_mfma_f32_16x16x32_bf16 v[112:115], v[236:239], a[48:51], v[112:115]\n"
"s_bfe_u32 s54, s17, 0x40018\n"
"s_lshl_b32 s54, s54, 14\n"
"v_mfma_f32_16x16x32_bf16 v[116:119], v[236:239], a[52:55], v[116:119]\n"
"v_mfma_f32_16x16x32_bf16 v[120:123], v[236:239], a[56:59], v[120:123]\n"
"v_mfma_f32_16x16x32_bf16 v[124:127], v[236:239], a[60:63], v[124:127]\n"
"s_waitcnt lgkmcnt(0)\n"
"v_mfma_f32_16x16x32_bf16 v[96:99], v[208:211], v[128:131], v[96:99]\n"
"v_mfma_f32_16x16x32_bf16 v[100:103], v[208:211], v[132:135], v[100:103]\n"
"v_mfma_f32_16x16x32_bf16 v[104:107], v[208:211], v[136:139], v[104:107]\n"
"v_mfma_f32_16x16x32_bf16 v[108:111], v[208:211], v[140:143], v[108:111]\n"
"v_mfma_f32_16x16x32_bf16 v[112:115], v[212:215], v[128:131], v[112:115]\n"
"s_barrier\n"
"ds_read_b64_tr_b16 v[224:225], v202 offset:20992\n"
"ds_read_b64_tr_b16 v[226:227], v202 offset:21248\n"
"v_mfma_f32_16x16x32_bf16 v[116:119], v[212:215], v[132:135], v[116:119]\n"
"s_add_u32 m0, s74, 0x21000\n"
"s_add_u32 s75, s86, 0x15000\n"
"v_mfma_f32_16x16x32_bf16 v[120:123], v[212:215], v[136:139], v[120:123]\n"
"buffer_load_dwordx4 v242, s[56:59], s75 offen lds\n"
"v_mfma_f32_16x16x32_bf16 v[124:127], v[212:215], v[140:143], v[124:127]\n"
"ds_read_b64_tr_b16 v[228:229], v202 offset:21024\n"
"ds_read_b64_tr_b16 v[230:231], v202 offset:21280\n"
"v_mfma_f32_16x16x32_bf16 v[96:99], v[216:219], a[64:67], v[96:99]\n"
"s_add_u32 m0, s74, 0x22080\n"
"v_mfma_f32_16x16x32_bf16 v[100:103], v[216:219], a[68:71], v[100:103]\n"
"buffer_load_dwordx4 v243, s[56:59], s75 offen lds\n"
"ds_read_b64_tr_b16 v[232:233], v205 offset:20992\n"
"ds_read_b64_tr_b16 v[234:235], v205 offset:21248\n"
"ds_read_b64_tr_b16 v[236:237], v205 offset:21024\n"
"ds_read_b64_tr_b16 v[238:239], v205 offset:21280\n"
"v_mfma_f32_16x16x32_bf16 v[104:107], v[216:219], a[72:75], v[104:107]\n"
"v_mfma_f32_16x16x32_bf16 v[108:111], v[216:219], a[76:79], v[108:111]\n"
"s_add_u32 m0, s74, 0x23100\n"
"s_add_u32 s75, s86, 0x1c000\n"
"v_mfma_f32_16x16x32_bf16 v[112:115], v[220:223], a[64:67], v[112:115]\n"
"buffer_load_dwordx4 v242, s[56:59], s75 offen lds\n"
"v_mfma_f32_16x16x32_bf16 v[116:119], v[220:223], a[68:71], v[116:119]\n"
"ds_read_b64_tr_b16 v[208:209], v201 offset:61184\n"
"ds_read_b64_tr_b16 v[210:211], v201 offset:61440\n"
"v_mfma_f32_16x16x32_bf16 v[120:123], v[220:223], a[72:75], v[120:123]\n"
"s_add_u32 m0, s74, 0x24180\n"
"v_mfma_f32_16x16x32_bf16 v[124:127], v[220:223], a[76:79], v[124:127]\n"
"buffer_load_dwordx4 v243, s[56:59], s75 offen lds\n"
"s_waitcnt lgkmcnt(2)\n"
"v_mfma_f32_16x16x32_bf16 v[96:99], v[224:227], v[144:147], v[96:99]\n"
"ds_read_b64_tr_b16 v[212:213], v201 offset:61216\n"
"ds_read_b64_tr_b16 v[214:215], v201 offset:61472\n"
"v_mfma_f32_16x16x32_bf16 v[100:103], v[224:227], v[148:151], v[100:103]\n"
"s_add_u32 m0, s74, 0x1ce00\n"
"s_add_u32 s75, s86, 0x23000\n"
"v_mfma_f32_16x16x32_bf16 v[104:107], v[224:227], v[152:155], v[104:107]\n"
"buffer_load_dwordx4 v242, s[56:59], s75 offen lds\n"
"v_mfma_f32_16x16x32_bf16 v[108:111], v[224:227], v[156:159], v[108:111]\n"
"ds_read_b64_tr_b16 v[216:217], v204 offset:61184\n"
"ds_read_b64_tr_b16 v[218:219], v204 offset:61440\n"
"v_mfma_f32_16x16x32_bf16 v[112:115], v[228:231], v[144:147], v[112:115]\n"
"s_add_u32 m0, s74, 0x1de80\n"
"v_mfma_f32_16x16x32_bf16 v[116:119], v[228:231], v[148:151], v[116:119]\n"
"buffer_load_dwordx4 v243, s[56:59], s75 offen lds\n"
"v_mfma_f32_16x16x32_bf16 v[120:123], v[228:231], v[152:155], v[120:123]\n"
"ds_read_b64_tr_b16 v[220:221], v204 offset:61216\n"
"ds_read_b64_tr_b16 v[222:223], v204 offset:61472\n"
"v_mfma_f32_16x16x32_bf16 v[124:127], v[228:231], v[156:159], v[124:127]\n"
"s_add_u32 m0, s74, 0x18c00\n"
"s_add_u32 s75, s86, 0x2a000\n"
"v_mfma_f32_16x16x32_bf16 v[96:99], v[232:235], a[80:83], v[96:99]\n"
"buffer_load_dwordx4 v242, s[56:59], s75 offen lds\n"
"v_mfma_f32_16x16x32_bf16 v[100:103], v[232:235], a[84:87], v[100:103]\n"
"v_mfma_f32_16x16x32_bf16 v[104:107], v[232:235], a[88:91], v[104:107]\n"
"s_add_u32 m0, s74, 0x19c80\n"
"v_mfma_f32_16x16x32_bf16 v[108:111], v[232:235], a[92:95], v[108:111]\n"
"buffer_load_dwordx4 v243, s[56:59], s75 offen lds\n"
"v_mfma_f32_16x16x32_bf16 v[112:115], v[236:239], a[80:83], v[112:115]\n"
"ds_read_b64_tr_b16 v[224:225], v201 offset:2048\n"
"ds_read_b64_tr_b16 v[226:227], v201 offset:2304\n"
"v_mfma_f32_16x16x32_bf16 v[116:119], v[236:239], a[84:87], v[116:119]\n"
"s_add_u32 m0, s74, 0x1ad00\n"
"s_add_u32 s75, s86, 0x31000\n"
"v_mfma_f32_16x16x32_bf16 v[120:123], v[236:239], a[88:91], v[120:123]\n"
"buffer_load_dwordx4 v242, s[56:59], s75 offen lds\n"
"v_mfma_f32_16x16x32_bf16 v[124:127], v[236:239], a[92:95], v[124:127]\n"
"s_waitcnt lgkmcnt(2)\n"
"v_mfma_f32_16x16x32_bf16 v[96:99], v[208:211], v[32:35], v[96:99]\n"
"s_add_u32 m0, s74, 0x1bd80\n"
"v_mfma_f32_16x16x32_bf16 v[100:103], v[208:211], v[36:39], v[100:103]\n"
"buffer_load_dwordx4 v243, s[56:59], s75 offen lds\n"
"v_mfma_f32_16x16x32_bf16 v[104:107], v[208:211], v[40:43], v[104:107]\n"
"ds_read_b64_tr_b16 v[228:229], v201 offset:2080\n"
"ds_read_b64_tr_b16 v[230:231], v201 offset:2336\n"
"v_mfma_f32_16x16x32_bf16 v[108:111], v[208:211], v[44:47], v[108:111]\n"
"ds_read_b64_tr_b16 v[232:233], v204 offset:2048\n"
"ds_read_b64_tr_b16 v[234:235], v204 offset:2304\n"
"v_mfma_f32_16x16x32_bf16 v[112:115], v[212:215], v[32:35], v[112:115]\n"
"ds_read_b64_tr_b16 v[236:237], v204 offset:2080\n"
"ds_read_b64_tr_b16 v[238:239], v204 offset:2336\n"
"v_mfma_f32_16x16x32_bf16 v[116:119], v[212:215], v[36:39], v[116:119]\n"
"v_mfma_f32_16x16x32_bf16 v[120:123], v[212:215], v[40:43], v[120:123]\n"
"v_mfma_f32_16x16x32_bf16 v[124:127], v[212:215], v[44:47], v[124:127]\n"
"v_mfma_f32_16x16x32_bf16 v[96:99], v[216:219], a[96:99], v[96:99]\n"
"v_mfma_f32_16x16x32_bf16 v[100:103], v[216:219], a[100:103], v[100:103]\n"
"v_mfma_f32_16x16x32_bf16 v[104:107], v[216:219], a[104:107], v[104:107]\n"
"v_mfma_f32_16x16x32_bf16 v[108:111], v[216:219], a[108:111], v[108:111]\n"
"v_mfma_f32_16x16x32_bf16 v[112:115], v[220:223], a[96:99], v[112:115]\n"
"ds_read_b64_tr_b16 v[208:209], v201 offset:10496\n"
"ds_read_b64_tr_b16 v[210:211], v201 offset:10752\n"
"v_mfma_f32_16x16x32_bf16 v[116:119], v[220:223], a[100:103], v[116:119]\n"
"ds_read_b64_tr_b16 v[212:213], v201 offset:10528\n"
"ds_read_b64_tr_b16 v[214:215], v201 offset:10784\n"
"v_mfma_f32_16x16x32_bf16 v[120:123], v[220:223], a[104:107], v[120:123]\n"
"ds_read_b64_tr_b16 v[216:217], v204 offset:10496\n"
"ds_read_b64_tr_b16 v[218:219], v204 offset:10752\n"
"v_mfma_f32_16x16x32_bf16 v[124:127], v[220:223], a[108:111], v[124:127]\n"
"s_waitcnt lgkmcnt(6)\n"
"v_mfma_f32_16x16x32_bf16 v[96:99], v[224:227], v[48:51], v[96:99]\n"
"ds_read_b64_tr_b16 v[220:221], v204 offset:10528\n"
"ds_read_b64_tr_b16 v[222:223], v204 offset:10784\n"
"v_mfma_f32_16x16x32_bf16 v[100:103], v[224:227], v[52:55], v[100:103]\n"
"v_mfma_f32_16x16x32_bf16 v[104:107], v[224:227], v[56:59], v[104:107]\n"
"v_mfma_f32_16x16x32_bf16 v[108:111], v[224:227], v[60:63], v[108:111]\n"
"v_mfma_f32_16x16x32_bf16 v[112:115], v[228:231], v[48:51], v[112:115]\n"
"v_mfma_f32_16x16x32_bf16 v[116:119], v[228:231], v[52:55], v[116:119]\n"
"v_mfma_f32_16x16x32_bf16 v[120:123], v[228:231], v[56:59], v[120:123]\n"
"s_lshl_b32 s76, s84, 2\n"
"s_add_u32 s76, s76, s29\n"
"s_add_u32 s76, s76, 0x1000\n"
"v_mfma_f32_16x16x32_bf16 v[124:127], v[228:231], v[60:63], v[124:127]\n"
"s_waitcnt vmcnt(10)\n"
"buffer_store_dword v241, off, s[68:71], s76 sc1\n"
"v_mfma_f32_16x16x32_bf16 v[96:99], v[232:235], a[112:115], v[96:99]\n"
"v_mfma_f32_16x16x32_bf16 v[100:103], v[232:235], a[116:119], v[100:103]\n"
"v_mfma_f32_16x16x32_bf16 v[104:107], v[232:235], a[120:123], v[104:107]\n"
"v_mfma_f32_16x16x32_bf16 v[108:111], v[232:235], a[124:127], v[108:111]\n"
"v_mfma_f32_16x16x32_bf16 v[112:115], v[236:239], a[112:115], v[112:115]\n"
"ds_read_b64_tr_b16 v[224:225], v201 offset:18944\n"
"ds_read_b64_tr_b16 v[226:227], v201 offset:19200\n"
"v_mfma_f32_16x16x32_bf16 v[116:119], v[236:239], a[116:119], v[116:119]\n"
"ds_read_b64_tr_b16 v[228:229], v201 offset:18976\n"
"ds_read_b64_tr_b16 v[230:231], v201 offset:19232\n"
"v_mfma_f32_16x16x32_bf16 v[120:123], v[236:239], a[120:123], v[120:123]\n"
"ds_read_b64_tr_b16 v[232:233], v204 offset:18944\n"
"ds_read_b64_tr_b16 v[234:235], v204 offset:19200\n"
"v_mfma_f32_16x16x32_bf16 v[124:127], v[236:239], a[124:127], v[124:127]\n"
"s_waitcnt lgkmcnt(6)\n"
"v_mfma_f32_16x16x32_bf16 v[160:163], v[208:211], v[0:3], 0\n"
"ds_read_b64_tr_b16 v[236:237], v204 offset:18976\n"
"ds_read_b64_tr_b16 v[238:239], v204 offset:19232\n"
"v_mfma_f32_16x16x32_bf16 v[164:167], v[208:211], v[4:7], 0\n"
"s_lshl_b32 s31, s85, 10\n"
"s_add_u32 s31, s31, s93\n"
"v_mfma_f32_16x16x32_bf16 v[168:171], v[208:211], v[8:11], 0\n"
"v_cvt_pk_bf16_f32 v96, v96, v97\n"
"v_cvt_pk_bf16_f32 v97, v98, v99\n"
"v_mfma_f32_16x16x32_bf16 v[172:175], v[208:211], v[12:15], 0\n"
"v_cvt_pk_bf16_f32 v98, v112, v113\n"
"v_cvt_pk_bf16_f32 v99, v114, v115\n"
"v_mfma_f32_16x16x32_bf16 v[176:179], v[212:215], v[0:3], 0\n"
"buffer_store_dwordx4 v[96:99], v248, s[60:63], s31 offen offset:0 sc1\n"
"v_mfma_f32_16x16x32_bf16 v[180:183], v[212:215], v[4:7], 0\n"
"v_cvt_pk_bf16_f32 v100, v100, v101\n"
"v_cvt_pk_bf16_f32 v101, v102, v103\n"
"v_mfma_f32_16x16x32_bf16 v[184:187], v[212:215], v[8:11], 0\n"
"v_cvt_pk_bf16_f32 v102, v116, v117\n"
"v_cvt_pk_bf16_f32 v103, v118, v119\n"
"v_mfma_f32_16x16x32_bf16 v[188:191], v[212:215], v[12:15], 0\n"
"buffer_store_dwordx4 v[100:103], v248, s[60:63], s31 offen offset:1024 sc1\n"
"v_mfma_f32_16x16x32_bf16 v[160:163], v[216:219], a[0:3], v[160:163]\n"
"v_cvt_pk_bf16_f32 v104, v104, v105\n"
"v_cvt_pk_bf16_f32 v105, v106, v107\n"
"v_mfma_f32_16x16x32_bf16 v[164:167], v[216:219], a[4:7], v[164:167]\n"
"v_cvt_pk_bf16_f32 v106, v120, v121\n"
"v_cvt_pk_bf16_f32 v107, v122, v123\n"
"v_mfma_f32_16x16x32_bf16 v[168:171], v[216:219], a[8:11], v[168:171]\n"
"buffer_store_dwordx4 v[104:107], v248, s[60:63], s31 offen offset:2048 sc1\n"
"v_mfma_f32_16x16x32_bf16 v[172:175], v[216:219], a[12:15], v[172:175]\n"
"v_cvt_pk_bf16_f32 v108, v108, v109\n"
"v_cvt_pk_bf16_f32 v109, v110, v111\n"
"v_mfma_f32_16x16x32_bf16 v[176:179], v[220:223], a[0:3], v[176:179]\n"
"ds_read_b64_tr_b16 v[208:209], v201 offset:27392\n"
"ds_read_b64_tr_b16 v[210:211], v201 offset:27648\n"
"v_mfma_f32_16x16x32_bf16 v[180:183], v[220:223], a[4:7], v[180:183]\n"
"ds_read_b64_tr_b16 v[212:213], v201 offset:27424\n"
"ds_read_b64_tr_b16 v[214:215], v201 offset:27680\n"
"v_mfma_f32_16x16x32_bf16 v[184:187], v[220:223], a[8:11], v[184:187]\n"
"ds_read_b64_tr_b16 v[216:217], v204 offset:27392\n"
"ds_read_b64_tr_b16 v[218:219], v204 offset:27648\n"
"v_mfma_f32_16x16x32_bf16 v[188:191], v[220:223], a[12:15], v[188:191]\n"
"v_cvt_pk_bf16_f32 v110, v124, v125\n"
"s_waitcnt lgkmcnt(6)\n"
"v_mfma_f32_16x16x32_bf16 v[160:163], v[224:227], v[16:19], v[160:163]\n"
"ds_read_b64_tr_b16 v[220:221], v204 offset:27424\n"
"ds_read_b64_tr_b16 v[222:223], v204 offset:27680\n"
"v_mfma_f32_16x16x32_bf16 v[164:167], v[224:227], v[20:23], v[164:167]\n"
"v_cvt_pk_bf16_f32 v111, v126, v127\n"
"v_mfma_f32_16x16x32_bf16 v[168:171], v[224:227], v[24:27], v[168:171]\n"
"buffer_store_dwordx4 v[108:111], v248, s[60:63], s31 offen offset:3072 sc1\n"
"v_mfma_f32_16x16x32_bf16 v[172:175], v[224:227], v[28:31], v[172:175]\n"
"v_mfma_f32_16x16x32_bf16 v[176:179], v[228:231], v[16:19], v[176:179]\n"
"v_mfma_f32_16x16x32_bf16 v[180:183], v[228:231], v[20:23], v[180:183]\n"
"v_mfma_f32_16x16x32_bf16 v[184:187], v[228:231], v[24:27], v[184:187]\n"
"v_mfma_f32_16x16x32_bf16 v[188:191], v[228:231], v[28:31], v[188:191]\n"
"v_mfma_f32_16x16x32_bf16 v[160:163], v[232:235], a[16:19], v[160:163]\n"
"v_mfma_f32_16x16x32_bf16 v[164:167], v[232:235], a[20:23], v[164:167]\n"
"v_mfma_f32_16x16x32_bf16 v[168:171], v[232:235], a[24:27], v[168:171]\n"
"v_mfma_f32_16x16x32_bf16 v[172:175], v[232:235], a[28:31], v[172:175]\n"
"v_mfma_f32_16x16x32_bf16 v[176:179], v[236:239], a[16:19], v[176:179]\n"
"v_mfma_f32_16x16x32_bf16 v[180:183], v[236:239], a[20:23], v[180:183]\n"
"v_mfma_f32_16x16x32_bf16 v[184:187], v[236:239], a[24:27], v[184:187]\n"
"v_mfma_f32_16x16x32_bf16 v[188:191], v[236:239], a[28:31], v[188:191]\n"
"s_waitcnt lgkmcnt(0)\n"
"v_mfma_f32_16x16x32_bf16 v[160:163], v[208:211], v[64:67], v[160:163]\n"
"v_mfma_f32_16x16x32_bf16 v[164:167], v[208:211], v[68:71], v[164:167]\n"
"v_mfma_f32_16x16x32_bf16 v[168:171], v[208:211], v[72:75], v[168:171]\n"
"v_mfma_f32_16x16x32_bf16 v[172:175], v[208:211], v[76:79], v[172:175]\n"
"v_mfma_f32_16x16x32_bf16 v[176:179], v[212:215], v[64:67], v[176:179]\n"
"s_waitcnt vmcnt(13)\n"
"s_barrier\n"
"ds_read_b64_tr_b16 v[224:225], v202 offset:4096\n"
"ds_read_b64_tr_b16 v[226:227], v202 offset:4352\n"
"v_mfma_f32_16x16x32_bf16 v[180:183], v[212:215], v[68:71], v[180:183]\n"
"s_add_u32 m0, s74, 0x25200\n"
"s_add_u32 s75, s87, 0x0\n"
"v_mfma_f32_16x16x32_bf16 v[184:187], v[212:215], v[72:75], v[184:187]\n"
"buffer_load_dwordx4 v242, s[56:59], s75 offen lds\n"
"v_mfma_f32_16x16x32_bf16 v[188:191], v[212:215], v[76:79], v[188:191]\n"
"ds_read_b64_tr_b16 v[228:229], v202 offset:4128\n"
"ds_read_b64_tr_b16 v[230:231], v202 offset:4384\n"
"v_mfma_f32_16x16x32_bf16 v[160:163], v[216:219], a[32:35], v[160:163]\n"
"s_add_u32 m0, s74, 0x26280\n"
"v_mfma_f32_16x16x32_bf16 v[164:167], v[216:219], a[36:39], v[164:167]\n"
"buffer_load_dwordx4 v243, s[56:59], s75 offen lds\n"
"ds_read_b64_tr_b16 v[232:233], v205 offset:4096\n"
"ds_read_b64_tr_b16 v[234:235], v205 offset:4352\n"
"ds_read_b64_tr_b16 v[236:237], v205 offset:4128\n"
"ds_read_b64_tr_b16 v[238:239], v205 offset:4384\n"
"v_mfma_f32_16x16x32_bf16 v[168:171], v[216:219], a[40:43], v[168:171]\n"
"v_mfma_f32_16x16x32_bf16 v[172:175], v[216:219], a[44:47], v[172:175]\n"
"s_add_u32 m0, s74, 0x1ef00\n"
"s_add_u32 s75, s87, 0x7000\n"
"v_mfma_f32_16x16x32_bf16 v[176:179], v[220:223], a[32:35], v[176:179]\n"
"buffer_load_dwordx4 v242, s[56:59], s75 offen lds\n"
"v_mfma_f32_16x16x32_bf16 v[180:183], v[220:223], a[36:39], v[180:183]\n"
"v_mfma_f32_16x16x32_bf16 v[184:187], v[220:223], a[40:43], v[184:187]\n"
"s_add_u32 m0, s74, 0x1ff80\n"
"v_mfma_f32_16x16x32_bf16 v[188:191], v[220:223], a[44:47], v[188:191]\n"
"buffer_load_dwordx4 v243, s[56:59], s75 offen lds\n"
"s_waitcnt lgkmcnt(0)\n"
"v_mfma_f32_16x16x32_bf16 v[160:163], v[224:227], v[80:83], v[160:163]\n"
"v_mfma_f32_16x16x32_bf16 v[164:167], v[224:227], v[84:87], v[164:167]\n"
"s_add_u32 m0, s74, 0x10800\n"
"s_add_u32 s75, s87, 0xe000\n"
"v_mfma_f32_16x16x32_bf16 v[168:171], v[224:227], v[88:91], v[168:171]\n"
"buffer_load_dwordx4 v242, s[56:59], s75 offen lds\n"
"v_mfma_f32_16x16x32_bf16 v[172:175], v[224:227], v[92:95], v[172:175]\n"
"v_mfma_f32_16x16x32_bf16 v[176:179], v[228:231], v[80:83], v[176:179]\n"
"s_add_u32 m0, s74, 0x11880\n"
"v_mfma_f32_16x16x32_bf16 v[180:183], v[228:231], v[84:87], v[180:183]\n"
"buffer_load_dwordx4 v243, s[56:59], s75 offen lds\n"
"v_mfma_f32_16x16x32_bf16 v[184:187], v[228:231], v[88:91], v[184:187]\n"
"s_waitcnt vmcnt(17)\n"
"s_barrier\n"
"ds_read_b64_tr_b16 v[208:209], v202 offset:12544\n"
"ds_read_b64_tr_b16 v[210:211], v202 offset:12800\n"
"v_mfma_f32_16x16x32_bf16 v[188:191], v[228:231], v[92:95], v[188:191]\n"
"s_add_u32 m0, s74, 0x21000\n"
"s_add_u32 s75, s87, 0x15000\n"
"v_mfma_f32_16x16x32_bf16 v[160:163], v[232:235], a[48:51], v[160:163]\n"
"buffer_load_dwordx4 v242, s[56:59], s75 offen lds\n"
"v_mfma_f32_16x16x32_bf16 v[164:167], v[232:235], a[52:55], v[164:167]\n"
"ds_read_b64_tr_b16 v[212:213], v202 offset:12576\n"
"ds_read_b64_tr_b16 v[214:215], v202 offset:12832\n"
"ds_read_b64_tr_b16 v[216:217], v205 offset:12544\n"
"ds_read_b64_tr_b16 v[218:219], v205 offset:12800\n"
"ds_read_b64_tr_b16 v[220:221], v205 offset:12576\n"
"ds_read_b64_tr_b16 v[222:223], v205 offset:12832\n"
"v_mfma_f32_16x16x32_bf16 v[168:171], v[232:235], a[56:59], v[168:171]\n"
"s_add_u32 m0, s74, 0x22080\n"
"v_mfma_f32_16x16x32_bf16 v[172:175], v[232:235], a[60:63], v[172:175]\n"
"buffer_load_dwordx4 v243, s[56:59], s75 offen lds\n"
"v_mfma_f32_16x16x32_bf16 v[176:179], v[236:239], a[48:51], v[176:179]\n"
"v_mfma_f32_16x16x32_bf16 v[180:183], v[236:239], a[52:55], v[180:183]\n"
"s_add_u32 m0, s74, 0x12900\n"
"s_add_u32 s75, s87, 0x1c000\n"
"v_mfma_f32_16x16x32_bf16 v[184:187], v[236:239], a[56:59], v[184:187]\n"
"buffer_load_dwordx4 v242, s[56:59], s75 offen lds\n"
"v_mfma_f32_16x16x32_bf16 v[188:191], v[236:239], a[60:63], v[188:191]\n"
"s_waitcnt lgkmcnt(0)\n"
"v_mfma_f32_16x16x32_bf16 v[160:163], v[208:211], v[128:131], v[160:163]\n"
"s_add_u32 m0, s74, 0x13980\n"
"v_mfma_f32_16x16x32_bf16 v[164:167], v[208:211], v[132:135], v[164:167]\n"
"buffer_load_dwordx4 v243, s[56:59], s75 offen lds\n"
"v_mfma_f32_16x16x32_bf16 v[168:171], v[208:211], v[136:139], v[168:171]\n"
"v_mfma_f32_16x16x32_bf16 v[172:175], v[208:211], v[140:143], v[172:175]\n"
"s_add_u32 m0, s74, 0x14a00\n"
"s_add_u32 s75, s87, 0x23000\n"
"v_mfma_f32_16x16x32_bf16 v[176:179], v[212:215], v[128:131], v[176:179]\n"
"buffer_load_dwordx4 v242, s[56:59], s75 offen lds\n"
"v_mfma_f32_16x16x32_bf16 v[180:183], v[212:215], v[132:135], v[180:183]\n"
"s_waitcnt vmcnt(16)\n"
"s_barrier\n"
"ds_read_b64_tr_b16 v[224:225], v201 offset:52736\n"
"ds_read_b64_tr_b16 v[226:227], v201 offset:52992\n"
"v_mfma_f32_16x16x32_bf16 v[184:187], v[212:215], v[136:139], v[184:187]\n"
"s_add_u32 m0, s74, 0x15a80\n"
"v_mfma_f32_16x16x32_bf16 v[188:191], v[212:215], v[140:143], v[188:191]\n"
"buffer_load_dwordx4 v243, s[56:59], s75 offen lds\n"
"v_mfma_f32_16x16x32_bf16 v[160:163], v[216:219], a[64:67], v[160:163]\n"
"ds_read_b64_tr_b16 v[228:229], v201 offset:52768\n"
"ds_read_b64_tr_b16 v[230:231], v201 offset:53024\n"
"v_mfma_f32_16x16x32_bf16 v[164:167], v[216:219], a[68:71], v[164:167]\n"
"ds_read_b64_tr_b16 v[232:233], v204 offset:52736\n"
"ds_read_b64_tr_b16 v[234:235], v204 offset:52992\n"
"ds_read_b64_tr_b16 v[236:237], v204 offset:52768\n"
"ds_read_b64_tr_b16 v[238:239], v204 offset:53024\n"
"s_add_u32 m0, s74, 0x23100\n"
"s_add_u32 s75, s87, 0x2a000\n"
"v_mfma_f32_16x16x32_bf16 v[168:171], v[216:219], a[72:75], v[168:171]\n"
"buffer_load_dwordx4 v242, s[56:59], s75 offen lds\n"
"v_mfma_f32_16x16x32_bf16 v[172:175], v[216:219], a[76:79], v[172:175]\n"
"v_mfma_f32_16x16x32_bf16 v[176:179], v[220:223], a[64:67], v[176:179]\n"
"s_add_u32 m0, s74, 0x24180\n"
"v_mfma_f32_16x16x32_bf16 v[180:183], v[220:223], a[68:71], v[180:183]\n"
"buffer_load_dwordx4 v243, s[56:59], s75 offen lds\n"
"v_mfma_f32_16x16x32_bf16 v[184:187], v[220:223], a[72:75], v[184:187]\n"
"ds_read_b64_tr_b16 v[208:209], v201 offset:35840\n"
"ds_read_b64_tr_b16 v[210:211], v201 offset:36096\n"
"v_mfma_f32_16x16x32_bf16 v[188:191], v[220:223], a[76:79], v[188:191]\n"
"s_add_u32 m0, s74, 0x16b00\n"
"s_add_u32 s75, s87, 0x31000\n"
"s_waitcnt lgkmcnt(2)\n"
"v_mfma_f32_16x16x32_bf16 v[160:163], v[224:227], v[144:147], v[160:163]\n"
"buffer_load_dwordx4 v242, s[56:59], s75 offen lds\n"
"v_mfma_f32_16x16x32_bf16 v[164:167], v[224:227], v[148:151], v[164:167]\n"
"ds_read_b64_tr_b16 v[212:213], v201 offset:35872\n"
"ds_read_b64_tr_b16 v[214:215], v201 offset:36128\n"
"v_mfma_f32_16x16x32_bf16 v[168:171], v[224:227], v[152:155], v[168:171]\n"
"s_add_u32 m0, s74, 0x17b80\n"
"v_mfma_f32_16x16x32_bf16 v[172:175], v[224:227], v[156:159], v[172:175]\n"
"buffer_load_dwordx4 v243, s[56:59], s75 offen lds\n"
"v_mfma_f32_16x16x32_bf16 v[176:179], v[228:231], v[144:147], v[176:179]\n"
"ds_read_b64_tr_b16 v[216:217], v204 offset:35840\n"
"ds_read_b64_tr_b16 v[218:219], v204 offset:36096\n"
"v_mfma_f32_16x16x32_bf16 v[180:183], v[228:231], v[148:151], v[180:183]\n"
"s_add_u32 m0, s74, 0x0\n"
"s_add_u32 s75, s88, 0x0\n"
"v_mfma_f32_16x16x32_bf16 v[184:187], v[228:231], v[152:155], v[184:187]\n"
"buffer_load_dwordx4 v242, s[56:59], s75 offen lds\n"
"v_mfma_f32_16x16x32_bf16 v[188:191], v[228:231], v[156:159], v[188:191]\n"
"ds_read_b64_tr_b16 v[220:221], v204 offset:35872\n"
"ds_read_b64_tr_b16 v[222:223], v204 offset:36128\n"
"v_mfma_f32_16x16x32_bf16 v[160:163], v[232:235], a[80:83], v[160:163]\n"
"s_add_u32 m0, s74, 0x1080\n"
"v_mfma_f32_16x16x32_bf16 v[164:167], v[232:235], a[84:87], v[164:167]\n"
"buffer_load_dwordx4 v243, s[56:59], s75 offen lds\n"
"v_mfma_f32_16x16x32_bf16 v[168:171], v[232:235], a[88:91], v[168:171]\n"
"v_mfma_f32_16x16x32_bf16 v[172:175], v[232:235], a[92:95], v[172:175]\n"
"s_add_u32 m0, s74, 0x2100\n"
"s_add_u32 s75, s88, 0x7000\n"
"v_mfma_f32_16x16x32_bf16 v[176:179], v[236:239], a[80:83], v[176:179]\n"
"buffer_load_dwordx4 v242, s[56:59], s75 offen lds\n"
"v_mfma_f32_16x16x32_bf16 v[180:183], v[236:239], a[84:87], v[180:183]\n"
"ds_read_b64_tr_b16 v[224:225], v201 offset:44288\n"
"ds_read_b64_tr_b16 v[226:227], v201 offset:44544\n"
"v_mfma_f32_16x16x32_bf16 v[184:187], v[236:239], a[88:91], v[184:187]\n"
"s_add_u32 m0, s74, 0x3180\n"
"v_mfma_f32_16x16x32_bf16 v[188:191], v[236:239], a[92:95], v[188:191]\n"
"buffer_load_dwordx4 v243, s[56:59], s75 offen lds\n"
"s_waitcnt lgkmcnt(2)\n"
"v_mfma_f32_16x16x32_bf16 v[160:163], v[208:211], v[32:35], v[160:163]\n"
"ds_read_b64_tr_b16 v[228:229], v201 offset:44320\n"
"ds_read_b64_tr_b16 v[230:231], v201 offset:44576\n"
"v_mfma_f32_16x16x32_bf16 v[164:167], v[208:211], v[36:39], v[164:167]\n"
"s_add_u32 m0, s74, 0x4200\n"
"s_add_u32 s75, s88, 0xe000\n"
"v_mfma_f32_16x16x32_bf16 v[168:171], v[208:211], v[40:43], v[168:171]\n"
"buffer_load_dwordx4 v242, s[56:59], s75 offen lds\n"
"v_mfma_f32_16x16x32_bf16 v[172:175], v[208:211], v[44:47], v[172:175]\n"
"ds_read_b64_tr_b16 v[232:233], v204 offset:44288\n"
"ds_read_b64_tr_b16 v[234:235], v204 offset:44544\n"
"v_mfma_f32_16x16x32_bf16 v[176:179], v[212:215], v[32:35], v[176:179]\n"
"s_add_u32 m0, s74, 0x5280\n"
"v_mfma_f32_16x16x32_bf16 v[180:183], v[212:215], v[36:39], v[180:183]\n"
"buffer_load_dwordx4 v243, s[56:59], s75 offen lds\n"
"v_mfma_f32_16x16x32_bf16 v[184:187], v[212:215], v[40:43], v[184:187]\n"
"ds_read_b64_tr_b16 v[236:237], v204 offset:44320\n"
"ds_read_b64_tr_b16 v[238:239], v204 offset:44576\n"
"v_mfma_f32_16x16x32_bf16 v[188:191], v[212:215], v[44:47], v[188:191]\n"
"s_add_u32 m0, s74, 0x6300\n"
"s_add_u32 s75, s88, 0x15000\n"
"v_mfma_f32_16x16x32_bf16 v[160:163], v[216:219], a[96:99], v[160:163]\n"
"buffer_load_dwordx4 v242, s[56:59], s75 offen lds\n"
"v_mfma_f32_16x16x32_bf16 v[164:167], v[216:219], a[100:103], v[164:167]\n"
"v_mfma_f32_16x16x32_bf16 v[168:171], v[216:219], a[104:107], v[168:171]\n"
"s_add_u32 m0, s74, 0x7380\n"
"v_mfma_f32_16x16x32_bf16 v[172:175], v[216:219], a[108:111], v[172:175]\n"
"buffer_load_dwordx4 v243, s[56:59], s75 offen lds\n"
"v_mfma_f32_16x16x32_bf16 v[176:179], v[220:223], a[96:99], v[176:179]\n"
"v_mfma_f32_16x16x32_bf16 v[180:183], v[220:223], a[100:103], v[180:183]\n"
"s_add_u32 m0, s74, 0x8400\n"
"s_add_u32 s75, s88, 0x1c000\n"
"v_mfma_f32_16x16x32_bf16 v[184:187], v[220:223], a[104:107], v[184:187]\n"
"buffer_load_dwordx4 v242, s[56:59], s75 offen lds\n"
"v_mfma_f32_16x16x32_bf16 v[188:191], v[220:223], a[108:111], v[188:191]\n"
"s_waitcnt lgkmcnt(0)\n"
"v_mfma_f32_16x16x32_bf16 v[160:163], v[224:227], v[48:51], v[160:163]\n"
"s_add_u32 m0, s74, 0x9480\n"
"v_mfma_f32_16x16x32_bf16 v[164:167], v[224:227], v[52:55], v[164:167]\n"
"buffer_load_dwordx4 v243, s[56:59], s75 offen lds\n"
"v_mfma_f32_16x16x32_bf16 v[168:171], v[224:227], v[56:59], v[168:171]\n"
"v_mfma_f32_16x16x32_bf16 v[172:175], v[224:227], v[60:63], v[172:175]\n"
"s_add_u32 m0, s74, 0xa500\n"
"s_add_u32 s75, s88, 0x23000\n"
"v_mfma_f32_16x16x32_bf16 v[176:179], v[228:231], v[48:51], v[176:179]\n"
"buffer_load_dwordx4 v242, s[56:59], s75 offen lds\n"
"v_mfma_f32_16x16x32_bf16 v[180:183], v[228:231], v[52:55], v[180:183]\n"
"s_waitcnt vmcnt(25)\n"
"s_barrier\n"
"ds_read_b64_tr_b16 v[208:209], v202 offset:20992\n"
"ds_read_b64_tr_b16 v[210:211], v202 offset:21248\n"
"v_mfma_f32_16x16x32_bf16 v[184:187], v[228:231], v[56:59], v[184:187]\n"
"s_add_u32 m0, s74, 0xb580\n"
"v_mfma_f32_16x16x32_bf16 v[188:191], v[228:231], v[60:63], v[188:191]\n"
"buffer_load_dwordx4 v243, s[56:59], s75 offen lds\n"
"s_lshl_b32 s76, s85, 2\n"
"s_add_u32 s76, s76, s29\n"
"s_add_u32 s76, s76, 0x1000\n"
"v_mfma_f32_16x16x32_bf16 v[160:163], v[232:235], a[112:115], v[160:163]\n"
"buffer_store_dword v241, off, s[68:71], s76 sc1\n"
"v_mfma_f32_16x16x32_bf16 v[164:167], v[232:235], a[116:119], v[164:167]\n"
"ds_read_b64_tr_b16 v[212:213], v202 offset:21024\n"
"ds_read_b64_tr_b16 v[214:215], v202 offset:21280\n"
"ds_read_b64_tr_b16 v[216:217], v205 offset:20992\n"
"ds_read_b64_tr_b16 v[218:219], v205 offset:21248\n"
"ds_read_b64_tr_b16 v[220:221], v205 offset:21024\n"
"ds_read_b64_tr_b16 v[222:223], v205 offset:21280\n"
"s_add_u32 m0, s74, 0xc600\n"
"s_add_u32 s75, s88, 0x2a000\n"
"v_mfma_f32_16x16x32_bf16 v[168:171], v[232:235], a[120:123], v[168:171]\n"
"buffer_load_dwordx4 v242, s[56:59], s75 offen lds\n"
"v_mfma_f32_16x16x32_bf16 v[172:175], v[232:235], a[124:127], v[172:175]\n"
"v_mfma_f32_16x16x32_bf16 v[176:179], v[236:239], a[112:115], v[176:179]\n"
"s_add_u32 m0, s74, 0xd680\n"
"v_mfma_f32_16x16x32_bf16 v[180:183], v[236:239], a[116:119], v[180:183]\n"
"buffer_load_dwordx4 v243, s[56:59], s75 offen lds\n"
"v_mfma_f32_16x16x32_bf16 v[184:187], v[236:239], a[120:123], v[184:187]\n"
"v_mfma_f32_16x16x32_bf16 v[188:191], v[236:239], a[124:127], v[188:191]\n"
"s_add_u32 m0, s74, 0xe700\n"
"s_add_u32 s75, s88, 0x31000\n"
"s_waitcnt lgkmcnt(0)\n"
"v_mfma_f32_16x16x32_bf16 v[96:99], v[208:211], v[0:3], 0\n"
"buffer_load_dwordx4 v242, s[56:59], s75 offen lds\n"
"v_mfma_f32_16x16x32_bf16 v[100:103], v[208:211], v[4:7], 0\n"
"s_lshl_b32 s31, s86, 10\n"
"s_add_u32 s31, s31, s93\n"
"v_mfma_f32_16x16x32_bf16 v[104:107], v[208:211], v[8:11], 0\n"
"v_cvt_pk_bf16_f32 v160, v160, v161\n"
"s_add_u32 m0, s74, 0xf780\n"
"v_mfma_f32_16x16x32_bf16 v[108:111], v[208:211], v[12:15], 0\n"
"buffer_load_dwordx4 v243, s[56:59], s75 offen lds\n"
"v_mfma_f32_16x16x32_bf16 v[112:115], v[212:215], v[0:3], 0\n"
"s_waitcnt vmcnt(29)\n"
"s_barrier\n"
"ds_read_b64_tr_b16 v[224:225], v201 offset:61184\n"
"ds_read_b64_tr_b16 v[226:227], v201 offset:61440\n"
"v_mfma_f32_16x16x32_bf16 v[116:119], v[212:215], v[4:7], 0\n"
"ds_read_b64_tr_b16 v[228:229], v201 offset:61216\n"
"ds_read_b64_tr_b16 v[230:231], v201 offset:61472\n"
"v_mfma_f32_16x16x32_bf16 v[120:123], v[212:215], v[8:11], 0\n"
"ds_read_b64_tr_b16 v[232:233], v204 offset:61184\n"
"ds_read_b64_tr_b16 v[234:235], v204 offset:61440\n"
"v_mfma_f32_16x16x32_bf16 v[124:127], v[212:215], v[12:15], 0\n"
"ds_read_b64_tr_b16 v[236:237], v204 offset:61216\n"
"ds_read_b64_tr_b16 v[238:239], v204 offset:61472\n"
"v_mfma_f32_16x16x32_bf16 v[96:99], v[216:219], a[0:3], v[96:99]\n"
"v_cvt_pk_bf16_f32 v161, v162, v163\n"
"v_cvt_pk_bf16_f32 v162, v176, v177\n"
"v_mfma_f32_16x16x32_bf16 v[100:103], v[216:219], a[4:7], v[100:103]\n"
"v_cvt_pk_bf16_f32 v163, v178, v179\n"
"v_cvt_pk_bf16_f32 v164, v164, v165\n"
"v_mfma_f32_16x16x32_bf16 v[104:107], v[216:219], a[8:11], v[104:107]\n"
"buffer_store_dwordx4 v[160:163], v248, s[60:63], s31 offen offset:0 sc1\n"
"v_mfma_f32_16x16x32_bf16 v[108:111], v[216:219], a[12:15], v[108:111]\n"
"v_cvt_pk_bf16_f32 v165, v166, v167\n"
"v_cvt_pk_bf16_f32 v166, v180, v181\n"
"v_mfma_f32_16x16x32_bf16 v[112:115], v[220:223], a[0:3], v[112:115]\n"
"v_cvt_pk_bf16_f32 v167, v182, v183\n"
"v_cvt_pk_bf16_f32 v168, v168, v169\n"
"v_mfma_f32_16x16x32_bf16 v[116:119], v[220:223], a[4:7], v[116:119]\n"
"buffer_store_dwordx4 v[164:167], v248, s[60:63], s31 offen offset:1024 sc1\n"
"v_mfma_f32_16x16x32_bf16 v[120:123], v[220:223], a[8:11], v[120:123]\n"
"v_cvt_pk_bf16_f32 v169, v170, v171\n"
"v_cvt_pk_bf16_f32 v170, v184, v185\n"
"v_mfma_f32_16x16x32_bf16 v[124:127], v[220:223], a[12:15], v[124:127]\n"
"v_cvt_pk_bf16_f32 v171, v186, v187\n"
"s_waitcnt lgkmcnt(0)\n"
"v_mfma_f32_16x16x32_bf16 v[96:99], v[224:227], v[16:19], v[96:99]\n"
"buffer_store_dwordx4 v[168:171], v248, s[60:63], s31 offen offset:2048 sc1\n"
"v_mfma_f32_16x16x32_bf16 v[100:103], v[224:227], v[20:23], v[100:103]\n"
"v_cvt_pk_bf16_f32 v172, v172, v173\n"
"v_cvt_pk_bf16_f32 v173, v174, v175\n"
"v_mfma_f32_16x16x32_bf16 v[104:107], v[224:227], v[24:27], v[104:107]\n"
"v_cvt_pk_bf16_f32 v174, v188, v189\n"
"v_cvt_pk_bf16_f32 v175, v190, v191\n"
"v_mfma_f32_16x16x32_bf16 v[108:111], v[224:227], v[28:31], v[108:111]\n"
"buffer_store_dwordx4 v[172:175], v248, s[60:63], s31 offen offset:3072 sc1\n"
"v_mfma_f32_16x16x32_bf16 v[112:115], v[228:231], v[16:19], v[112:115]\n"
"s_waitcnt vmcnt(31)\n"
"s_barrier\n"
"ds_read_b64_tr_b16 v[208:209], v201 offset:2048\n"
"ds_read_b64_tr_b16 v[210:211], v201 offset:2304\n"
"v_mfma_f32_16x16x32_bf16 v[116:119], v[228:231], v[20:23], v[116:119]\n"
"ds_read_b64_tr_b16 v[212:213], v201 offset:2080\n"
"ds_read_b64_tr_b16 v[214:215], v201 offset:2336\n"
"v_mfma_f32_16x16x32_bf16 v[120:123], v[228:231], v[24:27], v[120:123]\n"
"ds_read_b64_tr_b16 v[216:217], v204 offset:2048\n"
"ds_read_b64_tr_b16 v[218:219], v204 offset:2304\n"
"v_mfma_f32_16x16x32_bf16 v[124:127], v[228:231], v[28:31], v[124:127]\n"
"ds_read_b64_tr_b16 v[220:221], v204 offset:2080\n"
"ds_read_b64_tr_b16 v[222:223], v204 offset:2336\n"
"v_mfma_f32_16x16x32_bf16 v[96:99], v[232:235], a[16:19], v[96:99]\n"
"v_mfma_f32_16x16x32_bf16 v[100:103], v[232:235], a[20:23], v[100:103]\n"
"v_mfma_f32_16x16x32_bf16 v[104:107], v[232:235], a[24:27], v[104:107]\n"
"v_mfma_f32_16x16x32_bf16 v[108:111], v[232:235], a[28:31], v[108:111]\n"
"v_mfma_f32_16x16x32_bf16 v[112:115], v[236:239], a[16:19], v[112:115]\n"
"v_mfma_f32_16x16x32_bf16 v[116:119], v[236:239], a[20:23], v[116:119]\n"
"v_mfma_f32_16x16x32_bf16 v[120:123], v[236:239], a[24:27], v[120:123]\n"
"v_mfma_f32_16x16x32_bf16 v[124:127], v[236:239], a[28:31], v[124:127]\n"
"s_waitcnt lgkmcnt(0)\n"
"v_mfma_f32_16x16x32_bf16 v[96:99], v[208:211], v[64:67], v[96:99]\n"
"v_mfma_f32_16x16x32_bf16 v[100:103], v[208:211], v[68:71], v[100:103]\n"
"v_mfma_f32_16x16x32_bf16 v[104:107], v[208:211], v[72:75], v[104:107]\n"
"v_mfma_f32_16x16x32_bf16 v[108:111], v[208:211], v[76:79], v[108:111]\n"
"v_mfma_f32_16x16x32_bf16 v[112:115], v[212:215], v[64:67], v[112:115]\n"
"s_waitcnt vmcnt(29)\n"
"s_barrier\n"
"ds_read_b64_tr_b16 v[224:225], v202 offset:4096\n"
"ds_read_b64_tr_b16 v[226:227], v202 offset:4352\n"
"v_mfma_f32_16x16x32_bf16 v[116:119], v[212:215], v[68:71], v[116:119]\n"
"ds_read_b64_tr_b16 v[228:229], v202 offset:4128\n"
"ds_read_b64_tr_b16 v[230:231], v202 offset:4384\n"
"v_mfma_f32_16x16x32_bf16 v[120:123], v[212:215], v[72:75], v[120:123]\n"
"ds_read_b64_tr_b16 v[232:233], v205 offset:4096\n"
"ds_read_b64_tr_b16 v[234:235], v205 offset:4352\n"
"v_mfma_f32_16x16x32_bf16 v[124:127], v[212:215], v[76:79], v[124:127]\n"
"ds_read_b64_tr_b16 v[236:237], v205 offset:4128\n"
"ds_read_b64_tr_b16 v[238:239], v205 offset:4384\n"
"v_mfma_f32_16x16x32_bf16 v[96:99], v[216:219], a[32:35], v[96:99]\n"
"v_mfma_f32_16x16x32_bf16 v[100:103], v[216:219], a[36:39], v[100:103]\n"
"v_mfma_f32_16x16x32_bf16 v[104:107], v[216:219], a[40:43], v[104:107]\n"
"v_mfma_f32_16x16x32_bf16 v[108:111], v[216:219], a[44:47], v[108:111]\n"
"v_mfma_f32_16x16x32_bf16 v[112:115], v[220:223], a[32:35], v[112:115]\n"
"v_mfma_f32_16x16x32_bf16 v[116:119], v[220:223], a[36:39], v[116:119]\n"
"v_mfma_f32_16x16x32_bf16 v[120:123], v[220:223], a[40:43], v[120:123]\n"
"v_mfma_f32_16x16x32_bf16 v[124:127], v[220:223], a[44:47], v[124:127]\n"
"s_waitcnt lgkmcnt(0)\n"
"v_mfma_f32_16x16x32_bf16 v[96:99], v[224:227], v[80:83], v[96:99]\n"
"v_mfma_f32_16x16x32_bf16 v[100:103], v[224:227], v[84:87], v[100:103]\n"
"v_mfma_f32_16x16x32_bf16 v[104:107], v[224:227], v[88:91], v[104:107]\n"
"v_mfma_f32_16x16x32_bf16 v[108:111], v[224:227], v[92:95], v[108:111]\n"
"v_mfma_f32_16x16x32_bf16 v[112:115], v[228:231], v[80:83], v[112:115]\n"
"s_waitcnt vmcnt(23)\n"
"s_barrier\n"
"ds_read_b64_tr_b16 v[208:209], v201 offset:10496\n"
"ds_read_b64_tr_b16 v[210:211], v201 offset:10752\n"
"v_mfma_f32_16x16x32_bf16 v[116:119], v[228:231], v[84:87], v[116:119]\n"
"ds_read_b64_tr_b16 v[212:213], v201 offset:10528\n"
"ds_read_b64_tr_b16 v[214:215], v201 offset:10784\n"
"v_mfma_f32_16x16x32_bf16 v[120:123], v[228:231], v[88:91], v[120:123]\n"
"ds_read_b64_tr_b16 v[216:217], v204 offset:10496\n"
"ds_read_b64_tr_b16 v[218:219], v204 offset:10752\n"
"v_mfma_f32_16x16x32_bf16 v[124:127], v[228:231], v[92:95], v[124:127]\n"
"ds_read_b64_tr_b16 v[220:221], v204 offset:10528\n"
"ds_read_b64_tr_b16 v[222:223], v204 offset:10784\n"
"v_mfma_f32_16x16x32_bf16 v[96:99], v[232:235], a[48:51], v[96:99]\n"
"v_mfma_f32_16x16x32_bf16 v[100:103], v[232:235], a[52:55], v[100:103]\n"
"v_mfma_f32_16x16x32_bf16 v[104:107], v[232:235], a[56:59], v[104:107]\n"
"v_mfma_f32_16x16x32_bf16 v[108:111], v[232:235], a[60:63], v[108:111]\n"
"v_mfma_f32_16x16x32_bf16 v[112:115], v[236:239], a[48:51], v[112:115]\n"
"ds_read_b64_tr_b16 v[224:225], v201 offset:18944\n"
"ds_read_b64_tr_b16 v[226:227], v201 offset:19200\n"
"v_mfma_f32_16x16x32_bf16 v[116:119], v[236:239], a[52:55], v[116:119]\n"
"buffer_load_dword v254, v253, s[68:71], s98 offen sc1\n"
"v_mfma_f32_16x16x32_bf16 v[120:123], v[236:239], a[56:59], v[120:123]\n"
"ds_read_b64_tr_b16 v[228:229], v201 offset:18976\n"
"ds_read_b64_tr_b16 v[230:231], v201 offset:19232\n"
"v_mfma_f32_16x16x32_bf16 v[124:127], v[236:239], a[60:63], v[124:127]\n"
"s_waitcnt lgkmcnt(4)\n"
"v_mfma_f32_16x16x32_bf16 v[96:99], v[208:211], v[128:131], v[96:99]\n"
"ds_read_b64_tr_b16 v[232:233], v204 offset:18944\n"
"ds_read_b64_tr_b16 v[234:235], v204 offset:19200\n"
"v_mfma_f32_16x16x32_bf16 v[100:103], v[208:211], v[132:135], v[100:103]\n"
"ds_read_b64_tr_b16 v[236:237], v204 offset:18976\n"
"ds_read_b64_tr_b16 v[238:239], v204 offset:19232\n"
"v_mfma_f32_16x16x32_bf16 v[104:107], v[208:211], v[136:139], v[104:107]\n"
"v_mfma_f32_16x16x32_bf16 v[108:111], v[208:211], v[140:143], v[108:111]\n"
"v_mfma_f32_16x16x32_bf16 v[112:115], v[212:215], v[128:131], v[112:115]\n"
"v_mfma_f32_16x16x32_bf16 v[116:119], v[212:215], v[132:135], v[116:119]\n"
"v_mfma_f32_16x16x32_bf16 v[120:123], v[212:215], v[136:139], v[120:123]\n"
"v_mfma_f32_16x16x32_bf16 v[124:127], v[212:215], v[140:143], v[124:127]\n"
"v_mfma_f32_16x16x32_bf16 v[96:99], v[216:219], a[64:67], v[96:99]\n"
"v_mfma_f32_16x16x32_bf16 v[100:103], v[216:219], a[68:71], v[100:103]\n"
"v_mfma_f32_16x16x32_bf16 v[104:107], v[216:219], a[72:75], v[104:107]\n"
"v_mfma_f32_16x16x32_bf16 v[108:111], v[216:219], a[76:79], v[108:111]\n"
"v_mfma_f32_16x16x32_bf16 v[112:115], v[220:223], a[64:67], v[112:115]\n"
"ds_read_b64_tr_b16 v[208:209], v202 offset:12544\n"
"ds_read_b64_tr_b16 v[210:211], v202 offset:12800\n"
"v_mfma_f32_16x16x32_bf16 v[116:119], v[220:223], a[68:71], v[116:119]\n"
"ds_read_b64_tr_b16 v[212:213], v202 offset:12576\n"
"ds_read_b64_tr_b16 v[214:215], v202 offset:12832\n"
"v_mfma_f32_16x16x32_bf16 v[120:123], v[220:223], a[72:75], v[120:123]\n"
"ds_read_b64_tr_b16 v[216:217], v205 offset:12544\n"
"ds_read_b64_tr_b16 v[218:219], v205 offset:12800\n"
"v_mfma_f32_16x16x32_bf16 v[124:127], v[220:223], a[76:79], v[124:127]\n"
"s_waitcnt lgkmcnt(6)\n"
"v_mfma_f32_16x16x32_bf16 v[96:99], v[224:227], v[144:147], v[96:99]\n"
"ds_read_b64_tr_b16 v[220:221], v205 offset:12576\n"
"ds_read_b64_tr_b16 v[222:223], v205 offset:12832\n"
"v_mfma_f32_16x16x32_bf16 v[100:103], v[224:227], v[148:151], v[100:103]\n"
"v_mfma_f32_16x16x32_bf16 v[104:107], v[224:227], v[152:155], v[104:107]\n"
"v_mfma_f32_16x16x32_bf16 v[108:111], v[224:227], v[156:159], v[108:111]\n"
"v_mfma_f32_16x16x32_bf16 v[112:115], v[228:231], v[144:147], v[112:115]\n"
"v_mfma_f32_16x16x32_bf16 v[116:119], v[228:231], v[148:151], v[116:119]\n"
"v_mfma_f32_16x16x32_bf16 v[120:123], v[228:231], v[152:155], v[120:123]\n"
"v_mfma_f32_16x16x32_bf16 v[124:127], v[228:231], v[156:159], v[124:127]\n"
"v_mfma_f32_16x16x32_bf16 v[96:99], v[232:235], a[80:83], v[96:99]\n"
"v_mfma_f32_16x16x32_bf16 v[100:103], v[232:235], a[84:87], v[100:103]\n"
"v_mfma_f32_16x16x32_bf16 v[104:107], v[232:235], a[88:91], v[104:107]\n"
"v_mfma_f32_16x16x32_bf16 v[108:111], v[232:235], a[92:95], v[108:111]\n"
"v_mfma_f32_16x16x32_bf16 v[112:115], v[236:239], a[80:83], v[112:115]\n"
"v_mfma_f32_16x16x32_bf16 v[116:119], v[236:239], a[84:87], v[116:119]\n"
"v_mfma_f32_16x16x32_bf16 v[120:123], v[236:239], a[88:91], v[120:123]\n"
"v_mfma_f32_16x16x32_bf16 v[124:127], v[236:239], a[92:95], v[124:127]\n"
"s_waitcnt lgkmcnt(0)\n"
"v_mfma_f32_16x16x32_bf16 v[96:99], v[208:211], v[32:35], v[96:99]\n"
"v_mfma_f32_16x16x32_bf16 v[100:103], v[208:211], v[36:39], v[100:103]\n"
"v_mfma_f32_16x16x32_bf16 v[104:107], v[208:211], v[40:43], v[104:107]\n"
"v_mfma_f32_16x16x32_bf16 v[108:111], v[208:211], v[44:47], v[108:111]\n"
"v_mfma_f32_16x16x32_bf16 v[112:115], v[212:215], v[32:35], v[112:115]\n"
"s_waitcnt vmcnt(7)\n"
"s_barrier\n"
"ds_read_b64_tr_b16 v[224:225], v201 offset:27392\n"
"ds_read_b64_tr_b16 v[226:227], v201 offset:27648\n"
"v_mfma_f32_16x16x32_bf16 v[116:119], v[212:215], v[36:39], v[116:119]\n"
"ds_read_b64_tr_b16 v[228:229], v201 offset:27424\n"
"ds_read_b64_tr_b16 v[230:231], v201 offset:27680\n"
"v_mfma_f32_16x16x32_bf16 v[120:123], v[212:215], v[40:43], v[120:123]\n"
"ds_read_b64_tr_b16 v[232:233], v204 offset:27392\n"
"ds_read_b64_tr_b16 v[234:235], v204 offset:27648\n"
"v_mfma_f32_16x16x32_bf16 v[124:127], v[212:215], v[44:47], v[124:127]\n"
"ds_read_b64_tr_b16 v[236:237], v204 offset:27424\n"
"ds_read_b64_tr_b16 v[238:239], v204 offset:27680\n"
"v_mfma_f32_16x16x32_bf16 v[96:99], v[216:219], a[96:99], v[96:99]\n"
"v_mfma_f32_16x16x32_bf16 v[100:103], v[216:219], a[100:103], v[100:103]\n"
"v_mfma_f32_16x16x32_bf16 v[104:107], v[216:219], a[104:107], v[104:107]\n"
"v_mfma_f32_16x16x32_bf16 v[108:111], v[216:219], a[108:111], v[108:111]\n"
"v_mfma_f32_16x16x32_bf16 v[112:115], v[220:223], a[96:99], v[112:115]\n"
"ds_read_b64_tr_b16 v[208:209], v244 offset:0\n"
"ds_read_b64_tr_b16 v[210:211], v244 offset:256\n"
"v_mfma_f32_16x16x32_bf16 v[116:119], v[220:223], a[100:103], v[116:119]\n"
"ds_read_b64_tr_b16 v[212:213], v246 offset:0\n"
"ds_read_b64_tr_b16 v[214:215], v246 offset:256\n"
"v_mfma_f32_16x16x32_bf16 v[120:123], v[220:223], a[104:107], v[120:123]\n"
"ds_read_b64_tr_b16 v[216:217], v244 offset:8448\n"
"ds_read_b64_tr_b16 v[218:219], v244 offset:8704\n"
"v_mfma_f32_16x16x32_bf16 v[124:127], v[220:223], a[108:111], v[124:127]\n"
"s_waitcnt lgkmcnt(6)\n"
"v_mfma_f32_16x16x32_bf16 v[96:99], v[224:227], v[48:51], v[96:99]\n"
"ds_read_b64_tr_b16 v[220:221], v246 offset:8448\n"
"ds_read_b64_tr_b16 v[222:223], v246 offset:8704\n"
"v_mfma_f32_16x16x32_bf16 v[100:103], v[224:227], v[52:55], v[100:103]\n"
"v_mfma_f32_16x16x32_bf16 v[104:107], v[224:227], v[56:59], v[104:107]\n"
"v_mfma_f32_16x16x32_bf16 v[108:111], v[224:227], v[60:63], v[108:111]\n"
"v_mfma_f32_16x16x32_bf16 v[112:115], v[228:231], v[48:51], v[112:115]\n"
"v_mfma_f32_16x16x32_bf16 v[116:119], v[228:231], v[52:55], v[116:119]\n"
"v_mfma_f32_16x16x32_bf16 v[120:123], v[228:231], v[56:59], v[120:123]\n"
"s_waitcnt vmcnt(0)\n"
"v_cmp_eq_u32 vcc, 0, v254\n"
"s_lshl_b32 s76, s86, 2\n"
"s_add_u32 s76, s76, s29\n"
"s_add_u32 s76, s76, 0x1000\n"
"v_mfma_f32_16x16x32_bf16 v[124:127], v[228:231], v[60:63], v[124:127]\n"
"buffer_store_dword v241, off, s[68:71], s76 sc1\n"
"v_mfma_f32_16x16x32_bf16 v[96:99], v[232:235], a[112:115], v[96:99]\n"
"v_mfma_f32_16x16x32_bf16 v[100:103], v[232:235], a[116:119], v[100:103]\n"
"s_and_b32 s79, vcc_lo, s97\n"
"s_cbranch_scc1 L_tfl_%=\n"
"L_bfl_%=:\n"
"v_mfma_f32_16x16x32_bf16 v[104:107], v[232:235], a[120:123], v[104:107]\n"
"v_mfma_f32_16x16x32_bf16 v[108:111], v[232:235], a[124:127], v[108:111]\n"
"v_mfma_f32_16x16x32_bf16 v[112:115], v[236:239], a[112:115], v[112:115]\n"
"buffer_load_dwordx4 a[128:131], v248, s[32:35], s48 offen offset:0 sc1\n"
"v_mfma_f32_16x16x32_bf16 v[116:119], v[236:239], a[116:119], v[116:119]\n"
"ds_read_b64_tr_b16 v[224:225], v244 offset:16896\n"
"ds_read_b64_tr_b16 v[226:227], v244 offset:17152\n"
"v_mfma_f32_16x16x32_bf16 v[120:123], v[236:239], a[120:123], v[120:123]\n"
"ds_read_b64_tr_b16 v[228:229], v246 offset:16896\n"
"ds_read_b64_tr_b16 v[230:231], v246 offset:17152\n"
"v_mfma_f32_16x16x32_bf16 v[124:127], v[236:239], a[124:127], v[124:127]\n"
"buffer_load_dwordx4 a[132:135], v248, s[32:35], s48 offen offset:1024 sc1\n"
"s_waitcnt lgkmcnt(4)\n"
"v_mfma_f32_16x16x32_bf16 v[160:163], v[208:211], v[0:3], 0\n"
"ds_read_b64_tr_b16 v[232:233], v244 offset:25344\n"
"ds_read_b64_tr_b16 v[234:235], v244 offset:25600\n"
"v_mfma_f32_16x16x32_bf16 v[164:167], v[208:211], v[4:7], 0\n"
"ds_read_b64_tr_b16 v[236:237], v246 offset:25344\n"
"ds_read_b64_tr_b16 v[238:239], v246 offset:25600\n"
"v_mfma_f32_16x16x32_bf16 v[168:171], v[208:211], v[8:11], 0\n"
"buffer_load_dwordx4 a[136:139], v248, s[32:35], s48 offen offset:2048 sc1\n"
"v_mfma_f32_16x16x32_bf16 v[172:175], v[208:211], v[12:15], 0\n"
"s_lshl_b32 s31, s87, 10\n"
"s_add_u32 s31, s31, s93\n"
"v_mfma_f32_16x16x32_bf16 v[160:163], v[212:215], a[0:3], v[160:163]\n"
"v_cvt_pk_bf16_f32 v96, v96, v97\n"
"v_cvt_pk_bf16_f32 v97, v98, v99\n"
"v_mfma_f32_16x16x32_bf16 v[164:167], v[212:215], a[4:7], v[164:167]\n"
"buffer_load_dwordx4 a[140:143], v248, s[32:35], s48 offen offset:3072 sc1\n"
"v_mfma_f32_16x16x32_bf16 v[168:171], v[212:215], a[8:11], v[168:171]\n"
"v_cvt_pk_bf16_f32 v98, v112, v113\n"
"v_cvt_pk_bf16_f32 v99, v114, v115\n"
"v_mfma_f32_16x16x32_bf16 v[172:175], v[212:215], a[12:15], v[172:175]\n"
"buffer_store_dwordx4 v[96:99], v248, s[60:63], s31 offen offset:0 sc1\n"
"v_mfma_f32_16x16x32_bf16 v[160:163], v[216:219], v[16:19], v[160:163]\n"
"buffer_load_dwordx4 a[144:147], v248, s[32:35], s49 offen offset:0 sc1\n"
"v_mfma_f32_16x16x32_bf16 v[164:167], v[216:219], v[20:23], v[164:167]\n"
"v_cvt_pk_bf16_f32 v100, v100, v101\n"
"v_cvt_pk_bf16_f32 v101, v102, v103\n"
"v_mfma_f32_16x16x32_bf16 v[168:171], v[216:219], v[24:27], v[168:171]\n"
"v_cvt_pk_bf16_f32 v102, v116, v117\n"
"v_cvt_pk_bf16_f32 v103, v118, v119\n"
"v_mfma_f32_16x16x32_bf16 v[172:175], v[216:219], v[28:31], v[172:175]\n"
"buffer_load_dwordx4 a[148:151], v248, s[32:35], s49 offen offset:1024 sc1\n"
"v_mfma_f32_16x16x32_bf16 v[160:163], v[220:223], a[16:19], v[160:163]\n"
"ds_read_b64_tr_b16 v[208:209], v244 offset:33792\n"
"ds_read_b64_tr_b16 v[210:211], v244 offset:34048\n"
"v_mfma_f32_16x16x32_bf16 v[164:167], v[220:223], a[20:23], v[164:167]\n"
"ds_read_b64_tr_b16 v[212:213], v246 offset:33792\n"
"ds_read_b64_tr_b16 v[214:215], v246 offset:34048\n"
"v_mfma_f32_16x16x32_bf16 v[168:171], v[220:223], a[24:27], v[168:171]\n"
"buffer_load_dwordx4 a[152:155], v248, s[32:35], s49 offen offset:2048 sc1\n"
"v_mfma_f32_16x16x32_bf16 v[172:175], v[220:223], a[28:31], v[172:175]\n"
"v_cvt_pk_bf16_f32 v104, v104, v105\n"
"s_waitcnt lgkmcnt(4)\n"
"v_mfma_f32_16x16x32_bf16 v[160:163], v[224:227], v[64:67], v[160:163]\n"
"ds_read_b64_tr_b16 v[216:217], v244 offset:42240\n"
"ds_read_b64_tr_b16 v[218:219], v244 offset:42496\n"
"v_mfma_f32_16x16x32_bf16 v[164:167], v[224:227], v[68:71], v[164:167]\n"
"buffer_load_dwordx4 a[156:159], v248, s[32:35], s49 offen offset:3072 sc1\n"
"v_mfma_f32_16x16x32_bf16 v[168:171], v[224:227], v[72:75], v[168:171]\n"
"ds_read_b64_tr_b16 v[220:221], v246 offset:42240\n"
"ds_read_b64_tr_b16 v[222:223], v246 offset:42496\n"
"v_mfma_f32_16x16x32_bf16 v[172:175], v[224:227], v[76:79], v[172:175]\n"
"buffer_store_dwordx4 v[100:103], v248, s[60:63], s31 offen offset:1024 sc1\n"
"v_mfma_f32_16x16x32_bf16 v[160:163], v[228:231], a[32:35], v[160:163]\n"
"buffer_load_dwordx4 a[160:163], v248, s[32:35], s50 offen offset:0 sc1\n"
"v_mfma_f32_16x16x32_bf16 v[164:167], v[228:231], a[36:39], v[164:167]\n"
"v_cvt_pk_bf16_f32 v105, v106, v107\n"
"v_cvt_pk_bf16_f32 v106, v120, v121\n"
"v_mfma_f32_16x16x32_bf16 v[168:171], v[228:231], a[40:43], v[168:171]\n"
"v_cvt_pk_bf16_f32 v107, v122, v123\n"
"v_cvt_pk_bf16_f32 v108, v108, v109\n"
"v_mfma_f32_16x16x32_bf16 v[172:175], v[228:231], a[44:47], v[172:175]\n"
"buffer_load_dwordx4 a[164:167], v248, s[32:35], s50 offen offset:1024 sc1\n"
"v_mfma_f32_16x16x32_bf16 v[160:163], v[232:235], v[80:83], v[160:163]\n"
"buffer_store_dwordx4 v[104:107], v248, s[60:63], s31 offen offset:2048 sc1\n"
"v_mfma_f32_16x16x32_bf16 v[164:167], v[232:235], v[84:87], v[164:167]\n"
"v_cvt_pk_bf16_f32 v109, v110, v111\n"
"v_cvt_pk_bf16_f32 v110, v124, v125\n"
"v_mfma_f32_16x16x32_bf16 v[168:171], v[232:235], v[88:91], v[168:171]\n"
"buffer_load_dwordx4 a[168:171], v248, s[32:35], s50 offen offset:2048 sc1\n"
"v_mfma_f32_16x16x32_bf16 v[172:175], v[232:235], v[92:95], v[172:175]\n"
"v_cvt_pk_bf16_f32 v111, v126, v127\n"
"v_mfma_f32_16x16x32_bf16 v[160:163], v[236:239], a[48:51], v[160:163]\n"
"ds_read_b64_tr_b16 v[224:225], v244 offset:50688\n"
"ds_read_b64_tr_b16 v[226:227], v244 offset:50944\n"
"v_mfma_f32_16x16x32_bf16 v[164:167], v[236:239], a[52:55], v[164:167]\n"
"buffer_load_dwordx4 a[172:175], v248, s[32:35], s50 offen offset:3072 sc1\n"
"v_mfma_f32_16x16x32_bf16 v[168:171], v[236:239], a[56:59], v[168:171]\n"
"ds_read_b64_tr_b16 v[228:229], v246 offset:50688\n"
"ds_read_b64_tr_b16 v[230:231], v246 offset:50944\n"
"v_mfma_f32_16x16x32_bf16 v[172:175], v[236:239], a[60:63], v[172:175]\n"
"s_waitcnt lgkmcnt(4)\n"
"v_mfma_f32_16x16x32_bf16 v[160:163], v[208:211], v[128:131], v[160:163]\n"
"buffer_store_dwordx4 v[108:111], v248, s[60:63], s31 offen offset:3072 sc1\n"
"v_mfma_f32_16x16x32_bf16 v[164:167], v[208:211], v[132:135], v[164:167]\n"
"buffer_load_dwordx4 a[176:179], v248, s[32:35], s51 offen offset:0 sc1\n"
"v_mfma_f32_16x16x32_bf16 v[168:171], v[208:211], v[136:139], v[168:171]\n"
"v_mfma_f32_16x16x32_bf16 v[172:175], v[208:211], v[140:143], v[172:175]\n"
"buffer_load_dwordx4 a[180:183], v248, s[32:35], s51 offen offset:1024 sc1\n"
"v_mfma_f32_16x16x32_bf16 v[160:163], v[212:215], a[64:67], v[160:163]\n"
"s_barrier\n"
"ds_read_b64_tr_b16 v[232:233], v244 offset:59136\n"
"ds_read_b64_tr_b16 v[234:235], v244 offset:59392\n"
"v_mfma_f32_16x16x32_bf16 v[164:167], v[212:215], a[68:71], v[164:167]\n"
"ds_read_b64_tr_b16 v[236:237], v246 offset:59136\n"
"ds_read_b64_tr_b16 v[238:239], v246 offset:59392\n"
"v_mfma_f32_16x16x32_bf16 v[168:171], v[212:215], a[72:75], v[168:171]\n"
"buffer_load_dwordx4 a[184:187], v248, s[32:35], s51 offen offset:2048 sc1\n"
"v_mfma_f32_16x16x32_bf16 v[172:175], v[212:215], a[76:79], v[172:175]\n"
"v_mfma_f32_16x16x32_bf16 v[160:163], v[216:219], v[144:147], v[160:163]\n"
"v_mfma_f32_16x16x32_bf16 v[164:167], v[216:219], v[148:151], v[164:167]\n"
"buffer_load_dwordx4 a[188:191], v248, s[32:35], s51 offen offset:3072 sc1\n"
"v_mfma_f32_16x16x32_bf16 v[168:171], v[216:219], v[152:155], v[168:171]\n"
"v_mfma_f32_16x16x32_bf16 v[172:175], v[216:219], v[156:159], v[172:175]\n"
"v_mfma_f32_16x16x32_bf16 v[160:163], v[220:223], a[80:83], v[160:163]\n"
"ds_read_b64_tr_b16 v[208:209], v245 offset:32\n"
"ds_read_b64_tr_b16 v[210:211], v245 offset:288\n"
"v_mfma_f32_16x16x32_bf16 v[164:167], v[220:223], a[84:87], v[164:167]\n"
"ds_read_b64_tr_b16 v[212:213], v247 offset:32\n"
"ds_read_b64_tr_b16 v[214:215], v247 offset:288\n"
"v_mfma_f32_16x16x32_bf16 v[168:171], v[220:223], a[88:91], v[168:171]\n"
"ds_read_b64_tr_b16 v[216:217], v245 offset:8480\n"
"ds_read_b64_tr_b16 v[218:219], v245 offset:8736\n"
"v_mfma_f32_16x16x32_bf16 v[172:175], v[220:223], a[92:95], v[172:175]\n"
"s_waitcnt lgkmcnt(6)\n"
"v_mfma_f32_16x16x32_bf16 v[160:163], v[224:227], v[32:35], v[160:163]\n"
"ds_read_b64_tr_b16 v[220:221], v247 offset:8480\n"
"ds_read_b64_tr_b16 v[222:223], v247 offset:8736\n"
"v_mfma_f32_16x16x32_bf16 v[164:167], v[224:227], v[36:39], v[164:167]\n"
"v_mfma_f32_16x16x32_bf16 v[168:171], v[224:227], v[40:43], v[168:171]\n"
"v_mfma_f32_16x16x32_bf16 v[172:175], v[224:227], v[44:47], v[172:175]\n"
"v_mfma_f32_16x16x32_bf16 v[160:163], v[228:231], a[96:99], v[160:163]\n"
"v_mfma_f32_16x16x32_bf16 v[164:167], v[228:231], a[100:103], v[164:167]\n"
"v_mfma_f32_16x16x32_bf16 v[168:171], v[228:231], a[104:107], v[168:171]\n"
"v_mfma_f32_16x16x32_bf16 v[172:175], v[228:231], a[108:111], v[172:175]\n"
"v_mfma_f32_16x16x32_bf16 v[160:163], v[232:235], v[48:51], v[160:163]\n"
"v_mfma_f32_16x16x32_bf16 v[164:167], v[232:235], v[52:55], v[164:167]\n"
"v_mfma_f32_16x16x32_bf16 v[168:171], v[232:235], v[56:59], v[168:171]\n"
"v_mfma_f32_16x16x32_bf16 v[172:175], v[232:235], v[60:63], v[172:175]\n"
"v_mfma_f32_16x16x32_bf16 v[160:163], v[236:239], a[112:115], v[160:163]\n"
"v_mfma_f32_16x16x32_bf16 v[164:167], v[236:239], a[116:119], v[164:167]\n"
"v_mfma_f32_16x16x32_bf16 v[168:171], v[236:239], a[120:123], v[168:171]\n"
"v_mfma_f32_16x16x32_bf16 v[172:175], v[236:239], a[124:127], v[172:175]\n"
"s_waitcnt lgkmcnt(0)\n"
"v_mfma_f32_16x16x32_bf16 v[176:179], v[208:211], v[0:3], 0\n"
"s_lshl_b32 s31, s88, 10\n"
"s_add_u32 s31, s31, s93\n"
"v_mfma_f32_16x16x32_bf16 v[180:183], v[208:211], v[4:7], 0\n"
"v_mfma_f32_16x16x32_bf16 v[184:187], v[208:211], v[8:11], 0\n"
"v_cvt_pk_bf16_f32 v96, v160, v161\n"
"v_cvt_pk_bf16_f32 v97, v162, v163\n"
"v_mfma_f32_16x16x32_bf16 v[188:191], v[208:211], v[12:15], 0\n"
"v_cvt_pk_bf16_f32 v98, v160, v161\n"
"v_cvt_pk_bf16_f32 v99, v162, v163\n"
"v_mfma_f32_16x16x32_bf16 v[176:179], v[212:215], a[0:3], v[176:179]\n"
"ds_read_b64_tr_b16 v[224:225], v245 offset:16928\n"
"ds_read_b64_tr_b16 v[226:227], v245 offset:17184\n"
"v_mfma_f32_16x16x32_bf16 v[180:183], v[212:215], a[4:7], v[180:183]\n"
"ds_read_b64_tr_b16 v[228:229], v247 offset:16928\n"
"ds_read_b64_tr_b16 v[230:231], v247 offset:17184\n"
"v_mfma_f32_16x16x32_bf16 v[184:187], v[212:215], a[8:11], v[184:187]\n"
"ds_read_b64_tr_b16 v[232:233], v245 offset:25376\n"
"ds_read_b64_tr_b16 v[234:235], v245 offset:25632\n"
"v_mfma_f32_16x16x32_bf16 v[188:191], v[212:215], a[12:15], v[188:191]\n"
"ds_read_b64_tr_b16 v[236:237], v247 offset:25376\n"
"ds_read_b64_tr_b16 v[238:239], v247 offset:25632\n"
"v_mfma_f32_16x16x32_bf16 v[176:179], v[216:219], v[16:19], v[176:179]\n"
"v_min_u32 v96, -2, v96\n"
"v_min_u32 v97, -2, v97\n"
"v_mfma_f32_16x16x32_bf16 v[180:183], v[216:219], v[20:23], v[180:183]\n"
"v_min_u32 v98, -2, v98\n"
"v_min_u32 v99, -2, v99\n"
"v_mfma_f32_16x16x32_bf16 v[184:187], v[216:219], v[24:27], v[184:187]\n"
"buffer_store_dwordx4 v[96:99], v248, s[36:39], s31 offen offset:0 sc1\n"
"v_mfma_f32_16x16x32_bf16 v[188:191], v[216:219], v[28:31], v[188:191]\n"
"v_cvt_pk_bf16_f32 v100, v164, v165\n"
"v_cvt_pk_bf16_f32 v101, v166, v167\n"
"v_mfma_f32_16x16x32_bf16 v[176:179], v[220:223], a[16:19], v[176:179]\n"
"v_cvt_pk_bf16_f32 v102, v164, v165\n"
"v_cvt_pk_bf16_f32 v103, v166, v167\n"
"v_mfma_f32_16x16x32_bf16 v[180:183], v[220:223], a[20:23], v[180:183]\n"
"v_min_u32 v100, -2, v100\n"
"v_min_u32 v101, -2, v101\n"
"v_mfma_f32_16x16x32_bf16 v[184:187], v[220:223], a[24:27], v[184:187]\n"
"v_min_u32 v102, -2, v102\n"
"v_min_u32 v103, -2, v103\n"
"v_mfma_f32_16x16x32_bf16 v[188:191], v[220:223], a[28:31], v[188:191]\n"
"buffer_store_dwordx4 v[100:103], v248, s[36:39], s31 offen offset:1024 sc1\n"
"s_waitcnt vmcnt(21)\n"
"v_mfma_f32_16x16x32_bf16 v[160:163], a[248:251], a[128:131], v[160:163]\n"
"v_cvt_pk_bf16_f32 v104, v168, v169\n"
"v_cvt_pk_bf16_f32 v105, v170, v171\n"
"v_mfma_f32_16x16x32_bf16 v[176:179], a[250:253], a[128:131], v[176:179]\n"
"v_cvt_pk_bf16_f32 v106, v168, v169\n"
"v_cvt_pk_bf16_f32 v107, v170, v171\n"
"s_waitcnt vmcnt(20)\n"
"v_mfma_f32_16x16x32_bf16 v[164:167], a[248:251], a[132:135], v[164:167]\n"
"v_min_u32 v104, -2, v104\n"
"v_min_u32 v105, -2, v105\n"
"v_mfma_f32_16x16x32_bf16 v[180:183], a[250:253], a[132:135], v[180:183]\n"
"v_min_u32 v106, -2, v106\n"
"v_min_u32 v107, -2, v107\n"
"s_waitcnt vmcnt(19)\n"
"v_mfma_f32_16x16x32_bf16 v[168:171], a[248:251], a[136:139], v[168:171]\n"
"buffer_store_dwordx4 v[104:107], v248, s[36:39], s31 offen offset:2048 sc1\n"
"v_mfma_f32_16x16x32_bf16 v[184:187], a[250:253], a[136:139], v[184:187]\n"
"v_cvt_pk_bf16_f32 v108, v172, v173\n"
"v_cvt_pk_bf16_f32 v109, v174, v175\n"
"v_cvt_pk_bf16_f32 v110, v172, v173\n"
"v_cvt_pk_bf16_f32 v111, v174, v175\n"
"s_waitcnt vmcnt(19)\n"
"v_mfma_f32_16x16x32_bf16 v[172:175], a[248:251], a[140:143], v[172:175]\n"
"v_min_u32 v108, -2, v108\n"
"v_min_u32 v109, -2, v109\n"
"v_mfma_f32_16x16x32_bf16 v[188:191], a[250:253], a[140:143], v[188:191]\n"
"v_min_u32 v110, -2, v110\n"
"s_waitcnt lgkmcnt(0)\n"
"v_mfma_f32_16x16x32_bf16 v[176:179], v[224:227], v[64:67], v[176:179]\n"
"v_min_u32 v111, -2, v111\n"
"v_mfma_f32_16x16x32_bf16 v[180:183], v[224:227], v[68:71], v[180:183]\n"
"buffer_store_dwordx4 v[108:111], v248, s[36:39], s31 offen offset:3072 sc1\n"
"v_mfma_f32_16x16x32_bf16 v[184:187], v[224:227], v[72:75], v[184:187]\n"
"v_mfma_f32_16x16x32_bf16 v[188:191], v[224:227], v[76:79], v[188:191]\n"
"v_mfma_f32_16x16x32_bf16 v[176:179], v[228:231], a[32:35], v[176:179]\n"
"ds_read_b64_tr_b16 v[208:209], v245 offset:33824\n"
"ds_read_b64_tr_b16 v[210:211], v245 offset:34080\n"
"v_mfma_f32_16x16x32_bf16 v[180:183], v[228:231], a[36:39], v[180:183]\n"
"ds_read_b64_tr_b16 v[212:213], v247 offset:33824\n"
"ds_read_b64_tr_b16 v[214:215], v247 offset:34080\n"
"v_mfma_f32_16x16x32_bf16 v[184:187], v[228:231], a[40:43], v[184:187]\n"
"ds_read_b64_tr_b16 v[216:217], v245 offset:42272\n"
"ds_read_b64_tr_b16 v[218:219], v245 offset:42528\n"
"v_mfma_f32_16x16x32_bf16 v[188:191], v[228:231], a[44:47], v[188:191]\n"
"ds_read_b64_tr_b16 v[220:221], v247 offset:42272\n"
"ds_read_b64_tr_b16 v[222:223], v247 offset:42528\n"
"v_mfma_f32_16x16x32_bf16 v[176:179], v[232:235], v[80:83], v[176:179]\n"
"v_mfma_f32_16x16x32_bf16 v[180:183], v[232:235], v[84:87], v[180:183]\n"
"v_mfma_f32_16x16x32_bf16 v[184:187], v[232:235], v[88:91], v[184:187]\n"
"s_lshl_b32 s76, s87, 2\n"
"s_add_u32 s76, s76, s29\n"
"s_add_u32 s76, s76, 0x1000\n"
"v_mfma_f32_16x16x32_bf16 v[188:191], v[232:235], v[92:95], v[188:191]\n"
"s_waitcnt vmcnt(8)\n"
"buffer_store_dword v241, off, s[68:71], s76 sc1\n"
"v_mfma_f32_16x16x32_bf16 v[176:179], v[236:239], a[48:51], v[176:179]\n"
"v_mfma_f32_16x16x32_bf16 v[180:183], v[236:239], a[52:55], v[180:183]\n"
"v_mfma_f32_16x16x32_bf16 v[184:187], v[236:239], a[56:59], v[184:187]\n"
"v_mfma_f32_16x16x32_bf16 v[188:191], v[236:239], a[60:63], v[188:191]\n"
"v_mfma_f32_16x16x32_bf16 v[160:163], a[248:251], a[144:147], v[160:163]\n"
"v_mfma_f32_16x16x32_bf16 v[176:179], a[250:253], a[144:147], v[176:179]\n"
"v_mfma_f32_16x16x32_bf16 v[164:167], a[248:251], a[148:151], v[164:167]\n"
"v_mfma_f32_16x16x32_bf16 v[180:183], a[250:253], a[148:151], v[180:183]\n"
"v_mfma_f32_16x16x32_bf16 v[168:171], a[248:251], a[152:155], v[168:171]\n"
"v_mfma_f32_16x16x32_bf16 v[184:187], a[250:253], a[152:155], v[184:187]\n"
"v_mfma_f32_16x16x32_bf16 v[172:175], a[248:251], a[156:159], v[172:175]\n"
"buffer_load_dword v254, v253, s[68:71], s98 offen sc1\n"
"v_mfma_f32_16x16x32_bf16 v[188:191], a[250:253], a[156:159], v[188:191]\n"
"s_waitcnt lgkmcnt(0)\n"
"v_mfma_f32_16x16x32_bf16 v[176:179], v[208:211], v[128:131], v[176:179]\n"
"v_mfma_f32_16x16x32_bf16 v[180:183], v[208:211], v[132:135], v[180:183]\n"
"v_mfma_f32_16x16x32_bf16 v[184:187], v[208:211], v[136:139], v[184:187]\n"
"v_mfma_f32_16x16x32_bf16 v[188:191], v[208:211], v[140:143], v[188:191]\n"
"v_mfma_f32_16x16x32_bf16 v[176:179], v[212:215], a[64:67], v[176:179]\n"
"ds_read_b64_tr_b16 v[224:225], v245 offset:50720\n"
"ds_read_b64_tr_b16 v[226:227], v245 offset:50976\n"
"v_mfma_f32_16x16x32_bf16 v[180:183], v[212:215], a[68:71], v[180:183]\n"
"ds_read_b64_tr_b16 v[228:229], v247 offset:50720\n"
"ds_read_b64_tr_b16 v[230:231], v247 offset:50976\n"
"v_mfma_f32_16x16x32_bf16 v[184:187], v[212:215], a[72:75], v[184:187]\n"
"ds_read_b64_tr_b16 v[232:233], v245 offset:59168\n"
"ds_read_b64_tr_b16 v[234:235], v245 offset:59424\n"
"v_mfma_f32_16x16x32_bf16 v[188:191], v[212:215], a[76:79], v[188:191]\n"
"ds_read_b64_tr_b16 v[236:237], v247 offset:59168\n"
"ds_read_b64_tr_b16 v[238:239], v247 offset:59424\n"
"v_mfma_f32_16x16x32_bf16 v[176:179], v[216:219], v[144:147], v[176:179]\n"
"v_mfma_f32_16x16x32_bf16 v[180:183], v[216:219], v[148:151], v[180:183]\n"
"v_mfma_f32_16x16x32_bf16 v[184:187], v[216:219], v[152:155], v[184:187]\n"
"v_mfma_f32_16x16x32_bf16 v[188:191], v[216:219], v[156:159], v[188:191]\n"
"v_mfma_f32_16x16x32_bf16 v[176:179], v[220:223], a[80:83], v[176:179]\n"
"v_mfma_f32_16x16x32_bf16 v[180:183], v[220:223], a[84:87], v[180:183]\n"
"v_mfma_f32_16x16x32_bf16 v[184:187], v[220:223], a[88:91], v[184:187]\n"
"v_mfma_f32_16x16x32_bf16 v[188:191], v[220:223], a[92:95], v[188:191]\n"
"v_mfma_f32_16x16x32_bf16 v[160:163], a[248:251], a[160:163], v[160:163]\n"
"v_mfma_f32_16x16x32_bf16 v[176:179], a[250:253], a[160:163], v[176:179]\n"
"v_mfma_f32_16x16x32_bf16 v[164:167], a[248:251], a[164:167], v[164:167]\n"
"v_mfma_f32_16x16x32_bf16 v[180:183], a[250:253], a[164:167], v[180:183]\n"
"v_mfma_f32_16x16x32_bf16 v[168:171], a[248:251], a[168:171], v[168:171]\n"
"v_mfma_f32_16x16x32_bf16 v[184:187], a[250:253], a[168:171], v[184:187]\n"
"v_mfma_f32_16x16x32_bf16 v[172:175], a[248:251], a[172:175], v[172:175]\n"
"v_mfma_f32_16x16x32_bf16 v[188:191], a[250:253], a[172:175], v[188:191]\n"
"s_waitcnt lgkmcnt(0)\n"
"v_mfma_f32_16x16x32_bf16 v[176:179], v[224:227], v[32:35], v[176:179]\n"
"v_mfma_f32_16x16x32_bf16 v[180:183], v[224:227], v[36:39], v[180:183]\n"
"v_mfma_f32_16x16x32_bf16 v[184:187], v[224:227], v[40:43], v[184:187]\n"
"v_mfma_f32_16x16x32_bf16 v[188:191], v[224:227], v[44:47], v[188:191]\n"
"v_mfma_f32_16x16x32_bf16 v[176:179], v[228:231], a[96:99], v[176:179]\n"
"v_mfma_f32_16x16x32_bf16 v[180:183], v[228:231], a[100:103], v[180:183]\n"
"v_mfma_f32_16x16x32_bf16 v[184:187], v[228:231], a[104:107], v[184:187]\n"
"v_mfma_f32_16x16x32_bf16 v[188:191], v[228:231], a[108:111], v[188:191]\n"
"v_mfma_f32_16x16x32_bf16 v[176:179], v[232:235], v[48:51], v[176:179]\n"
"v_mfma_f32_16x16x32_bf16 v[180:183], v[232:235], v[52:55], v[180:183]\n"
"v_mfma_f32_16x16x32_bf16 v[184:187], v[232:235], v[56:59], v[184:187]\n"
"v_mfma_f32_16x16x32_bf16 v[188:191], v[232:235], v[60:63], v[188:191]\n"
"v_mfma_f32_16x16x32_bf16 v[176:179], v[236:239], a[112:115], v[176:179]\n"
"v_mfma_f32_16x16x32_bf16 v[180:183], v[236:239], a[116:119], v[180:183]\n"
"s_waitcnt vmcnt(0)\n"
"v_cmp_eq_u32 vcc, 0, v254\n"
"v_mfma_f32_16x16x32_bf16 v[184:187], v[236:239], a[120:123], v[184:187]\n"
"v_mfma_f32_16x16x32_bf16 v[188:191], v[236:239], a[124:127], v[188:191]\n"
"v_mfma_f32_16x16x32_bf16 v[160:163], a[248:251], a[176:179], v[160:163]\n"
"s_nop 0\n"
"s_and_b32 s79, vcc_lo, s92\n"
"s_cbranch_scc1 L_tfm_%=\n"
"L_bfm_%=:\n"
"v_mfma_f32_16x16x32_bf16 v[176:179], a[250:253], a[176:179], v[176:179]\n"
"buffer_load_dwordx4 v[0:3], v248, s[32:35], s52 offen offset:0 sc1\n"
"v_mfma_f32_16x16x32_bf16 v[164:167], a[248:251], a[180:183], v[164:167]\n"
"v_mfma_f32_16x16x32_bf16 v[180:183], a[250:253], a[180:183], v[180:183]\n"
"buffer_load_dwordx4 v[4:7], v248, s[32:35], s52 offen offset:1024 sc1\n"
"v_mfma_f32_16x16x32_bf16 v[168:171], a[248:251], a[184:187], v[168:171]\n"
"v_mfma_f32_16x16x32_bf16 v[184:187], a[250:253], a[184:187], v[184:187]\n"
"v_mfma_f32_16x16x32_bf16 v[172:175], a[248:251], a[188:191], v[172:175]\n"
"buffer_load_dwordx4 v[8:11], v248, s[32:35], s52 offen offset:2048 sc1\n"
"v_mfma_f32_16x16x32_bf16 v[188:191], a[250:253], a[188:191], v[188:191]\n"
"buffer_load_dwordx4 v[12:15], v248, s[32:35], s52 offen offset:3072 sc1\n"
"buffer_load_dwordx4 v[16:19], v248, s[32:35], s53 offen offset:0 sc1\n"
"buffer_load_dwordx4 v[20:23], v248, s[32:35], s53 offen offset:1024 sc1\n"
"buffer_load_dwordx4 v[24:27], v248, s[32:35], s53 offen offset:2048 sc1\n"
"buffer_load_dwordx4 v[28:31], v248, s[32:35], s53 offen offset:3072 sc1\n"
"buffer_load_dwordx4 v[64:67], v248, s[32:35], s54 offen offset:0 sc1\n"
"buffer_load_dwordx4 v[68:71], v248, s[32:35], s54 offen offset:1024 sc1\n"
"buffer_load_dwordx4 v[72:75], v248, s[32:35], s54 offen offset:2048 sc1\n"
"buffer_load_dwordx4 v[76:79], v248, s[32:35], s54 offen offset:3072 sc1\n"
"s_waitcnt vmcnt(11)\n"
"v_mfma_f32_16x16x32_bf16 v[160:163], a[248:251], v[0:3], v[160:163]\n"
"v_mfma_f32_16x16x32_bf16 v[176:179], a[250:253], v[0:3], v[176:179]\n"
"s_waitcnt vmcnt(10)\n"
"v_mfma_f32_16x16x32_bf16 v[164:167], a[248:251], v[4:7], v[164:167]\n"
"v_mfma_f32_16x16x32_bf16 v[180:183], a[250:253], v[4:7], v[180:183]\n"
"s_waitcnt vmcnt(9)\n"
"v_mfma_f32_16x16x32_bf16 v[168:171], a[248:251], v[8:11], v[168:171]\n"
"v_mfma_f32_16x16x32_bf16 v[184:187], a[250:253], v[8:11], v[184:187]\n"
"s_waitcnt vmcnt(8)\n"
"v_mfma_f32_16x16x32_bf16 v[172:175], a[248:251], v[12:15], v[172:175]\n"
"v_mfma_f32_16x16x32_bf16 v[188:191], a[250:253], v[12:15], v[188:191]\n"
"s_waitcnt vmcnt(7)\n"
"v_mfma_f32_16x16x32_bf16 v[160:163], a[248:251], v[16:19], v[160:163]\n"
"v_mfma_f32_16x16x32_bf16 v[176:179], a[250:253], v[16:19], v[176:179]\n"
"s_waitcnt vmcnt(6)\n"
"v_mfma_f32_16x16x32_bf16 v[164:167], a[248:251], v[20:23], v[164:167]\n"
"v_mfma_f32_16x16x32_bf16 v[180:183], a[250:253], v[20:23], v[180:183]\n"
"s_waitcnt vmcnt(5)\n"
"v_mfma_f32_16x16x32_bf16 v[168:171], a[248:251], v[24:27], v[168:171]\n"
"v_mfma_f32_16x16x32_bf16 v[184:187], a[250:253], v[24:27], v[184:187]\n"
"s_waitcnt vmcnt(4)\n"
"v_mfma_f32_16x16x32_bf16 v[172:175], a[248:251], v[28:31], v[172:175]\n"
"v_mfma_f32_16x16x32_bf16 v[188:191], a[250:253], v[28:31], v[188:191]\n"
"s_waitcnt vmcnt(3)\n"
"v_max_u32 v192, v64, v65\n"
"v_max3_u32 v192, v192, v66, v67\n"
"s_waitcnt vmcnt(2)\n"
"v_max3_u32 v192, v192, v68, v69\n"
"v_max3_u32 v192, v192, v70, v71\n"
"s_waitcnt vmcnt(1)\n"
"v_max3_u32 v192, v192, v72, v73\n"
"v_max3_u32 v192, v192, v74, v75\n"
"s_waitcnt vmcnt(0)\n"
"v_max3_u32 v192, v192, v76, v77\n"
"v_max3_u32 v192, v192, v78, v79\n"
"v_cmp_eq_u32 vcc, -1, v192\n"
"s_nop 3\n"
"s_cbranch_vccnz L_tre6_%=\n"
"L_bre6_%=:\n"
"v_mfma_f32_16x16x32_bf16 v[160:163], a[248:251], v[64:67], v[160:163]\n"
"v_mfma_f32_16x16x32_bf16 v[176:179], a[250:253], v[64:67], v[176:179]\n"
"v_mfma_f32_16x16x32_bf16 v[164:167], a[248:251], v[68:71], v[164:167]\n"
"v_mfma_f32_16x16x32_bf16 v[180:183], a[250:253], v[68:71], v[180:183]\n"
"v_mfma_f32_16x16x32_bf16 v[168:171], a[248:251], v[72:75], v[168:171]\n"
"v_mfma_f32_16x16x32_bf16 v[184:187], a[250:253], v[72:75], v[184:187]\n"
"v_mfma_f32_16x16x32_bf16 v[172:175], a[248:251], v[76:79], v[172:175]\n"
"v_mfma_f32_16x16x32_bf16 v[188:191], a[250:253], v[76:79], v[188:191]\n"
"s_nop 0\n"
"v_cvt_pk_bf16_f32 v160, v160, v161\n"
"v_cvt_pk_bf16_f32 v161, v162, v163\n"
"buffer_store_dwordx2 v[160:161], v240, s[64:67], s4 offen\n"
"v_cvt_pk_bf16_f32 v176, v176, v177\n"
"v_cvt_pk_bf16_f32 v177, v178, v179\n"
"buffer_store_dwordx2 v[176:177], v240, s[40:43], s4 offen\n"
"v_cvt_pk_bf16_f32 v164, v164, v165\n"
"v_cvt_pk_bf16_f32 v165, v166, v167\n"
"buffer_store_dwordx2 v[164:165], v240, s[64:67], s5 offen\n"
"v_cvt_pk_bf16_f32 v180, v180, v181\n"
"v_cvt_pk_bf16_f32 v181, v182, v183\n"
"buffer_store_dwordx2 v[180:181], v240, s[40:43], s5 offen\n"
"v_cvt_pk_bf16_f32 v168, v168, v169\n"
"v_cvt_pk_bf16_f32 v169, v170, v171\n"
"buffer_store_dwordx2 v[168:169], v240, s[64:67], s6 offen\n"
"v_cvt_pk_bf16_f32 v184, v184, v185\n"
"v_cvt_pk_bf16_f32 v185, v186, v187\n"
"buffer_store_dwordx2 v[184:185], v240, s[40:43], s6 offen\n"
"v_cvt_pk_bf16_f32 v172, v172, v173\n"
"v_cvt_pk_bf16_f32 v173, v174, v175\n"
"buffer_store_dwordx2 v[172:173], v240, s[64:67], s7 offen\n"
"v_cvt_pk_bf16_f32 v188, v188, v189\n"
"v_cvt_pk_bf16_f32 v189, v190, v191\n"
"buffer_store_dwordx2 v[188:189], v240, s[40:43], s7 offen\n"
"s_branch L_fin_%=\n"
"L_hb_%=:\n"
"s_mov_b32 s78, 0x40000\n"
"L_hbl_%=:\n"
"s_add_u32 s28, s28, 1\n"
"v_mov_b32 v252, s28\n"
"buffer_store_dword v252, off, s[68:71], s29 sc1\n"
"s_sleep 2\n"
"buffer_load_dword v254, v253, s[68:71], s30 offen sc1\n"
"s_waitcnt vmcnt(0)\n"
"v_cmp_eq_u32 vcc, v254, v249\n"
"s_nop 4\n"
"s_cbranch_vccz L_hbd_%=\n"
"s_sub_u32 s78, s78, 1\n"
"s_cmp_lg_u32 s78, 0\n"
"s_cbranch_scc1 L_hbl_%=\n"
"L_hbd_%=:\n"
"s_setpc_b64 s[80:81]\n"
"L_tB0_%=:\n"
"s_call_b64 s[80:81], L_rareB1_%=\n"
"s_branch L_bB0_%=\n"
"L_tC0_%=:\n"
"s_call_b64 s[80:81], L_rareC0_%=\n"
"s_branch L_bC0_%=\n"
"L_tBl0_%=:\n"
"s_call_b64 s[80:81], L_rareB0_%=\n"
"s_branch L_bBl0_%=\n"
"L_tCl0_%=:\n"
"s_call_b64 s[80:81], L_rareC1_%=\n"
"s_branch L_bCl0_%=\n"
"L_tBl1_%=:\n"
"s_call_b64 s[80:81], L_rareB1_%=\n"
"s_branch L_bBl1_%=\n"
"L_tCl1_%=:\n"
"s_call_b64 s[80:81], L_rareC0_%=\n"
"s_branch L_bCl1_%=\n"
"L_tBl2_%=:\n"
"s_call_b64 s[80:81], L_rareB0_%=\n"
"s_branch L_bBl2_%=\n"
"L_tCl2_%=:\n"
"s_call_b64 s[80:81], L_rareC1_%=\n"
"s_branch L_bCl2_%=\n"
"L_tBl3_%=:\n"
"s_call_b64 s[80:81], L_rareB1_%=\n"
"s_branch L_bBl3_%=\n"
"L_tCl3_%=:\n"
"s_call_b64 s[80:81], L_rareC0_%=\n"
"s_branch L_bCl3_%=\n"
"L_tB13_%=:\n"
"s_call_b64 s[80:81], L_rareB0_%=\n"
"s_branch L_bB13_%=\n"
"L_tC13_%=:\n"
"s_call_b64 s[80:81], L_rareC1_%=\n"
"s_branch L_bC13_%=\n"
"L_tB14_%=:\n"
"s_call_b64 s[80:81], L_rareB1_%=\n"
"s_branch L_bB14_%=\n"
"L_tC14_%=:\n"
"s_call_b64 s[80:81], L_rareC0_%=\n"
"s_branch L_bC14_%=\n"
"L_thb_%=:\n"
"s_call_b64 s[80:81], L_hb_%=\n"
"s_branch L_bhb_%=\n"
"L_tfl_%=:\n"
"s_call_b64 s[80:81], L_fl_%=\n"
"s_branch L_bfl_%=\n"
"L_fl_%=:\n"
"s_mov_b32 s78, 0x40000\n"
"L_fll_%=:\n"
"s_add_u32 s28, s28, 1\n"
"v_mov_b32 v252, s28\n"
"buffer_store_dword v252, off, s[68:71], s29 sc1\n"
"buffer_load_dword v254, v253, s[68:71], s98 offen sc1\n"
"s_waitcnt vmcnt(0)\n"
"v_cmp_eq_u32 vcc, 0, v254\n"
"s_nop 4\n"
"s_and_b32 s79, vcc_lo, s97\n"
"s_cbranch_scc0 L_fld_%=\n"
"s_sub_u32 s78, s78, 1\n"
"s_cmp_lg_u32 s78, 0\n"
"s_cbranch_scc1 L_fll_%=\n"
"L_fld_%=:\n"
"s_setpc_b64 s[80:81]\n"
"L_tfm_%=:\n"
"s_call_b64 s[80:81], L_fm_%=\n"
"s_branch L_bfm_%=\n"
"L_fm_%=:\n"
"s_mov_b32 s78, 0x40000\n"
"L_fml_%=:\n"
"s_add_u32 s28, s28, 1\n"
"v_mov_b32 v252, s28\n"
"buffer_store_dword v252, off, s[68:71], s29 sc1\n"
"buffer_load_dword v254, v253, s[68:71], s98 offen sc1\n"
"s_waitcnt vmcnt(0)\n"
"v_cmp_eq_u32 vcc, 0, v254\n"
"s_nop 4\n"
"s_and_b32 s79, vcc_lo, s92\n"
"s_cbranch_scc0 L_fmd_%=\n"
"s_sub_u32 s78, s78, 1\n"
"s_cmp_lg_u32 s78, 0\n"
"s_cbranch_scc1 L_fml_%=\n"
"L_fmd_%=:\n"
"s_setpc_b64 s[80:81]\n"
"L_tre6_%=:\n"
"s_call_b64 s[80:81], L_re6_%=\n"
"s_branch L_bre6_%=\n"
"L_re6_%=:\n"
"s_cmp_lt_u32 s90, 6\n"
"s_cbranch_scc1 L_rd6_%=\n"
"s_mov_b32 s78, 0x40000\n"
"L_rl6_%=:\n"
"s_add_u32 s28, s28, 1\n"
"v_mov_b32 v252, s28\n"
"buffer_store_dword v252, off, s[68:71], s29 sc1\n"
"buffer_load_dwordx4 v[64:67], v248, s[32:35], s54 offen offset:0 sc1\n"
"buffer_load_dwordx4 v[68:71], v248, s[32:35], s54 offen offset:1024 sc1\n"
"buffer_load_dwordx4 v[72:75], v248, s[32:35], s54 offen offset:2048 sc1\n"
"buffer_load_dwordx4 v[76:79], v248, s[32:35], s54 offen offset:3072 sc1\n"
"s_waitcnt vmcnt(0)\n"
"v_max_u32 v192, v64, v65\n"
"v_max3_u32 v192, v192, v66, v67\n"
"v_max3_u32 v192, v192, v68, v69\n"
"v_max3_u32 v192, v192, v70, v71\n"
"v_max3_u32 v192, v192, v72, v73\n"
"v_max3_u32 v192, v192, v74, v75\n"
"v_max3_u32 v192, v192, v76, v77\n"
"v_max3_u32 v192, v192, v78, v79\n"
"v_cmp_eq_u32 vcc, -1, v192\n"
"s_nop 4\n"
"s_cbranch_vccz L_rd6_%=\n"
"s_sub_u32 s78, s78, 1\n"
"s_cmp_lg_u32 s78, 0\n"
"s_cbranch_scc1 L_rl6_%=\n"
"L_rd6_%=:\n"
"s_setpc_b64 s[80:81]\n"
"L_end1_%=:\n"
"L_Y_%=:\n"
"v_subrev_u32 v192, 0xe000, v192\n"
"v_subrev_u32 v193, 0xe000, v193\n"
"v_subrev_u32 v194, 0xe000, v194\n"
"v_subrev_u32 v195, 0xe000, v195\n"
"s_add_u32 s83, s82, 0x80000\n"
"s_cmp_eq_u32 s83, 0x380000\n"
"s_cselect_b32 s83, 0, s83\n"
"s_add_u32 s84, s83, 0x80000\n"
"s_cmp_eq_u32 s84, 0x380000\n"
"s_cselect_b32 s84, 0, s84\n"
"v_and_b32 v254, 3, v253\n"
"v_lshrrev_b32 v253, 2, v253\n"
"v_lshl_add_u32 v254, v253, 4, v254\n"
"s_lshl_b32 s79, s89, 2\n"
"v_add_u32 v254, s79, v254\n"
"v_lshl_add_u32 v243, v254, 9, v252\n"
"v_and_b32 v252, 31, v251\n"
"v_lshlrev_b32 v252, 4, v252\n"
"v_xor_b32 v252, s94, v252\n"
"v_lshrrev_b32 v253, 5, v251\n"
"v_lshl_add_u32 v252, v253, 12, v252\n"
"s_lshr_b32 s79, s89, 1\n"
"s_lshl_b32 s78, s79, 13\n"
"s_lshl_b32 s79, s79, 13\n"
"s_and_b32 s94, s89, 1\n"
"s_lshl_b32 s31, s94, 11\n"
"s_add_u32 s31, s31, s78\n"
"v_add_u32 v242, s31, v252\n"
"v_and_b32 v252, 15, v251\n"
"v_lshlrev_b32 v252, 7, v252\n"
"v_bfe_u32 v253, v251, 1, 3\n"
"v_lshrrev_b32 v254, 4, v251\n"
"v_xor_b32 v253, v253, v254\n"
"v_lshl_add_u32 v252, v253, 4, v252\n"
"s_add_u32 s79, s79, 0x8400\n"
"v_add_u32 v196, s79, v252\n"
"v_xor_b32 v197, 64, v196\n"
"v_and_b32 v252, 15, v251\n"
"v_lshlrev_b32 v252, 4, v252\n"
"v_lshl_add_u32 v252, v254, 10, v252\n"
"s_lshl_b32 s94, s94, 12\n"
"v_add_u32 v198, s94, v252\n"
"v_add_u32 v244, 0x10800, v198\n"
"v_add_u32 v245, 0x21000, v198\n"
"s_add_u32 s85, s84, 0x80000\n"
"s_cmp_eq_u32 s85, 0x380000\n"
"s_cselect_b32 s85, 0, s85\n"
"s_add_u32 s86, s85, 0x80000\n"
"s_cmp_eq_u32 s86, 0x380000\n"
"s_cselect_b32 s86, 0, s86\n"
"s_add_u32 s87, s86, 0x80000\n"
"s_cmp_eq_u32 s87, 0x380000\n"
"s_cselect_b32 s87, 0, s87\n"
"s_add_u32 s88, s87, 0x80000\n"
"s_cmp_eq_u32 s88, 0x380000\n"
"s_cselect_b32 s88, 0, s88\n"
"s_add_u32 s48, s6, %3\n"
"s_addc_u32 s49, s7, 0\n"
"s_mov_b32 s50, 262144\n"
"s_mov_b32 s51, 0x20000\n"
"s_add_u32 s52, s8, %3\n"
"s_addc_u32 s53, s9, 0\n"
"s_mov_b32 s54, 262144\n"
"s_mov_b32 s55, 0x20000\n"
"s_mov_b64 s[8:9], s[28:29]\n"
"s_mov_b64 s[68:69], s[18:19]\n"
"s_mov_b32 s70, 8192\n"
"s_mov_b32 s71, 0x20000\n"
"s_bfe_u32 s79, %7, 0x50005\n"
"s_lshl_b32 s79, s79, 13\n"
"s_add_u32 s79, s79, 0x2000000\n"
"s_add_u32 s68, s68, s79\n"
"s_addc_u32 s69, s69, 0\n"
"s_lshl_b32 s30, s89, 7\n"
"s_lshl_b32 s29, s90, 2\n"
"s_add_u32 s29, s29, s30\n"
"s_add_u32 s56, s12, %4\n"
"s_addc_u32 s57, s13, 0\n"
"s_mov_b32 s58, 458752\n"
"s_mov_b32 s59, 0x20000\n"
"s_add_u32 s64, s16, %5\n"
"s_addc_u32 s65, s17, 0\n"
"s_mov_b32 s66, 228480\n"
"s_mov_b32 s67, 0x20000\n"
"s_mov_b64 s[60:61], s[18:19]\n"
"s_mov_b32 s62, 917504\n"
"s_mov_b32 s63, 0x20000\n"
"s_bfe_u32 s79, %7, 0x50005\n"
"s_lshl_b32 s79, s79, 20\n"
"s_add_u32 s60, s60, s79\n"
"s_addc_u32 s61, s61, 0\n"
"s_min_u32 s91, s90, 6\n"
"s_mov_b64 s[32:33], s[60:61]\n"
"s_mov_b32 s34, 131072\n"
"s_mov_b32 s35, 0x20000\n"
"s_lshl_b32 s79, s91, 17\n"
"s_add_u32 s32, s32, s79\n"
"s_addc_u32 s33, s33, 0\n"
"s_mov_b64 s[36:37], s[60:61]\n"
"s_mov_b32 s38, 917504\n"
"s_mov_b32 s39, 0x20000\n"
"s_cmp_lt_u32 s90, 6\n"
"s_cselect_b32 s38, 0, s38\n"
"s_cmp_eq_u32 s90, 6\n"
"s_cselect_b32 s99, 32, 0\n"
"s_bfe_u32 s92, %7, 0x3000d\n"
"s_mul_i32 s73, s89, 4224\n"
"s_mul_i32 s74, s89, 1056\n"
"s_mov_b32 s95, 0x3db8aa3b\n"
"s_mov_b32 s76, 0x10000\n"
"s_lshl_b32 s98, s91, 9\n"
"s_add_u32 s98, s98, s30\n"
"s_add_u32 s98, s98, 0x1000\n"
"v_subrev_u32 v199, 0x2000, v242\n"
"v_subrev_u32 v200, 0x1000, v243\n"
"s_lshl_b32 s93, s90, 14\n"
"s_add_u32 s93, s93, s96\n"
"v_and_b32 v240, 7, v251\n"
"v_mul_u32_u24 v240, 0x420, v240\n"
"v_bfe_u32 v253, v251, 3, 1\n"
"v_lshl_add_u32 v240, v253, 9, v240\n"
"v_lshrrev_b32 v254, 4, v251\n"
"v_lshl_add_u32 v240, v254, 4, v240\n"
"v_mul_u32_u24 v241, 0x420, v254\n"
"v_bfe_u32 v253, v251, 2, 2\n"
"v_lshl_add_u32 v241, v253, 6, v241\n"
"v_and_b32 v253, 3, v251\n"
"v_lshl_add_u32 v241, v253, 3, v241\n"
"v_add_u32 v241, 0x10800, v241\n"
"s_lshl_b32 s92, s92, 2\n"
"s_lshl_b32 s79, s91, 2\n"
"s_mov_b32 s14, 0x6543210\n"
"s_mov_b32 s15, 0x654321\n"
"s_add_u32 s94, s79, 4\n"
"s_lshr_b64 s[14:15], s[14:15], s94\n"
"s_and_b32 s14, s14, 0xffffff\n"
"s_lshr_b32 s15, s14, 8\n"
"s_lshl_b32 s94, s14, 24\n"
"s_or_b32 s14, s14, s94\n"
"s_lshr_b64 s[14:15], s[14:15], s92\n"
"s_and_b32 s16, s14, 0xffffff\n"
"s_mov_b32 s14, 0x60123456\n"
"s_mov_b32 s15, 0x12345\n"
"s_sub_u32 s94, 28, s79\n"
"s_lshr_b64 s[14:15], s[14:15], s94\n"
"s_and_b32 s14, s14, 0xffffff\n"
"s_lshr_b32 s15, s14, 8\n"
"s_lshl_b32 s94, s14, 24\n"
"s_or_b32 s14, s14, s94\n"
"s_lshr_b64 s[14:15], s[14:15], s92\n"
"s_and_b32 s17, s14, 0xffffff\n"
"s_lshl_b32 s79, s91, 24\n"
"s_or_b32 s16, s16, s79\n"
"s_cmp_eq_u32 s90, 7\n"
"s_cselect_b32 s79, 6, 7\n"
"s_lshl_b32 s79, s79, 24\n"
"s_or_b32 s17, s17, s79\n"
"s_lshl_b32 s4, s91, 7\n"
"s_add_u32 s5, s4, 0x7000\n"
"s_add_u32 s6, s4, 0xe000\n"
"s_add_u32 s7, s4, 0x15000\n"
"v_and_b32 v252, 7, v251\n"
"v_lshlrev_b32 v252, 2, v252\n"
"v_mov_b32 v253, 0\n"
"s_bfe_u32 s79, %7, 0x40005\n"
"s_lshl_b32 s79, s79, 10\n"
"s_and_b32 s94, %7, 7\n"
"s_lshl_b32 s94, s94, 7\n"
"s_add_u32 s79, s79, s94\n"
"s_add_u32 s79, s79, L_u13_%=-L_pc0_%=\n"
"v_and_b32 v251, 3, v251\n"
"v_lshlrev_b32 v251, 14, v251\n"
"v_add_u32 v251, s79, v251\n"
"v_min_u32 v251, L_end1_%=-L_pc0_%=, v251\n"
"global_load_dword v255, v255, s[8:9]\n"
"s_add_u32 m0, s96, 0xa600\n"
"s_lshr_b32 s75, s83, 12\n"
"buffer_load_dwordx4 v192, s[40:43], s75 offen nt lds\n"
"s_add_u32 m0, s96, 0xaa00\n"
"s_nop 0\n"
"buffer_load_dwordx4 v193, s[40:43], s75 offen nt lds\n"
"s_add_u32 m0, s96, 0xae00\n"
"s_nop 0\n"
"buffer_load_dwordx4 v194, s[40:43], s75 offen nt lds\n"
"s_add_u32 m0, s96, 0xb200\n"
"s_nop 0\n"
"buffer_load_dwordx4 v195, s[40:43], s75 offen nt lds\n"
"s_add_u32 m0, s96, 0xc600\n"
"s_add_u32 s75, s75, 0xe000\n"
"buffer_load_dwordx4 v192, s[40:43], s75 offen nt lds\n"
"s_add_u32 m0, s96, 0xca00\n"
"s_nop 0\n"
"buffer_load_dwordx4 v193, s[40:43], s75 offen nt lds\n"
"s_add_u32 m0, s96, 0xce00\n"
"s_nop 0\n"
"buffer_load_dwordx4 v194, s[40:43], s75 offen nt lds\n"
"s_add_u32 m0, s96, 0xd200\n"
"s_nop 0\n"
"buffer_load_dwordx4 v195, s[40:43], s75 offen nt lds\n"
"s_waitcnt vmcnt(13)\n"
"s_barrier\n"
"ds_read_b128 a[0:3], v196 offset:0\n"
"ds_read_b128 a[4:7], v196 offset:2048\n"
"ds_read_b128 a[8:11], v196 offset:4096\n"
"ds_read_b128 a[12:15], v196 offset:6144\n"
"s_waitcnt vmcnt(11)\n"
"s_barrier\n"
"ds_read_b64_tr_b16 v[208:209], v244 offset:33792\n"
"ds_read_b64_tr_b16 v[210:211], v244 offset:34048\n"
"s_waitcnt lgkmcnt(0)\n"
"v_mfma_f32_16x16x32_bf16 v[0:3], v[208:211], a[0:3], 0\n"
"ds_read_b64_tr_b16 v[212:213], v244 offset:33800\n"
"ds_read_b64_tr_b16 v[214:215], v244 offset:34056\n"
"s_add_u32 m0, s96, 0xe800\n"
"s_lshr_b32 s75, s84, 12\n"
"v_mfma_f32_16x16x32_bf16 v[4:7], v[208:211], a[4:7], 0\n"
"buffer_load_dwordx4 v192, s[40:43], s75 offen nt lds\n"
"ds_read_b64_tr_b16 v[216:217], v244 offset:34304\n"
"ds_read_b64_tr_b16 v[218:219], v244 offset:34560\n"
"v_mfma_f32_16x16x32_bf16 v[8:11], v[208:211], a[8:11], 0\n"
"ds_read_b64_tr_b16 v[220:221], v244 offset:34312\n"
"ds_read_b64_tr_b16 v[222:223], v244 offset:34568\n"
"v_mfma_f32_16x16x32_bf16 v[12:15], v[208:211], a[12:15], 0\n"
"s_add_u32 m0, s96, 0xec00\n"
"s_waitcnt lgkmcnt(4)\n"
"v_mfma_f32_16x16x32_bf16 v[32:35], v[212:215], a[0:3], 0\n"
"buffer_load_dwordx4 v193, s[40:43], s75 offen nt lds\n"
"v_mfma_f32_16x16x32_bf16 v[36:39], v[212:215], a[4:7], 0\n"
"s_waitcnt vmcnt(11)\n"
"s_barrier\n"
"ds_read_b64_tr_b16 v[224:225], v244 offset:42240\n"
"ds_read_b64_tr_b16 v[226:227], v244 offset:42496\n"
"v_mfma_f32_16x16x32_bf16 v[40:43], v[212:215], a[8:11], 0\n"
"s_add_u32 m0, s96, 0xf000\n"
"v_mfma_f32_16x16x32_bf16 v[44:47], v[212:215], a[12:15], 0\n"
"buffer_load_dwordx4 v194, s[40:43], s75 offen nt lds\n"
"s_waitcnt lgkmcnt(4)\n"
"v_mfma_f32_16x16x32_bf16 v[16:19], v[216:219], a[0:3], 0\n"
"ds_read_b64_tr_b16 v[228:229], v244 offset:42248\n"
"ds_read_b64_tr_b16 v[230:231], v244 offset:42504\n"
"v_mfma_f32_16x16x32_bf16 v[20:23], v[216:219], a[4:7], 0\n"
"ds_read_b64_tr_b16 v[232:233], v244 offset:42752\n"
"ds_read_b64_tr_b16 v[234:235], v244 offset:43008\n"
"ds_read_b64_tr_b16 v[236:237], v244 offset:42760\n"
"ds_read_b64_tr_b16 v[238:239], v244 offset:43016\n"
"s_add_u32 m0, s96, 0xf400\n"
"v_mfma_f32_16x16x32_bf16 v[24:27], v[216:219], a[8:11], 0\n"
"buffer_load_dwordx4 v195, s[40:43], s75 offen nt lds\n"
"v_mfma_f32_16x16x32_bf16 v[28:31], v[216:219], a[12:15], 0\n"
"s_waitcnt lgkmcnt(8)\n"
"v_mfma_f32_16x16x32_bf16 v[48:51], v[220:223], a[0:3], 0\n"
"s_add_u32 m0, s96, 0x10800\n"
"s_add_u32 s75, s75, 0xe000\n"
"v_mfma_f32_16x16x32_bf16 v[52:55], v[220:223], a[4:7], 0\n"
"buffer_load_dwordx4 v192, s[40:43], s75 offen nt lds\n"
"v_mfma_f32_16x16x32_bf16 v[56:59], v[220:223], a[8:11], 0\n"
"ds_read_b128 a[16:19], v197 offset:0\n"
"ds_read_b128 a[20:23], v197 offset:2048\n"
"v_mfma_f32_16x16x32_bf16 v[60:63], v[220:223], a[12:15], 0\n"
"s_add_u32 m0, s96, 0x10c00\n"
"s_waitcnt lgkmcnt(2)\n"
"v_mfma_f32_16x16x32_bf16 v[64:67], v[224:227], a[0:3], 0\n"
"buffer_load_dwordx4 v193, s[40:43], s75 offen nt lds\n"
"v_mfma_f32_16x16x32_bf16 v[68:71], v[224:227], a[4:7], 0\n"
"ds_read_b128 a[24:27], v197 offset:4096\n"
"ds_read_b128 a[28:31], v197 offset:6144\n"
"v_mfma_f32_16x16x32_bf16 v[72:75], v[224:227], a[8:11], 0\n"
"s_add_u32 m0, s96, 0x11000\n"
"v_mfma_f32_16x16x32_bf16 v[76:79], v[224:227], a[12:15], 0\n"
"buffer_load_dwordx4 v194, s[40:43], s75 offen nt lds\n"
"v_mfma_f32_16x16x32_bf16 v[96:99], v[228:231], a[0:3], 0\n"
"s_barrier\n"
"ds_read_b64_tr_b16 v[208:209], v244 offset:50688\n"
"ds_read_b64_tr_b16 v[210:211], v244 offset:50944\n"
"v_mfma_f32_16x16x32_bf16 v[100:103], v[228:231], a[4:7], 0\n"
"s_add_u32 m0, s96, 0x11400\n"
"v_mfma_f32_16x16x32_bf16 v[104:107], v[228:231], a[8:11], 0\n"
"buffer_load_dwordx4 v195, s[40:43], s75 offen nt lds\n"
"v_mfma_f32_16x16x32_bf16 v[108:111], v[228:231], a[12:15], 0\n"
"ds_read_b64_tr_b16 v[212:213], v244 offset:50696\n"
"ds_read_b64_tr_b16 v[214:215], v244 offset:50952\n"
"v_mfma_f32_16x16x32_bf16 v[80:83], v[232:235], a[0:3], 0\n"
"v_mfma_f32_16x16x32_bf16 v[84:87], v[232:235], a[4:7], 0\n"
"ds_read_b64_tr_b16 v[216:217], v244 offset:51200\n"
"ds_read_b64_tr_b16 v[218:219], v244 offset:51456\n"
"ds_read_b64_tr_b16 v[220:221], v244 offset:51208\n"
"ds_read_b64_tr_b16 v[222:223], v244 offset:51464\n"
"v_mfma_f32_16x16x32_bf16 v[88:91], v[232:235], a[8:11], 0\n"
"v_mfma_f32_16x16x32_bf16 v[92:95], v[232:235], a[12:15], 0\n"
"v_mfma_f32_16x16x32_bf16 v[112:115], v[236:239], a[0:3], 0\n"
"v_mfma_f32_16x16x32_bf16 v[116:119], v[236:239], a[4:7], 0\n"
"v_mfma_f32_16x16x32_bf16 v[120:123], v[236:239], a[8:11], 0\n"
"v_mfma_f32_16x16x32_bf16 v[124:127], v[236:239], a[12:15], 0\n"
"s_waitcnt lgkmcnt(0)\n"
"v_mfma_f32_16x16x32_bf16 v[0:3], v[208:211], a[16:19], v[0:3]\n"
"v_mfma_f32_16x16x32_bf16 v[4:7], v[208:211], a[20:23], v[4:7]\n"
"v_mfma_f32_16x16x32_bf16 v[8:11], v[208:211], a[24:27], v[8:11]\n"
"v_mfma_f32_16x16x32_bf16 v[12:15], v[208:211], a[28:31], v[12:15]\n"
"v_mfma_f32_16x16x32_bf16 v[32:35], v[212:215], a[16:19], v[32:35]\n"
"v_mfma_f32_16x16x32_bf16 v[36:39], v[212:215], a[20:23], v[36:39]\n"
"v_mfma_f32_16x16x32_bf16 v[40:43], v[212:215], a[24:27], v[40:43]\n"
"s_barrier\n"
"ds_read_b64_tr_b16 v[224:225], v244 offset:59136\n"
"ds_read_b64_tr_b16 v[226:227], v244 offset:59392\n"
"v_mfma_f32_16x16x32_bf16 v[44:47], v[212:215], a[28:31], v[44:47]\n"
"s_add_u32 m0, s96, 0x6400\n"
"s_lshr_b32 s75, s85, 12\n"
"v_mfma_f32_16x16x32_bf16 v[16:19], v[216:219], a[16:19], v[16:19]\n"
"buffer_load_dwordx4 v192, s[40:43], s75 offen nt lds\n"
"v_mfma_f32_16x16x32_bf16 v[20:23], v[216:219], a[20:23], v[20:23]\n"
"ds_read_b64_tr_b16 v[228:229], v244 offset:59144\n"
"ds_read_b64_tr_b16 v[230:231], v244 offset:59400\n"
"ds_read_b64_tr_b16 v[232:233], v244 offset:59648\n"
"ds_read_b64_tr_b16 v[234:235], v244 offset:59904\n"
"ds_read_b64_tr_b16 v[236:237], v244 offset:59656\n"
"ds_read_b64_tr_b16 v[238:239], v244 offset:59912\n"
"v_mfma_f32_16x16x32_bf16 v[24:27], v[216:219], a[24:27], v[24:27]\n"
"s_add_u32 m0, s96, 0x6800\n"
"v_mfma_f32_16x16x32_bf16 v[28:31], v[216:219], a[28:31], v[28:31]\n"
"buffer_load_dwordx4 v193, s[40:43], s75 offen nt lds\n"
"v_mfma_f32_16x16x32_bf16 v[48:51], v[220:223], a[16:19], v[48:51]\n"
"v_mfma_f32_16x16x32_bf16 v[52:55], v[220:223], a[20:23], v[52:55]\n"
"s_add_u32 m0, s96, 0x6c00\n"
"v_mfma_f32_16x16x32_bf16 v[56:59], v[220:223], a[24:27], v[56:59]\n"
"buffer_load_dwordx4 v194, s[40:43], s75 offen nt lds\n"
"v_mfma_f32_16x16x32_bf16 v[60:63], v[220:223], a[28:31], v[60:63]\n"
"s_waitcnt lgkmcnt(0)\n"
"v_mfma_f32_16x16x32_bf16 v[64:67], v[224:227], a[16:19], v[64:67]\n"
"s_add_u32 m0, s96, 0x7000\n"
"v_mfma_f32_16x16x32_bf16 v[68:71], v[224:227], a[20:23], v[68:71]\n"
"buffer_load_dwordx4 v195, s[40:43], s75 offen nt lds\n"
"v_mfma_f32_16x16x32_bf16 v[72:75], v[224:227], a[24:27], v[72:75]\n"
"v_mfma_f32_16x16x32_bf16 v[76:79], v[224:227], a[28:31], v[76:79]\n"
"s_add_u32 m0, s96, 0x8400\n"
"s_add_u32 s75, s75, 0xe000\n"
"v_mfma_f32_16x16x32_bf16 v[96:99], v[228:231], a[16:19], v[96:99]\n"
"buffer_load_dwordx4 v192, s[40:43], s75 offen nt lds\n"
"v_mfma_f32_16x16x32_bf16 v[100:103], v[228:231], a[20:23], v[100:103]\n"
"s_waitcnt vmcnt(13)\n"
"s_barrier\n"
"ds_read_b128 a[32:35], v196 offset:16896\n"
"v_mfma_f32_16x16x32_bf16 v[104:107], v[228:231], a[24:27], v[104:107]\n"
"ds_read_b128 a[36:39], v196 offset:18944\n"
"s_add_u32 m0, s96, 0x8800\n"
"v_mfma_f32_16x16x32_bf16 v[108:111], v[228:231], a[28:31], v[108:111]\n"
"buffer_load_dwordx4 v193, s[40:43], s75 offen nt lds\n"
"v_mfma_f32_16x16x32_bf16 v[80:83], v[232:235], a[16:19], v[80:83]\n"
"ds_read_b128 a[40:43], v196 offset:20992\n"
"ds_read_b128 a[44:47], v196 offset:23040\n"
"v_mfma_f32_16x16x32_bf16 v[84:87], v[232:235], a[20:23], v[84:87]\n"
"s_barrier\n"
"ds_read_b64_tr_b16 v[208:209], v245 offset:0\n"
"ds_read_b64_tr_b16 v[210:211], v245 offset:256\n"
"ds_read_b64_tr_b16 v[212:213], v245 offset:8\n"
"ds_read_b64_tr_b16 v[214:215], v245 offset:264\n"
"ds_read_b64_tr_b16 v[216:217], v245 offset:512\n"
"ds_read_b64_tr_b16 v[218:219], v245 offset:768\n"
"ds_read_b64_tr_b16 v[220:221], v245 offset:520\n"
"ds_read_b64_tr_b16 v[222:223], v245 offset:776\n"
"s_add_u32 m0, s96, 0x8c00\n"
"v_mfma_f32_16x16x32_bf16 v[88:91], v[232:235], a[24:27], v[88:91]\n"
"buffer_load_dwordx4 v194, s[40:43], s75 offen nt lds\n"
"v_mfma_f32_16x16x32_bf16 v[92:95], v[232:235], a[28:31], v[92:95]\n"
"v_mfma_f32_16x16x32_bf16 v[112:115], v[236:239], a[16:19], v[112:115]\n"
"s_add_u32 m0, s96, 0x9000\n"
"v_mfma_f32_16x16x32_bf16 v[116:119], v[236:239], a[20:23], v[116:119]\n"
"buffer_load_dwordx4 v195, s[40:43], s75 offen nt lds\n"
"v_mfma_f32_16x16x32_bf16 v[120:123], v[236:239], a[24:27], v[120:123]\n"
"v_mfma_f32_16x16x32_bf16 v[124:127], v[236:239], a[28:31], v[124:127]\n"
"s_waitcnt lgkmcnt(0)\n"
"v_mfma_f32_16x16x32_bf16 v[0:3], v[208:211], a[32:35], v[0:3]\n"
"v_mfma_f32_16x16x32_bf16 v[4:7], v[208:211], a[36:39], v[4:7]\n"
"v_mfma_f32_16x16x32_bf16 v[8:11], v[208:211], a[40:43], v[8:11]\n"
"v_mfma_f32_16x16x32_bf16 v[12:15], v[208:211], a[44:47], v[12:15]\n"
"v_mfma_f32_16x16x32_bf16 v[32:35], v[212:215], a[32:35], v[32:35]\n"
"s_barrier\n"
"ds_read_b64_tr_b16 v[224:225], v245 offset:8448\n"
"ds_read_b64_tr_b16 v[226:227], v245 offset:8704\n"
"v_mfma_f32_16x16x32_bf16 v[36:39], v[212:215], a[36:39], v[36:39]\n"
"v_mfma_f32_16x16x32_bf16 v[40:43], v[212:215], a[40:43], v[40:43]\n"
"v_mfma_f32_16x16x32_bf16 v[44:47], v[212:215], a[44:47], v[44:47]\n"
"ds_read_b64_tr_b16 v[228:229], v245 offset:8456\n"
"ds_read_b64_tr_b16 v[230:231], v245 offset:8712\n"
"v_mfma_f32_16x16x32_bf16 v[16:19], v[216:219], a[32:35], v[16:19]\n"
"v_mfma_f32_16x16x32_bf16 v[20:23], v[216:219], a[36:39], v[20:23]\n"
"ds_read_b64_tr_b16 v[232:233], v245 offset:8960\n"
"ds_read_b64_tr_b16 v[234:235], v245 offset:9216\n"
"ds_read_b64_tr_b16 v[236:237], v245 offset:8968\n"
"ds_read_b64_tr_b16 v[238:239], v245 offset:9224\n"
"v_mfma_f32_16x16x32_bf16 v[24:27], v[216:219], a[40:43], v[24:27]\n"
"v_mfma_f32_16x16x32_bf16 v[28:31], v[216:219], a[44:47], v[28:31]\n"
"v_mfma_f32_16x16x32_bf16 v[48:51], v[220:223], a[32:35], v[48:51]\n"
"v_mfma_f32_16x16x32_bf16 v[52:55], v[220:223], a[36:39], v[52:55]\n"
"ds_read_b128 a[48:51], v197 offset:16896\n"
"ds_read_b128 a[52:55], v197 offset:18944\n"
"v_mfma_f32_16x16x32_bf16 v[56:59], v[220:223], a[40:43], v[56:59]\n"
"ds_read_b128 a[56:59], v197 offset:20992\n"
"v_mfma_f32_16x16x32_bf16 v[60:63], v[220:223], a[44:47], v[60:63]\n"
"s_waitcnt lgkmcnt(3)\n"
"v_mfma_f32_16x16x32_bf16 v[64:67], v[224:227], a[32:35], v[64:67]\n"
"ds_read_b128 a[60:63], v197 offset:23040\n"
"v_mfma_f32_16x16x32_bf16 v[68:71], v[224:227], a[36:39], v[68:71]\n"
"v_mfma_f32_16x16x32_bf16 v[72:75], v[224:227], a[40:43], v[72:75]\n"
"v_mfma_f32_16x16x32_bf16 v[76:79], v[224:227], a[44:47], v[76:79]\n"
"v_mfma_f32_16x16x32_bf16 v[96:99], v[228:231], a[32:35], v[96:99]\n"
"v_mfma_f32_16x16x32_bf16 v[100:103], v[228:231], a[36:39], v[100:103]\n"
"v_mfma_f32_16x16x32_bf16 v[104:107], v[228:231], a[40:43], v[104:107]\n"
"s_waitcnt vmcnt(8)\n"
"s_barrier\n"
"ds_read_b64_tr_b16 v[208:209], v244 offset:33792\n"
"ds_read_b64_tr_b16 v[210:211], v244 offset:34048\n"
"v_mfma_f32_16x16x32_bf16 v[108:111], v[228:231], a[44:47], v[108:111]\n"
"v_mfma_f32_16x16x32_bf16 v[80:83], v[232:235], a[32:35], v[80:83]\n"
"v_mfma_f32_16x16x32_bf16 v[84:87], v[232:235], a[36:39], v[84:87]\n"
"ds_read_b64_tr_b16 v[212:213], v244 offset:33800\n"
"ds_read_b64_tr_b16 v[214:215], v244 offset:34056\n"
"ds_read_b64_tr_b16 v[216:217], v244 offset:34304\n"
"ds_read_b64_tr_b16 v[218:219], v244 offset:34560\n"
"ds_read_b64_tr_b16 v[220:221], v244 offset:34312\n"
"ds_read_b64_tr_b16 v[222:223], v244 offset:34568\n"
"v_mfma_f32_16x16x32_bf16 v[88:91], v[232:235], a[40:43], v[88:91]\n"
"v_mfma_f32_16x16x32_bf16 v[92:95], v[232:235], a[44:47], v[92:95]\n"
"v_mfma_f32_16x16x32_bf16 v[112:115], v[236:239], a[32:35], v[112:115]\n"
"v_mfma_f32_16x16x32_bf16 v[116:119], v[236:239], a[36:39], v[116:119]\n"
"v_mfma_f32_16x16x32_bf16 v[120:123], v[236:239], a[40:43], v[120:123]\n"
"v_mfma_f32_16x16x32_bf16 v[124:127], v[236:239], a[44:47], v[124:127]\n"
"s_waitcnt lgkmcnt(0)\n"
"v_mfma_f32_16x16x32_bf16 v[0:3], v[208:211], a[48:51], v[0:3]\n"
"v_mfma_f32_16x16x32_bf16 v[4:7], v[208:211], a[52:55], v[4:7]\n"
"v_mfma_f32_16x16x32_bf16 v[8:11], v[208:211], a[56:59], v[8:11]\n"
"v_mfma_f32_16x16x32_bf16 v[12:15], v[208:211], a[60:63], v[12:15]\n"
"v_mfma_f32_16x16x32_bf16 v[32:35], v[212:215], a[48:51], v[32:35]\n"
"v_mfma_f32_16x16x32_bf16 v[36:39], v[212:215], a[52:55], v[36:39]\n"
"s_barrier\n"
"ds_read_b64_tr_b16 v[224:225], v244 offset:42240\n"
"ds_read_b64_tr_b16 v[226:227], v244 offset:42496\n"
"v_mfma_f32_16x16x32_bf16 v[40:43], v[212:215], a[56:59], v[40:43]\n"
"s_add_u32 m0, s96, 0xa600\n"
"s_lshr_b32 s75, s86, 12\n"
"v_mfma_f32_16x16x32_bf16 v[44:47], v[212:215], a[60:63], v[44:47]\n"
"buffer_load_dwordx4 v192, s[40:43], s75 offen nt lds\n"
"v_mfma_f32_16x16x32_bf16 v[16:19], v[216:219], a[48:51], v[16:19]\n"
"ds_read_b64_tr_b16 v[228:229], v244 offset:42248\n"
"ds_read_b64_tr_b16 v[230:231], v244 offset:42504\n"
"v_mfma_f32_16x16x32_bf16 v[20:23], v[216:219], a[52:55], v[20:23]\n"
"ds_read_b64_tr_b16 v[232:233], v244 offset:42752\n"
"ds_read_b64_tr_b16 v[234:235], v244 offset:43008\n"
"ds_read_b64_tr_b16 v[236:237], v244 offset:42760\n"
"ds_read_b64_tr_b16 v[238:239], v244 offset:43016\n"
"s_add_u32 m0, s96, 0xaa00\n"
"v_mfma_f32_16x16x32_bf16 v[24:27], v[216:219], a[56:59], v[24:27]\n"
"buffer_load_dwordx4 v193, s[40:43], s75 offen nt lds\n"
"v_mfma_f32_16x16x32_bf16 v[28:31], v[216:219], a[60:63], v[28:31]\n"
"v_mfma_f32_16x16x32_bf16 v[48:51], v[220:223], a[48:51], v[48:51]\n"
"ds_read_b128 a[64:67], v196 offset:33792\n"
"s_add_u32 m0, s96, 0xae00\n"
"v_mfma_f32_16x16x32_bf16 v[52:55], v[220:223], a[52:55], v[52:55]\n"
"buffer_load_dwordx4 v194, s[40:43], s75 offen nt lds\n"
"v_mfma_f32_16x16x32_bf16 v[56:59], v[220:223], a[56:59], v[56:59]\n"
"ds_read_b128 a[68:71], v196 offset:35840\n"
"ds_read_b128 a[72:75], v196 offset:37888\n"
"v_mfma_f32_16x16x32_bf16 v[60:63], v[220:223], a[60:63], v[60:63]\n"
"s_add_u32 m0, s96, 0xb200\n"
"s_waitcnt lgkmcnt(3)\n"
"v_mfma_f32_16x16x32_bf16 v[64:67], v[224:227], a[48:51], v[64:67]\n"
"buffer_load_dwordx4 v195, s[40:43], s75 offen nt lds\n"
"v_mfma_f32_16x16x32_bf16 v[68:71], v[224:227], a[52:55], v[68:71]\n"
"ds_read_b128 a[76:79], v196 offset:39936\n"
"v_mfma_f32_16x16x32_bf16 v[72:75], v[224:227], a[56:59], v[72:75]\n"
"s_add_u32 m0, s96, 0xc600\n"
"s_add_u32 s75, s75, 0xe000\n"
"v_mfma_f32_16x16x32_bf16 v[76:79], v[224:227], a[60:63], v[76:79]\n"
"buffer_load_dwordx4 v192, s[40:43], s75 offen nt lds\n"
"v_mfma_f32_16x16x32_bf16 v[96:99], v[228:231], a[48:51], v[96:99]\n"
"s_barrier\n"
"ds_read_b64_tr_b16 v[208:209], v244 offset:50688\n"
"ds_read_b64_tr_b16 v[210:211], v244 offset:50944\n"
"v_mfma_f32_16x16x32_bf16 v[100:103], v[228:231], a[52:55], v[100:103]\n"
"s_add_u32 m0, s96, 0xca00\n"
"v_mfma_f32_16x16x32_bf16 v[104:107], v[228:231], a[56:59], v[104:107]\n"
"buffer_load_dwordx4 v193, s[40:43], s75 offen nt lds\n"
"v_mfma_f32_16x16x32_bf16 v[108:111], v[228:231], a[60:63], v[108:111]\n"
"ds_read_b64_tr_b16 v[212:213], v244 offset:50696\n"
"ds_read_b64_tr_b16 v[214:215], v244 offset:50952\n"
"v_mfma_f32_16x16x32_bf16 v[80:83], v[232:235], a[48:51], v[80:83]\n"
"s_add_u32 m0, s96, 0xce00\n"
"v_mfma_f32_16x16x32_bf16 v[84:87], v[232:235], a[52:55], v[84:87]\n"
"buffer_load_dwordx4 v194, s[40:43], s75 offen nt lds\n"
"ds_read_b64_tr_b16 v[216:217], v244 offset:51200\n"
"ds_read_b64_tr_b16 v[218:219], v244 offset:51456\n"
"ds_read_b64_tr_b16 v[220:221], v244 offset:51208\n"
"ds_read_b64_tr_b16 v[222:223], v244 offset:51464\n"
"v_mfma_f32_16x16x32_bf16 v[88:91], v[232:235], a[56:59], v[88:91]\n"
"v_mfma_f32_16x16x32_bf16 v[92:95], v[232:235], a[60:63], v[92:95]\n"
"s_add_u32 m0, s96, 0xd200\n"
"v_mfma_f32_16x16x32_bf16 v[112:115], v[236:239], a[48:51], v[112:115]\n"
"buffer_load_dwordx4 v195, s[40:43], s75 offen nt lds\n"
"v_mfma_f32_16x16x32_bf16 v[116:119], v[236:239], a[52:55], v[116:119]\n"
"v_mfma_f32_16x16x32_bf16 v[120:123], v[236:239], a[56:59], v[120:123]\n"
"v_mfma_f32_16x16x32_bf16 v[124:127], v[236:239], a[60:63], v[124:127]\n"
"s_waitcnt lgkmcnt(0)\n"
"v_mfma_f32_16x16x32_bf16 v[0:3], v[208:211], a[64:67], v[0:3]\n"
"v_mfma_f32_16x16x32_bf16 v[4:7], v[208:211], a[68:71], v[4:7]\n"
"v_mfma_f32_16x16x32_bf16 v[8:11], v[208:211], a[72:75], v[8:11]\n"
"v_mfma_f32_16x16x32_bf16 v[12:15], v[208:211], a[76:79], v[12:15]\n"
"v_mfma_f32_16x16x32_bf16 v[32:35], v[212:215], a[64:67], v[32:35]\n"
"v_mfma_f32_16x16x32_bf16 v[36:39], v[212:215], a[68:71], v[36:39]\n"
"v_mfma_f32_16x16x32_bf16 v[40:43], v[212:215], a[72:75], v[40:43]\n"
"s_waitcnt vmcnt(8)\n"
"s_barrier\n"
"ds_read_b64_tr_b16 v[224:225], v244 offset:59136\n"
"ds_read_b64_tr_b16 v[226:227], v244 offset:59392\n"
"v_mfma_f32_16x16x32_bf16 v[44:47], v[212:215], a[76:79], v[44:47]\n"
"v_mfma_f32_16x16x32_bf16 v[16:19], v[216:219], a[64:67], v[16:19]\n"
"v_mfma_f32_16x16x32_bf16 v[20:23], v[216:219], a[68:71], v[20:23]\n"
"ds_read_b64_tr_b16 v[228:229], v244 offset:59144\n"
"ds_read_b64_tr_b16 v[230:231], v244 offset:59400\n"
"ds_read_b64_tr_b16 v[232:233], v244 offset:59648\n"
"ds_read_b64_tr_b16 v[234:235], v244 offset:59904\n"
"ds_read_b64_tr_b16 v[236:237], v244 offset:59656\n"
"ds_read_b64_tr_b16 v[238:239], v244 offset:59912\n"
"v_mfma_f32_16x16x32_bf16 v[24:27], v[216:219], a[72:75], v[24:27]\n"
"v_mfma_f32_16x16x32_bf16 v[28:31], v[216:219], a[76:79], v[28:31]\n"
"v_mfma_f32_16x16x32_bf16 v[48:51], v[220:223], a[64:67], v[48:51]\n"
"ds_read_b128 a[80:83], v197 offset:33792\n"
"ds_read_b128 a[84:87], v197 offset:35840\n"
"v_mfma_f32_16x16x32_bf16 v[52:55], v[220:223], a[68:71], v[52:55]\n"
"ds_read_b128 a[88:91], v197 offset:37888\n"
"v_mfma_f32_16x16x32_bf16 v[56:59], v[220:223], a[72:75], v[56:59]\n"
"v_mfma_f32_16x16x32_bf16 v[60:63], v[220:223], a[76:79], v[60:63]\n"
"ds_read_b128 a[92:95], v197 offset:39936\n"
"s_waitcnt lgkmcnt(4)\n"
"v_mfma_f32_16x16x32_bf16 v[64:67], v[224:227], a[64:67], v[64:67]\n"
"v_mfma_f32_16x16x32_bf16 v[68:71], v[224:227], a[68:71], v[68:71]\n"
"v_mfma_f32_16x16x32_bf16 v[72:75], v[224:227], a[72:75], v[72:75]\n"
"v_mfma_f32_16x16x32_bf16 v[76:79], v[224:227], a[76:79], v[76:79]\n"
"v_mfma_f32_16x16x32_bf16 v[96:99], v[228:231], a[64:67], v[96:99]\n"
"v_mfma_f32_16x16x32_bf16 v[100:103], v[228:231], a[68:71], v[100:103]\n"
"s_barrier\n"
"ds_read_b64_tr_b16 v[208:209], v245 offset:0\n"
"ds_read_b64_tr_b16 v[210:211], v245 offset:256\n"
"v_mfma_f32_16x16x32_bf16 v[104:107], v[228:231], a[72:75], v[104:107]\n"
"v_mfma_f32_16x16x32_bf16 v[108:111], v[228:231], a[76:79], v[108:111]\n"
"v_mfma_f32_16x16x32_bf16 v[80:83], v[232:235], a[64:67], v[80:83]\n"
"ds_read_b64_tr_b16 v[212:213], v245 offset:8\n"
"ds_read_b64_tr_b16 v[214:215], v245 offset:264\n"
"v_mfma_f32_16x16x32_bf16 v[84:87], v[232:235], a[68:71], v[84:87]\n"
"ds_read_b64_tr_b16 v[216:217], v245 offset:512\n"
"ds_read_b64_tr_b16 v[218:219], v245 offset:768\n"
"ds_read_b64_tr_b16 v[220:221], v245 offset:520\n"
"ds_read_b64_tr_b16 v[222:223], v245 offset:776\n"
"v_mfma_f32_16x16x32_bf16 v[88:91], v[232:235], a[72:75], v[88:91]\n"
"v_mfma_f32_16x16x32_bf16 v[92:95], v[232:235], a[76:79], v[92:95]\n"
"v_mfma_f32_16x16x32_bf16 v[112:115], v[236:239], a[64:67], v[112:115]\n"
"v_mfma_f32_16x16x32_bf16 v[116:119], v[236:239], a[68:71], v[116:119]\n"
"v_mfma_f32_16x16x32_bf16 v[120:123], v[236:239], a[72:75], v[120:123]\n"
"v_mfma_f32_16x16x32_bf16 v[124:127], v[236:239], a[76:79], v[124:127]\n"
"s_waitcnt lgkmcnt(0)\n"
"v_mfma_f32_16x16x32_bf16 v[0:3], v[208:211], a[80:83], v[0:3]\n"
"v_mfma_f32_16x16x32_bf16 v[4:7], v[208:211], a[84:87], v[4:7]\n"
"v_mfma_f32_16x16x32_bf16 v[8:11], v[208:211], a[88:91], v[8:11]\n"
"v_mfma_f32_16x16x32_bf16 v[12:15], v[208:211], a[92:95], v[12:15]\n"
"v_mfma_f32_16x16x32_bf16 v[32:35], v[212:215], a[80:83], v[32:35]\n"
"s_barrier\n"
"ds_read_b64_tr_b16 v[224:225], v245 offset:16896\n"
"ds_read_b64_tr_b16 v[226:227], v245 offset:17152\n"
"v_mfma_f32_16x16x32_bf16 v[36:39], v[212:215], a[84:87], v[36:39]\n"
"s_add_u32 m0, s96, 0xe800\n"
"s_lshr_b32 s75, s87, 12\n"
"v_mfma_f32_16x16x32_bf16 v[40:43], v[212:215], a[88:91], v[40:43]\n"
"buffer_load_dwordx4 v192, s[40:43], s75 offen nt lds\n"
"v_mfma_f32_16x16x32_bf16 v[44:47], v[212:215], a[92:95], v[44:47]\n"
"ds_read_b64_tr_b16 v[228:229], v245 offset:16904\n"
"ds_read_b64_tr_b16 v[230:231], v245 offset:17160\n"
"v_mfma_f32_16x16x32_bf16 v[16:19], v[216:219], a[80:83], v[16:19]\n"
"s_add_u32 m0, s96, 0xec00\n"
"v_mfma_f32_16x16x32_bf16 v[20:23], v[216:219], a[84:87], v[20:23]\n"
"buffer_load_dwordx4 v193, s[40:43], s75 offen nt lds\n"
"ds_read_b64_tr_b16 v[232:233], v245 offset:17408\n"
"ds_read_b64_tr_b16 v[234:235], v245 offset:17664\n"
"ds_read_b64_tr_b16 v[236:237], v245 offset:17416\n"
"ds_read_b64_tr_b16 v[238:239], v245 offset:17672\n"
"v_mfma_f32_16x16x32_bf16 v[24:27], v[216:219], a[88:91], v[24:27]\n"
"v_mfma_f32_16x16x32_bf16 v[28:31], v[216:219], a[92:95], v[28:31]\n"
"s_add_u32 m0, s96, 0xf000\n"
"v_mfma_f32_16x16x32_bf16 v[48:51], v[220:223], a[80:83], v[48:51]\n"
"buffer_load_dwordx4 v194, s[40:43], s75 offen nt lds\n"
"v_mfma_f32_16x16x32_bf16 v[52:55], v[220:223], a[84:87], v[52:55]\n"
"ds_read_b128 a[96:99], v196 offset:0\n"
"ds_read_b128 a[100:103], v196 offset:2048\n"
"v_mfma_f32_16x16x32_bf16 v[56:59], v[220:223], a[88:91], v[56:59]\n"
"ds_read_b128 a[104:107], v196 offset:4096\n"
"s_add_u32 m0, s96, 0xf400\n"
"v_mfma_f32_16x16x32_bf16 v[60:63], v[220:223], a[92:95], v[60:63]\n"
"buffer_load_dwordx4 v195, s[40:43], s75 offen nt lds\n"
"s_waitcnt lgkmcnt(3)\n"
"v_mfma_f32_16x16x32_bf16 v[64:67], v[224:227], a[80:83], v[64:67]\n"
"ds_read_b128 a[108:111], v196 offset:6144\n"
"v_mfma_f32_16x16x32_bf16 v[68:71], v[224:227], a[84:87], v[68:71]\n"
"s_add_u32 m0, s96, 0x10800\n"
"s_add_u32 s75, s75, 0xe000\n"
"v_mfma_f32_16x16x32_bf16 v[72:75], v[224:227], a[88:91], v[72:75]\n"
"buffer_load_dwordx4 v192, s[40:43], s75 offen nt lds\n"
"v_mfma_f32_16x16x32_bf16 v[76:79], v[224:227], a[92:95], v[76:79]\n"
"v_mfma_f32_16x16x32_bf16 v[96:99], v[228:231], a[80:83], v[96:99]\n"
"s_barrier\n"
"s_add_u32 m0, s96, 0x10c00\n"
"v_mfma_f32_16x16x32_bf16 v[100:103], v[228:231], a[84:87], v[100:103]\n"
"buffer_load_dwordx4 v193, s[40:43], s75 offen nt lds\n"
"v_mfma_f32_16x16x32_bf16 v[104:107], v[228:231], a[88:91], v[104:107]\n"
"ds_read_b64_tr_b16 v[208:209], v245 offset:8448\n"
"ds_read_b64_tr_b16 v[210:211], v245 offset:8704\n"
"v_mfma_f32_16x16x32_bf16 v[108:111], v[228:231], a[92:95], v[108:111]\n"
"s_add_u32 m0, s96, 0x11000\n"
"v_mfma_f32_16x16x32_bf16 v[80:83], v[232:235], a[80:83], v[80:83]\n"
"buffer_load_dwordx4 v194, s[40:43], s75 offen nt lds\n"
"v_mfma_f32_16x16x32_bf16 v[84:87], v[232:235], a[84:87], v[84:87]\n"
"ds_read_b64_tr_b16 v[212:213], v245 offset:8456\n"
"ds_read_b64_tr_b16 v[214:215], v245 offset:8712\n"
"ds_read_b64_tr_b16 v[216:217], v245 offset:8960\n"
"ds_read_b64_tr_b16 v[218:219], v245 offset:9216\n"
"ds_read_b64_tr_b16 v[220:221], v245 offset:8968\n"
"ds_read_b64_tr_b16 v[222:223], v245 offset:9224\n"
"v_mfma_f32_16x16x32_bf16 v[88:91], v[232:235], a[88:91], v[88:91]\n"
"s_add_u32 m0, s96, 0x11400\n"
"v_mfma_f32_16x16x32_bf16 v[92:95], v[232:235], a[92:95], v[92:95]\n"
"buffer_load_dwordx4 v195, s[40:43], s75 offen nt lds\n"
"v_mfma_f32_16x16x32_bf16 v[112:115], v[236:239], a[80:83], v[112:115]\n"
"v_mfma_f32_16x16x32_bf16 v[116:119], v[236:239], a[84:87], v[116:119]\n"
"v_mfma_f32_16x16x32_bf16 v[120:123], v[236:239], a[88:91], v[120:123]\n"
"v_mfma_f32_16x16x32_bf16 v[124:127], v[236:239], a[92:95], v[124:127]\n"
"s_waitcnt lgkmcnt(0)\n"
"v_mfma_f32_16x16x32_bf16 v[0:3], v[208:211], a[96:99], v[0:3]\n"
"s_waitcnt vmcnt(8)\n"
"s_barrier\n"
"v_mfma_f32_16x16x32_bf16 v[4:7], v[208:211], a[100:103], v[4:7]\n"
"v_mfma_f32_16x16x32_bf16 v[8:11], v[208:211], a[104:107], v[8:11]\n"
"ds_read_b64_tr_b16 v[224:225], v244 offset:33792\n"
"ds_read_b64_tr_b16 v[226:227], v244 offset:34048\n"
"v_mfma_f32_16x16x32_bf16 v[12:15], v[208:211], a[108:111], v[12:15]\n"
"v_mfma_f32_16x16x32_bf16 v[32:35], v[212:215], a[96:99], v[32:35]\n"
"v_mfma_f32_16x16x32_bf16 v[36:39], v[212:215], a[100:103], v[36:39]\n"
"ds_read_b64_tr_b16 v[228:229], v244 offset:33800\n"
"ds_read_b64_tr_b16 v[230:231], v244 offset:34056\n"
"v_mfma_f32_16x16x32_bf16 v[40:43], v[212:215], a[104:107], v[40:43]\n"
"v_mfma_f32_16x16x32_bf16 v[44:47], v[212:215], a[108:111], v[44:47]\n"
"v_mfma_f32_16x16x32_bf16 v[16:19], v[216:219], a[96:99], v[16:19]\n"
"ds_read_b64_tr_b16 v[232:233], v244 offset:34304\n"
"ds_read_b64_tr_b16 v[234:235], v244 offset:34560\n"
"v_mfma_f32_16x16x32_bf16 v[20:23], v[216:219], a[100:103], v[20:23]\n"
"ds_read_b64_tr_b16 v[236:237], v244 offset:34312\n"
"ds_read_b64_tr_b16 v[238:239], v244 offset:34568\n"
"v_mfma_f32_16x16x32_bf16 v[24:27], v[216:219], a[104:107], v[24:27]\n"
"v_mfma_f32_16x16x32_bf16 v[28:31], v[216:219], a[108:111], v[28:31]\n"
"v_mfma_f32_16x16x32_bf16 v[48:51], v[220:223], a[96:99], v[48:51]\n"
"ds_read_b128 a[112:115], v197 offset:0\n"
"ds_read_b128 a[116:119], v197 offset:2048\n"
"v_mfma_f32_16x16x32_bf16 v[52:55], v[220:223], a[100:103], v[52:55]\n"
"ds_read_b128 a[120:123], v197 offset:4096\n"
"ds_read_b128 a[124:127], v197 offset:6144\n"
"v_mfma_f32_16x16x32_bf16 v[56:59], v[220:223], a[104:107], v[56:59]\n"
"v_mfma_f32_16x16x32_bf16 v[60:63], v[220:223], a[108:111], v[60:63]\n"
"s_waitcnt lgkmcnt(4)\n"
"v_mfma_f32_16x16x32_bf16 v[64:67], v[224:227], a[96:99], v[64:67]\n"
"v_mfma_f32_16x16x32_bf16 v[68:71], v[224:227], a[100:103], v[68:71]\n"
"v_mfma_f32_16x16x32_bf16 v[72:75], v[224:227], a[104:107], v[72:75]\n"
"s_barrier\n"
"v_mfma_f32_16x16x32_bf16 v[76:79], v[224:227], a[108:111], v[76:79]\n"
"v_mfma_f32_16x16x32_bf16 v[96:99], v[228:231], a[96:99], v[96:99]\n"
"ds_read_b64_tr_b16 v[208:209], v244 offset:42240\n"
"ds_read_b64_tr_b16 v[210:211], v244 offset:42496\n"
"v_mfma_f32_16x16x32_bf16 v[100:103], v[228:231], a[100:103], v[100:103]\n"
"v_mfma_f32_16x16x32_bf16 v[104:107], v[228:231], a[104:107], v[104:107]\n"
"v_mfma_f32_16x16x32_bf16 v[108:111], v[228:231], a[108:111], v[108:111]\n"
"ds_read_b64_tr_b16 v[212:213], v244 offset:42248\n"
"ds_read_b64_tr_b16 v[214:215], v244 offset:42504\n"
"v_mfma_f32_16x16x32_bf16 v[80:83], v[232:235], a[96:99], v[80:83]\n"
"v_mfma_f32_16x16x32_bf16 v[84:87], v[232:235], a[100:103], v[84:87]\n"
"ds_read_b64_tr_b16 v[216:217], v244 offset:42752\n"
"ds_read_b64_tr_b16 v[218:219], v244 offset:43008\n"
"ds_read_b64_tr_b16 v[220:221], v244 offset:42760\n"
"ds_read_b64_tr_b16 v[222:223], v244 offset:43016\n"
"v_mfma_f32_16x16x32_bf16 v[88:91], v[232:235], a[104:107], v[88:91]\n"
"v_mfma_f32_16x16x32_bf16 v[92:95], v[232:235], a[108:111], v[92:95]\n"
"v_mfma_f32_16x16x32_bf16 v[112:115], v[236:239], a[96:99], v[112:115]\n"
"v_mfma_f32_16x16x32_bf16 v[116:119], v[236:239], a[100:103], v[116:119]\n"
"ds_read_b64_tr_b16 v[224:225], v198 offset:8448\n"
"ds_read_b64_tr_b16 v[226:227], v198 offset:8704\n"
"v_mfma_f32_16x16x32_bf16 v[120:123], v[236:239], a[104:107], v[120:123]\n"
"v_mfma_f32_16x16x32_bf16 v[124:127], v[236:239], a[108:111], v[124:127]\n"
"s_waitcnt lgkmcnt(2)\n"
"v_mfma_f32_16x16x32_bf16 v[0:3], v[208:211], a[112:115], v[0:3]\n"
"ds_read_b64_tr_b16 v[228:229], v198 offset:8456\n"
"ds_read_b64_tr_b16 v[230:231], v198 offset:8712\n"
"v_mfma_f32_16x16x32_bf16 v[4:7], v[208:211], a[116:119], v[4:7]\n"
"s_barrier\n"
"s_add_u32 m0, s96, 0x6400\n"
"s_lshr_b32 s75, s88, 12\n"
"v_mfma_f32_16x16x32_bf16 v[8:11], v[208:211], a[120:123], v[8:11]\n"
"buffer_load_dwordx4 v192, s[40:43], s75 offen nt lds\n"
"v_mfma_f32_16x16x32_bf16 v[12:15], v[208:211], a[124:127], v[12:15]\n"
"ds_read_b64_tr_b16 v[232:233], v198 offset:8960\n"
"ds_read_b64_tr_b16 v[234:235], v198 offset:9216\n"
"v_mfma_f32_16x16x32_bf16 v[32:35], v[212:215], a[112:115], v[32:35]\n"
"s_add_u32 m0, s96, 0x6800\n"
"v_mfma_f32_16x16x32_bf16 v[36:39], v[212:215], a[116:119], v[36:39]\n"
"buffer_load_dwordx4 v193, s[40:43], s75 offen nt lds\n"
"v_mfma_f32_16x16x32_bf16 v[40:43], v[212:215], a[120:123], v[40:43]\n"
"ds_read_b64_tr_b16 v[236:237], v198 offset:8968\n"
"ds_read_b64_tr_b16 v[238:239], v198 offset:9224\n"
"v_mfma_f32_16x16x32_bf16 v[44:47], v[212:215], a[124:127], v[44:47]\n"
"s_add_u32 m0, s96, 0x6c00\n"
"v_mfma_f32_16x16x32_bf16 v[16:19], v[216:219], a[112:115], v[16:19]\n"
"buffer_load_dwordx4 v194, s[40:43], s75 offen nt lds\n"
"v_mfma_f32_16x16x32_bf16 v[20:23], v[216:219], a[116:119], v[20:23]\n"
"v_mfma_f32_16x16x32_bf16 v[24:27], v[216:219], a[120:123], v[24:27]\n"
"s_add_u32 m0, s96, 0x7000\n"
"v_mfma_f32_16x16x32_bf16 v[28:31], v[216:219], a[124:127], v[28:31]\n"
"buffer_load_dwordx4 v195, s[40:43], s75 offen nt lds\n"
"v_mfma_f32_16x16x32_bf16 v[48:51], v[220:223], a[112:115], v[48:51]\n"
"ds_read_b128 a[128:131], v196 offset:16896\n"
"ds_read_b128 a[132:135], v196 offset:18944\n"
"v_mfma_f32_16x16x32_bf16 v[52:55], v[220:223], a[116:119], v[52:55]\n"
"s_add_u32 m0, s96, 0x8400\n"
"s_add_u32 s75, s75, 0xe000\n"
"v_mfma_f32_16x16x32_bf16 v[56:59], v[220:223], a[120:123], v[56:59]\n"
"buffer_load_dwordx4 v192, s[40:43], s75 offen nt lds\n"
"v_mfma_f32_16x16x32_bf16 v[60:63], v[220:223], a[124:127], v[60:63]\n"
"ds_read_b128 a[136:139], v196 offset:20992\n"
"s_waitcnt lgkmcnt(3)\n"
"v_mfma_f32_16x16x32_bf16 v[64:67], v[224:227], a[112:115], v[64:67]\n"
"ds_read_b128 a[140:143], v196 offset:23040\n"
"s_add_u32 m0, s96, 0x8800\n"
"v_mfma_f32_16x16x32_bf16 v[68:71], v[224:227], a[116:119], v[68:71]\n"
"buffer_load_dwordx4 v193, s[40:43], s75 offen nt lds\n"
"v_mfma_f32_16x16x32_bf16 v[72:75], v[224:227], a[120:123], v[72:75]\n"
"ds_read_b64_tr_b16 v[208:209], v244 offset:50688\n"
"ds_read_b64_tr_b16 v[210:211], v244 offset:50944\n"
"v_mfma_f32_16x16x32_bf16 v[76:79], v[224:227], a[124:127], v[76:79]\n"
"s_add_u32 m0, s96, 0x8c00\n"
"v_mfma_f32_16x16x32_bf16 v[96:99], v[228:231], a[112:115], v[96:99]\n"
"buffer_load_dwordx4 v194, s[40:43], s75 offen nt lds\n"
"v_mfma_f32_16x16x32_bf16 v[100:103], v[228:231], a[116:119], v[100:103]\n"
"ds_read_b64_tr_b16 v[212:213], v244 offset:50696\n"
"ds_read_b64_tr_b16 v[214:215], v244 offset:50952\n"
"v_mfma_f32_16x16x32_bf16 v[104:107], v[228:231], a[120:123], v[104:107]\n"
"s_add_u32 m0, s96, 0x9000\n"
"v_mfma_f32_16x16x32_bf16 v[108:111], v[228:231], a[124:127], v[108:111]\n"
"buffer_load_dwordx4 v195, s[40:43], s75 offen nt lds\n"
"v_mfma_f32_16x16x32_bf16 v[80:83], v[232:235], a[112:115], v[80:83]\n"
"ds_read_b64_tr_b16 v[216:217], v244 offset:51200\n"
"ds_read_b64_tr_b16 v[218:219], v244 offset:51456\n"
"v_mfma_f32_16x16x32_bf16 v[84:87], v[232:235], a[116:119], v[84:87]\n"
"ds_read_b64_tr_b16 v[220:221], v244 offset:51208\n"
"ds_read_b64_tr_b16 v[222:223], v244 offset:51464\n"
"v_mfma_f32_16x16x32_bf16 v[88:91], v[232:235], a[120:123], v[88:91]\n"
"v_mfma_f32_16x16x32_bf16 v[92:95], v[232:235], a[124:127], v[92:95]\n"
"v_mfma_f32_16x16x32_bf16 v[112:115], v[236:239], a[112:115], v[112:115]\n"
"v_mfma_f32_16x16x32_bf16 v[116:119], v[236:239], a[116:119], v[116:119]\n"
"v_mfma_f32_16x16x32_bf16 v[120:123], v[236:239], a[120:123], v[120:123]\n"
"v_mfma_f32_16x16x32_bf16 v[124:127], v[236:239], a[124:127], v[124:127]\n"
"s_waitcnt lgkmcnt(0)\n"
"v_mfma_f32_16x16x32_bf16 v[0:3], v[208:211], a[128:131], v[0:3]\n"
"v_mfma_f32_16x16x32_bf16 v[4:7], v[208:211], a[132:135], v[4:7]\n"
"v_mfma_f32_16x16x32_bf16 v[8:11], v[208:211], a[136:139], v[8:11]\n"
"s_waitcnt vmcnt(8)\n"
"s_barrier\n"
"s_add_u32 m0, s73, 0xffffdf00\n"
"s_mov_b32 s75, 0x0\n"
"v_mfma_f32_16x16x32_bf16 v[12:15], v[208:211], a[140:143], v[12:15]\n"
"buffer_load_dwordx4 v199, s[48:51], s75 offen offset:0 nt lds\n"
"v_mfma_f32_16x16x32_bf16 v[32:35], v[212:215], a[128:131], v[32:35]\n"
"ds_read_b64_tr_b16 v[224:225], v244 offset:59136\n"
"ds_read_b64_tr_b16 v[226:227], v244 offset:59392\n"
"v_mfma_f32_16x16x32_bf16 v[36:39], v[212:215], a[132:135], v[36:39]\n"
"v_mfma_f32_16x16x32_bf16 v[40:43], v[212:215], a[136:139], v[40:43]\n"
"v_mfma_f32_16x16x32_bf16 v[44:47], v[212:215], a[140:143], v[44:47]\n"
"ds_read_b64_tr_b16 v[228:229], v244 offset:59144\n"
"ds_read_b64_tr_b16 v[230:231], v244 offset:59400\n"
"v_mfma_f32_16x16x32_bf16 v[16:19], v[216:219], a[128:131], v[16:19]\n"
"v_mfma_f32_16x16x32_bf16 v[20:23], v[216:219], a[132:135], v[20:23]\n"
"ds_read_b64_tr_b16 v[232:233], v244 offset:59648\n"
"ds_read_b64_tr_b16 v[234:235], v244 offset:59904\n"
"ds_read_b64_tr_b16 v[236:237], v244 offset:59656\n"
"ds_read_b64_tr_b16 v[238:239], v244 offset:59912\n"
"v_mfma_f32_16x16x32_bf16 v[24:27], v[216:219], a[136:139], v[24:27]\n"
"v_mfma_f32_16x16x32_bf16 v[28:31], v[216:219], a[140:143], v[28:31]\n"
"v_mfma_f32_16x16x32_bf16 v[48:51], v[220:223], a[128:131], v[48:51]\n"
"v_mfma_f32_16x16x32_bf16 v[52:55], v[220:223], a[132:135], v[52:55]\n"
"ds_read_b128 a[144:147], v197 offset:16896\n"
"ds_read_b128 a[148:151], v197 offset:18944\n"
"v_mfma_f32_16x16x32_bf16 v[56:59], v[220:223], a[136:139], v[56:59]\n"
"ds_read_b128 a[152:155], v197 offset:20992\n"
"s_add_u32 m0, s73, 0x0\n"
"v_mfma_f32_16x16x32_bf16 v[60:63], v[220:223], a[140:143], v[60:63]\n"
"buffer_load_dwordx4 v242, s[48:51], s75 offen offset:0 nt lds\n"
"s_waitcnt lgkmcnt(3)\n"
"v_mfma_f32_16x16x32_bf16 v[64:67], v[224:227], a[128:131], v[64:67]\n"
"ds_read_b128 a[156:159], v197 offset:23040\n"
"v_mfma_f32_16x16x32_bf16 v[68:71], v[224:227], a[132:135], v[68:71]\n"
"s_barrier\n"
"v_mfma_f32_16x16x32_bf16 v[72:75], v[224:227], a[136:139], v[72:75]\n"
"v_mfma_f32_16x16x32_bf16 v[76:79], v[224:227], a[140:143], v[76:79]\n"
"ds_read_b64_tr_b16 v[208:209], v245 offset:0\n"
"ds_read_b64_tr_b16 v[210:211], v245 offset:256\n"
"v_mfma_f32_16x16x32_bf16 v[96:99], v[228:231], a[128:131], v[96:99]\n"
"v_mfma_f32_16x16x32_bf16 v[100:103], v[228:231], a[132:135], v[100:103]\n"
"v_mfma_f32_16x16x32_bf16 v[104:107], v[228:231], a[136:139], v[104:107]\n"
"ds_read_b64_tr_b16 v[212:213], v245 offset:8\n"
"ds_read_b64_tr_b16 v[214:215], v245 offset:264\n"
"v_mfma_f32_16x16x32_bf16 v[108:111], v[228:231], a[140:143], v[108:111]\n"
"v_mfma_f32_16x16x32_bf16 v[80:83], v[232:235], a[128:131], v[80:83]\n"
"v_mfma_f32_16x16x32_bf16 v[84:87], v[232:235], a[132:135], v[84:87]\n"
"ds_read_b64_tr_b16 v[216:217], v245 offset:512\n"
"ds_read_b64_tr_b16 v[218:219], v245 offset:768\n"
"ds_read_b64_tr_b16 v[220:221], v245 offset:520\n"
"ds_read_b64_tr_b16 v[222:223], v245 offset:776\n"
"v_mfma_f32_16x16x32_bf16 v[88:91], v[232:235], a[136:139], v[88:91]\n"
"s_add_u32 m0, s73, 0xffffe120\n"
"v_mfma_f32_16x16x32_bf16 v[92:95], v[232:235], a[140:143], v[92:95]\n"
"buffer_load_dwordx4 v199, s[48:51], s75 offen offset:512 nt lds\n"
"v_mfma_f32_16x16x32_bf16 v[112:115], v[236:239], a[128:131], v[112:115]\n"
"ds_read_b64_tr_b16 v[224:225], v245 offset:16896\n"
"ds_read_b64_tr_b16 v[226:227], v245 offset:17152\n"
"v_mfma_f32_16x16x32_bf16 v[116:119], v[236:239], a[132:135], v[116:119]\n"
"v_mfma_f32_16x16x32_bf16 v[120:123], v[236:239], a[136:139], v[120:123]\n"
"v_mfma_f32_16x16x32_bf16 v[124:127], v[236:239], a[140:143], v[124:127]\n"
"s_waitcnt lgkmcnt(2)\n"
"v_mfma_f32_16x16x32_bf16 v[0:3], v[208:211], a[144:147], v[0:3]\n"
"s_barrier\n"
"v_mfma_f32_16x16x32_bf16 v[4:7], v[208:211], a[148:151], v[4:7]\n"
"v_mfma_f32_16x16x32_bf16 v[8:11], v[208:211], a[152:155], v[8:11]\n"
"ds_read_b64_tr_b16 v[228:229], v245 offset:16904\n"
"ds_read_b64_tr_b16 v[230:231], v245 offset:17160\n"
"v_mfma_f32_16x16x32_bf16 v[12:15], v[208:211], a[156:159], v[12:15]\n"
"v_mfma_f32_16x16x32_bf16 v[32:35], v[212:215], a[144:147], v[32:35]\n"
"v_mfma_f32_16x16x32_bf16 v[36:39], v[212:215], a[148:151], v[36:39]\n"
"ds_read_b64_tr_b16 v[232:233], v245 offset:17408\n"
"ds_read_b64_tr_b16 v[234:235], v245 offset:17664\n"
"v_mfma_f32_16x16x32_bf16 v[40:43], v[212:215], a[152:155], v[40:43]\n"
"s_add_u32 m0, s73, 0x220\n"
"v_mfma_f32_16x16x32_bf16 v[44:47], v[212:215], a[156:159], v[44:47]\n"
"buffer_load_dwordx4 v242, s[48:51], s75 offen offset:512 nt lds\n"
"v_mfma_f32_16x16x32_bf16 v[16:19], v[216:219], a[144:147], v[16:19]\n"
"ds_read_b64_tr_b16 v[236:237], v245 offset:17416\n"
"ds_read_b64_tr_b16 v[238:239], v245 offset:17672\n"
"v_mfma_f32_16x16x32_bf16 v[20:23], v[216:219], a[148:151], v[20:23]\n"
"v_mfma_f32_16x16x32_bf16 v[24:27], v[216:219], a[152:155], v[24:27]\n"
"v_mfma_f32_16x16x32_bf16 v[28:31], v[216:219], a[156:159], v[28:31]\n"
"v_mfma_f32_16x16x32_bf16 v[48:51], v[220:223], a[144:147], v[48:51]\n"
"ds_read_b128 a[160:163], v196 offset:33792\n"
"ds_read_b128 a[164:167], v196 offset:35840\n"
"v_mfma_f32_16x16x32_bf16 v[52:55], v[220:223], a[148:151], v[52:55]\n"
"ds_read_b128 a[168:171], v196 offset:37888\n"
"ds_read_b128 a[172:175], v196 offset:39936\n"
"v_mfma_f32_16x16x32_bf16 v[56:59], v[220:223], a[152:155], v[56:59]\n"
"ds_read_b64_tr_b16 v[208:209], v245 offset:8448\n"
"ds_read_b64_tr_b16 v[210:211], v245 offset:8704\n"
"v_mfma_f32_16x16x32_bf16 v[60:63], v[220:223], a[156:159], v[60:63]\n"
"s_waitcnt lgkmcnt(6)\n"
"v_mfma_f32_16x16x32_bf16 v[64:67], v[224:227], a[144:147], v[64:67]\n"
"ds_read_b64_tr_b16 v[212:213], v245 offset:8456\n"
"ds_read_b64_tr_b16 v[214:215], v245 offset:8712\n"
"v_mfma_f32_16x16x32_bf16 v[68:71], v[224:227], a[148:151], v[68:71]\n"
"ds_read_b64_tr_b16 v[216:217], v245 offset:8960\n"
"ds_read_b64_tr_b16 v[218:219], v245 offset:9216\n"
"v_mfma_f32_16x16x32_bf16 v[72:75], v[224:227], a[152:155], v[72:75]\n"
"s_barrier\n"
"s_add_u32 m0, s73, 0xffffe340\n"
"v_mfma_f32_16x16x32_bf16 v[76:79], v[224:227], a[156:159], v[76:79]\n"
"buffer_load_dwordx4 v199, s[48:51], s75 offen offset:1024 nt lds\n"
"v_mfma_f32_16x16x32_bf16 v[96:99], v[228:231], a[144:147], v[96:99]\n"
"ds_read_b64_tr_b16 v[220:221], v245 offset:8968\n"
"ds_read_b64_tr_b16 v[222:223], v245 offset:9224\n"
"v_mfma_f32_16x16x32_bf16 v[100:103], v[228:231], a[148:151], v[100:103]\n"
"v_mfma_f32_16x16x32_bf16 v[104:107], v[228:231], a[152:155], v[104:107]\n"
"v_mfma_f32_16x16x32_bf16 v[108:111], v[228:231], a[156:159], v[108:111]\n"
"v_mfma_f32_16x16x32_bf16 v[80:83], v[232:235], a[144:147], v[80:83]\n"
"v_mfma_f32_16x16x32_bf16 v[84:87], v[232:235], a[148:151], v[84:87]\n"
"v_mfma_f32_16x16x32_bf16 v[88:91], v[232:235], a[152:155], v[88:91]\n"
"v_mfma_f32_16x16x32_bf16 v[92:95], v[232:235], a[156:159], v[92:95]\n"
"v_mfma_f32_16x16x32_bf16 v[112:115], v[236:239], a[144:147], v[112:115]\n"
"v_mfma_f32_16x16x32_bf16 v[116:119], v[236:239], a[148:151], v[116:119]\n"
"ds_read_b64_tr_b16 v[224:225], v244 offset:33792\n"
"ds_read_b64_tr_b16 v[226:227], v244 offset:34048\n"
"v_mfma_f32_16x16x32_bf16 v[120:123], v[236:239], a[152:155], v[120:123]\n"
"s_add_u32 m0, s73, 0x440\n"
"v_mfma_f32_16x16x32_bf16 v[124:127], v[236:239], a[156:159], v[124:127]\n"
"buffer_load_dwordx4 v242, s[48:51], s75 offen offset:1024 nt lds\n"
"s_waitcnt lgkmcnt(2)\n"
"v_mfma_f32_16x16x32_bf16 v[0:3], v[208:211], a[160:163], v[0:3]\n"
"ds_read_b64_tr_b16 v[228:229], v244 offset:33800\n"
"ds_read_b64_tr_b16 v[230:231], v244 offset:34056\n"
"v_mfma_f32_16x16x32_bf16 v[4:7], v[208:211], a[164:167], v[4:7]\n"
"s_waitcnt vmcnt(6)\n"
"s_barrier\n"
"v_mfma_f32_16x16x32_bf16 v[8:11], v[208:211], a[168:171], v[8:11]\n"
"v_mfma_f32_16x16x32_bf16 v[12:15], v[208:211], a[172:175], v[12:15]\n"
"ds_read_b64_tr_b16 v[232:233], v244 offset:34304\n"
"ds_read_b64_tr_b16 v[234:235], v244 offset:34560\n"
"v_mfma_f32_16x16x32_bf16 v[32:35], v[212:215], a[160:163], v[32:35]\n"
"v_mfma_f32_16x16x32_bf16 v[36:39], v[212:215], a[164:167], v[36:39]\n"
"v_mfma_f32_16x16x32_bf16 v[40:43], v[212:215], a[168:171], v[40:43]\n"
"ds_read_b64_tr_b16 v[236:237], v244 offset:34312\n"
"ds_read_b64_tr_b16 v[238:239], v244 offset:34568\n"
"v_mfma_f32_16x16x32_bf16 v[44:47], v[212:215], a[172:175], v[44:47]\n"
"v_mfma_f32_16x16x32_bf16 v[16:19], v[216:219], a[160:163], v[16:19]\n"
"v_mfma_f32_16x16x32_bf16 v[20:23], v[216:219], a[164:167], v[20:23]\n"
"v_mfma_f32_16x16x32_bf16 v[24:27], v[216:219], a[168:171], v[24:27]\n"
"s_add_u32 m0, s73, 0xffffe560\n"
"v_mfma_f32_16x16x32_bf16 v[28:31], v[216:219], a[172:175], v[28:31]\n"
"buffer_load_dwordx4 v199, s[48:51], s75 offen offset:1536 nt lds\n"
"v_mfma_f32_16x16x32_bf16 v[48:51], v[220:223], a[160:163], v[48:51]\n"
"ds_read_b128 a[176:179], v197 offset:33792\n"
"ds_read_b128 a[180:183], v197 offset:35840\n"
"v_mfma_f32_16x16x32_bf16 v[52:55], v[220:223], a[164:167], v[52:55]\n"
"ds_read_b128 a[184:187], v197 offset:37888\n"
"ds_read_b128 a[188:191], v197 offset:39936\n"
"v_mfma_f32_16x16x32_bf16 v[56:59], v[220:223], a[168:171], v[56:59]\n"
"ds_read_b64_tr_b16 v[208:209], v244 offset:42240\n"
"ds_read_b64_tr_b16 v[210:211], v244 offset:42496\n"
"v_mfma_f32_16x16x32_bf16 v[60:63], v[220:223], a[172:175], v[60:63]\n"
"s_waitcnt lgkmcnt(6)\n"
"v_mfma_f32_16x16x32_bf16 v[64:67], v[224:227], a[160:163], v[64:67]\n"
"s_barrier\n"
"v_mfma_f32_16x16x32_bf16 v[68:71], v[224:227], a[164:167], v[68:71]\n"
"v_mfma_f32_16x16x32_bf16 v[72:75], v[224:227], a[168:171], v[72:75]\n"
"ds_read_b64_tr_b16 v[212:213], v244 offset:42248\n"
"ds_read_b64_tr_b16 v[214:215], v244 offset:42504\n"
"v_mfma_f32_16x16x32_bf16 v[76:79], v[224:227], a[172:175], v[76:79]\n"
"v_mfma_f32_16x16x32_bf16 v[96:99], v[228:231], a[160:163], v[96:99]\n"
"v_mfma_f32_16x16x32_bf16 v[100:103], v[228:231], a[164:167], v[100:103]\n"
"ds_read_b64_tr_b16 v[216:217], v244 offset:42752\n"
"ds_read_b64_tr_b16 v[218:219], v244 offset:43008\n"
"v_mfma_f32_16x16x32_bf16 v[104:107], v[228:231], a[168:171], v[104:107]\n"
"s_add_u32 m0, s73, 0x660\n"
"v_mfma_f32_16x16x32_bf16 v[108:111], v[228:231], a[172:175], v[108:111]\n"
"buffer_load_dwordx4 v242, s[48:51], s75 offen offset:1536 nt lds\n"
"v_mfma_f32_16x16x32_bf16 v[80:83], v[232:235], a[160:163], v[80:83]\n"
"ds_read_b64_tr_b16 v[220:221], v244 offset:42760\n"
"ds_read_b64_tr_b16 v[222:223], v244 offset:43016\n"
"v_mfma_f32_16x16x32_bf16 v[84:87], v[232:235], a[164:167], v[84:87]\n"
"v_mfma_f32_16x16x32_bf16 v[88:91], v[232:235], a[168:171], v[88:91]\n"
"v_mfma_f32_16x16x32_bf16 v[92:95], v[232:235], a[172:175], v[92:95]\n"
"v_mfma_f32_16x16x32_bf16 v[112:115], v[236:239], a[160:163], v[112:115]\n"
"ds_read_b64_tr_b16 v[224:225], v244 offset:50688\n"
"ds_read_b64_tr_b16 v[226:227], v244 offset:50944\n"
"v_mfma_f32_16x16x32_bf16 v[116:119], v[236:239], a[164:167], v[116:119]\n"
"v_mfma_f32_16x16x32_bf16 v[120:123], v[236:239], a[168:171], v[120:123]\n"
"v_mfma_f32_16x16x32_bf16 v[124:127], v[236:239], a[172:175], v[124:127]\n"
"s_waitcnt lgkmcnt(2)\n"
"v_mfma_f32_16x16x32_bf16 v[0:3], v[208:211], a[176:179], v[0:3]\n"
"ds_read_b64_tr_b16 v[228:229], v244 offset:50696\n"
"ds_read_b64_tr_b16 v[230:231], v244 offset:50952\n"
"v_mfma_f32_16x16x32_bf16 v[4:7], v[208:211], a[180:183], v[4:7]\n"
"ds_read_b64_tr_b16 v[232:233], v244 offset:51200\n"
"ds_read_b64_tr_b16 v[234:235], v244 offset:51456\n"
"v_mfma_f32_16x16x32_bf16 v[8:11], v[208:211], a[184:187], v[8:11]\n"
"s_barrier\n"
"s_add_u32 m0, s73, 0x2100\n"
"s_mov_b32 s75, 0x4000\n"
"v_mfma_f32_16x16x32_bf16 v[12:15], v[208:211], a[188:191], v[12:15]\n"
"buffer_load_dwordx4 v199, s[48:51], s75 offen offset:0 nt lds\n"
"v_mfma_f32_16x16x32_bf16 v[32:35], v[212:215], a[176:179], v[32:35]\n"
"ds_read_b64_tr_b16 v[236:237], v244 offset:51208\n"
"ds_read_b64_tr_b16 v[238:239], v244 offset:51464\n"
"v_mfma_f32_16x16x32_bf16 v[36:39], v[212:215], a[180:183], v[36:39]\n"
"v_mfma_f32_16x16x32_bf16 v[40:43], v[212:215], a[184:187], v[40:43]\n"
"v_mfma_f32_16x16x32_bf16 v[44:47], v[212:215], a[188:191], v[44:47]\n"
"v_mfma_f32_16x16x32_bf16 v[16:19], v[216:219], a[176:179], v[16:19]\n"
"v_mfma_f32_16x16x32_bf16 v[20:23], v[216:219], a[180:183], v[20:23]\n"
"v_mfma_f32_16x16x32_bf16 v[24:27], v[216:219], a[184:187], v[24:27]\n"
"v_mfma_f32_16x16x32_bf16 v[28:31], v[216:219], a[188:191], v[28:31]\n"
"v_mfma_f32_16x16x32_bf16 v[48:51], v[220:223], a[176:179], v[48:51]\n"
"v_mfma_f32_16x16x32_bf16 v[52:55], v[220:223], a[180:183], v[52:55]\n"
"ds_read_b128 a[192:195], v196 offset:0\n"
"ds_read_b128 a[196:199], v196 offset:2048\n"
"v_mfma_f32_16x16x32_bf16 v[56:59], v[220:223], a[184:187], v[56:59]\n"
"ds_read_b128 a[200:203], v196 offset:4096\n"
"s_add_u32 m0, s73, 0x4200\n"
"v_mfma_f32_16x16x32_bf16 v[60:63], v[220:223], a[188:191], v[60:63]\n"
"buffer_load_dwordx4 v242, s[48:51], s75 offen offset:0 nt lds\n"
"s_waitcnt lgkmcnt(3)\n"
"v_mfma_f32_16x16x32_bf16 v[64:67], v[224:227], a[176:179], v[64:67]\n"
"ds_read_b128 a[204:207], v196 offset:6144\n"
"v_mfma_f32_16x16x32_bf16 v[68:71], v[224:227], a[180:183], v[68:71]\n"
"s_barrier\n"
"v_mfma_f32_16x16x32_bf16 v[72:75], v[224:227], a[184:187], v[72:75]\n"
"v_mfma_f32_16x16x32_bf16 v[76:79], v[224:227], a[188:191], v[76:79]\n"
"ds_read_b64_tr_b16 v[208:209], v244 offset:59136\n"
"ds_read_b64_tr_b16 v[210:211], v244 offset:59392\n"
"v_mfma_f32_16x16x32_bf16 v[96:99], v[228:231], a[176:179], v[96:99]\n"
"v_mfma_f32_16x16x32_bf16 v[100:103], v[228:231], a[180:183], v[100:103]\n"
"v_mfma_f32_16x16x32_bf16 v[104:107], v[228:231], a[184:187], v[104:107]\n"
"ds_read_b64_tr_b16 v[212:213], v244 offset:59144\n"
"ds_read_b64_tr_b16 v[214:215], v244 offset:59400\n"
"v_mfma_f32_16x16x32_bf16 v[108:111], v[228:231], a[188:191], v[108:111]\n"
"v_mfma_f32_16x16x32_bf16 v[80:83], v[232:235], a[176:179], v[80:83]\n"
"v_mfma_f32_16x16x32_bf16 v[84:87], v[232:235], a[180:183], v[84:87]\n"
"ds_read_b64_tr_b16 v[216:217], v244 offset:59648\n"
"ds_read_b64_tr_b16 v[218:219], v244 offset:59904\n"
"ds_read_b64_tr_b16 v[220:221], v244 offset:59656\n"
"ds_read_b64_tr_b16 v[222:223], v244 offset:59912\n"
"v_mfma_f32_16x16x32_bf16 v[88:91], v[232:235], a[184:187], v[88:91]\n"
"s_add_u32 m0, s73, 0x2320\n"
"v_mfma_f32_16x16x32_bf16 v[92:95], v[232:235], a[188:191], v[92:95]\n"
"buffer_load_dwordx4 v199, s[48:51], s75 offen offset:512 nt lds\n"
"v_mfma_f32_16x16x32_bf16 v[112:115], v[236:239], a[176:179], v[112:115]\n"
"ds_read_b128 a[208:211], v197 offset:0\n"
"ds_read_b128 a[212:215], v197 offset:2048\n"
"v_mfma_f32_16x16x32_bf16 v[116:119], v[236:239], a[180:183], v[116:119]\n"
"ds_read_b128 a[216:219], v197 offset:4096\n"
"v_mfma_f32_16x16x32_bf16 v[120:123], v[236:239], a[184:187], v[120:123]\n"
"v_mfma_f32_16x16x32_bf16 v[124:127], v[236:239], a[188:191], v[124:127]\n"
"s_waitcnt lgkmcnt(3)\n"
"v_mfma_f32_16x16x32_bf16 v[0:3], v[208:211], a[192:195], v[0:3]\n"
"s_barrier\n"
"v_mfma_f32_16x16x32_bf16 v[4:7], v[208:211], a[196:199], v[4:7]\n"
"v_mfma_f32_16x16x32_bf16 v[8:11], v[208:211], a[200:203], v[8:11]\n"
"ds_read_b128 a[220:223], v197 offset:6144\n"
"v_mfma_f32_16x16x32_bf16 v[12:15], v[208:211], a[204:207], v[12:15]\n"
"v_mfma_f32_16x16x32_bf16 v[32:35], v[212:215], a[192:195], v[32:35]\n"
"v_mfma_f32_16x16x32_bf16 v[36:39], v[212:215], a[196:199], v[36:39]\n"
"ds_read_b64_tr_b16 v[224:225], v245 offset:0\n"
"ds_read_b64_tr_b16 v[226:227], v245 offset:256\n"
"v_mfma_f32_16x16x32_bf16 v[40:43], v[212:215], a[200:203], v[40:43]\n"
"s_add_u32 m0, s73, 0x4420\n"
"v_mfma_f32_16x16x32_bf16 v[44:47], v[212:215], a[204:207], v[44:47]\n"
"buffer_load_dwordx4 v242, s[48:51], s75 offen offset:512 nt lds\n"
"v_mfma_f32_16x16x32_bf16 v[16:19], v[216:219], a[192:195], v[16:19]\n"
"ds_read_b64_tr_b16 v[228:229], v245 offset:8\n"
"ds_read_b64_tr_b16 v[230:231], v245 offset:264\n"
"v_mfma_f32_16x16x32_bf16 v[20:23], v[216:219], a[196:199], v[20:23]\n"
"ds_read_b64_tr_b16 v[232:233], v245 offset:512\n"
"ds_read_b64_tr_b16 v[234:235], v245 offset:768\n"
"ds_read_b64_tr_b16 v[236:237], v245 offset:520\n"
"ds_read_b64_tr_b16 v[238:239], v245 offset:776\n"
"v_mfma_f32_16x16x32_bf16 v[24:27], v[216:219], a[200:203], v[24:27]\n"
"v_mfma_f32_16x16x32_bf16 v[28:31], v[216:219], a[204:207], v[28:31]\n"
"v_mfma_f32_16x16x32_bf16 v[48:51], v[220:223], a[192:195], v[48:51]\n"
"ds_read_b64_tr_b16 v[208:209], v245 offset:16896\n"
"ds_read_b64_tr_b16 v[210:211], v245 offset:17152\n"
"v_mfma_f32_16x16x32_bf16 v[52:55], v[220:223], a[196:199], v[52:55]\n"
"v_mfma_f32_16x16x32_bf16 v[56:59], v[220:223], a[200:203], v[56:59]\n"
"v_mfma_f32_16x16x32_bf16 v[60:63], v[220:223], a[204:207], v[60:63]\n"
"s_waitcnt lgkmcnt(2)\n"
"v_mfma_f32_16x16x32_bf16 v[0:3], v[224:227], a[208:211], v[0:3]\n"
"ds_read_b64_tr_b16 v[212:213], v245 offset:16904\n"
"ds_read_b64_tr_b16 v[214:215], v245 offset:17160\n"
"v_mfma_f32_16x16x32_bf16 v[4:7], v[224:227], a[212:215], v[4:7]\n"
"ds_read_b64_tr_b16 v[216:217], v245 offset:17408\n"
"ds_read_b64_tr_b16 v[218:219], v245 offset:17664\n"
"v_mfma_f32_16x16x32_bf16 v[8:11], v[224:227], a[216:219], v[8:11]\n"
"s_barrier\n"
"s_add_u32 m0, s73, 0x2540\n"
"v_mfma_f32_16x16x32_bf16 v[12:15], v[224:227], a[220:223], v[12:15]\n"
"buffer_load_dwordx4 v199, s[48:51], s75 offen offset:1024 nt lds\n"
"v_mfma_f32_16x16x32_bf16 v[32:35], v[228:231], a[208:211], v[32:35]\n"
"ds_read_b64_tr_b16 v[220:221], v245 offset:17416\n"
"ds_read_b64_tr_b16 v[222:223], v245 offset:17672\n"
"v_mfma_f32_16x16x32_bf16 v[36:39], v[228:231], a[212:215], v[36:39]\n"
"v_mul_f32 v0, s95, v0\n"
"v_mfma_f32_16x16x32_bf16 v[40:43], v[228:231], a[216:219], v[40:43]\n"
"v_mfma_f32_16x16x32_bf16 v[44:47], v[228:231], a[220:223], v[44:47]\n"
"v_mul_f32 v1, s95, v1\n"
"v_mul_f32 v2, s95, v2\n"
"v_mfma_f32_16x16x32_bf16 v[16:19], v[232:235], a[208:211], v[16:19]\n"
"v_mul_f32 v3, s95, v3\n"
"v_mfma_f32_16x16x32_bf16 v[20:23], v[232:235], a[212:215], v[20:23]\n"
"v_mfma_f32_16x16x32_bf16 v[24:27], v[232:235], a[216:219], v[24:27]\n"
"v_mul_f32 v32, s95, v32\n"
"v_mul_f32 v33, s95, v33\n"
"v_mfma_f32_16x16x32_bf16 v[28:31], v[232:235], a[220:223], v[28:31]\n"
"v_mul_f32 v34, s95, v34\n"
"v_mfma_f32_16x16x32_bf16 v[48:51], v[236:239], a[208:211], v[48:51]\n"
"v_mfma_f32_16x16x32_bf16 v[52:55], v[236:239], a[212:215], v[52:55]\n"
"ds_read_b64_tr_b16 v[224:225], v245 offset:8448\n"
"ds_read_b64_tr_b16 v[226:227], v245 offset:8704\n"
"v_mfma_f32_16x16x32_bf16 v[56:59], v[236:239], a[216:219], v[56:59]\n"
"v_mul_f32 v35, s95, v35\n"
"s_add_u32 m0, s73, 0x4640\n"
"v_mfma_f32_16x16x32_bf16 v[60:63], v[236:239], a[220:223], v[60:63]\n"
"buffer_load_dwordx4 v242, s[48:51], s75 offen offset:1024 nt lds\n"
"s_waitcnt lgkmcnt(2)\n"
"v_mfma_f32_16x16x32_bf16 v[64:67], v[208:211], a[192:195], v[64:67]\n"
"ds_read_b64_tr_b16 v[228:229], v245 offset:8456\n"
"ds_read_b64_tr_b16 v[230:231], v245 offset:8712\n"
"v_mfma_f32_16x16x32_bf16 v[68:71], v[208:211], a[196:199], v[68:71]\n"
"s_barrier\n"
"v_mfma_f32_16x16x32_bf16 v[72:75], v[208:211], a[200:203], v[72:75]\n"
"v_mfma_f32_16x16x32_bf16 v[76:79], v[208:211], a[204:207], v[76:79]\n"
"ds_read_b64_tr_b16 v[232:233], v245 offset:8960\n"
"ds_read_b64_tr_b16 v[234:235], v245 offset:9216\n"
"v_mfma_f32_16x16x32_bf16 v[96:99], v[212:215], a[192:195], v[96:99]\n"
"v_cvt_pk_bf16_f32 v0, v0, v1\n"
"v_mfma_f32_16x16x32_bf16 v[100:103], v[212:215], a[196:199], v[100:103]\n"
"v_mfma_f32_16x16x32_bf16 v[104:107], v[212:215], a[200:203], v[104:107]\n"
"ds_read_b64_tr_b16 v[236:237], v245 offset:8968\n"
"ds_read_b64_tr_b16 v[238:239], v245 offset:9224\n"
"v_mfma_f32_16x16x32_bf16 v[108:111], v[212:215], a[204:207], v[108:111]\n"
"v_cvt_pk_bf16_f32 v1, v2, v3\n"
"v_mfma_f32_16x16x32_bf16 v[80:83], v[216:219], a[192:195], v[80:83]\n"
"v_mfma_f32_16x16x32_bf16 v[84:87], v[216:219], a[196:199], v[84:87]\n"
"v_cvt_pk_bf16_f32 v2, v32, v33\n"
"v_cvt_pk_bf16_f32 v3, v34, v35\n"
"v_mfma_f32_16x16x32_bf16 v[88:91], v[216:219], a[200:203], v[88:91]\n"
"v_mul_f32 v4, s95, v4\n"
"s_add_u32 m0, s73, 0x2760\n"
"v_mfma_f32_16x16x32_bf16 v[92:95], v[216:219], a[204:207], v[92:95]\n"
"buffer_load_dwordx4 v199, s[48:51], s75 offen offset:1536 nt lds\n"
"v_mfma_f32_16x16x32_bf16 v[112:115], v[220:223], a[192:195], v[112:115]\n"
"ds_read_b64_tr_b16 v[208:209], v244 offset:33792\n"
"ds_read_b64_tr_b16 v[210:211], v244 offset:34048\n"
"v_mfma_f32_16x16x32_bf16 v[116:119], v[220:223], a[196:199], v[116:119]\n"
"ds_read_b64_tr_b16 v[212:213], v244 offset:33800\n"
"ds_read_b64_tr_b16 v[214:215], v244 offset:34056\n"
"v_mfma_f32_16x16x32_bf16 v[120:123], v[220:223], a[200:203], v[120:123]\n"
"ds_read_b64_tr_b16 v[216:217], v244 offset:34304\n"
"ds_read_b64_tr_b16 v[218:219], v244 offset:34560\n"
"v_mfma_f32_16x16x32_bf16 v[124:127], v[220:223], a[204:207], v[124:127]\n"
"v_mul_f32 v5, s95, v5\n"
"s_waitcnt lgkmcnt(6)\n"
"v_mfma_f32_16x16x32_bf16 v[64:67], v[224:227], a[208:211], v[64:67]\n"
"s_barrier\n"
"v_mfma_f32_16x16x32_bf16 v[68:71], v[224:227], a[212:215], v[68:71]\n"
"v_mfma_f32_16x16x32_bf16 v[72:75], v[224:227], a[216:219], v[72:75]\n"
"ds_read_b64_tr_b16 v[220:221], v244 offset:34312\n"
"ds_read_b64_tr_b16 v[222:223], v244 offset:34568\n"
"v_mfma_f32_16x16x32_bf16 v[76:79], v[224:227], a[220:223], v[76:79]\n"
"v_mul_f32 v6, s95, v6\n"
"v_mfma_f32_16x16x32_bf16 v[96:99], v[228:231], a[208:211], v[96:99]\n"
"v_mfma_f32_16x16x32_bf16 v[100:103], v[228:231], a[212:215], v[100:103]\n"
"v_mul_f32 v7, s95, v7\n"
"v_mul_f32 v36, s95, v36\n"
"v_mfma_f32_16x16x32_bf16 v[104:107], v[228:231], a[216:219], v[104:107]\n"
"v_mul_f32 v37, s95, v37\n"
"s_add_u32 m0, s73, 0x4860\n"
"v_mfma_f32_16x16x32_bf16 v[108:111], v[228:231], a[220:223], v[108:111]\n"
"buffer_load_dwordx4 v242, s[48:51], s75 offen offset:1536 nt lds\n"
"v_mfma_f32_16x16x32_bf16 v[80:83], v[232:235], a[208:211], v[80:83]\n"
"v_mul_f32 v38, s95, v38\n"
"v_mul_f32 v39, s95, v39\n"
"v_mfma_f32_16x16x32_bf16 v[84:87], v[232:235], a[212:215], v[84:87]\n"
"v_cvt_pk_bf16_f32 v4, v4, v5\n"
"v_mfma_f32_16x16x32_bf16 v[88:91], v[232:235], a[216:219], v[88:91]\n"
"v_mfma_f32_16x16x32_bf16 v[92:95], v[232:235], a[220:223], v[92:95]\n"
"v_cvt_pk_bf16_f32 v5, v6, v7\n"
"v_cvt_pk_bf16_f32 v6, v36, v37\n"
"v_mfma_f32_16x16x32_bf16 v[112:115], v[236:239], a[208:211], v[112:115]\n"
"ds_read_b64_tr_b16 v[224:225], v244 offset:42240\n"
"ds_read_b64_tr_b16 v[226:227], v244 offset:42496\n"
"v_mfma_f32_16x16x32_bf16 v[116:119], v[236:239], a[212:215], v[116:119]\n"
"ds_read_b64_tr_b16 v[228:229], v244 offset:42248\n"
"ds_read_b64_tr_b16 v[230:231], v244 offset:42504\n"
"v_mfma_f32_16x16x32_bf16 v[120:123], v[236:239], a[216:219], v[120:123]\n"
"ds_read_b64_tr_b16 v[232:233], v244 offset:42752\n"
"ds_read_b64_tr_b16 v[234:235], v244 offset:43008\n"
"v_mfma_f32_16x16x32_bf16 v[124:127], v[236:239], a[220:223], v[124:127]\n"
"v_cvt_pk_bf16_f32 v7, v38, v39\n"
"s_waitcnt lgkmcnt(6)\n"
"v_mfma_f32_16x16x32_bf16 v[128:131], v[208:211], a[0:3], 0\n"
"ds_read_b64_tr_b16 v[236:237], v244 offset:42760\n"
"ds_read_b64_tr_b16 v[238:239], v244 offset:43016\n"
"v_mfma_f32_16x16x32_bf16 v[132:135], v[208:211], a[4:7], 0\n"
"v_mul_f32 v8, s95, v8\n"
"v_mul_f32 v9, s95, v9\n"
"v_mfma_f32_16x16x32_bf16 v[136:139], v[208:211], a[8:11], 0\n"
"s_barrier\n"
"s_add_u32 m0, s74, 0x141c0\n"
"s_mov_b32 s75, 0x4000\n"
"v_mfma_f32_16x16x32_bf16 v[140:143], v[208:211], a[12:15], 0\n"
"buffer_load_dwordx4 v200, s[52:55], s75 offen offset:0 nt lds\n"
"v_mfma_f32_16x16x32_bf16 v[160:163], v[212:215], a[0:3], 0\n"
"v_mul_f32 v10, s95, v10\n"
"v_mul_f32 v11, s95, v11\n"
"v_mfma_f32_16x16x32_bf16 v[164:167], v[212:215], a[4:7], 0\n"
"v_mul_f32 v40, s95, v40\n"
"v_mfma_f32_16x16x32_bf16 v[168:171], v[212:215], a[8:11], 0\n"
"v_mfma_f32_16x16x32_bf16 v[172:175], v[212:215], a[12:15], 0\n"
"v_mul_f32 v41, s95, v41\n"
"v_mul_f32 v42, s95, v42\n"
"v_mfma_f32_16x16x32_bf16 v[144:147], v[216:219], a[0:3], 0\n"
"v_mul_f32 v43, s95, v43\n"
"v_mfma_f32_16x16x32_bf16 v[148:151], v[216:219], a[4:7], 0\n"
"v_mfma_f32_16x16x32_bf16 v[152:155], v[216:219], a[8:11], 0\n"
"v_cvt_pk_bf16_f32 v8, v8, v9\n"
"v_cvt_pk_bf16_f32 v9, v10, v11\n"
"v_mfma_f32_16x16x32_bf16 v[156:159], v[216:219], a[12:15], 0\n"
"v_cvt_pk_bf16_f32 v10, v40, v41\n"
"v_mfma_f32_16x16x32_bf16 v[176:179], v[220:223], a[0:3], 0\n"
"v_mfma_f32_16x16x32_bf16 v[180:183], v[220:223], a[4:7], 0\n"
"ds_read_b64_tr_b16 v[208:209], v244 offset:50688\n"
"ds_read_b64_tr_b16 v[210:211], v244 offset:50944\n"
"v_mfma_f32_16x16x32_bf16 v[184:187], v[220:223], a[8:11], 0\n"
"v_cvt_pk_bf16_f32 v11, v42, v43\n"
"s_add_u32 m0, s74, 0x14a00\n"
"v_mfma_f32_16x16x32_bf16 v[188:191], v[220:223], a[12:15], 0\n"
"buffer_load_dwordx4 v243, s[52:55], s75 offen offset:0 nt lds\n"
"s_waitcnt lgkmcnt(2)\n"
"v_mfma_f32_16x16x32_bf16 v[128:131], v[224:227], a[16:19], v[128:131]\n"
"ds_read_b64_tr_b16 v[212:213], v244 offset:50696\n"
"ds_read_b64_tr_b16 v[214:215], v244 offset:50952\n"
"v_mfma_f32_16x16x32_bf16 v[132:135], v[224:227], a[20:23], v[132:135]\n"
"s_barrier\n"
"v_mfma_f32_16x16x32_bf16 v[136:139], v[224:227], a[24:27], v[136:139]\n"
"v_mfma_f32_16x16x32_bf16 v[140:143], v[224:227], a[28:31], v[140:143]\n"
"ds_read_b64_tr_b16 v[216:217], v244 offset:51200\n"
"ds_read_b64_tr_b16 v[218:219], v244 offset:51456\n"
"v_mfma_f32_16x16x32_bf16 v[160:163], v[228:231], a[16:19], v[160:163]\n"
"v_mul_f32 v12, s95, v12\n"
"v_mfma_f32_16x16x32_bf16 v[164:167], v[228:231], a[20:23], v[164:167]\n"
"v_mfma_f32_16x16x32_bf16 v[168:171], v[228:231], a[24:27], v[168:171]\n"
"ds_read_b64_tr_b16 v[220:221], v244 offset:51208\n"
"ds_read_b64_tr_b16 v[222:223], v244 offset:51464\n"
"v_mfma_f32_16x16x32_bf16 v[172:175], v[228:231], a[28:31], v[172:175]\n"
"v_mul_f32 v13, s95, v13\n"
"v_mfma_f32_16x16x32_bf16 v[144:147], v[232:235], a[16:19], v[144:147]\n"
"v_mfma_f32_16x16x32_bf16 v[148:151], v[232:235], a[20:23], v[148:151]\n"
"v_mul_f32 v14, s95, v14\n"
"v_mul_f32 v15, s95, v15\n"
"v_mfma_f32_16x16x32_bf16 v[152:155], v[232:235], a[24:27], v[152:155]\n"
"v_mul_f32 v44, s95, v44\n"
"s_add_u32 m0, s74, 0x151c0\n"
"v_mfma_f32_16x16x32_bf16 v[156:159], v[232:235], a[28:31], v[156:159]\n"
"buffer_load_dwordx4 v200, s[52:55], s75 offen offset:128 nt lds\n"
"v_mfma_f32_16x16x32_bf16 v[176:179], v[236:239], a[16:19], v[176:179]\n"
"ds_read_b64_tr_b16 v[224:225], v244 offset:59136\n"
"ds_read_b64_tr_b16 v[226:227], v244 offset:59392\n"
"v_mfma_f32_16x16x32_bf16 v[180:183], v[236:239], a[20:23], v[180:183]\n"
"ds_read_b64_tr_b16 v[228:229], v244 offset:59144\n"
"ds_read_b64_tr_b16 v[230:231], v244 offset:59400\n"
"v_mfma_f32_16x16x32_bf16 v[184:187], v[236:239], a[24:27], v[184:187]\n"
"ds_read_b64_tr_b16 v[232:233], v244 offset:59648\n"
"ds_read_b64_tr_b16 v[234:235], v244 offset:59904\n"
"v_mfma_f32_16x16x32_bf16 v[188:191], v[236:239], a[28:31], v[188:191]\n"
"v_mul_f32 v45, s95, v45\n"
"s_waitcnt lgkmcnt(6)\n"
"v_mfma_f32_16x16x32_bf16 v[128:131], v[208:211], a[32:35], v[128:131]\n"
"s_barrier\n"
"v_mfma_f32_16x16x32_bf16 v[132:135], v[208:211], a[36:39], v[132:135]\n"
"v_mfma_f32_16x16x32_bf16 v[136:139], v[208:211], a[40:43], v[136:139]\n"
"ds_read_b64_tr_b16 v[236:237], v244 offset:59656\n"
"ds_read_b64_tr_b16 v[238:239], v244 offset:59912\n"
"v_mfma_f32_16x16x32_bf16 v[140:143], v[208:211], a[44:47], v[140:143]\n"
"v_mul_f32 v46, s95, v46\n"
"v_mfma_f32_16x16x32_bf16 v[160:163], v[212:215], a[32:35], v[160:163]\n"
"v_mfma_f32_16x16x32_bf16 v[164:167], v[212:215], a[36:39], v[164:167]\n"
"v_mul_f32 v47, s95, v47\n"
"v_cvt_pk_bf16_f32 v12, v12, v13\n"
"v_mfma_f32_16x16x32_bf16 v[168:171], v[212:215], a[40:43], v[168:171]\n"
"v_cvt_pk_bf16_f32 v13, v14, v15\n"
"s_add_u32 m0, s74, 0x15a00\n"
"v_mfma_f32_16x16x32_bf16 v[172:175], v[212:215], a[44:47], v[172:175]\n"
"buffer_load_dwordx4 v243, s[52:55], s75 offen offset:128 nt lds\n"
"v_mfma_f32_16x16x32_bf16 v[144:147], v[216:219], a[32:35], v[144:147]\n"
"v_cvt_pk_bf16_f32 v14, v44, v45\n"
"v_cvt_pk_bf16_f32 v15, v46, v47\n"
"v_mfma_f32_16x16x32_bf16 v[148:151], v[216:219], a[36:39], v[148:151]\n"
"v_mul_f32 v16, s95, v16\n"
"v_mfma_f32_16x16x32_bf16 v[152:155], v[216:219], a[40:43], v[152:155]\n"
"v_mfma_f32_16x16x32_bf16 v[156:159], v[216:219], a[44:47], v[156:159]\n"
"v_mul_f32 v17, s95, v17\n"
"v_mul_f32 v18, s95, v18\n"
"v_mfma_f32_16x16x32_bf16 v[176:179], v[220:223], a[32:35], v[176:179]\n"
"ds_read_b64_tr_b16 v[208:209], v245 offset:0\n"
"ds_read_b64_tr_b16 v[210:211], v245 offset:256\n"
"v_mfma_f32_16x16x32_bf16 v[180:183], v[220:223], a[36:39], v[180:183]\n"
"ds_read_b64_tr_b16 v[212:213], v245 offset:8\n"
"ds_read_b64_tr_b16 v[214:215], v245 offset:264\n"
"v_mfma_f32_16x16x32_bf16 v[184:187], v[220:223], a[40:43], v[184:187]\n"
"ds_read_b64_tr_b16 v[216:217], v245 offset:512\n"
"ds_read_b64_tr_b16 v[218:219], v245 offset:768\n"
"v_mfma_f32_16x16x32_bf16 v[188:191], v[220:223], a[44:47], v[188:191]\n"
"v_mul_f32 v19, s95, v19\n"
"s_waitcnt lgkmcnt(6)\n"
"v_mfma_f32_16x16x32_bf16 v[128:131], v[224:227], a[48:51], v[128:131]\n"
"ds_read_b64_tr_b16 v[220:221], v245 offset:520\n"
"ds_read_b64_tr_b16 v[222:223], v245 offset:776\n"
"v_mfma_f32_16x16x32_bf16 v[132:135], v[224:227], a[52:55], v[132:135]\n"
"v_mul_f32 v48, s95, v48\n"
"v_mul_f32 v49, s95, v49\n"
"v_mfma_f32_16x16x32_bf16 v[136:139], v[224:227], a[56:59], v[136:139]\n"
"s_barrier\n"
"s_add_u32 m0, s74, 0x161c0\n"
"v_mfma_f32_16x16x32_bf16 v[140:143], v[224:227], a[60:63], v[140:143]\n"
"buffer_load_dwordx4 v200, s[52:55], s75 offen offset:256 nt lds\n"
"v_mfma_f32_16x16x32_bf16 v[160:163], v[228:231], a[48:51], v[160:163]\n"
"v_mul_f32 v50, s95, v50\n"
"v_mul_f32 v51, s95, v51\n"
"v_mfma_f32_16x16x32_bf16 v[164:167], v[228:231], a[52:55], v[164:167]\n"
"v_cvt_pk_bf16_f32 v16, v16, v17\n"
"v_mfma_f32_16x16x32_bf16 v[168:171], v[228:231], a[56:59], v[168:171]\n"
"v_mfma_f32_16x16x32_bf16 v[172:175], v[228:231], a[60:63], v[172:175]\n"
"v_cvt_pk_bf16_f32 v17, v18, v19\n"
"v_cvt_pk_bf16_f32 v18, v48, v49\n"
"v_mfma_f32_16x16x32_bf16 v[144:147], v[232:235], a[48:51], v[144:147]\n"
"v_cvt_pk_bf16_f32 v19, v50, v51\n"
"v_mfma_f32_16x16x32_bf16 v[148:151], v[232:235], a[52:55], v[148:151]\n"
"v_mfma_f32_16x16x32_bf16 v[152:155], v[232:235], a[56:59], v[152:155]\n"
"v_mul_f32 v20, s95, v20\n"
"v_mul_f32 v21, s95, v21\n"
"v_mfma_f32_16x16x32_bf16 v[156:159], v[232:235], a[60:63], v[156:159]\n"
"v_mul_f32 v22, s95, v22\n"
"v_mfma_f32_16x16x32_bf16 v[176:179], v[236:239], a[48:51], v[176:179]\n"
"v_mfma_f32_16x16x32_bf16 v[180:183], v[236:239], a[52:55], v[180:183]\n"
"ds_read_b64_tr_b16 v[224:225], v245 offset:16896\n"
"ds_read_b64_tr_b16 v[226:227], v245 offset:17152\n"
"v_mfma_f32_16x16x32_bf16 v[184:187], v[236:239], a[56:59], v[184:187]\n"
"v_mul_f32 v23, s95, v23\n"
"s_add_u32 m0, s74, 0x16a00\n"
"v_mfma_f32_16x16x32_bf16 v[188:191], v[236:239], a[60:63], v[188:191]\n"
"buffer_load_dwordx4 v243, s[52:55], s75 offen offset:256 nt lds\n"
"s_waitcnt lgkmcnt(2)\n"
"v_mfma_f32_16x16x32_bf16 v[128:131], v[208:211], a[64:67], v[128:131]\n"
"ds_read_b64_tr_b16 v[228:229], v245 offset:16904\n"
"ds_read_b64_tr_b16 v[230:231], v245 offset:17160\n"
"v_mfma_f32_16x16x32_bf16 v[132:135], v[208:211], a[68:71], v[132:135]\n"
"s_barrier\n"
"v_mfma_f32_16x16x32_bf16 v[136:139], v[208:211], a[72:75], v[136:139]\n"
"v_mfma_f32_16x16x32_bf16 v[140:143], v[208:211], a[76:79], v[140:143]\n"
"ds_read_b64_tr_b16 v[232:233], v245 offset:17408\n"
"ds_read_b64_tr_b16 v[234:235], v245 offset:17664\n"
"v_mfma_f32_16x16x32_bf16 v[160:163], v[212:215], a[64:67], v[160:163]\n"
"v_mul_f32 v52, s95, v52\n"
"v_mfma_f32_16x16x32_bf16 v[164:167], v[212:215], a[68:71], v[164:167]\n"
"v_mfma_f32_16x16x32_bf16 v[168:171], v[212:215], a[72:75], v[168:171]\n"
"ds_read_b64_tr_b16 v[236:237], v245 offset:17416\n"
"ds_read_b64_tr_b16 v[238:239], v245 offset:17672\n"
"v_mfma_f32_16x16x32_bf16 v[172:175], v[212:215], a[76:79], v[172:175]\n"
"v_mul_f32 v53, s95, v53\n"
"v_mfma_f32_16x16x32_bf16 v[144:147], v[216:219], a[64:67], v[144:147]\n"
"v_mfma_f32_16x16x32_bf16 v[148:151], v[216:219], a[68:71], v[148:151]\n"
"v_mul_f32 v54, s95, v54\n"
"v_mul_f32 v55, s95, v55\n"
"v_mfma_f32_16x16x32_bf16 v[152:155], v[216:219], a[72:75], v[152:155]\n"
"v_cvt_pk_bf16_f32 v20, v20, v21\n"
"s_add_u32 m0, s74, 0x171c0\n"
"v_mfma_f32_16x16x32_bf16 v[156:159], v[216:219], a[76:79], v[156:159]\n"
"buffer_load_dwordx4 v200, s[52:55], s75 offen offset:384 nt lds\n"
"v_mfma_f32_16x16x32_bf16 v[176:179], v[220:223], a[64:67], v[176:179]\n"
"ds_read_b64_tr_b16 v[208:209], v245 offset:8448\n"
"ds_read_b64_tr_b16 v[210:211], v245 offset:8704\n"
"v_mfma_f32_16x16x32_bf16 v[180:183], v[220:223], a[68:71], v[180:183]\n"
"ds_read_b64_tr_b16 v[212:213], v245 offset:8456\n"
"ds_read_b64_tr_b16 v[214:215], v245 offset:8712\n"
"v_mfma_f32_16x16x32_bf16 v[184:187], v[220:223], a[72:75], v[184:187]\n"
"ds_read_b64_tr_b16 v[216:217], v245 offset:8960\n"
"ds_read_b64_tr_b16 v[218:219], v245 offset:9216\n"
"v_mfma_f32_16x16x32_bf16 v[188:191], v[220:223], a[76:79], v[188:191]\n"
"v_cvt_pk_bf16_f32 v21, v22, v23\n"
"s_waitcnt lgkmcnt(6)\n"
"v_mfma_f32_16x16x32_bf16 v[128:131], v[224:227], a[80:83], v[128:131]\n"
"s_barrier\n"
"v_mfma_f32_16x16x32_bf16 v[132:135], v[224:227], a[84:87], v[132:135]\n"
"v_mfma_f32_16x16x32_bf16 v[136:139], v[224:227], a[88:91], v[136:139]\n"
"ds_read_b64_tr_b16 v[220:221], v245 offset:8968\n"
"ds_read_b64_tr_b16 v[222:223], v245 offset:9224\n"
"v_mfma_f32_16x16x32_bf16 v[140:143], v[224:227], a[92:95], v[140:143]\n"
"v_cvt_pk_bf16_f32 v22, v52, v53\n"
"v_mfma_f32_16x16x32_bf16 v[160:163], v[228:231], a[80:83], v[160:163]\n"
"v_mfma_f32_16x16x32_bf16 v[164:167], v[228:231], a[84:87], v[164:167]\n"
"v_cvt_pk_bf16_f32 v23, v54, v55\n"
"v_mul_f32 v24, s95, v24\n"
"v_mfma_f32_16x16x32_bf16 v[168:171], v[228:231], a[88:91], v[168:171]\n"
"v_mul_f32 v25, s95, v25\n"
"s_add_u32 m0, s74, 0x17a00\n"
"v_mfma_f32_16x16x32_bf16 v[172:175], v[228:231], a[92:95], v[172:175]\n"
"buffer_load_dwordx4 v243, s[52:55], s75 offen offset:384 nt lds\n"
"v_mfma_f32_16x16x32_bf16 v[144:147], v[232:235], a[80:83], v[144:147]\n"
"v_mul_f32 v26, s95, v26\n"
"v_mul_f32 v27, s95, v27\n"
"v_mfma_f32_16x16x32_bf16 v[148:151], v[232:235], a[84:87], v[148:151]\n"
"v_mul_f32 v56, s95, v56\n"
"v_mfma_f32_16x16x32_bf16 v[152:155], v[232:235], a[88:91], v[152:155]\n"
"v_mfma_f32_16x16x32_bf16 v[156:159], v[232:235], a[92:95], v[156:159]\n"
"v_mul_f32 v57, s95, v57\n"
"v_mul_f32 v58, s95, v58\n"
"v_mfma_f32_16x16x32_bf16 v[176:179], v[236:239], a[80:83], v[176:179]\n"
"ds_read_b64_tr_b16 v[224:225], v244 offset:33792\n"
"ds_read_b64_tr_b16 v[226:227], v244 offset:34048\n"
"v_mfma_f32_16x16x32_bf16 v[180:183], v[236:239], a[84:87], v[180:183]\n"
"ds_read_b64_tr_b16 v[228:229], v244 offset:33800\n"
"ds_read_b64_tr_b16 v[230:231], v244 offset:34056\n"
"v_mfma_f32_16x16x32_bf16 v[184:187], v[236:239], a[88:91], v[184:187]\n"
"ds_read_b64_tr_b16 v[232:233], v244 offset:34304\n"
"ds_read_b64_tr_b16 v[234:235], v244 offset:34560\n"
"v_mfma_f32_16x16x32_bf16 v[188:191], v[236:239], a[92:95], v[188:191]\n"
"v_mul_f32 v59, s95, v59\n"
"s_waitcnt lgkmcnt(6)\n"
"v_mfma_f32_16x16x32_bf16 v[128:131], v[208:211], a[96:99], v[128:131]\n"
"ds_read_b64_tr_b16 v[236:237], v244 offset:34312\n"
"ds_read_b64_tr_b16 v[238:239], v244 offset:34568\n"
"v_mfma_f32_16x16x32_bf16 v[132:135], v[208:211], a[100:103], v[132:135]\n"
"v_cvt_pk_bf16_f32 v24, v24, v25\n"
"v_cvt_pk_bf16_f32 v25, v26, v27\n"
"v_mfma_f32_16x16x32_bf16 v[136:139], v[208:211], a[104:107], v[136:139]\n"
"s_barrier\n"
"s_add_u32 m0, s74, 0xffc0\n"
"s_mov_b32 s75, 0x0\n"
"v_mfma_f32_16x16x32_bf16 v[140:143], v[208:211], a[108:111], v[140:143]\n"
"buffer_load_dwordx4 v200, s[52:55], s75 offen offset:0 nt lds\n"
"v_mfma_f32_16x16x32_bf16 v[160:163], v[212:215], a[96:99], v[160:163]\n"
"v_cvt_pk_bf16_f32 v26, v56, v57\n"
"v_cvt_pk_bf16_f32 v27, v58, v59\n"
"v_mfma_f32_16x16x32_bf16 v[164:167], v[212:215], a[100:103], v[164:167]\n"
"v_mul_f32 v28, s95, v28\n"
"v_mfma_f32_16x16x32_bf16 v[168:171], v[212:215], a[104:107], v[168:171]\n"
"v_mfma_f32_16x16x32_bf16 v[172:175], v[212:215], a[108:111], v[172:175]\n"
"v_mul_f32 v29, s95, v29\n"
"v_mul_f32 v30, s95, v30\n"
"v_mfma_f32_16x16x32_bf16 v[144:147], v[216:219], a[96:99], v[144:147]\n"
"v_mul_f32 v31, s95, v31\n"
"v_mfma_f32_16x16x32_bf16 v[148:151], v[216:219], a[100:103], v[148:151]\n"
"v_mfma_f32_16x16x32_bf16 v[152:155], v[216:219], a[104:107], v[152:155]\n"
"v_mul_f32 v60, s95, v60\n"
"v_mul_f32 v61, s95, v61\n"
"v_mfma_f32_16x16x32_bf16 v[156:159], v[216:219], a[108:111], v[156:159]\n"
"v_mul_f32 v62, s95, v62\n"
"v_mfma_f32_16x16x32_bf16 v[176:179], v[220:223], a[96:99], v[176:179]\n"
"v_mfma_f32_16x16x32_bf16 v[180:183], v[220:223], a[100:103], v[180:183]\n"
"ds_read_b64_tr_b16 v[208:209], v244 offset:42240\n"
"ds_read_b64_tr_b16 v[210:211], v244 offset:42496\n"
"v_mfma_f32_16x16x32_bf16 v[184:187], v[220:223], a[104:107], v[184:187]\n"
"v_mul_f32 v63, s95, v63\n"
"s_add_u32 m0, s74, 0x10800\n"
"v_mfma_f32_16x16x32_bf16 v[188:191], v[220:223], a[108:111], v[188:191]\n"
"buffer_load_dwordx4 v243, s[52:55], s75 offen offset:0 nt lds\n"
"s_waitcnt lgkmcnt(2)\n"
"v_mfma_f32_16x16x32_bf16 v[128:131], v[224:227], a[112:115], v[128:131]\n"
"ds_read_b64_tr_b16 v[212:213], v244 offset:42248\n"
"ds_read_b64_tr_b16 v[214:215], v244 offset:42504\n"
"v_mfma_f32_16x16x32_bf16 v[132:135], v[224:227], a[116:119], v[132:135]\n"
"s_barrier\n"
"v_mfma_f32_16x16x32_bf16 v[136:139], v[224:227], a[120:123], v[136:139]\n"
"v_mfma_f32_16x16x32_bf16 v[140:143], v[224:227], a[124:127], v[140:143]\n"
"ds_read_b64_tr_b16 v[216:217], v244 offset:42752\n"
"ds_read_b64_tr_b16 v[218:219], v244 offset:43008\n"
"v_mfma_f32_16x16x32_bf16 v[160:163], v[228:231], a[112:115], v[160:163]\n"
"v_cvt_pk_bf16_f32 v28, v28, v29\n"
"v_mfma_f32_16x16x32_bf16 v[164:167], v[228:231], a[116:119], v[164:167]\n"
"v_mfma_f32_16x16x32_bf16 v[168:171], v[228:231], a[120:123], v[168:171]\n"
"ds_read_b64_tr_b16 v[220:221], v244 offset:42760\n"
"ds_read_b64_tr_b16 v[222:223], v244 offset:43016\n"
"v_mfma_f32_16x16x32_bf16 v[172:175], v[228:231], a[124:127], v[172:175]\n"
"v_cvt_pk_bf16_f32 v29, v30, v31\n"
"v_mfma_f32_16x16x32_bf16 v[144:147], v[232:235], a[112:115], v[144:147]\n"
"v_mfma_f32_16x16x32_bf16 v[148:151], v[232:235], a[116:119], v[148:151]\n"
"v_cvt_pk_bf16_f32 v30, v60, v61\n"
"v_cvt_pk_bf16_f32 v31, v62, v63\n"
"v_mfma_f32_16x16x32_bf16 v[152:155], v[232:235], a[120:123], v[152:155]\n"
"v_mul_f32 v64, s95, v64\n"
"s_add_u32 m0, s74, 0x10fc0\n"
"v_mfma_f32_16x16x32_bf16 v[156:159], v[232:235], a[124:127], v[156:159]\n"
"buffer_load_dwordx4 v200, s[52:55], s75 offen offset:128 nt lds\n"
"v_mfma_f32_16x16x32_bf16 v[176:179], v[236:239], a[112:115], v[176:179]\n"
"ds_read_b64_tr_b16 v[224:225], v244 offset:50688\n"
"ds_read_b64_tr_b16 v[226:227], v244 offset:50944\n"
"v_mfma_f32_16x16x32_bf16 v[180:183], v[236:239], a[116:119], v[180:183]\n"
"ds_read_b64_tr_b16 v[228:229], v244 offset:50696\n"
"ds_read_b64_tr_b16 v[230:231], v244 offset:50952\n"
"v_mfma_f32_16x16x32_bf16 v[184:187], v[236:239], a[120:123], v[184:187]\n"
"ds_read_b64_tr_b16 v[232:233], v244 offset:51200\n"
"ds_read_b64_tr_b16 v[234:235], v244 offset:51456\n"
"v_mfma_f32_16x16x32_bf16 v[188:191], v[236:239], a[124:127], v[188:191]\n"
"v_mul_f32 v65, s95, v65\n"
"s_waitcnt lgkmcnt(6)\n"
"v_mfma_f32_16x16x32_bf16 v[128:131], v[208:211], a[128:131], v[128:131]\n"
"s_barrier\n"
"v_mfma_f32_16x16x32_bf16 v[132:135], v[208:211], a[132:135], v[132:135]\n"
"v_mfma_f32_16x16x32_bf16 v[136:139], v[208:211], a[136:139], v[136:139]\n"
"ds_read_b64_tr_b16 v[236:237], v244 offset:51208\n"
"ds_read_b64_tr_b16 v[238:239], v244 offset:51464\n"
"v_mfma_f32_16x16x32_bf16 v[140:143], v[208:211], a[140:143], v[140:143]\n"
"v_mul_f32 v66, s95, v66\n"
"v_mfma_f32_16x16x32_bf16 v[160:163], v[212:215], a[128:131], v[160:163]\n"
"v_mfma_f32_16x16x32_bf16 v[164:167], v[212:215], a[132:135], v[164:167]\n"
"v_mul_f32 v67, s95, v67\n"
"v_mul_f32 v96, s95, v96\n"
"v_mfma_f32_16x16x32_bf16 v[168:171], v[212:215], a[136:139], v[168:171]\n"
"v_mul_f32 v97, s95, v97\n"
"s_add_u32 m0, s74, 0x11800\n"
"v_mfma_f32_16x16x32_bf16 v[172:175], v[212:215], a[140:143], v[172:175]\n"
"buffer_load_dwordx4 v243, s[52:55], s75 offen offset:128 nt lds\n"
"v_mfma_f32_16x16x32_bf16 v[144:147], v[216:219], a[128:131], v[144:147]\n"
"v_mul_f32 v98, s95, v98\n"
"v_mul_f32 v99, s95, v99\n"
"v_mfma_f32_16x16x32_bf16 v[148:151], v[216:219], a[132:135], v[148:151]\n"
"v_cvt_pk_bf16_f32 v64, v64, v65\n"
"v_mfma_f32_16x16x32_bf16 v[152:155], v[216:219], a[136:139], v[152:155]\n"
"v_mfma_f32_16x16x32_bf16 v[156:159], v[216:219], a[140:143], v[156:159]\n"
"v_cvt_pk_bf16_f32 v65, v66, v67\n"
"v_cvt_pk_bf16_f32 v66, v96, v97\n"
"v_mfma_f32_16x16x32_bf16 v[176:179], v[220:223], a[128:131], v[176:179]\n"
"ds_read_b64_tr_b16 v[208:209], v244 offset:59136\n"
"ds_read_b64_tr_b16 v[210:211], v244 offset:59392\n"
"v_mfma_f32_16x16x32_bf16 v[180:183], v[220:223], a[132:135], v[180:183]\n"
"ds_read_b64_tr_b16 v[212:213], v244 offset:59144\n"
"ds_read_b64_tr_b16 v[214:215], v244 offset:59400\n"
"v_mfma_f32_16x16x32_bf16 v[184:187], v[220:223], a[136:139], v[184:187]\n"
"ds_read_b64_tr_b16 v[216:217], v244 offset:59648\n"
"ds_read_b64_tr_b16 v[218:219], v244 offset:59904\n"
"v_mfma_f32_16x16x32_bf16 v[188:191], v[220:223], a[140:143], v[188:191]\n"
"v_cvt_pk_bf16_f32 v67, v98, v99\n"
"s_waitcnt lgkmcnt(6)\n"
"v_mfma_f32_16x16x32_bf16 v[128:131], v[224:227], a[144:147], v[128:131]\n"
"ds_read_b64_tr_b16 v[220:221], v244 offset:59656\n"
"ds_read_b64_tr_b16 v[222:223], v244 offset:59912\n"
"v_mfma_f32_16x16x32_bf16 v[132:135], v[224:227], a[148:151], v[132:135]\n"
"v_mul_f32 v68, s95, v68\n"
"v_mul_f32 v69, s95, v69\n"
"v_mfma_f32_16x16x32_bf16 v[136:139], v[224:227], a[152:155], v[136:139]\n"
"s_barrier\n"
"s_add_u32 m0, s74, 0x11fc0\n"
"v_mfma_f32_16x16x32_bf16 v[140:143], v[224:227], a[156:159], v[140:143]\n"
"buffer_load_dwordx4 v200, s[52:55], s75 offen offset:256 nt lds\n"
"v_mfma_f32_16x16x32_bf16 v[160:163], v[228:231], a[144:147], v[160:163]\n"
"v_mul_f32 v70, s95, v70\n"
"v_mul_f32 v71, s95, v71\n"
"v_mfma_f32_16x16x32_bf16 v[164:167], v[228:231], a[148:151], v[164:167]\n"
"v_mul_f32 v100, s95, v100\n"
"v_mfma_f32_16x16x32_bf16 v[168:171], v[228:231], a[152:155], v[168:171]\n"
"v_mfma_f32_16x16x32_bf16 v[172:175], v[228:231], a[156:159], v[172:175]\n"
"v_mul_f32 v101, s95, v101\n"
"v_mul_f32 v102, s95, v102\n"
"v_mfma_f32_16x16x32_bf16 v[144:147], v[232:235], a[144:147], v[144:147]\n"
"v_mul_f32 v103, s95, v103\n"
"v_mfma_f32_16x16x32_bf16 v[148:151], v[232:235], a[148:151], v[148:151]\n"
"v_mfma_f32_16x16x32_bf16 v[152:155], v[232:235], a[152:155], v[152:155]\n"
"v_cvt_pk_bf16_f32 v68, v68, v69\n"
"v_cvt_pk_bf16_f32 v69, v70, v71\n"
"v_mfma_f32_16x16x32_bf16 v[156:159], v[232:235], a[156:159], v[156:159]\n"
"v_cvt_pk_bf16_f32 v70, v100, v101\n"
"v_mfma_f32_16x16x32_bf16 v[176:179], v[236:239], a[144:147], v[176:179]\n"
"v_mfma_f32_16x16x32_bf16 v[180:183], v[236:239], a[148:151], v[180:183]\n"
"ds_read_b64_tr_b16 v[224:225], v245 offset:0\n"
"ds_read_b64_tr_b16 v[226:227], v245 offset:256\n"
"v_mfma_f32_16x16x32_bf16 v[184:187], v[236:239], a[152:155], v[184:187]\n"
"v_cvt_pk_bf16_f32 v71, v102, v103\n"
"s_add_u32 m0, s74, 0x12800\n"
"v_mfma_f32_16x16x32_bf16 v[188:191], v[236:239], a[156:159], v[188:191]\n"
"buffer_load_dwordx4 v243, s[52:55], s75 offen offset:256 nt lds\n"
"s_waitcnt lgkmcnt(2)\n"
"v_mfma_f32_16x16x32_bf16 v[128:131], v[208:211], a[160:163], v[128:131]\n"
"ds_read_b64_tr_b16 v[228:229], v245 offset:8\n"
"ds_read_b64_tr_b16 v[230:231], v245 offset:264\n"
"v_mfma_f32_16x16x32_bf16 v[132:135], v[208:211], a[164:167], v[132:135]\n"
"s_barrier\n"
"v_mfma_f32_16x16x32_bf16 v[136:139], v[208:211], a[168:171], v[136:139]\n"
"v_mfma_f32_16x16x32_bf16 v[140:143], v[208:211], a[172:175], v[140:143]\n"
"ds_read_b64_tr_b16 v[232:233], v245 offset:512\n"
"ds_read_b64_tr_b16 v[234:235], v245 offset:768\n"
"v_mfma_f32_16x16x32_bf16 v[160:163], v[212:215], a[160:163], v[160:163]\n"
"v_mul_f32 v72, s95, v72\n"
"v_mfma_f32_16x16x32_bf16 v[164:167], v[212:215], a[164:167], v[164:167]\n"
"v_mfma_f32_16x16x32_bf16 v[168:171], v[212:215], a[168:171], v[168:171]\n"
"ds_read_b64_tr_b16 v[236:237], v245 offset:520\n"
"ds_read_b64_tr_b16 v[238:239], v245 offset:776\n"
"v_mfma_f32_16x16x32_bf16 v[172:175], v[212:215], a[172:175], v[172:175]\n"
"v_mul_f32 v73, s95, v73\n"
"v_mfma_f32_16x16x32_bf16 v[144:147], v[216:219], a[160:163], v[144:147]\n"
"v_mfma_f32_16x16x32_bf16 v[148:151], v[216:219], a[164:167], v[148:151]\n"
"v_mul_f32 v74, s95, v74\n"
"v_mul_f32 v75, s95, v75\n"
"v_mfma_f32_16x16x32_bf16 v[152:155], v[216:219], a[168:171], v[152:155]\n"
"v_mul_f32 v104, s95, v104\n"
"s_add_u32 m0, s74, 0x12fc0\n"
"v_mfma_f32_16x16x32_bf16 v[156:159], v[216:219], a[172:175], v[156:159]\n"
"buffer_load_dwordx4 v200, s[52:55], s75 offen offset:384 nt lds\n"
"v_mfma_f32_16x16x32_bf16 v[176:179], v[220:223], a[160:163], v[176:179]\n"
"ds_read_b64_tr_b16 v[208:209], v245 offset:16896\n"
"ds_read_b64_tr_b16 v[210:211], v245 offset:17152\n"
"v_mfma_f32_16x16x32_bf16 v[180:183], v[220:223], a[164:167], v[180:183]\n"
"ds_read_b64_tr_b16 v[212:213], v245 offset:16904\n"
"ds_read_b64_tr_b16 v[214:215], v245 offset:17160\n"
"v_mfma_f32_16x16x32_bf16 v[184:187], v[220:223], a[168:171], v[184:187]\n"
"ds_read_b64_tr_b16 v[216:217], v245 offset:17408\n"
"ds_read_b64_tr_b16 v[218:219], v245 offset:17664\n"
"v_mfma_f32_16x16x32_bf16 v[188:191], v[220:223], a[172:175], v[188:191]\n"
"v_mul_f32 v105, s95, v105\n"
"s_waitcnt lgkmcnt(6)\n"
"v_mfma_f32_16x16x32_bf16 v[128:131], v[224:227], a[176:179], v[128:131]\n"
"s_barrier\n"
"v_mfma_f32_16x16x32_bf16 v[132:135], v[224:227], a[180:183], v[132:135]\n"
"v_mfma_f32_16x16x32_bf16 v[136:139], v[224:227], a[184:187], v[136:139]\n"
"ds_read_b64_tr_b16 v[220:221], v245 offset:17416\n"
"ds_read_b64_tr_b16 v[222:223], v245 offset:17672\n"
"v_mfma_f32_16x16x32_bf16 v[140:143], v[224:227], a[188:191], v[140:143]\n"
"v_mul_f32 v106, s95, v106\n"
"v_mfma_f32_16x16x32_bf16 v[160:163], v[228:231], a[176:179], v[160:163]\n"
"v_mfma_f32_16x16x32_bf16 v[164:167], v[228:231], a[180:183], v[164:167]\n"
"v_mul_f32 v107, s95, v107\n"
"v_cvt_pk_bf16_f32 v72, v72, v73\n"
"v_mfma_f32_16x16x32_bf16 v[168:171], v[228:231], a[184:187], v[168:171]\n"
"v_cvt_pk_bf16_f32 v73, v74, v75\n"
"s_add_u32 m0, s74, 0x13800\n"
"v_mfma_f32_16x16x32_bf16 v[172:175], v[228:231], a[188:191], v[172:175]\n"
"buffer_load_dwordx4 v243, s[52:55], s75 offen offset:384 nt lds\n"
"v_mfma_f32_16x16x32_bf16 v[144:147], v[232:235], a[176:179], v[144:147]\n"
"v_cvt_pk_bf16_f32 v74, v104, v105\n"
"v_cvt_pk_bf16_f32 v75, v106, v107\n"
"v_mfma_f32_16x16x32_bf16 v[148:151], v[232:235], a[180:183], v[148:151]\n"
"v_mul_f32 v76, s95, v76\n"
"v_mfma_f32_16x16x32_bf16 v[152:155], v[232:235], a[184:187], v[152:155]\n"
"v_mfma_f32_16x16x32_bf16 v[156:159], v[232:235], a[188:191], v[156:159]\n"
"v_mul_f32 v77, s95, v77\n"
"v_mul_f32 v78, s95, v78\n"
"v_mfma_f32_16x16x32_bf16 v[176:179], v[236:239], a[176:179], v[176:179]\n"
"ds_read_b64_tr_b16 v[224:225], v245 offset:8448\n"
"ds_read_b64_tr_b16 v[226:227], v245 offset:8704\n"
"v_mfma_f32_16x16x32_bf16 v[180:183], v[236:239], a[180:183], v[180:183]\n"
"ds_read_b64_tr_b16 v[228:229], v245 offset:8456\n"
"ds_read_b64_tr_b16 v[230:231], v245 offset:8712\n"
"v_mfma_f32_16x16x32_bf16 v[184:187], v[236:239], a[184:187], v[184:187]\n"
"ds_read_b64_tr_b16 v[232:233], v245 offset:8960\n"
"ds_read_b64_tr_b16 v[234:235], v245 offset:9216\n"
"v_mfma_f32_16x16x32_bf16 v[188:191], v[236:239], a[188:191], v[188:191]\n"
"v_mul_f32 v79, s95, v79\n"
"s_waitcnt lgkmcnt(6)\n"
"v_mfma_f32_16x16x32_bf16 v[128:131], v[208:211], a[192:195], v[128:131]\n"
"ds_read_b64_tr_b16 v[236:237], v245 offset:8968\n"
"ds_read_b64_tr_b16 v[238:239], v245 offset:9224\n"
"v_mfma_f32_16x16x32_bf16 v[132:135], v[208:211], a[196:199], v[132:135]\n"
"v_mul_f32 v108, s95, v108\n"
"v_mul_f32 v109, s95, v109\n"
"v_mfma_f32_16x16x32_bf16 v[136:139], v[208:211], a[200:203], v[136:139]\n"
"s_barrier\n"
"s_add_u32 m0, s73, 0x6300\n"
"s_mov_b32 s75, 0x8000\n"
"v_mfma_f32_16x16x32_bf16 v[140:143], v[208:211], a[204:207], v[140:143]\n"
"buffer_load_dwordx4 v199, s[48:51], s75 offen offset:0 nt lds\n"
"v_mfma_f32_16x16x32_bf16 v[160:163], v[212:215], a[192:195], v[160:163]\n"
"v_mul_f32 v110, s95, v110\n"
"v_mul_f32 v111, s95, v111\n"
"v_mfma_f32_16x16x32_bf16 v[164:167], v[212:215], a[196:199], v[164:167]\n"
"v_cvt_pk_bf16_f32 v76, v76, v77\n"
"v_mfma_f32_16x16x32_bf16 v[168:171], v[212:215], a[200:203], v[168:171]\n"
"v_mfma_f32_16x16x32_bf16 v[172:175], v[212:215], a[204:207], v[172:175]\n"
"v_cvt_pk_bf16_f32 v77, v78, v79\n"
"v_cvt_pk_bf16_f32 v78, v108, v109\n"
"v_mfma_f32_16x16x32_bf16 v[144:147], v[216:219], a[192:195], v[144:147]\n"
"v_cvt_pk_bf16_f32 v79, v110, v111\n"
"v_mfma_f32_16x16x32_bf16 v[148:151], v[216:219], a[196:199], v[148:151]\n"
"v_mfma_f32_16x16x32_bf16 v[152:155], v[216:219], a[200:203], v[152:155]\n"
"v_mul_f32 v80, s95, v80\n"
"v_mul_f32 v81, s95, v81\n"
"v_mfma_f32_16x16x32_bf16 v[156:159], v[216:219], a[204:207], v[156:159]\n"
"v_mul_f32 v82, s95, v82\n"
"v_mfma_f32_16x16x32_bf16 v[176:179], v[220:223], a[192:195], v[176:179]\n"
"v_mfma_f32_16x16x32_bf16 v[180:183], v[220:223], a[196:199], v[180:183]\n"
"ds_read_b64_tr_b16 v[208:209], v244 offset:33792\n"
"ds_read_b64_tr_b16 v[210:211], v244 offset:34048\n"
"v_mfma_f32_16x16x32_bf16 v[184:187], v[220:223], a[200:203], v[184:187]\n"
"v_mul_f32 v83, s95, v83\n"
"s_add_u32 m0, s73, 0x8400\n"
"v_mfma_f32_16x16x32_bf16 v[188:191], v[220:223], a[204:207], v[188:191]\n"
"buffer_load_dwordx4 v242, s[48:51], s75 offen offset:0 nt lds\n"
"s_waitcnt lgkmcnt(2)\n"
"v_mfma_f32_16x16x32_bf16 v[128:131], v[224:227], a[208:211], v[128:131]\n"
"ds_read_b64_tr_b16 v[212:213], v244 offset:33800\n"
"ds_read_b64_tr_b16 v[214:215], v244 offset:34056\n"
"v_mfma_f32_16x16x32_bf16 v[132:135], v[224:227], a[212:215], v[132:135]\n"
"s_waitcnt vmcnt(26)\n"
"s_barrier\n"
"v_mfma_f32_16x16x32_bf16 v[136:139], v[224:227], a[216:219], v[136:139]\n"
"v_mfma_f32_16x16x32_bf16 v[140:143], v[224:227], a[220:223], v[140:143]\n"
"ds_read_b64_tr_b16 v[216:217], v244 offset:34304\n"
"ds_read_b64_tr_b16 v[218:219], v244 offset:34560\n"
"v_mfma_f32_16x16x32_bf16 v[160:163], v[228:231], a[208:211], v[160:163]\n"
"v_mul_f32 v112, s95, v112\n"
"v_mfma_f32_16x16x32_bf16 v[164:167], v[228:231], a[212:215], v[164:167]\n"
"v_mfma_f32_16x16x32_bf16 v[168:171], v[228:231], a[216:219], v[168:171]\n"
"ds_read_b64_tr_b16 v[220:221], v244 offset:34312\n"
"ds_read_b64_tr_b16 v[222:223], v244 offset:34568\n"
"v_mfma_f32_16x16x32_bf16 v[172:175], v[228:231], a[220:223], v[172:175]\n"
"v_mul_f32 v113, s95, v113\n"
"v_mfma_f32_16x16x32_bf16 v[144:147], v[232:235], a[208:211], v[144:147]\n"
"v_mfma_f32_16x16x32_bf16 v[148:151], v[232:235], a[212:215], v[148:151]\n"
"v_mul_f32 v114, s95, v114\n"
"v_mul_f32 v115, s95, v115\n"
"v_mfma_f32_16x16x32_bf16 v[152:155], v[232:235], a[216:219], v[152:155]\n"
"v_cvt_pk_bf16_f32 v80, v80, v81\n"
"s_add_u32 m0, s73, 0x6520\n"
"v_mfma_f32_16x16x32_bf16 v[156:159], v[232:235], a[220:223], v[156:159]\n"
"buffer_load_dwordx4 v199, s[48:51], s75 offen offset:512 nt lds\n"
"v_mfma_f32_16x16x32_bf16 v[176:179], v[236:239], a[208:211], v[176:179]\n"
"ds_read_b64_tr_b16 v[224:225], v244 offset:42240\n"
"ds_read_b64_tr_b16 v[226:227], v244 offset:42496\n"
"v_mfma_f32_16x16x32_bf16 v[180:183], v[236:239], a[212:215], v[180:183]\n"
"ds_read_b64_tr_b16 v[228:229], v244 offset:42248\n"
"ds_read_b64_tr_b16 v[230:231], v244 offset:42504\n"
"v_mfma_f32_16x16x32_bf16 v[184:187], v[236:239], a[216:219], v[184:187]\n"
"ds_read_b64_tr_b16 v[232:233], v244 offset:42752\n"
"ds_read_b64_tr_b16 v[234:235], v244 offset:43008\n"
"v_mfma_f32_16x16x32_bf16 v[188:191], v[236:239], a[220:223], v[188:191]\n"
"v_cvt_pk_bf16_f32 v81, v82, v83\n"
"s_waitcnt lgkmcnt(6)\n"
"v_mfma_f32_16x16x32_bf16 v[32:35], v[208:211], a[0:3], 0\n"
"s_barrier\n"
"v_mfma_f32_16x16x32_bf16 v[36:39], v[208:211], a[4:7], 0\n"
"v_mfma_f32_16x16x32_bf16 v[40:43], v[208:211], a[8:11], 0\n"
"ds_read_b64_tr_b16 v[236:237], v244 offset:42760\n"
"ds_read_b64_tr_b16 v[238:239], v244 offset:43016\n"
"v_mfma_f32_16x16x32_bf16 v[44:47], v[208:211], a[12:15], 0\n"
"v_cvt_pk_bf16_f32 v82, v112, v113\n"
"v_mfma_f32_16x16x32_bf16 v[96:99], v[212:215], a[0:3], 0\n"
"v_mfma_f32_16x16x32_bf16 v[100:103], v[212:215], a[4:7], 0\n"
"v_cvt_pk_bf16_f32 v83, v114, v115\n"
"v_mul_f32 v84, s95, v84\n"
"v_mfma_f32_16x16x32_bf16 v[104:107], v[212:215], a[8:11], 0\n"
"v_mul_f32 v85, s95, v85\n"
"s_add_u32 m0, s73, 0x8620\n"
"v_mfma_f32_16x16x32_bf16 v[108:111], v[212:215], a[12:15], 0\n"
"buffer_load_dwordx4 v242, s[48:51], s75 offen offset:512 nt lds\n"
"v_mfma_f32_16x16x32_bf16 v[48:51], v[216:219], a[0:3], 0\n"
"v_mul_f32 v86, s95, v86\n"
"v_mul_f32 v87, s95, v87\n"
"v_mfma_f32_16x16x32_bf16 v[52:55], v[216:219], a[4:7], 0\n"
"v_mul_f32 v116, s95, v116\n"
"v_mfma_f32_16x16x32_bf16 v[56:59], v[216:219], a[8:11], 0\n"
"v_mfma_f32_16x16x32_bf16 v[60:63], v[216:219], a[12:15], 0\n"
"v_mul_f32 v117, s95, v117\n"
"v_mul_f32 v118, s95, v118\n"
"v_mfma_f32_16x16x32_bf16 v[112:115], v[220:223], a[0:3], 0\n"
"ds_read_b64_tr_b16 v[208:209], v244 offset:50688\n"
"ds_read_b64_tr_b16 v[210:211], v244 offset:50944\n"
"v_mul_f32 v119, s95, v119\n"
"v_cvt_pk_bf16_f32 v84, v84, v85\n"
"v_cvt_pk_bf16_f32 v85, v86, v87\n"
"v_cvt_pk_bf16_f32 v86, v116, v117\n"
"v_cvt_pk_bf16_f32 v87, v118, v119\n"
"v_mfma_f32_16x16x32_bf16 v[116:119], v[220:223], a[4:7], 0\n"
"ds_read_b64_tr_b16 v[212:213], v244 offset:50696\n"
"ds_read_b64_tr_b16 v[214:215], v244 offset:50952\n"
"v_mul_f32 v88, s95, v88\n"
"v_mul_f32 v89, s95, v89\n"
"v_mul_f32 v90, s95, v90\n"
"v_mul_f32 v91, s95, v91\n"
"v_mul_f32 v120, s95, v120\n"
"v_mul_f32 v121, s95, v121\n"
"v_mul_f32 v122, s95, v122\n"
"v_mul_f32 v123, s95, v123\n"
"v_cvt_pk_bf16_f32 v88, v88, v89\n"
"v_cvt_pk_bf16_f32 v89, v90, v91\n"
"v_cvt_pk_bf16_f32 v90, v120, v121\n"
"v_cvt_pk_bf16_f32 v91, v122, v123\n"
"v_mfma_f32_16x16x32_bf16 v[120:123], v[220:223], a[8:11], 0\n"
"ds_read_b64_tr_b16 v[216:217], v244 offset:51200\n"
"ds_read_b64_tr_b16 v[218:219], v244 offset:51456\n"
"v_mul_f32 v92, s95, v92\n"
"v_mul_f32 v93, s95, v93\n"
"v_mul_f32 v94, s95, v94\n"
"v_mul_f32 v95, s95, v95\n"
"v_mul_f32 v124, s95, v124\n"
"v_mul_f32 v125, s95, v125\n"
"v_mul_f32 v126, s95, v126\n"
"v_mul_f32 v127, s95, v127\n"
"v_cvt_pk_bf16_f32 v92, v92, v93\n"
"v_cvt_pk_bf16_f32 v93, v94, v95\n"
"v_cvt_pk_bf16_f32 v94, v124, v125\n"
"v_cvt_pk_bf16_f32 v95, v126, v127\n"
"v_mfma_f32_16x16x32_bf16 v[124:127], v[220:223], a[12:15], 0\n"
"s_sub_u32 s31, 32, s99\n"
"s_waitcnt lgkmcnt(6)\n"
"v_mfma_f32_16x16x32_bf16 v[32:35], v[224:227], a[16:19], v[32:35]\n"
"ds_read_b64_tr_b16 v[220:221], v244 offset:51208\n"
"ds_read_b64_tr_b16 v[222:223], v244 offset:51464\n"
"v_mfma_f32_16x16x32_bf16 v[36:39], v[224:227], a[20:23], v[36:39]\n"
"s_add_u32 s40, s64, s31\n"
"s_addc_u32 s41, s65, 0\n"
"v_mfma_f32_16x16x32_bf16 v[40:43], v[224:227], a[24:27], v[40:43]\n"
"s_sub_u32 s42, s66, s31\n"
"s_add_u32 m0, s73, 0x6740\n"
"v_mfma_f32_16x16x32_bf16 v[44:47], v[224:227], a[28:31], v[44:47]\n"
"buffer_load_dwordx4 v199, s[48:51], s75 offen offset:1024 nt lds\n"
"v_mfma_f32_16x16x32_bf16 v[96:99], v[228:231], a[16:19], v[96:99]\n"
"s_cmp_lt_u32 s90, 6\n"
"s_cselect_b32 s66, s66, 0\n"
"v_mfma_f32_16x16x32_bf16 v[100:103], v[228:231], a[20:23], v[100:103]\n"
"v_mul_f32 v128, s95, v128\n"
"v_mul_f32 v129, s95, v129\n"
"v_mfma_f32_16x16x32_bf16 v[104:107], v[228:231], a[24:27], v[104:107]\n"
"v_mul_f32 v130, s95, v130\n"
"v_mul_f32 v131, s95, v131\n"
"v_mfma_f32_16x16x32_bf16 v[108:111], v[228:231], a[28:31], v[108:111]\n"
"v_mul_f32 v160, s95, v160\n"
"v_mul_f32 v161, s95, v161\n"
"v_mfma_f32_16x16x32_bf16 v[48:51], v[232:235], a[16:19], v[48:51]\n"
"v_mul_f32 v162, s95, v162\n"
"v_mul_f32 v163, s95, v163\n"
"v_mfma_f32_16x16x32_bf16 v[52:55], v[232:235], a[20:23], v[52:55]\n"
"v_cvt_pk_bf16_f32 v128, v128, v129\n"
"v_cvt_pk_bf16_f32 v129, v130, v131\n"
"v_mfma_f32_16x16x32_bf16 v[56:59], v[232:235], a[24:27], v[56:59]\n"
"v_cvt_pk_bf16_f32 v130, v160, v161\n"
"v_cvt_pk_bf16_f32 v131, v162, v163\n"
"v_mfma_f32_16x16x32_bf16 v[60:63], v[232:235], a[28:31], v[60:63]\n"
"v_mul_f32 v132, s95, v132\n"
"v_mul_f32 v133, s95, v133\n"
"v_mfma_f32_16x16x32_bf16 v[112:115], v[236:239], a[16:19], v[112:115]\n"
"v_mul_f32 v134, s95, v134\n"
"v_mul_f32 v135, s95, v135\n"
"v_mfma_f32_16x16x32_bf16 v[116:119], v[236:239], a[20:23], v[116:119]\n"
"v_mul_f32 v164, s95, v164\n"
"v_mul_f32 v165, s95, v165\n"
"v_mfma_f32_16x16x32_bf16 v[120:123], v[236:239], a[24:27], v[120:123]\n"
"v_mul_f32 v166, s95, v166\n"
"s_add_u32 m0, s73, 0x8840\n"
"v_mfma_f32_16x16x32_bf16 v[124:127], v[236:239], a[28:31], v[124:127]\n"
"buffer_load_dwordx4 v242, s[48:51], s75 offen offset:1024 nt lds\n"
"s_waitcnt lgkmcnt(0)\n"
"v_mfma_f32_16x16x32_bf16 v[32:35], v[208:211], a[32:35], v[32:35]\n"
"v_mul_f32 v167, s95, v167\n"
"v_cvt_pk_bf16_f32 v132, v132, v133\n"
"v_mfma_f32_16x16x32_bf16 v[36:39], v[208:211], a[36:39], v[36:39]\n"
"s_barrier\n"
"v_mfma_f32_16x16x32_bf16 v[40:43], v[208:211], a[40:43], v[40:43]\n"
"v_mfma_f32_16x16x32_bf16 v[44:47], v[208:211], a[44:47], v[44:47]\n"
"ds_read_b64_tr_b16 v[224:225], v244 offset:59136\n"
"ds_read_b64_tr_b16 v[226:227], v244 offset:59392\n"
"v_mfma_f32_16x16x32_bf16 v[96:99], v[212:215], a[32:35], v[96:99]\n"
"v_cvt_pk_bf16_f32 v133, v134, v135\n"
"v_mfma_f32_16x16x32_bf16 v[100:103], v[212:215], a[36:39], v[100:103]\n"
"v_mfma_f32_16x16x32_bf16 v[104:107], v[212:215], a[40:43], v[104:107]\n"
"ds_read_b64_tr_b16 v[228:229], v244 offset:59144\n"
"ds_read_b64_tr_b16 v[230:231], v244 offset:59400\n"
"v_mfma_f32_16x16x32_bf16 v[108:111], v[212:215], a[44:47], v[108:111]\n"
"v_cvt_pk_bf16_f32 v134, v164, v165\n"
"v_mfma_f32_16x16x32_bf16 v[48:51], v[216:219], a[32:35], v[48:51]\n"
"v_mfma_f32_16x16x32_bf16 v[52:55], v[216:219], a[36:39], v[52:55]\n"
"ds_read_b64_tr_b16 v[232:233], v244 offset:59648\n"
"ds_read_b64_tr_b16 v[234:235], v244 offset:59904\n"
"ds_read_b64_tr_b16 v[236:237], v244 offset:59656\n"
"ds_read_b64_tr_b16 v[238:239], v244 offset:59912\n"
"v_mfma_f32_16x16x32_bf16 v[56:59], v[216:219], a[40:43], v[56:59]\n"
"v_cvt_pk_bf16_f32 v135, v166, v167\n"
"s_add_u32 m0, s73, 0x6960\n"
"v_mfma_f32_16x16x32_bf16 v[60:63], v[216:219], a[44:47], v[60:63]\n"
"buffer_load_dwordx4 v199, s[48:51], s75 offen offset:1536 nt lds\n"
"v_mfma_f32_16x16x32_bf16 v[112:115], v[220:223], a[32:35], v[112:115]\n"
"ds_read_b64_tr_b16 v[208:209], v245 offset:0\n"
"ds_read_b64_tr_b16 v[210:211], v245 offset:256\n"
"v_mfma_f32_16x16x32_bf16 v[116:119], v[220:223], a[36:39], v[116:119]\n"
"ds_read_b64_tr_b16 v[212:213], v245 offset:8\n"
"ds_read_b64_tr_b16 v[214:215], v245 offset:264\n"
"v_mfma_f32_16x16x32_bf16 v[120:123], v[220:223], a[40:43], v[120:123]\n"
"ds_read_b64_tr_b16 v[216:217], v245 offset:512\n"
"ds_read_b64_tr_b16 v[218:219], v245 offset:768\n"
"v_mfma_f32_16x16x32_bf16 v[124:127], v[220:223], a[44:47], v[124:127]\n"
"v_mul_f32 v136, s95, v136\n"
"s_waitcnt lgkmcnt(6)\n"
"v_mfma_f32_16x16x32_bf16 v[32:35], v[224:227], a[48:51], v[32:35]\n"
"s_barrier\n"
"v_mfma_f32_16x16x32_bf16 v[36:39], v[224:227], a[52:55], v[36:39]\n"
"v_mfma_f32_16x16x32_bf16 v[40:43], v[224:227], a[56:59], v[40:43]\n"
"ds_read_b64_tr_b16 v[220:221], v245 offset:520\n"
"ds_read_b64_tr_b16 v[222:223], v245 offset:776\n"
"v_mfma_f32_16x16x32_bf16 v[44:47], v[224:227], a[60:63], v[44:47]\n"
"v_mul_f32 v137, s95, v137\n"
"v_mfma_f32_16x16x32_bf16 v[96:99], v[228:231], a[48:51], v[96:99]\n"
"v_mfma_f32_16x16x32_bf16 v[100:103], v[228:231], a[52:55], v[100:103]\n"
"v_mul_f32 v138, s95, v138\n"
"v_mul_f32 v139, s95, v139\n"
"v_mfma_f32_16x16x32_bf16 v[104:107], v[228:231], a[56:59], v[104:107]\n"
"v_mul_f32 v168, s95, v168\n"
"s_add_u32 m0, s73, 0x8a60\n"
"v_mfma_f32_16x16x32_bf16 v[108:111], v[228:231], a[60:63], v[108:111]\n"
"buffer_load_dwordx4 v242, s[48:51], s75 offen offset:1536 nt lds\n"
"v_mfma_f32_16x16x32_bf16 v[48:51], v[232:235], a[48:51], v[48:51]\n"
"v_mul_f32 v169, s95, v169\n"
"v_mul_f32 v170, s95, v170\n"
"v_mfma_f32_16x16x32_bf16 v[52:55], v[232:235], a[52:55], v[52:55]\n"
"v_mul_f32 v171, s95, v171\n"
"v_mfma_f32_16x16x32_bf16 v[56:59], v[232:235], a[56:59], v[56:59]\n"
"v_mfma_f32_16x16x32_bf16 v[60:63], v[232:235], a[60:63], v[60:63]\n"
"v_cvt_pk_bf16_f32 v136, v136, v137\n"
"v_cvt_pk_bf16_f32 v137, v138, v139\n"
"v_mfma_f32_16x16x32_bf16 v[112:115], v[236:239], a[48:51], v[112:115]\n"
"ds_read_b64_tr_b16 v[224:225], v245 offset:16896\n"
"ds_read_b64_tr_b16 v[226:227], v245 offset:17152\n"
"v_mfma_f32_16x16x32_bf16 v[116:119], v[236:239], a[52:55], v[116:119]\n"
"ds_read_b64_tr_b16 v[228:229], v245 offset:16904\n"
"ds_read_b64_tr_b16 v[230:231], v245 offset:17160\n"
"v_mfma_f32_16x16x32_bf16 v[120:123], v[236:239], a[56:59], v[120:123]\n"
"ds_read_b64_tr_b16 v[232:233], v245 offset:17408\n"
"ds_read_b64_tr_b16 v[234:235], v245 offset:17664\n"
"v_mfma_f32_16x16x32_bf16 v[124:127], v[236:239], a[60:63], v[124:127]\n"
"v_cvt_pk_bf16_f32 v138, v168, v169\n"
"s_waitcnt lgkmcnt(6)\n"
"v_mfma_f32_16x16x32_bf16 v[32:35], v[208:211], a[64:67], v[32:35]\n"
"ds_read_b64_tr_b16 v[236:237], v245 offset:17416\n"
"ds_read_b64_tr_b16 v[238:239], v245 offset:17672\n"
"v_mfma_f32_16x16x32_bf16 v[36:39], v[208:211], a[68:71], v[36:39]\n"
"v_cvt_pk_bf16_f32 v139, v170, v171\n"
"v_mul_f32 v140, s95, v140\n"
"v_mfma_f32_16x16x32_bf16 v[40:43], v[208:211], a[72:75], v[40:43]\n"
"s_barrier\n"
"s_add_u32 m0, s73, 0xa500\n"
"s_mov_b32 s75, 0xc000\n"
"v_mfma_f32_16x16x32_bf16 v[44:47], v[208:211], a[76:79], v[44:47]\n"
"buffer_load_dwordx4 v199, s[48:51], s75 offen offset:0 nt lds\n"
"v_mfma_f32_16x16x32_bf16 v[96:99], v[212:215], a[64:67], v[96:99]\n"
"v_mul_f32 v141, s95, v141\n"
"v_mul_f32 v142, s95, v142\n"
"v_mfma_f32_16x16x32_bf16 v[100:103], v[212:215], a[68:71], v[100:103]\n"
"v_mul_f32 v143, s95, v143\n"
"v_mfma_f32_16x16x32_bf16 v[104:107], v[212:215], a[72:75], v[104:107]\n"
"v_mfma_f32_16x16x32_bf16 v[108:111], v[212:215], a[76:79], v[108:111]\n"
"v_mul_f32 v172, s95, v172\n"
"v_mul_f32 v173, s95, v173\n"
"v_mfma_f32_16x16x32_bf16 v[48:51], v[216:219], a[64:67], v[48:51]\n"
"v_mul_f32 v174, s95, v174\n"
"v_mfma_f32_16x16x32_bf16 v[52:55], v[216:219], a[68:71], v[52:55]\n"
"v_mfma_f32_16x16x32_bf16 v[56:59], v[216:219], a[72:75], v[56:59]\n"
"v_mul_f32 v175, s95, v175\n"
"v_cvt_pk_bf16_f32 v140, v140, v141\n"
"v_mfma_f32_16x16x32_bf16 v[60:63], v[216:219], a[76:79], v[60:63]\n"
"v_cvt_pk_bf16_f32 v141, v142, v143\n"
"v_mfma_f32_16x16x32_bf16 v[112:115], v[220:223], a[64:67], v[112:115]\n"
"v_mfma_f32_16x16x32_bf16 v[116:119], v[220:223], a[68:71], v[116:119]\n"
"ds_read_b64_tr_b16 v[208:209], v245 offset:8448\n"
"ds_read_b64_tr_b16 v[210:211], v245 offset:8704\n"
"v_mfma_f32_16x16x32_bf16 v[120:123], v[220:223], a[72:75], v[120:123]\n"
"v_cvt_pk_bf16_f32 v142, v172, v173\n"
"s_add_u32 m0, s73, 0xc600\n"
"v_mfma_f32_16x16x32_bf16 v[124:127], v[220:223], a[76:79], v[124:127]\n"
"buffer_load_dwordx4 v242, s[48:51], s75 offen offset:0 nt lds\n"
"s_waitcnt lgkmcnt(2)\n"
"v_mfma_f32_16x16x32_bf16 v[32:35], v[224:227], a[80:83], v[32:35]\n"
"ds_read_b64_tr_b16 v[212:213], v245 offset:8456\n"
"ds_read_b64_tr_b16 v[214:215], v245 offset:8712\n"
"v_mfma_f32_16x16x32_bf16 v[36:39], v[224:227], a[84:87], v[36:39]\n"
"s_barrier\n"
"v_mfma_f32_16x16x32_bf16 v[40:43], v[224:227], a[88:91], v[40:43]\n"
"v_mfma_f32_16x16x32_bf16 v[44:47], v[224:227], a[92:95], v[44:47]\n"
"ds_read_b64_tr_b16 v[216:217], v245 offset:8960\n"
"ds_read_b64_tr_b16 v[218:219], v245 offset:9216\n"
"v_mfma_f32_16x16x32_bf16 v[96:99], v[228:231], a[80:83], v[96:99]\n"
"v_cvt_pk_bf16_f32 v143, v174, v175\n"
"v_mfma_f32_16x16x32_bf16 v[100:103], v[228:231], a[84:87], v[100:103]\n"
"v_mfma_f32_16x16x32_bf16 v[104:107], v[228:231], a[88:91], v[104:107]\n"
"ds_read_b64_tr_b16 v[220:221], v245 offset:8968\n"
"ds_read_b64_tr_b16 v[222:223], v245 offset:9224\n"
"v_mfma_f32_16x16x32_bf16 v[108:111], v[228:231], a[92:95], v[108:111]\n"
"v_mul_f32 v144, s95, v144\n"
"v_mfma_f32_16x16x32_bf16 v[48:51], v[232:235], a[80:83], v[48:51]\n"
"v_mfma_f32_16x16x32_bf16 v[52:55], v[232:235], a[84:87], v[52:55]\n"
"v_mul_f32 v145, s95, v145\n"
"v_mul_f32 v146, s95, v146\n"
"v_mfma_f32_16x16x32_bf16 v[56:59], v[232:235], a[88:91], v[56:59]\n"
"v_mul_f32 v147, s95, v147\n"
"s_add_u32 m0, s73, 0xa720\n"
"v_mfma_f32_16x16x32_bf16 v[60:63], v[232:235], a[92:95], v[60:63]\n"
"buffer_load_dwordx4 v199, s[48:51], s75 offen offset:512 nt lds\n"
"v_mfma_f32_16x16x32_bf16 v[112:115], v[236:239], a[80:83], v[112:115]\n"
"ds_read_b64_tr_b16 v[224:225], v244 offset:33792\n"
"ds_read_b64_tr_b16 v[226:227], v244 offset:34048\n"
"v_mfma_f32_16x16x32_bf16 v[116:119], v[236:239], a[84:87], v[116:119]\n"
"ds_read_b64_tr_b16 v[228:229], v244 offset:33800\n"
"ds_read_b64_tr_b16 v[230:231], v244 offset:34056\n"
"v_mfma_f32_16x16x32_bf16 v[120:123], v[236:239], a[88:91], v[120:123]\n"
"ds_read_b64_tr_b16 v[232:233], v244 offset:34304\n"
"ds_read_b64_tr_b16 v[234:235], v244 offset:34560\n"
"v_mfma_f32_16x16x32_bf16 v[124:127], v[236:239], a[92:95], v[124:127]\n"
"v_mul_f32 v176, s95, v176\n"
"s_waitcnt lgkmcnt(6)\n"
"v_mfma_f32_16x16x32_bf16 v[32:35], v[208:211], a[96:99], v[32:35]\n"
"s_barrier\n"
"v_mfma_f32_16x16x32_bf16 v[36:39], v[208:211], a[100:103], v[36:39]\n"
"v_mfma_f32_16x16x32_bf16 v[40:43], v[208:211], a[104:107], v[40:43]\n"
"ds_read_b64_tr_b16 v[236:237], v244 offset:34312\n"
"ds_read_b64_tr_b16 v[238:239], v244 offset:34568\n"
"v_mfma_f32_16x16x32_bf16 v[44:47], v[208:211], a[108:111], v[44:47]\n"
"v_mul_f32 v177, s95, v177\n"
"v_mfma_f32_16x16x32_bf16 v[96:99], v[212:215], a[96:99], v[96:99]\n"
"v_mfma_f32_16x16x32_bf16 v[100:103], v[212:215], a[100:103], v[100:103]\n"
"v_mul_f32 v178, s95, v178\n"
"v_mul_f32 v179, s95, v179\n"
"v_mfma_f32_16x16x32_bf16 v[104:107], v[212:215], a[104:107], v[104:107]\n"
"v_cvt_pk_bf16_f32 v144, v144, v145\n"
"s_add_u32 m0, s73, 0xc820\n"
"v_mfma_f32_16x16x32_bf16 v[108:111], v[212:215], a[108:111], v[108:111]\n"
"buffer_load_dwordx4 v242, s[48:51], s75 offen offset:512 nt lds\n"
"v_mfma_f32_16x16x32_bf16 v[48:51], v[216:219], a[96:99], v[48:51]\n"
"v_cvt_pk_bf16_f32 v145, v146, v147\n"
"v_cvt_pk_bf16_f32 v146, v176, v177\n"
"v_mfma_f32_16x16x32_bf16 v[52:55], v[216:219], a[100:103], v[52:55]\n"
"v_cvt_pk_bf16_f32 v147, v178, v179\n"
"v_mfma_f32_16x16x32_bf16 v[56:59], v[216:219], a[104:107], v[56:59]\n"
"v_mfma_f32_16x16x32_bf16 v[60:63], v[216:219], a[108:111], v[60:63]\n"
"v_mul_f32 v148, s95, v148\n"
"v_mul_f32 v149, s95, v149\n"
"v_mfma_f32_16x16x32_bf16 v[112:115], v[220:223], a[96:99], v[112:115]\n"
"v_mul_f32 v150, s95, v150\n"
"v_mul_f32 v151, s95, v151\n"
"v_mfma_f32_16x16x32_bf16 v[116:119], v[220:223], a[100:103], v[116:119]\n"
"v_mul_f32 v180, s95, v180\n"
"v_mul_f32 v181, s95, v181\n"
"v_mfma_f32_16x16x32_bf16 v[120:123], v[220:223], a[104:107], v[120:123]\n"
"v_mul_f32 v182, s95, v182\n"
"v_mul_f32 v183, s95, v183\n"
"v_mfma_f32_16x16x32_bf16 v[124:127], v[220:223], a[108:111], v[124:127]\n"
"v_cvt_pk_bf16_f32 v148, v148, v149\n"
"s_waitcnt lgkmcnt(0)\n"
"v_mfma_f32_16x16x32_bf16 v[32:35], v[224:227], a[112:115], v[32:35]\n"
"v_cvt_pk_bf16_f32 v149, v150, v151\n"
"v_cvt_pk_bf16_f32 v150, v180, v181\n"
"v_mfma_f32_16x16x32_bf16 v[36:39], v[224:227], a[116:119], v[36:39]\n"
"v_cvt_pk_bf16_f32 v151, v182, v183\n"
"v_mul_f32 v152, s95, v152\n"
"v_mfma_f32_16x16x32_bf16 v[40:43], v[224:227], a[120:123], v[40:43]\n"
"v_mul_f32 v153, s95, v153\n"
"s_add_u32 m0, s73, 0xa940\n"
"v_mfma_f32_16x16x32_bf16 v[44:47], v[224:227], a[124:127], v[44:47]\n"
"buffer_load_dwordx4 v199, s[48:51], s75 offen offset:1024 nt lds\n"
"v_mfma_f32_16x16x32_bf16 v[96:99], v[228:231], a[112:115], v[96:99]\n"
"s_barrier\n"
"ds_read_b64_tr_b16 v[208:209], v244 offset:50688\n"
"ds_read_b64_tr_b16 v[210:211], v244 offset:50944\n"
"v_mfma_f32_16x16x32_bf16 v[100:103], v[228:231], a[116:119], v[100:103]\n"
"ds_read_b64_tr_b16 v[212:213], v244 offset:50696\n"
"ds_read_b64_tr_b16 v[214:215], v244 offset:50952\n"
"v_mfma_f32_16x16x32_bf16 v[104:107], v[228:231], a[120:123], v[104:107]\n"
"ds_read_b64_tr_b16 v[216:217], v244 offset:51200\n"
"ds_read_b64_tr_b16 v[218:219], v244 offset:51456\n"
"v_mfma_f32_16x16x32_bf16 v[108:111], v[228:231], a[124:127], v[108:111]\n"
"ds_read_b64_tr_b16 v[220:221], v244 offset:51208\n"
"ds_read_b64_tr_b16 v[222:223], v244 offset:51464\n"
"v_mfma_f32_16x16x32_bf16 v[48:51], v[232:235], a[112:115], v[48:51]\n"
"v_mul_f32 v154, s95, v154\n"
"v_mul_f32 v155, s95, v155\n"
"v_mfma_f32_16x16x32_bf16 v[52:55], v[232:235], a[116:119], v[52:55]\n"
"v_mul_f32 v184, s95, v184\n"
"v_mul_f32 v185, s95, v185\n"
"v_mfma_f32_16x16x32_bf16 v[56:59], v[232:235], a[120:123], v[56:59]\n"
"v_mul_f32 v186, s95, v186\n"
"v_mul_f32 v187, s95, v187\n"
"v_mfma_f32_16x16x32_bf16 v[60:63], v[232:235], a[124:127], v[60:63]\n"
"v_cvt_pk_bf16_f32 v152, v152, v153\n"
"v_cvt_pk_bf16_f32 v153, v154, v155\n"
"v_mfma_f32_16x16x32_bf16 v[112:115], v[236:239], a[112:115], v[112:115]\n"
"v_cvt_pk_bf16_f32 v154, v184, v185\n"
"v_cvt_pk_bf16_f32 v155, v186, v187\n"
"v_mfma_f32_16x16x32_bf16 v[116:119], v[236:239], a[116:119], v[116:119]\n"
"v_mul_f32 v156, s95, v156\n"
"v_mul_f32 v157, s95, v157\n"
"v_mfma_f32_16x16x32_bf16 v[120:123], v[236:239], a[120:123], v[120:123]\n"
"v_mul_f32 v158, s95, v158\n"
"s_add_u32 m0, s73, 0xca40\n"
"v_mfma_f32_16x16x32_bf16 v[124:127], v[236:239], a[124:127], v[124:127]\n"
"buffer_load_dwordx4 v242, s[48:51], s75 offen offset:1024 nt lds\n"
"s_waitcnt lgkmcnt(0)\n"
"v_mfma_f32_16x16x32_bf16 v[32:35], v[208:211], a[128:131], v[32:35]\n"
"v_mul_f32 v159, s95, v159\n"
"v_mul_f32 v188, s95, v188\n"
"v_mfma_f32_16x16x32_bf16 v[36:39], v[208:211], a[132:135], v[36:39]\n"
"s_barrier\n"
"v_mfma_f32_16x16x32_bf16 v[40:43], v[208:211], a[136:139], v[40:43]\n"
"v_mfma_f32_16x16x32_bf16 v[44:47], v[208:211], a[140:143], v[44:47]\n"
"ds_read_b64_tr_b16 v[224:225], v244 offset:59136\n"
"ds_read_b64_tr_b16 v[226:227], v244 offset:59392\n"
"v_mfma_f32_16x16x32_bf16 v[96:99], v[212:215], a[128:131], v[96:99]\n"
"v_mul_f32 v189, s95, v189\n"
"v_mfma_f32_16x16x32_bf16 v[100:103], v[212:215], a[132:135], v[100:103]\n"
"v_mfma_f32_16x16x32_bf16 v[104:107], v[212:215], a[136:139], v[104:107]\n"
"ds_read_b64_tr_b16 v[228:229], v244 offset:59144\n"
"ds_read_b64_tr_b16 v[230:231], v244 offset:59400\n"
"v_mfma_f32_16x16x32_bf16 v[108:111], v[212:215], a[140:143], v[108:111]\n"
"v_mul_f32 v190, s95, v190\n"
"v_mfma_f32_16x16x32_bf16 v[48:51], v[216:219], a[128:131], v[48:51]\n"
"v_mfma_f32_16x16x32_bf16 v[52:55], v[216:219], a[132:135], v[52:55]\n"
"ds_read_b64_tr_b16 v[232:233], v244 offset:59648\n"
"ds_read_b64_tr_b16 v[234:235], v244 offset:59904\n"
"ds_read_b64_tr_b16 v[236:237], v244 offset:59656\n"
"ds_read_b64_tr_b16 v[238:239], v244 offset:59912\n"
"v_mfma_f32_16x16x32_bf16 v[56:59], v[216:219], a[136:139], v[56:59]\n"
"v_mul_f32 v191, s95, v191\n"
"s_add_u32 m0, s73, 0xab60\n"
"v_mfma_f32_16x16x32_bf16 v[60:63], v[216:219], a[140:143], v[60:63]\n"
"buffer_load_dwordx4 v199, s[48:51], s75 offen offset:1536 nt lds\n"
"v_mfma_f32_16x16x32_bf16 v[112:115], v[220:223], a[128:131], v[112:115]\n"
"v_cvt_pk_bf16_f32 v156, v156, v157\n"
"v_cvt_pk_bf16_f32 v157, v158, v159\n"
"v_mfma_f32_16x16x32_bf16 v[116:119], v[220:223], a[132:135], v[116:119]\n"
"v_cvt_pk_bf16_f32 v158, v188, v189\n"
"v_cvt_pk_bf16_f32 v159, v190, v191\n"
"v_mfma_f32_16x16x32_bf16 v[120:123], v[220:223], a[136:139], v[120:123]\n"
"v_mfma_f32_16x16x32_bf16 v[124:127], v[220:223], a[140:143], v[124:127]\n"
"s_waitcnt lgkmcnt(0)\n"
"v_mfma_f32_16x16x32_bf16 v[32:35], v[224:227], a[144:147], v[32:35]\n"
"v_mfma_f32_16x16x32_bf16 v[36:39], v[224:227], a[148:151], v[36:39]\n"
"v_mfma_f32_16x16x32_bf16 v[40:43], v[224:227], a[152:155], v[40:43]\n"
"v_mfma_f32_16x16x32_bf16 v[44:47], v[224:227], a[156:159], v[44:47]\n"
"v_mfma_f32_16x16x32_bf16 v[96:99], v[228:231], a[144:147], v[96:99]\n"
"s_barrier\n"
"ds_read_b64_tr_b16 v[208:209], v245 offset:0\n"
"ds_read_b64_tr_b16 v[210:211], v245 offset:256\n"
"v_mfma_f32_16x16x32_bf16 v[100:103], v[228:231], a[148:151], v[100:103]\n"
"ds_read_b64_tr_b16 v[212:213], v245 offset:8\n"
"ds_read_b64_tr_b16 v[214:215], v245 offset:264\n"
"v_mfma_f32_16x16x32_bf16 v[104:107], v[228:231], a[152:155], v[104:107]\n"
"s_add_u32 m0, s73, 0xcc60\n"
"v_mfma_f32_16x16x32_bf16 v[108:111], v[228:231], a[156:159], v[108:111]\n"
"buffer_load_dwordx4 v242, s[48:51], s75 offen offset:1536 nt lds\n"
"v_mfma_f32_16x16x32_bf16 v[48:51], v[232:235], a[144:147], v[48:51]\n"
"ds_read_b64_tr_b16 v[216:217], v245 offset:512\n"
"ds_read_b64_tr_b16 v[218:219], v245 offset:768\n"
"v_mfma_f32_16x16x32_bf16 v[52:55], v[232:235], a[148:151], v[52:55]\n"
"ds_read_b64_tr_b16 v[220:221], v245 offset:520\n"
"ds_read_b64_tr_b16 v[222:223], v245 offset:776\n"
"v_mfma_f32_16x16x32_bf16 v[56:59], v[232:235], a[152:155], v[56:59]\n"
"v_mfma_f32_16x16x32_bf16 v[60:63], v[232:235], a[156:159], v[60:63]\n"
"v_mfma_f32_16x16x32_bf16 v[112:115], v[236:239], a[144:147], v[112:115]\n"
"v_mfma_f32_16x16x32_bf16 v[116:119], v[236:239], a[148:151], v[116:119]\n"
"v_mfma_f32_16x16x32_bf16 v[120:123], v[236:239], a[152:155], v[120:123]\n"
"v_mfma_f32_16x16x32_bf16 v[124:127], v[236:239], a[156:159], v[124:127]\n"
"s_waitcnt lgkmcnt(0)\n"
"v_mfma_f32_16x16x32_bf16 v[32:35], v[208:211], a[160:163], v[32:35]\n"
"v_mfma_f32_16x16x32_bf16 v[36:39], v[208:211], a[164:167], v[36:39]\n"
"v_mfma_f32_16x16x32_bf16 v[40:43], v[208:211], a[168:171], v[40:43]\n"
"v_mfma_f32_16x16x32_bf16 v[44:47], v[208:211], a[172:175], v[44:47]\n"
"v_mfma_f32_16x16x32_bf16 v[96:99], v[212:215], a[160:163], v[96:99]\n"
"s_barrier\n"
"ds_read_b64_tr_b16 v[224:225], v245 offset:16896\n"
"ds_read_b64_tr_b16 v[226:227], v245 offset:17152\n"
"v_mfma_f32_16x16x32_bf16 v[100:103], v[212:215], a[164:167], v[100:103]\n"
"ds_read_b64_tr_b16 v[228:229], v245 offset:16904\n"
"ds_read_b64_tr_b16 v[230:231], v245 offset:17160\n"
"v_mfma_f32_16x16x32_bf16 v[104:107], v[212:215], a[168:171], v[104:107]\n"
"ds_read_b64_tr_b16 v[232:233], v245 offset:17408\n"
"ds_read_b64_tr_b16 v[234:235], v245 offset:17664\n"
"v_mfma_f32_16x16x32_bf16 v[108:111], v[212:215], a[172:175], v[108:111]\n"
"ds_read_b64_tr_b16 v[236:237], v245 offset:17416\n"
"ds_read_b64_tr_b16 v[238:239], v245 offset:17672\n"
"v_mfma_f32_16x16x32_bf16 v[48:51], v[216:219], a[160:163], v[48:51]\n"
"v_mfma_f32_16x16x32_bf16 v[52:55], v[216:219], a[164:167], v[52:55]\n"
"v_mfma_f32_16x16x32_bf16 v[56:59], v[216:219], a[168:171], v[56:59]\n"
"v_mfma_f32_16x16x32_bf16 v[60:63], v[216:219], a[172:175], v[60:63]\n"
"v_mfma_f32_16x16x32_bf16 v[112:115], v[220:223], a[160:163], v[112:115]\n"
"v_mfma_f32_16x16x32_bf16 v[116:119], v[220:223], a[164:167], v[116:119]\n"
"v_mfma_f32_16x16x32_bf16 v[120:123], v[220:223], a[168:171], v[120:123]\n"
"v_mfma_f32_16x16x32_bf16 v[124:127], v[220:223], a[172:175], v[124:127]\n"
"s_waitcnt lgkmcnt(0)\n"
"v_mfma_f32_16x16x32_bf16 v[32:35], v[224:227], a[176:179], v[32:35]\n"
"v_mfma_f32_16x16x32_bf16 v[36:39], v[224:227], a[180:183], v[36:39]\n"
"v_mfma_f32_16x16x32_bf16 v[40:43], v[224:227], a[184:187], v[40:43]\n"
"v_mfma_f32_16x16x32_bf16 v[44:47], v[224:227], a[188:191], v[44:47]\n"
"v_mfma_f32_16x16x32_bf16 v[96:99], v[228:231], a[176:179], v[96:99]\n"
"s_barrier\n"
"ds_read_b64_tr_b16 v[208:209], v245 offset:8448\n"
"ds_read_b64_tr_b16 v[210:211], v245 offset:8704\n"
"v_mfma_f32_16x16x32_bf16 v[100:103], v[228:231], a[180:183], v[100:103]\n"
"ds_read_b64_tr_b16 v[212:213], v245 offset:8456\n"
"ds_read_b64_tr_b16 v[214:215], v245 offset:8712\n"
"v_mfma_f32_16x16x32_bf16 v[104:107], v[228:231], a[184:187], v[104:107]\n"
"ds_read_b64_tr_b16 v[216:217], v245 offset:8960\n"
"ds_read_b64_tr_b16 v[218:219], v245 offset:9216\n"
"v_mfma_f32_16x16x32_bf16 v[108:111], v[228:231], a[188:191], v[108:111]\n"
"ds_read_b64_tr_b16 v[220:221], v245 offset:8968\n"
"ds_read_b64_tr_b16 v[222:223], v245 offset:9224\n"
"v_mfma_f32_16x16x32_bf16 v[48:51], v[232:235], a[176:179], v[48:51]\n"
"v_mfma_f32_16x16x32_bf16 v[52:55], v[232:235], a[180:183], v[52:55]\n"
"v_mfma_f32_16x16x32_bf16 v[56:59], v[232:235], a[184:187], v[56:59]\n"
"v_mfma_f32_16x16x32_bf16 v[60:63], v[232:235], a[188:191], v[60:63]\n"
"v_mfma_f32_16x16x32_bf16 v[112:115], v[236:239], a[176:179], v[112:115]\n"
"v_mfma_f32_16x16x32_bf16 v[116:119], v[236:239], a[180:183], v[116:119]\n"
"v_mfma_f32_16x16x32_bf16 v[120:123], v[236:239], a[184:187], v[120:123]\n"
"v_mfma_f32_16x16x32_bf16 v[124:127], v[236:239], a[188:191], v[124:127]\n"
"s_waitcnt lgkmcnt(0)\n"
"v_mfma_f32_16x16x32_bf16 v[32:35], v[208:211], a[192:195], v[32:35]\n"
"v_mfma_f32_16x16x32_bf16 v[36:39], v[208:211], a[196:199], v[36:39]\n"
"v_mfma_f32_16x16x32_bf16 v[40:43], v[208:211], a[200:203], v[40:43]\n"
"v_mfma_f32_16x16x32_bf16 v[44:47], v[208:211], a[204:207], v[44:47]\n"
"v_mfma_f32_16x16x32_bf16 v[96:99], v[212:215], a[192:195], v[96:99]\n"
"s_barrier\n"
"ds_read_b64_tr_b16 v[224:225], v244 offset:50688\n"
"ds_read_b64_tr_b16 v[226:227], v244 offset:50944\n"
"v_mfma_f32_16x16x32_bf16 v[100:103], v[212:215], a[196:199], v[100:103]\n"
"ds_read_b64_tr_b16 v[228:229], v244 offset:50696\n"
"ds_read_b64_tr_b16 v[230:231], v244 offset:50952\n"
"v_mfma_f32_16x16x32_bf16 v[104:107], v[212:215], a[200:203], v[104:107]\n"
"ds_read_b64_tr_b16 v[232:233], v244 offset:51200\n"
"ds_read_b64_tr_b16 v[234:235], v244 offset:51456\n"
"v_mfma_f32_16x16x32_bf16 v[108:111], v[212:215], a[204:207], v[108:111]\n"
"ds_read_b64_tr_b16 v[236:237], v244 offset:51208\n"
"ds_read_b64_tr_b16 v[238:239], v244 offset:51464\n"
"v_mfma_f32_16x16x32_bf16 v[48:51], v[216:219], a[192:195], v[48:51]\n"
"v_mfma_f32_16x16x32_bf16 v[52:55], v[216:219], a[196:199], v[52:55]\n"
"v_mfma_f32_16x16x32_bf16 v[56:59], v[216:219], a[200:203], v[56:59]\n"
"v_mfma_f32_16x16x32_bf16 v[60:63], v[216:219], a[204:207], v[60:63]\n"
"v_mfma_f32_16x16x32_bf16 v[112:115], v[220:223], a[192:195], v[112:115]\n"
"ds_read_b128 v[208:211], v240 offset:0\n"
"ds_read_b128 v[212:215], v240 offset:8448\n"
"v_mfma_f32_16x16x32_bf16 v[116:119], v[220:223], a[196:199], v[116:119]\n"
"ds_read_b128 v[216:219], v240 offset:64\n"
"v_mfma_f32_16x16x32_bf16 v[120:123], v[220:223], a[200:203], v[120:123]\n"
"v_mfma_f32_16x16x32_bf16 v[124:127], v[220:223], a[204:207], v[124:127]\n"
"ds_read_b128 v[220:223], v240 offset:8512\n"
"s_waitcnt lgkmcnt(4)\n"
"v_mfma_f32_16x16x32_bf16 v[32:35], v[224:227], a[208:211], v[32:35]\n"
"v_mfma_f32_16x16x32_bf16 v[36:39], v[224:227], a[212:215], v[36:39]\n"
"v_mfma_f32_16x16x32_bf16 v[40:43], v[224:227], a[216:219], v[40:43]\n"
"v_mfma_f32_16x16x32_bf16 v[44:47], v[224:227], a[220:223], v[44:47]\n"
"v_mfma_f32_16x16x32_bf16 v[96:99], v[228:231], a[208:211], v[96:99]\n"
"v_mfma_f32_16x16x32_bf16 v[100:103], v[228:231], a[212:215], v[100:103]\n"
"v_mfma_f32_16x16x32_bf16 v[104:107], v[228:231], a[216:219], v[104:107]\n"
"v_mfma_f32_16x16x32_bf16 v[108:111], v[228:231], a[220:223], v[108:111]\n"
"v_mfma_f32_16x16x32_bf16 v[48:51], v[232:235], a[208:211], v[48:51]\n"
"v_mul_f32 v32, s95, v32\n"
"v_mul_f32 v33, s95, v33\n"
"v_mfma_f32_16x16x32_bf16 v[52:55], v[232:235], a[212:215], v[52:55]\n"
"v_mul_f32 v34, s95, v34\n"
"v_mul_f32 v35, s95, v35\n"
"v_mfma_f32_16x16x32_bf16 v[56:59], v[232:235], a[216:219], v[56:59]\n"
"v_mul_f32 v96, s95, v96\n"
"v_mul_f32 v97, s95, v97\n"
"v_mfma_f32_16x16x32_bf16 v[60:63], v[232:235], a[220:223], v[60:63]\n"
"v_mul_f32 v98, s95, v98\n"
"v_mul_f32 v99, s95, v99\n"
"v_mfma_f32_16x16x32_bf16 v[112:115], v[236:239], a[208:211], v[112:115]\n"
"ds_read_b128 v[224:227], v240 offset:128\n"
"ds_read_b128 v[228:231], v240 offset:8576\n"
"v_mfma_f32_16x16x32_bf16 v[116:119], v[236:239], a[212:215], v[116:119]\n"
"ds_read_b128 v[232:235], v240 offset:192\n"
"v_cvt_pk_bf16_f32 v32, v32, v33\n"
"v_mfma_f32_16x16x32_bf16 v[120:123], v[236:239], a[216:219], v[120:123]\n"
"v_cvt_pk_bf16_f32 v33, v34, v35\n"
"v_cvt_pk_bf16_f32 v34, v96, v97\n"
"v_mfma_f32_16x16x32_bf16 v[124:127], v[236:239], a[220:223], v[124:127]\n"
"ds_read_b128 v[236:239], v240 offset:8640\n"
"s_branch L_join_%=\n"
"L_end_%=:\n"
"L_rareB0_%=:\n"
"s_nop 3\n"
"v_max3_f32 v113, v160, v161, v162\n"
"v_max3_f32 v113, v113, v163, v176\n"
"v_max3_f32 v113, v113, v177, v178\n"
"v_max3_f32 v113, v113, v179, 0\n"
"v_mov_b32 v114, v113\n"
"s_nop 1\n"
"v_permlane32_swap_b32 v113, v114\n"
"s_nop 1\n"
"v_max_f32 v113, v113, v114\n"
"v_mov_b32 v114, v113\n"
"s_nop 1\n"
"v_permlane16_swap_b32 v113, v114\n"
"s_nop 1\n"
"v_max_f32 v113, v113, v114\n"
"v_exp_f32 v112, -v113\n"
"v_sub_f32 v160, v160, v113\n"
"v_sub_f32 v161, v161, v113\n"
"v_sub_f32 v162, v162, v113\n"
"v_sub_f32 v163, v163, v113\n"
"v_sub_f32 v176, v176, v113\n"
"v_sub_f32 v177, v177, v113\n"
"v_sub_f32 v178, v178, v113\n"
"v_sub_f32 v179, v179, v113\n"
"v_sub_f32 v192, v192, v113\n"
"v_sub_f32 v193, v193, v113\n"
"v_sub_f32 v194, v194, v113\n"
"v_sub_f32 v195, v195, v113\n"
"v_mul_f32 v244, v244, v112\n"
"v_max3_f32 v117, v164, v165, v166\n"
"v_max3_f32 v117, v117, v167, v180\n"
"v_max3_f32 v117, v117, v181, v182\n"
"v_max3_f32 v117, v117, v183, 0\n"
"v_mov_b32 v118, v117\n"
"s_nop 1\n"
"v_permlane32_swap_b32 v117, v118\n"
"s_nop 1\n"
"v_max_f32 v117, v117, v118\n"
"v_mov_b32 v118, v117\n"
"s_nop 1\n"
"v_permlane16_swap_b32 v117, v118\n"
"s_nop 1\n"
"v_max_f32 v117, v117, v118\n"
"v_exp_f32 v116, -v117\n"
"v_sub_f32 v164, v164, v117\n"
"v_sub_f32 v165, v165, v117\n"
"v_sub_f32 v166, v166, v117\n"
"v_sub_f32 v167, v167, v117\n"
"v_sub_f32 v180, v180, v117\n"
"v_sub_f32 v181, v181, v117\n"
"v_sub_f32 v182, v182, v117\n"
"v_sub_f32 v183, v183, v117\n"
"v_sub_f32 v196, v196, v117\n"
"v_sub_f32 v197, v197, v117\n"
"v_sub_f32 v198, v198, v117\n"
"v_sub_f32 v199, v199, v117\n"
"v_mul_f32 v245, v245, v116\n"
"v_max3_f32 v121, v168, v169, v170\n"
"v_max3_f32 v121, v121, v171, v184\n"
"v_max3_f32 v121, v121, v185, v186\n"
"v_max3_f32 v121, v121, v187, 0\n"
"v_mov_b32 v122, v121\n"
"s_nop 1\n"
"v_permlane32_swap_b32 v121, v122\n"
"s_nop 1\n"
"v_max_f32 v121, v121, v122\n"
"v_mov_b32 v122, v121\n"
"s_nop 1\n"
"v_permlane16_swap_b32 v121, v122\n"
"s_nop 1\n"
"v_max_f32 v121, v121, v122\n"
"v_exp_f32 v120, -v121\n"
"v_sub_f32 v168, v168, v121\n"
"v_sub_f32 v169, v169, v121\n"
"v_sub_f32 v170, v170, v121\n"
"v_sub_f32 v171, v171, v121\n"
"v_sub_f32 v184, v184, v121\n"
"v_sub_f32 v185, v185, v121\n"
"v_sub_f32 v186, v186, v121\n"
"v_sub_f32 v187, v187, v121\n"
"v_sub_f32 v200, v200, v121\n"
"v_sub_f32 v201, v201, v121\n"
"v_sub_f32 v202, v202, v121\n"
"v_sub_f32 v203, v203, v121\n"
"v_mul_f32 v246, v246, v120\n"
"v_max3_f32 v125, v172, v173, v174\n"
"v_max3_f32 v125, v125, v175, v188\n"
"v_max3_f32 v125, v125, v189, v190\n"
"v_max3_f32 v125, v125, v191, 0\n"
"v_mov_b32 v126, v125\n"
"s_nop 1\n"
"v_permlane32_swap_b32 v125, v126\n"
"s_nop 1\n"
"v_max_f32 v125, v125, v126\n"
"v_mov_b32 v126, v125\n"
"s_nop 1\n"
"v_permlane16_swap_b32 v125, v126\n"
"s_nop 1\n"
"v_max_f32 v125, v125, v126\n"
"v_exp_f32 v124, -v125\n"
"v_sub_f32 v172, v172, v125\n"
"v_sub_f32 v173, v173, v125\n"
"v_sub_f32 v174, v174, v125\n"
"v_sub_f32 v175, v175, v125\n"
"v_sub_f32 v188, v188, v125\n"
"v_sub_f32 v189, v189, v125\n"
"v_sub_f32 v190, v190, v125\n"
"v_sub_f32 v191, v191, v125\n"
"v_sub_f32 v204, v204, v125\n"
"v_sub_f32 v205, v205, v125\n"
"v_sub_f32 v206, v206, v125\n"
"v_sub_f32 v207, v207, v125\n"
"v_mul_f32 v247, v247, v124\n"
"s_nop 3\n"
"s_setpc_b64 s[80:81]\n"
"L_rareB1_%=:\n"
"s_nop 3\n"
"v_max3_f32 v177, v96, v97, v98\n"
"v_max3_f32 v177, v177, v99, v112\n"
"v_max3_f32 v177, v177, v113, v114\n"
"v_max3_f32 v177, v177, v115, 0\n"
"v_mov_b32 v178, v177\n"
"s_nop 1\n"
"v_permlane32_swap_b32 v177, v178\n"
"s_nop 1\n"
"v_max_f32 v177, v177, v178\n"
"v_mov_b32 v178, v177\n"
"s_nop 1\n"
"v_permlane16_swap_b32 v177, v178\n"
"s_nop 1\n"
"v_max_f32 v177, v177, v178\n"
"v_exp_f32 v176, -v177\n"
"v_sub_f32 v96, v96, v177\n"
"v_sub_f32 v97, v97, v177\n"
"v_sub_f32 v98, v98, v177\n"
"v_sub_f32 v99, v99, v177\n"
"v_sub_f32 v112, v112, v177\n"
"v_sub_f32 v113, v113, v177\n"
"v_sub_f32 v114, v114, v177\n"
"v_sub_f32 v115, v115, v177\n"
"v_sub_f32 v192, v192, v177\n"
"v_sub_f32 v193, v193, v177\n"
"v_sub_f32 v194, v194, v177\n"
"v_sub_f32 v195, v195, v177\n"
"v_mul_f32 v244, v244, v176\n"
"v_max3_f32 v181, v100, v101, v102\n"
"v_max3_f32 v181, v181, v103, v116\n"
"v_max3_f32 v181, v181, v117, v118\n"
"v_max3_f32 v181, v181, v119, 0\n"
"v_mov_b32 v182, v181\n"
"s_nop 1\n"
"v_permlane32_swap_b32 v181, v182\n"
"s_nop 1\n"
"v_max_f32 v181, v181, v182\n"
"v_mov_b32 v182, v181\n"
"s_nop 1\n"
"v_permlane16_swap_b32 v181, v182\n"
"s_nop 1\n"
"v_max_f32 v181, v181, v182\n"
"v_exp_f32 v180, -v181\n"
"v_sub_f32 v100, v100, v181\n"
"v_sub_f32 v101, v101, v181\n"
"v_sub_f32 v102, v102, v181\n"
"v_sub_f32 v103, v103, v181\n"
"v_sub_f32 v116, v116, v181\n"
"v_sub_f32 v117, v117, v181\n"
"v_sub_f32 v118, v118, v181\n"
"v_sub_f32 v119, v119, v181\n"
"v_sub_f32 v196, v196, v181\n"
"v_sub_f32 v197, v197, v181\n"
"v_sub_f32 v198, v198, v181\n"
"v_sub_f32 v199, v199, v181\n"
"v_mul_f32 v245, v245, v180\n"
"v_max3_f32 v185, v104, v105, v106\n"
"v_max3_f32 v185, v185, v107, v120\n"
"v_max3_f32 v185, v185, v121, v122\n"
"v_max3_f32 v185, v185, v123, 0\n"
"v_mov_b32 v186, v185\n"
"s_nop 1\n"
"v_permlane32_swap_b32 v185, v186\n"
"s_nop 1\n"
"v_max_f32 v185, v185, v186\n"
"v_mov_b32 v186, v185\n"
"s_nop 1\n"
"v_permlane16_swap_b32 v185, v186\n"
"s_nop 1\n"
"v_max_f32 v185, v185, v186\n"
"v_exp_f32 v184, -v185\n"
"v_sub_f32 v104, v104, v185\n"
"v_sub_f32 v105, v105, v185\n"
"v_sub_f32 v106, v106, v185\n"
"v_sub_f32 v107, v107, v185\n"
"v_sub_f32 v120, v120, v185\n"
"v_sub_f32 v121, v121, v185\n"
"v_sub_f32 v122, v122, v185\n"
"v_sub_f32 v123, v123, v185\n"
"v_sub_f32 v200, v200, v185\n"
"v_sub_f32 v201, v201, v185\n"
"v_sub_f32 v202, v202, v185\n"
"v_sub_f32 v203, v203, v185\n"
"v_mul_f32 v246, v246, v184\n"
"v_max3_f32 v189, v108, v109, v110\n"
"v_max3_f32 v189, v189, v111, v124\n"
"v_max3_f32 v189, v189, v125, v126\n"
"v_max3_f32 v189, v189, v127, 0\n"
"v_mov_b32 v190, v189\n"
"s_nop 1\n"
"v_permlane32_swap_b32 v189, v190\n"
"s_nop 1\n"
"v_max_f32 v189, v189, v190\n"
"v_mov_b32 v190, v189\n"
"s_nop 1\n"
"v_permlane16_swap_b32 v189, v190\n"
"s_nop 1\n"
"v_max_f32 v189, v189, v190\n"
"v_exp_f32 v188, -v189\n"
"v_sub_f32 v108, v108, v189\n"
"v_sub_f32 v109, v109, v189\n"
"v_sub_f32 v110, v110, v189\n"
"v_sub_f32 v111, v111, v189\n"
"v_sub_f32 v124, v124, v189\n"
"v_sub_f32 v125, v125, v189\n"
"v_sub_f32 v126, v126, v189\n"
"v_sub_f32 v127, v127, v189\n"
"v_sub_f32 v204, v204, v189\n"
"v_sub_f32 v205, v205, v189\n"
"v_sub_f32 v206, v206, v189\n"
"v_sub_f32 v207, v207, v189\n"
"v_mul_f32 v247, v247, v188\n"
"s_nop 3\n"
"s_setpc_b64 s[80:81]\n"
"L_rareC0_%=:\n"
"s_nop 15\n"
"v_accvgpr_read_b32 v160, a0\n"
"v_accvgpr_read_b32 v161, a1\n"
"v_accvgpr_read_b32 v162, a2\n"
"v_accvgpr_read_b32 v163, a3\n"
"v_mul_f32 v160, v160, v176\n"
"v_mul_f32 v161, v161, v176\n"
"v_mul_f32 v162, v162, v176\n"
"v_mul_f32 v163, v163, v176\n"
"v_accvgpr_write_b32 a0, v160\n"
"v_accvgpr_write_b32 a1, v161\n"
"v_accvgpr_write_b32 a2, v162\n"
"v_accvgpr_write_b32 a3, v163\n"
"v_accvgpr_read_b32 v164, a4\n"
"v_accvgpr_read_b32 v165, a5\n"
"v_accvgpr_read_b32 v166, a6\n"
"v_accvgpr_read_b32 v167, a7\n"
"v_mul_f32 v164, v164, v180\n"
"v_mul_f32 v165, v165, v180\n"
"v_mul_f32 v166, v166, v180\n"
"v_mul_f32 v167, v167, v180\n"
"v_accvgpr_write_b32 a4, v164\n"
"v_accvgpr_write_b32 a5, v165\n"
"v_accvgpr_write_b32 a6, v166\n"
"v_accvgpr_write_b32 a7, v167\n"
"v_accvgpr_read_b32 v168, a8\n"
"v_accvgpr_read_b32 v169, a9\n"
"v_accvgpr_read_b32 v170, a10\n"
"v_accvgpr_read_b32 v171, a11\n"
"v_mul_f32 v168, v168, v184\n"
"v_mul_f32 v169, v169, v184\n"
"v_mul_f32 v170, v170, v184\n"
"v_mul_f32 v171, v171, v184\n"
"v_accvgpr_write_b32 a8, v168\n"
"v_accvgpr_write_b32 a9, v169\n"
"v_accvgpr_write_b32 a10, v170\n"
"v_accvgpr_write_b32 a11, v171\n"
"v_accvgpr_read_b32 v172, a12\n"
"v_accvgpr_read_b32 v173, a13\n"
"v_accvgpr_read_b32 v174, a14\n"
"v_accvgpr_read_b32 v175, a15\n"
"v_mul_f32 v172, v172, v188\n"
"v_mul_f32 v173, v173, v188\n"
"v_mul_f32 v174, v174, v188\n"
"v_mul_f32 v175, v175, v188\n"
"v_accvgpr_write_b32 a12, v172\n"
"v_accvgpr_write_b32 a13, v173\n"
"v_accvgpr_write_b32 a14, v174\n"
"v_accvgpr_write_b32 a15, v175\n"
"v_accvgpr_read_b32 v160, a16\n"
"v_accvgpr_read_b32 v161, a17\n"
"v_accvgpr_read_b32 v162, a18\n"
"v_accvgpr_read_b32 v163, a19\n"
"v_mul_f32 v160, v160, v176\n"
"v_mul_f32 v161, v161, v176\n"
"v_mul_f32 v162, v162, v176\n"
"v_mul_f32 v163, v163, v176\n"
"v_accvgpr_write_b32 a16, v160\n"
"v_accvgpr_write_b32 a17, v161\n"
"v_accvgpr_write_b32 a18, v162\n"
"v_accvgpr_write_b32 a19, v163\n"
"v_accvgpr_read_b32 v164, a20\n"
"v_accvgpr_read_b32 v165, a21\n"
"v_accvgpr_read_b32 v166, a22\n"
"v_accvgpr_read_b32 v167, a23\n"
"v_mul_f32 v164, v164, v180\n"
"v_mul_f32 v165, v165, v180\n"
"v_mul_f32 v166, v166, v180\n"
"v_mul_f32 v167, v167, v180\n"
"v_accvgpr_write_b32 a20, v164\n"
"v_accvgpr_write_b32 a21, v165\n"
"v_accvgpr_write_b32 a22, v166\n"
"v_accvgpr_write_b32 a23, v167\n"
"v_accvgpr_read_b32 v168, a24\n"
"v_accvgpr_read_b32 v169, a25\n"
"v_accvgpr_read_b32 v170, a26\n"
"v_accvgpr_read_b32 v171, a27\n"
"v_mul_f32 v168, v168, v184\n"
"v_mul_f32 v169, v169, v184\n"
"v_mul_f32 v170, v170, v184\n"
"v_mul_f32 v171, v171, v184\n"
"v_accvgpr_write_b32 a24, v168\n"
"v_accvgpr_write_b32 a25, v169\n"
"v_accvgpr_write_b32 a26, v170\n"
"v_accvgpr_write_b32 a27, v171\n"
"v_accvgpr_read_b32 v172, a28\n"
"v_accvgpr_read_b32 v173, a29\n"
"v_accvgpr_read_b32 v174, a30\n"
"v_accvgpr_read_b32 v175, a31\n"
"v_mul_f32 v172, v172, v188\n"
"v_mul_f32 v173, v173, v188\n"
"v_mul_f32 v174, v174, v188\n"
"v_mul_f32 v175, v175, v188\n"
"v_accvgpr_write_b32 a28, v172\n"
"v_accvgpr_write_b32 a29, v173\n"
"v_accvgpr_write_b32 a30, v174\n"
"v_accvgpr_write_b32 a31, v175\n"
"v_accvgpr_read_b32 v160, a32\n"
"v_accvgpr_read_b32 v161, a33\n"
"v_accvgpr_read_b32 v162, a34\n"
"v_accvgpr_read_b32 v163, a35\n"
"v_mul_f32 v160, v160, v176\n"
"v_mul_f32 v161, v161, v176\n"
"v_mul_f32 v162, v162, v176\n"
"v_mul_f32 v163, v163, v176\n"
"v_accvgpr_write_b32 a32, v160\n"
"v_accvgpr_write_b32 a33, v161\n"
"v_accvgpr_write_b32 a34, v162\n"
"v_accvgpr_write_b32 a35, v163\n"
"v_accvgpr_read_b32 v164, a36\n"
"v_accvgpr_read_b32 v165, a37\n"
"v_accvgpr_read_b32 v166, a38\n"
"v_accvgpr_read_b32 v167, a39\n"
"v_mul_f32 v164, v164, v180\n"
"v_mul_f32 v165, v165, v180\n"
"v_mul_f32 v166, v166, v180\n"
"v_mul_f32 v167, v167, v180\n"
"v_accvgpr_write_b32 a36, v164\n"
"v_accvgpr_write_b32 a37, v165\n"
"v_accvgpr_write_b32 a38, v166\n"
"v_accvgpr_write_b32 a39, v167\n"
"v_accvgpr_read_b32 v168, a40\n"
"v_accvgpr_read_b32 v169, a41\n"
"v_accvgpr_read_b32 v170, a42\n"
"v_accvgpr_read_b32 v171, a43\n"
"v_mul_f32 v168, v168, v184\n"
"v_mul_f32 v169, v169, v184\n"
"v_mul_f32 v170, v170, v184\n"
"v_mul_f32 v171, v171, v184\n"
"v_accvgpr_write_b32 a40, v168\n"
"v_accvgpr_write_b32 a41, v169\n"
"v_accvgpr_write_b32 a42, v170\n"
"v_accvgpr_write_b32 a43, v171\n"
"v_accvgpr_read_b32 v172, a44\n"
"v_accvgpr_read_b32 v173, a45\n"
"v_accvgpr_read_b32 v174, a46\n"
"v_accvgpr_read_b32 v175, a47\n"
"v_mul_f32 v172, v172, v188\n"
"v_mul_f32 v173, v173, v188\n"
"v_mul_f32 v174, v174, v188\n"
"v_mul_f32 v175, v175, v188\n"
"v_accvgpr_write_b32 a44, v172\n"
"v_accvgpr_write_b32 a45, v173\n"
"v_accvgpr_write_b32 a46, v174\n"
"v_accvgpr_write_b32 a47, v175\n"
"v_accvgpr_read_b32 v160, a48\n"
"v_accvgpr_read_b32 v161, a49\n"
"v_accvgpr_read_b32 v162, a50\n"
"v_accvgpr_read_b32 v163, a51\n"
"v_mul_f32 v160, v160, v176\n"
"v_mul_f32 v161, v161, v176\n"
"v_mul_f32 v162, v162, v176\n"
"v_mul_f32 v163, v163, v176\n"
"v_accvgpr_write_b32 a48, v160\n"
"v_accvgpr_write_b32 a49, v161\n"
"v_accvgpr_write_b32 a50, v162\n"
"v_accvgpr_write_b32 a51, v163\n"
"v_accvgpr_read_b32 v164, a52\n"
"v_accvgpr_read_b32 v165, a53\n"
"v_accvgpr_read_b32 v166, a54\n"
"v_accvgpr_read_b32 v167, a55\n"
"v_mul_f32 v164, v164, v180\n"
"v_mul_f32 v165, v165, v180\n"
"v_mul_f32 v166, v166, v180\n"
"v_mul_f32 v167, v167, v180\n"
"v_accvgpr_write_b32 a52, v164\n"
"v_accvgpr_write_b32 a53, v165\n"
"v_accvgpr_write_b32 a54, v166\n"
"v_accvgpr_write_b32 a55, v167\n"
"v_accvgpr_read_b32 v168, a56\n"
"v_accvgpr_read_b32 v169, a57\n"
"v_accvgpr_read_b32 v170, a58\n"
"v_accvgpr_read_b32 v171, a59\n"
"v_mul_f32 v168, v168, v184\n"
"v_mul_f32 v169, v169, v184\n"
"v_mul_f32 v170, v170, v184\n"
"v_mul_f32 v171, v171, v184\n"
"v_accvgpr_write_b32 a56, v168\n"
"v_accvgpr_write_b32 a57, v169\n"
"v_accvgpr_write_b32 a58, v170\n"
"v_accvgpr_write_b32 a59, v171\n"
"v_accvgpr_read_b32 v172, a60\n"
"v_accvgpr_read_b32 v173, a61\n"
"v_accvgpr_read_b32 v174, a62\n"
"v_accvgpr_read_b32 v175, a63\n"
"v_mul_f32 v172, v172, v188\n"
"v_mul_f32 v173, v173, v188\n"
"v_mul_f32 v174, v174, v188\n"
"v_mul_f32 v175, v175, v188\n"
"v_accvgpr_write_b32 a60, v172\n"
"v_accvgpr_write_b32 a61, v173\n"
"v_accvgpr_write_b32 a62, v174\n"
"v_accvgpr_write_b32 a63, v175\n"
"v_accvgpr_read_b32 v160, a64\n"
"v_accvgpr_read_b32 v161, a65\n"
"v_accvgpr_read_b32 v162, a66\n"
"v_accvgpr_read_b32 v163, a67\n"
"v_mul_f32 v160, v160, v176\n"
"v_mul_f32 v161, v161, v176\n"
"v_mul_f32 v162, v162, v176\n"
"v_mul_f32 v163, v163, v176\n"
"v_accvgpr_write_b32 a64, v160\n"
"v_accvgpr_write_b32 a65, v161\n"
"v_accvgpr_write_b32 a66, v162\n"
"v_accvgpr_write_b32 a67, v163\n"
"v_accvgpr_read_b32 v164, a68\n"
"v_accvgpr_read_b32 v165, a69\n"
"v_accvgpr_read_b32 v166, a70\n"
"v_accvgpr_read_b32 v167, a71\n"
"v_mul_f32 v164, v164, v180\n"
"v_mul_f32 v165, v165, v180\n"
"v_mul_f32 v166, v166, v180\n"
"v_mul_f32 v167, v167, v180\n"
"v_accvgpr_write_b32 a68, v164\n"
"v_accvgpr_write_b32 a69, v165\n"
"v_accvgpr_write_b32 a70, v166\n"
"v_accvgpr_write_b32 a71, v167\n"
"v_accvgpr_read_b32 v168, a72\n"
"v_accvgpr_read_b32 v169, a73\n"
"v_accvgpr_read_b32 v170, a74\n"
"v_accvgpr_read_b32 v171, a75\n"
"v_mul_f32 v168, v168, v184\n"
"v_mul_f32 v169, v169, v184\n"
"v_mul_f32 v170, v170, v184\n"
"v_mul_f32 v171, v171, v184\n"
"v_accvgpr_write_b32 a72, v168\n"
"v_accvgpr_write_b32 a73, v169\n"
"v_accvgpr_write_b32 a74, v170\n"
"v_accvgpr_write_b32 a75, v171\n"
"v_accvgpr_read_b32 v172, a76\n"
"v_accvgpr_read_b32 v173, a77\n"
"v_accvgpr_read_b32 v174, a78\n"
"v_accvgpr_read_b32 v175, a79\n"
"v_mul_f32 v172, v172, v188\n"
"v_mul_f32 v173, v173, v188\n"
"v_mul_f32 v174, v174, v188\n"
"v_mul_f32 v175, v175, v188\n"
"v_accvgpr_write_b32 a76, v172\n"
"v_accvgpr_write_b32 a77, v173\n"
"v_accvgpr_write_b32 a78, v174\n"
"v_accvgpr_write_b32 a79, v175\n"
"v_accvgpr_read_b32 v160, a80\n"
"v_accvgpr_read_b32 v161, a81\n"
"v_accvgpr_read_b32 v162, a82\n"
"v_accvgpr_read_b32 v163, a83\n"
"v_mul_f32 v160, v160, v176\n"
"v_mul_f32 v161, v161, v176\n"
"v_mul_f32 v162, v162, v176\n"
"v_mul_f32 v163, v163, v176\n"
"v_accvgpr_write_b32 a80, v160\n"
"v_accvgpr_write_b32 a81, v161\n"
"v_accvgpr_write_b32 a82, v162\n"
"v_accvgpr_write_b32 a83, v163\n"
"v_accvgpr_read_b32 v164, a84\n"
"v_accvgpr_read_b32 v165, a85\n"
"v_accvgpr_read_b32 v166, a86\n"
"v_accvgpr_read_b32 v167, a87\n"
"v_mul_f32 v164, v164, v180\n"
"v_mul_f32 v165, v165, v180\n"
"v_mul_f32 v166, v166, v180\n"
"v_mul_f32 v167, v167, v180\n"
"v_accvgpr_write_b32 a84, v164\n"
"v_accvgpr_write_b32 a85, v165\n"
"v_accvgpr_write_b32 a86, v166\n"
"v_accvgpr_write_b32 a87, v167\n"
"v_accvgpr_read_b32 v168, a88\n"
"v_accvgpr_read_b32 v169, a89\n"
"v_accvgpr_read_b32 v170, a90\n"
"v_accvgpr_read_b32 v171, a91\n"
"v_mul_f32 v168, v168, v184\n"
"v_mul_f32 v169, v169, v184\n"
"v_mul_f32 v170, v170, v184\n"
"v_mul_f32 v171, v171, v184\n"
"v_accvgpr_write_b32 a88, v168\n"
"v_accvgpr_write_b32 a89, v169\n"
"v_accvgpr_write_b32 a90, v170\n"
"v_accvgpr_write_b32 a91, v171\n"
"v_accvgpr_read_b32 v172, a92\n"
"v_accvgpr_read_b32 v173, a93\n"
"v_accvgpr_read_b32 v174, a94\n"
"v_accvgpr_read_b32 v175, a95\n"
"v_mul_f32 v172, v172, v188\n"
"v_mul_f32 v173, v173, v188\n"
"v_mul_f32 v174, v174, v188\n"
"v_mul_f32 v175, v175, v188\n"
"v_accvgpr_write_b32 a92, v172\n"
"v_accvgpr_write_b32 a93, v173\n"
"v_accvgpr_write_b32 a94, v174\n"
"v_accvgpr_write_b32 a95, v175\n"
"v_accvgpr_read_b32 v160, a96\n"
"v_accvgpr_read_b32 v161, a97\n"
"v_accvgpr_read_b32 v162, a98\n"
"v_accvgpr_read_b32 v163, a99\n"
"v_mul_f32 v160, v160, v176\n"
"v_mul_f32 v161, v161, v176\n"
"v_mul_f32 v162, v162, v176\n"
"v_mul_f32 v163, v163, v176\n"
"v_accvgpr_write_b32 a96, v160\n"
"v_accvgpr_write_b32 a97, v161\n"
"v_accvgpr_write_b32 a98, v162\n"
"v_accvgpr_write_b32 a99, v163\n"
"v_accvgpr_read_b32 v164, a100\n"
"v_accvgpr_read_b32 v165, a101\n"
"v_accvgpr_read_b32 v166, a102\n"
"v_accvgpr_read_b32 v167, a103\n"
"v_mul_f32 v164, v164, v180\n"
"v_mul_f32 v165, v165, v180\n"
"v_mul_f32 v166, v166, v180\n"
"v_mul_f32 v167, v167, v180\n"
"v_accvgpr_write_b32 a100, v164\n"
"v_accvgpr_write_b32 a101, v165\n"
"v_accvgpr_write_b32 a102, v166\n"
"v_accvgpr_write_b32 a103, v167\n"
"v_accvgpr_read_b32 v168, a104\n"
"v_accvgpr_read_b32 v169, a105\n"
"v_accvgpr_read_b32 v170, a106\n"
"v_accvgpr_read_b32 v171, a107\n"
"v_mul_f32 v168, v168, v184\n"
"v_mul_f32 v169, v169, v184\n"
"v_mul_f32 v170, v170, v184\n"
"v_mul_f32 v171, v171, v184\n"
"v_accvgpr_write_b32 a104, v168\n"
"v_accvgpr_write_b32 a105, v169\n"
"v_accvgpr_write_b32 a106, v170\n"
"v_accvgpr_write_b32 a107, v171\n"
"v_accvgpr_read_b32 v172, a108\n"
"v_accvgpr_read_b32 v173, a109\n"
"v_accvgpr_read_b32 v174, a110\n"
"v_accvgpr_read_b32 v175, a111\n"
"v_mul_f32 v172, v172, v188\n"
"v_mul_f32 v173, v173, v188\n"
"v_mul_f32 v174, v174, v188\n"
"v_mul_f32 v175, v175, v188\n"
"v_accvgpr_write_b32 a108, v172\n"
"v_accvgpr_write_b32 a109, v173\n"
"v_accvgpr_write_b32 a110, v174\n"
"v_accvgpr_write_b32 a111, v175\n"
"v_accvgpr_read_b32 v160, a112\n"
"v_accvgpr_read_b32 v161, a113\n"
"v_accvgpr_read_b32 v162, a114\n"
"v_accvgpr_read_b32 v163, a115\n"
"v_mul_f32 v160, v160, v176\n"
"v_mul_f32 v161, v161, v176\n"
"v_mul_f32 v162, v162, v176\n"
"v_mul_f32 v163, v163, v176\n"
"v_accvgpr_write_b32 a112, v160\n"
"v_accvgpr_write_b32 a113, v161\n"
"v_accvgpr_write_b32 a114, v162\n"
"v_accvgpr_write_b32 a115, v163\n"
"v_accvgpr_read_b32 v164, a116\n"
"v_accvgpr_read_b32 v165, a117\n"
"v_accvgpr_read_b32 v166, a118\n"
"v_accvgpr_read_b32 v167, a119\n"
"v_mul_f32 v164, v164, v180\n"
"v_mul_f32 v165, v165, v180\n"
"v_mul_f32 v166, v166, v180\n"
"v_mul_f32 v167, v167, v180\n"
"v_accvgpr_write_b32 a116, v164\n"
"v_accvgpr_write_b32 a117, v165\n"
"v_accvgpr_write_b32 a118, v166\n"
"v_accvgpr_write_b32 a119, v167\n"
"v_accvgpr_read_b32 v168, a120\n"
"v_accvgpr_read_b32 v169, a121\n"
"v_accvgpr_read_b32 v170, a122\n"
"v_accvgpr_read_b32 v171, a123\n"
"v_mul_f32 v168, v168, v184\n"
"v_mul_f32 v169, v169, v184\n"
"v_mul_f32 v170, v170, v184\n"
"v_mul_f32 v171, v171, v184\n"
"v_accvgpr_write_b32 a120, v168\n"
"v_accvgpr_write_b32 a121, v169\n"
"v_accvgpr_write_b32 a122, v170\n"
"v_accvgpr_write_b32 a123, v171\n"
"v_accvgpr_read_b32 v172, a124\n"
"v_accvgpr_read_b32 v173, a125\n"
"v_accvgpr_read_b32 v174, a126\n"
"v_accvgpr_read_b32 v175, a127\n"
"v_mul_f32 v172, v172, v188\n"
"v_mul_f32 v173, v173, v188\n"
"v_mul_f32 v174, v174, v188\n"
"v_mul_f32 v175, v175, v188\n"
"v_accvgpr_write_b32 a124, v172\n"
"v_accvgpr_write_b32 a125, v173\n"
"v_accvgpr_write_b32 a126, v174\n"
"v_accvgpr_write_b32 a127, v175\n"
"v_accvgpr_read_b32 v160, a128\n"
"v_accvgpr_read_b32 v161, a129\n"
"v_accvgpr_read_b32 v162, a130\n"
"v_accvgpr_read_b32 v163, a131\n"
"v_mul_f32 v160, v160, v176\n"
"v_mul_f32 v161, v161, v176\n"
"v_mul_f32 v162, v162, v176\n"
"v_mul_f32 v163, v163, v176\n"
"v_accvgpr_write_b32 a128, v160\n"
"v_accvgpr_write_b32 a129, v161\n"
"v_accvgpr_write_b32 a130, v162\n"
"v_accvgpr_write_b32 a131, v163\n"
"v_accvgpr_read_b32 v164, a132\n"
"v_accvgpr_read_b32 v165, a133\n"
"v_accvgpr_read_b32 v166, a134\n"
"v_accvgpr_read_b32 v167, a135\n"
"v_mul_f32 v164, v164, v180\n"
"v_mul_f32 v165, v165, v180\n"
"v_mul_f32 v166, v166, v180\n"
"v_mul_f32 v167, v167, v180\n"
"v_accvgpr_write_b32 a132, v164\n"
"v_accvgpr_write_b32 a133, v165\n"
"v_accvgpr_write_b32 a134, v166\n"
"v_accvgpr_write_b32 a135, v167\n"
"v_accvgpr_read_b32 v168, a136\n"
"v_accvgpr_read_b32 v169, a137\n"
"v_accvgpr_read_b32 v170, a138\n"
"v_accvgpr_read_b32 v171, a139\n"
"v_mul_f32 v168, v168, v184\n"
"v_mul_f32 v169, v169, v184\n"
"v_mul_f32 v170, v170, v184\n"
"v_mul_f32 v171, v171, v184\n"
"v_accvgpr_write_b32 a136, v168\n"
"v_accvgpr_write_b32 a137, v169\n"
"v_accvgpr_write_b32 a138, v170\n"
"v_accvgpr_write_b32 a139, v171\n"
"v_accvgpr_read_b32 v172, a140\n"
"v_accvgpr_read_b32 v173, a141\n"
"v_accvgpr_read_b32 v174, a142\n"
"v_accvgpr_read_b32 v175, a143\n"
"v_mul_f32 v172, v172, v188\n"
"v_mul_f32 v173, v173, v188\n"
"v_mul_f32 v174, v174, v188\n"
"v_mul_f32 v175, v175, v188\n"
"v_accvgpr_write_b32 a140, v172\n"
"v_accvgpr_write_b32 a141, v173\n"
"v_accvgpr_write_b32 a142, v174\n"
"v_accvgpr_write_b32 a143, v175\n"
"v_accvgpr_read_b32 v160, a144\n"
"v_accvgpr_read_b32 v161, a145\n"
"v_accvgpr_read_b32 v162, a146\n"
"v_accvgpr_read_b32 v163, a147\n"
"v_mul_f32 v160, v160, v176\n"
"v_mul_f32 v161, v161, v176\n"
"v_mul_f32 v162, v162, v176\n"
"v_mul_f32 v163, v163, v176\n"
"v_accvgpr_write_b32 a144, v160\n"
"v_accvgpr_write_b32 a145, v161\n"
"v_accvgpr_write_b32 a146, v162\n"
"v_accvgpr_write_b32 a147, v163\n"
"v_accvgpr_read_b32 v164, a148\n"
"v_accvgpr_read_b32 v165, a149\n"
"v_accvgpr_read_b32 v166, a150\n"
"v_accvgpr_read_b32 v167, a151\n"
"v_mul_f32 v164, v164, v180\n"
"v_mul_f32 v165, v165, v180\n"
"v_mul_f32 v166, v166, v180\n"
"v_mul_f32 v167, v167, v180\n"
"v_accvgpr_write_b32 a148, v164\n"
"v_accvgpr_write_b32 a149, v165\n"
"v_accvgpr_write_b32 a150, v166\n"
"v_accvgpr_write_b32 a151, v167\n"
"v_accvgpr_read_b32 v168, a152\n"
"v_accvgpr_read_b32 v169, a153\n"
"v_accvgpr_read_b32 v170, a154\n"
"v_accvgpr_read_b32 v171, a155\n"
"v_mul_f32 v168, v168, v184\n"
"v_mul_f32 v169, v169, v184\n"
"v_mul_f32 v170, v170, v184\n"
"v_mul_f32 v171, v171, v184\n"
"v_accvgpr_write_b32 a152, v168\n"
"v_accvgpr_write_b32 a153, v169\n"
"v_accvgpr_write_b32 a154, v170\n"
"v_accvgpr_write_b32 a155, v171\n"
"v_accvgpr_read_b32 v172, a156\n"
"v_accvgpr_read_b32 v173, a157\n"
"v_accvgpr_read_b32 v174, a158\n"
"v_accvgpr_read_b32 v175, a159\n"
"v_mul_f32 v172, v172, v188\n"
"v_mul_f32 v173, v173, v188\n"
"v_mul_f32 v174, v174, v188\n"
"v_mul_f32 v175, v175, v188\n"
"v_accvgpr_write_b32 a156, v172\n"
"v_accvgpr_write_b32 a157, v173\n"
"v_accvgpr_write_b32 a158, v174\n"
"v_accvgpr_write_b32 a159, v175\n"
"v_accvgpr_read_b32 v160, a160\n"
"v_accvgpr_read_b32 v161, a161\n"
"v_accvgpr_read_b32 v162, a162\n"
"v_accvgpr_read_b32 v163, a163\n"
"v_mul_f32 v160, v160, v176\n"
"v_mul_f32 v161, v161, v176\n"
"v_mul_f32 v162, v162, v176\n"
"v_mul_f32 v163, v163, v176\n"
"v_accvgpr_write_b32 a160, v160\n"
"v_accvgpr_write_b32 a161, v161\n"
"v_accvgpr_write_b32 a162, v162\n"
"v_accvgpr_write_b32 a163, v163\n"
"v_accvgpr_read_b32 v164, a164\n"
"v_accvgpr_read_b32 v165, a165\n"
"v_accvgpr_read_b32 v166, a166\n"
"v_accvgpr_read_b32 v167, a167\n"
"v_mul_f32 v164, v164, v180\n"
"v_mul_f32 v165, v165, v180\n"
"v_mul_f32 v166, v166, v180\n"
"v_mul_f32 v167, v167, v180\n"
"v_accvgpr_write_b32 a164, v164\n"
"v_accvgpr_write_b32 a165, v165\n"
"v_accvgpr_write_b32 a166, v166\n"
"v_accvgpr_write_b32 a167, v167\n"
"v_accvgpr_read_b32 v168, a168\n"
"v_accvgpr_read_b32 v169, a169\n"
"v_accvgpr_read_b32 v170, a170\n"
"v_accvgpr_read_b32 v171, a171\n"
"v_mul_f32 v168, v168, v184\n"
"v_mul_f32 v169, v169, v184\n"
"v_mul_f32 v170, v170, v184\n"
"v_mul_f32 v171, v171, v184\n"
"v_accvgpr_write_b32 a168, v168\n"
"v_accvgpr_write_b32 a169, v169\n"
"v_accvgpr_write_b32 a170, v170\n"
"v_accvgpr_write_b32 a171, v171\n"
"v_accvgpr_read_b32 v172, a172\n"
"v_accvgpr_read_b32 v173, a173\n"
"v_accvgpr_read_b32 v174, a174\n"
"v_accvgpr_read_b32 v175, a175\n"
"v_mul_f32 v172, v172, v188\n"
"v_mul_f32 v173, v173, v188\n"
"v_mul_f32 v174, v174, v188\n"
"v_mul_f32 v175, v175, v188\n"
"v_accvgpr_write_b32 a172, v172\n"
"v_accvgpr_write_b32 a173, v173\n"
"v_accvgpr_write_b32 a174, v174\n"
"v_accvgpr_write_b32 a175, v175\n"
"v_accvgpr_read_b32 v160, a176\n"
"v_accvgpr_read_b32 v161, a177\n"
"v_accvgpr_read_b32 v162, a178\n"
"v_accvgpr_read_b32 v163, a179\n"
"v_mul_f32 v160, v160, v176\n"
"v_mul_f32 v161, v161, v176\n"
"v_mul_f32 v162, v162, v176\n"
"v_mul_f32 v163, v163, v176\n"
"v_accvgpr_write_b32 a176, v160\n"
"v_accvgpr_write_b32 a177, v161\n"
"v_accvgpr_write_b32 a178, v162\n"
"v_accvgpr_write_b32 a179, v163\n"
"v_accvgpr_read_b32 v164, a180\n"
"v_accvgpr_read_b32 v165, a181\n"
"v_accvgpr_read_b32 v166, a182\n"
"v_accvgpr_read_b32 v167, a183\n"
"v_mul_f32 v164, v164, v180\n"
"v_mul_f32 v165, v165, v180\n"
"v_mul_f32 v166, v166, v180\n"
"v_mul_f32 v167, v167, v180\n"
"v_accvgpr_write_b32 a180, v164\n"
"v_accvgpr_write_b32 a181, v165\n"
"v_accvgpr_write_b32 a182, v166\n"
"v_accvgpr_write_b32 a183, v167\n"
"v_accvgpr_read_b32 v168, a184\n"
"v_accvgpr_read_b32 v169, a185\n"
"v_accvgpr_read_b32 v170, a186\n"
"v_accvgpr_read_b32 v171, a187\n"
"v_mul_f32 v168, v168, v184\n"
"v_mul_f32 v169, v169, v184\n"
"v_mul_f32 v170, v170, v184\n"
"v_mul_f32 v171, v171, v184\n"
"v_accvgpr_write_b32 a184, v168\n"
"v_accvgpr_write_b32 a185, v169\n"
"v_accvgpr_write_b32 a186, v170\n"
"v_accvgpr_write_b32 a187, v171\n"
"v_accvgpr_read_b32 v172, a188\n"
"v_accvgpr_read_b32 v173, a189\n"
"v_accvgpr_read_b32 v174, a190\n"
"v_accvgpr_read_b32 v175, a191\n"
"v_mul_f32 v172, v172, v188\n"
"v_mul_f32 v173, v173, v188\n"
"v_mul_f32 v174, v174, v188\n"
"v_mul_f32 v175, v175, v188\n"
"v_accvgpr_write_b32 a188, v172\n"
"v_accvgpr_write_b32 a189, v173\n"
"v_accvgpr_write_b32 a190, v174\n"
"v_accvgpr_write_b32 a191, v175\n"
"v_accvgpr_read_b32 v160, a192\n"
"v_accvgpr_read_b32 v161, a193\n"
"v_accvgpr_read_b32 v162, a194\n"
"v_accvgpr_read_b32 v163, a195\n"
"v_mul_f32 v160, v160, v176\n"
"v_mul_f32 v161, v161, v176\n"
"v_mul_f32 v162, v162, v176\n"
"v_mul_f32 v163, v163, v176\n"
"v_accvgpr_write_b32 a192, v160\n"
"v_accvgpr_write_b32 a193, v161\n"
"v_accvgpr_write_b32 a194, v162\n"
"v_accvgpr_write_b32 a195, v163\n"
"v_accvgpr_read_b32 v164, a196\n"
"v_accvgpr_read_b32 v165, a197\n"
"v_accvgpr_read_b32 v166, a198\n"
"v_accvgpr_read_b32 v167, a199\n"
"v_mul_f32 v164, v164, v180\n"
"v_mul_f32 v165, v165, v180\n"
"v_mul_f32 v166, v166, v180\n"
"v_mul_f32 v167, v167, v180\n"
"v_accvgpr_write_b32 a196, v164\n"
"v_accvgpr_write_b32 a197, v165\n"
"v_accvgpr_write_b32 a198, v166\n"
"v_accvgpr_write_b32 a199, v167\n"
"v_accvgpr_read_b32 v168, a200\n"
"v_accvgpr_read_b32 v169, a201\n"
"v_accvgpr_read_b32 v170, a202\n"
"v_accvgpr_read_b32 v171, a203\n"
"v_mul_f32 v168, v168, v184\n"
"v_mul_f32 v169, v169, v184\n"
"v_mul_f32 v170, v170, v184\n"
"v_mul_f32 v171, v171, v184\n"
"v_accvgpr_write_b32 a200, v168\n"
"v_accvgpr_write_b32 a201, v169\n"
"v_accvgpr_write_b32 a202, v170\n"
"v_accvgpr_write_b32 a203, v171\n"
"v_accvgpr_read_b32 v172, a204\n"
"v_accvgpr_read_b32 v173, a205\n"
"v_accvgpr_read_b32 v174, a206\n"
"v_accvgpr_read_b32 v175, a207\n"
"v_mul_f32 v172, v172, v188\n"
"v_mul_f32 v173, v173, v188\n"
"v_mul_f32 v174, v174, v188\n"
"v_mul_f32 v175, v175, v188\n"
"v_accvgpr_write_b32 a204, v172\n"
"v_accvgpr_write_b32 a205, v173\n"
"v_accvgpr_write_b32 a206, v174\n"
"v_accvgpr_write_b32 a207, v175\n"
"v_accvgpr_read_b32 v160, a208\n"
"v_accvgpr_read_b32 v161, a209\n"
"v_accvgpr_read_b32 v162, a210\n"
"v_accvgpr_read_b32 v163, a211\n"
"v_mul_f32 v160, v160, v176\n"
"v_mul_f32 v161, v161, v176\n"
"v_mul_f32 v162, v162, v176\n"
"v_mul_f32 v163, v163, v176\n"
"v_accvgpr_write_b32 a208, v160\n"
"v_accvgpr_write_b32 a209, v161\n"
"v_accvgpr_write_b32 a210, v162\n"
"v_accvgpr_write_b32 a211, v163\n"
"v_accvgpr_read_b32 v164, a212\n"
"v_accvgpr_read_b32 v165, a213\n"
"v_accvgpr_read_b32 v166, a214\n"
"v_accvgpr_read_b32 v167, a215\n"
"v_mul_f32 v164, v164, v180\n"
"v_mul_f32 v165, v165, v180\n"
"v_mul_f32 v166, v166, v180\n"
"v_mul_f32 v167, v167, v180\n"
"v_accvgpr_write_b32 a212, v164\n"
"v_accvgpr_write_b32 a213, v165\n"
"v_accvgpr_write_b32 a214, v166\n"
"v_accvgpr_write_b32 a215, v167\n"
"v_accvgpr_read_b32 v168, a216\n"
"v_accvgpr_read_b32 v169, a217\n"
"v_accvgpr_read_b32 v170, a218\n"
"v_accvgpr_read_b32 v171, a219\n"
"v_mul_f32 v168, v168, v184\n"
"v_mul_f32 v169, v169, v184\n"
"v_mul_f32 v170, v170, v184\n"
"v_mul_f32 v171, v171, v184\n"
"v_accvgpr_write_b32 a216, v168\n"
"v_accvgpr_write_b32 a217, v169\n"
"v_accvgpr_write_b32 a218, v170\n"
"v_accvgpr_write_b32 a219, v171\n"
"v_accvgpr_read_b32 v172, a220\n"
"v_accvgpr_read_b32 v173, a221\n"
"v_accvgpr_read_b32 v174, a222\n"
"v_accvgpr_read_b32 v175, a223\n"
"v_mul_f32 v172, v172, v188\n"
"v_mul_f32 v173, v173, v188\n"
"v_mul_f32 v174, v174, v188\n"
"v_mul_f32 v175, v175, v188\n"
"v_accvgpr_write_b32 a220, v172\n"
"v_accvgpr_write_b32 a221, v173\n"
"v_accvgpr_write_b32 a222, v174\n"
"v_accvgpr_write_b32 a223, v175\n"
"v_accvgpr_read_b32 v160, a224\n"
"v_accvgpr_read_b32 v161, a225\n"
"v_accvgpr_read_b32 v162, a226\n"
"v_accvgpr_read_b32 v163, a227\n"
"v_mul_f32 v160, v160, v176\n"
"v_mul_f32 v161, v161, v176\n"
"v_mul_f32 v162, v162, v176\n"
"v_mul_f32 v163, v163, v176\n"
"v_accvgpr_write_b32 a224, v160\n"
"v_accvgpr_write_b32 a225, v161\n"
"v_accvgpr_write_b32 a226, v162\n"
"v_accvgpr_write_b32 a227, v163\n"
"v_accvgpr_read_b32 v164, a228\n"
"v_accvgpr_read_b32 v165, a229\n"
"v_accvgpr_read_b32 v166, a230\n"
"v_accvgpr_read_b32 v167, a231\n"
"v_mul_f32 v164, v164, v180\n"
"v_mul_f32 v165, v165, v180\n"
"v_mul_f32 v166, v166, v180\n"
"v_mul_f32 v167, v167, v180\n"
"v_accvgpr_write_b32 a228, v164\n"
"v_accvgpr_write_b32 a229, v165\n"
"v_accvgpr_write_b32 a230, v166\n"
"v_accvgpr_write_b32 a231, v167\n"
"v_accvgpr_read_b32 v168, a232\n"
"v_accvgpr_read_b32 v169, a233\n"
"v_accvgpr_read_b32 v170, a234\n"
"v_accvgpr_read_b32 v171, a235\n"
"v_mul_f32 v168, v168, v184\n"
"v_mul_f32 v169, v169, v184\n"
"v_mul_f32 v170, v170, v184\n"
"v_mul_f32 v171, v171, v184\n"
"v_accvgpr_write_b32 a232, v168\n"
"v_accvgpr_write_b32 a233, v169\n"
"v_accvgpr_write_b32 a234, v170\n"
"v_accvgpr_write_b32 a235, v171\n"
"v_accvgpr_read_b32 v172, a236\n"
"v_accvgpr_read_b32 v173, a237\n"
"v_accvgpr_read_b32 v174, a238\n"
"v_accvgpr_read_b32 v175, a239\n"
"v_mul_f32 v172, v172, v188\n"
"v_mul_f32 v173, v173, v188\n"
"v_mul_f32 v174, v174, v188\n"
"v_mul_f32 v175, v175, v188\n"
"v_accvgpr_write_b32 a236, v172\n"
"v_accvgpr_write_b32 a237, v173\n"
"v_accvgpr_write_b32 a238, v174\n"
"v_accvgpr_write_b32 a239, v175\n"
"v_accvgpr_read_b32 v160, a240\n"
"v_accvgpr_read_b32 v161, a241\n"
"v_accvgpr_read_b32 v162, a242\n"
"v_accvgpr_read_b32 v163, a243\n"
"v_mul_f32 v160, v160, v176\n"
"v_mul_f32 v161, v161, v176\n"
"v_mul_f32 v162, v162, v176\n"
"v_mul_f32 v163, v163, v176\n"
"v_accvgpr_write_b32 a240, v160\n"
"v_accvgpr_write_b32 a241, v161\n"
"v_accvgpr_write_b32 a242, v162\n"
"v_accvgpr_write_b32 a243, v163\n"
"v_accvgpr_read_b32 v164, a244\n"
"v_accvgpr_read_b32 v165, a245\n"
"v_accvgpr_read_b32 v166, a246\n"
"v_accvgpr_read_b32 v167, a247\n"
"v_mul_f32 v164, v164, v180\n"
"v_mul_f32 v165, v165, v180\n"
"v_mul_f32 v166, v166, v180\n"
"v_mul_f32 v167, v167, v180\n"
"v_accvgpr_write_b32 a244, v164\n"
"v_accvgpr_write_b32 a245, v165\n"
"v_accvgpr_write_b32 a246, v166\n"
"v_accvgpr_write_b32 a247, v167\n"
"v_accvgpr_read_b32 v168, a248\n"
"v_accvgpr_read_b32 v169, a249\n"
"v_accvgpr_read_b32 v170, a250\n"
"v_accvgpr_read_b32 v171, a251\n"
"v_mul_f32 v168, v168, v184\n"
"v_mul_f32 v169, v169, v184\n"
"v_mul_f32 v170, v170, v184\n"
"v_mul_f32 v171, v171, v184\n"
"v_accvgpr_write_b32 a248, v168\n"
"v_accvgpr_write_b32 a249, v169\n"
"v_accvgpr_write_b32 a250, v170\n"
"v_accvgpr_write_b32 a251, v171\n"
"v_accvgpr_read_b32 v172, a252\n"
"v_accvgpr_read_b32 v173, a253\n"
"v_accvgpr_read_b32 v174, a254\n"
"v_accvgpr_read_b32 v175, a255\n"
"v_mul_f32 v172, v172, v188\n"
"v_mul_f32 v173, v173, v188\n"
"v_mul_f32 v174, v174, v188\n"
"v_mul_f32 v175, v175, v188\n"
"v_accvgpr_write_b32 a252, v172\n"
"v_accvgpr_write_b32 a253, v173\n"
"v_accvgpr_write_b32 a254, v174\n"
"v_accvgpr_write_b32 a255, v175\n"
"s_nop 3\n"
"s_setpc_b64 s[80:81]\n"
"L_rareC1_%=:\n"
"s_nop 15\n"
"v_accvgpr_read_b32 v96, a0\n"
"v_accvgpr_read_b32 v97, a1\n"
"v_accvgpr_read_b32 v98, a2\n"
"v_accvgpr_read_b32 v99, a3\n"
"v_mul_f32 v96, v96, v112\n"
"v_mul_f32 v97, v97, v112\n"
"v_mul_f32 v98, v98, v112\n"
"v_mul_f32 v99, v99, v112\n"
"v_accvgpr_write_b32 a0, v96\n"
"v_accvgpr_write_b32 a1, v97\n"
"v_accvgpr_write_b32 a2, v98\n"
"v_accvgpr_write_b32 a3, v99\n"
"v_accvgpr_read_b32 v100, a4\n"
"v_accvgpr_read_b32 v101, a5\n"
"v_accvgpr_read_b32 v102, a6\n"
"v_accvgpr_read_b32 v103, a7\n"
"v_mul_f32 v100, v100, v116\n"
"v_mul_f32 v101, v101, v116\n"
"v_mul_f32 v102, v102, v116\n"
"v_mul_f32 v103, v103, v116\n"
"v_accvgpr_write_b32 a4, v100\n"
"v_accvgpr_write_b32 a5, v101\n"
"v_accvgpr_write_b32 a6, v102\n"
"v_accvgpr_write_b32 a7, v103\n"
"v_accvgpr_read_b32 v104, a8\n"
"v_accvgpr_read_b32 v105, a9\n"
"v_accvgpr_read_b32 v106, a10\n"
"v_accvgpr_read_b32 v107, a11\n"
"v_mul_f32 v104, v104, v120\n"
"v_mul_f32 v105, v105, v120\n"
"v_mul_f32 v106, v106, v120\n"
"v_mul_f32 v107, v107, v120\n"
"v_accvgpr_write_b32 a8, v104\n"
"v_accvgpr_write_b32 a9, v105\n"
"v_accvgpr_write_b32 a10, v106\n"
"v_accvgpr_write_b32 a11, v107\n"
"v_accvgpr_read_b32 v108, a12\n"
"v_accvgpr_read_b32 v109, a13\n"
"v_accvgpr_read_b32 v110, a14\n"
"v_accvgpr_read_b32 v111, a15\n"
"v_mul_f32 v108, v108, v124\n"
"v_mul_f32 v109, v109, v124\n"
"v_mul_f32 v110, v110, v124\n"
"v_mul_f32 v111, v111, v124\n"
"v_accvgpr_write_b32 a12, v108\n"
"v_accvgpr_write_b32 a13, v109\n"
"v_accvgpr_write_b32 a14, v110\n"
"v_accvgpr_write_b32 a15, v111\n"
"v_accvgpr_read_b32 v96, a16\n"
"v_accvgpr_read_b32 v97, a17\n"
"v_accvgpr_read_b32 v98, a18\n"
"v_accvgpr_read_b32 v99, a19\n"
"v_mul_f32 v96, v96, v112\n"
"v_mul_f32 v97, v97, v112\n"
"v_mul_f32 v98, v98, v112\n"
"v_mul_f32 v99, v99, v112\n"
"v_accvgpr_write_b32 a16, v96\n"
"v_accvgpr_write_b32 a17, v97\n"
"v_accvgpr_write_b32 a18, v98\n"
"v_accvgpr_write_b32 a19, v99\n"
"v_accvgpr_read_b32 v100, a20\n"
"v_accvgpr_read_b32 v101, a21\n"
"v_accvgpr_read_b32 v102, a22\n"
"v_accvgpr_read_b32 v103, a23\n"
"v_mul_f32 v100, v100, v116\n"
"v_mul_f32 v101, v101, v116\n"
"v_mul_f32 v102, v102, v116\n"
"v_mul_f32 v103, v103, v116\n"
"v_accvgpr_write_b32 a20, v100\n"
"v_accvgpr_write_b32 a21, v101\n"
"v_accvgpr_write_b32 a22, v102\n"
"v_accvgpr_write_b32 a23, v103\n"
"v_accvgpr_read_b32 v104, a24\n"
"v_accvgpr_read_b32 v105, a25\n"
"v_accvgpr_read_b32 v106, a26\n"
"v_accvgpr_read_b32 v107, a27\n"
"v_mul_f32 v104, v104, v120\n"
"v_mul_f32 v105, v105, v120\n"
"v_mul_f32 v106, v106, v120\n"
"v_mul_f32 v107, v107, v120\n"
"v_accvgpr_write_b32 a24, v104\n"
"v_accvgpr_write_b32 a25, v105\n"
"v_accvgpr_write_b32 a26, v106\n"
"v_accvgpr_write_b32 a27, v107\n"
"v_accvgpr_read_b32 v108, a28\n"
"v_accvgpr_read_b32 v109, a29\n"
"v_accvgpr_read_b32 v110, a30\n"
"v_accvgpr_read_b32 v111, a31\n"
"v_mul_f32 v108, v108, v124\n"
"v_mul_f32 v109, v109, v124\n"
"v_mul_f32 v110, v110, v124\n"
"v_mul_f32 v111, v111, v124\n"
"v_accvgpr_write_b32 a28, v108\n"
"v_accvgpr_write_b32 a29, v109\n"
"v_accvgpr_write_b32 a30, v110\n"
"v_accvgpr_write_b32 a31, v111\n"
"v_accvgpr_read_b32 v96, a32\n"
"v_accvgpr_read_b32 v97, a33\n"
"v_accvgpr_read_b32 v98, a34\n"
"v_accvgpr_read_b32 v99, a35\n"
"v_mul_f32 v96, v96, v112\n"
"v_mul_f32 v97, v97, v112\n"
"v_mul_f32 v98, v98, v112\n"
"v_mul_f32 v99, v99, v112\n"
"v_accvgpr_write_b32 a32, v96\n"
"v_accvgpr_write_b32 a33, v97\n"
"v_accvgpr_write_b32 a34, v98\n"
"v_accvgpr_write_b32 a35, v99\n"
"v_accvgpr_read_b32 v100, a36\n"
"v_accvgpr_read_b32 v101, a37\n"
"v_accvgpr_read_b32 v102, a38\n"
"v_accvgpr_read_b32 v103, a39\n"
"v_mul_f32 v100, v100, v116\n"
"v_mul_f32 v101, v101, v116\n"
"v_mul_f32 v102, v102, v116\n"
"v_mul_f32 v103, v103, v116\n"
"v_accvgpr_write_b32 a36, v100\n"
"v_accvgpr_write_b32 a37, v101\n"
"v_accvgpr_write_b32 a38, v102\n"
"v_accvgpr_write_b32 a39, v103\n"
"v_accvgpr_read_b32 v104, a40\n"
"v_accvgpr_read_b32 v105, a41\n"
"v_accvgpr_read_b32 v106, a42\n"
"v_accvgpr_read_b32 v107, a43\n"
"v_mul_f32 v104, v104, v120\n"
"v_mul_f32 v105, v105, v120\n"
"v_mul_f32 v106, v106, v120\n"
"v_mul_f32 v107, v107, v120\n"
"v_accvgpr_write_b32 a40, v104\n"
"v_accvgpr_write_b32 a41, v105\n"
"v_accvgpr_write_b32 a42, v106\n"
"v_accvgpr_write_b32 a43, v107\n"
"v_accvgpr_read_b32 v108, a44\n"
"v_accvgpr_read_b32 v109, a45\n"
"v_accvgpr_read_b32 v110, a46\n"
"v_accvgpr_read_b32 v111, a47\n"
"v_mul_f32 v108, v108, v124\n"
"v_mul_f32 v109, v109, v124\n"
"v_mul_f32 v110, v110, v124\n"
"v_mul_f32 v111, v111, v124\n"
"v_accvgpr_write_b32 a44, v108\n"
"v_accvgpr_write_b32 a45, v109\n"
"v_accvgpr_write_b32 a46, v110\n"
"v_accvgpr_write_b32 a47, v111\n"
"v_accvgpr_read_b32 v96, a48\n"
"v_accvgpr_read_b32 v97, a49\n"
"v_accvgpr_read_b32 v98, a50\n"
"v_accvgpr_read_b32 v99, a51\n"
"v_mul_f32 v96, v96, v112\n"
"v_mul_f32 v97, v97, v112\n"
"v_mul_f32 v98, v98, v112\n"
"v_mul_f32 v99, v99, v112\n"
"v_accvgpr_write_b32 a48, v96\n"
"v_accvgpr_write_b32 a49, v97\n"
"v_accvgpr_write_b32 a50, v98\n"
"v_accvgpr_write_b32 a51, v99\n"
"v_accvgpr_read_b32 v100, a52\n"
"v_accvgpr_read_b32 v101, a53\n"
"v_accvgpr_read_b32 v102, a54\n"
"v_accvgpr_read_b32 v103, a55\n"
"v_mul_f32 v100, v100, v116\n"
"v_mul_f32 v101, v101, v116\n"
"v_mul_f32 v102, v102, v116\n"
"v_mul_f32 v103, v103, v116\n"
"v_accvgpr_write_b32 a52, v100\n"
"v_accvgpr_write_b32 a53, v101\n"
"v_accvgpr_write_b32 a54, v102\n"
"v_accvgpr_write_b32 a55, v103\n"
"v_accvgpr_read_b32 v104, a56\n"
"v_accvgpr_read_b32 v105, a57\n"
"v_accvgpr_read_b32 v106, a58\n"
"v_accvgpr_read_b32 v107, a59\n"
"v_mul_f32 v104, v104, v120\n"
"v_mul_f32 v105, v105, v120\n"
"v_mul_f32 v106, v106, v120\n"
"v_mul_f32 v107, v107, v120\n"
"v_accvgpr_write_b32 a56, v104\n"
"v_accvgpr_write_b32 a57, v105\n"
"v_accvgpr_write_b32 a58, v106\n"
"v_accvgpr_write_b32 a59, v107\n"
"v_accvgpr_read_b32 v108, a60\n"
"v_accvgpr_read_b32 v109, a61\n"
"v_accvgpr_read_b32 v110, a62\n"
"v_accvgpr_read_b32 v111, a63\n"
"v_mul_f32 v108, v108, v124\n"
"v_mul_f32 v109, v109, v124\n"
"v_mul_f32 v110, v110, v124\n"
"v_mul_f32 v111, v111, v124\n"
"v_accvgpr_write_b32 a60, v108\n"
"v_accvgpr_write_b32 a61, v109\n"
"v_accvgpr_write_b32 a62, v110\n"
"v_accvgpr_write_b32 a63, v111\n"
"v_accvgpr_read_b32 v96, a64\n"
"v_accvgpr_read_b32 v97, a65\n"
"v_accvgpr_read_b32 v98, a66\n"
"v_accvgpr_read_b32 v99, a67\n"
"v_mul_f32 v96, v96, v112\n"
"v_mul_f32 v97, v97, v112\n"
"v_mul_f32 v98, v98, v112\n"
"v_mul_f32 v99, v99, v112\n"
"v_accvgpr_write_b32 a64, v96\n"
"v_accvgpr_write_b32 a65, v97\n"
"v_accvgpr_write_b32 a66, v98\n"
"v_accvgpr_write_b32 a67, v99\n"
"v_accvgpr_read_b32 v100, a68\n"
"v_accvgpr_read_b32 v101, a69\n"
"v_accvgpr_read_b32 v102, a70\n"
"v_accvgpr_read_b32 v103, a71\n"
"v_mul_f32 v100, v100, v116\n"
"v_mul_f32 v101, v101, v116\n"
"v_mul_f32 v102, v102, v116\n"
"v_mul_f32 v103, v103, v116\n"
"v_accvgpr_write_b32 a68, v100\n"
"v_accvgpr_write_b32 a69, v101\n"
"v_accvgpr_write_b32 a70, v102\n"
"v_accvgpr_write_b32 a71, v103\n"
"v_accvgpr_read_b32 v104, a72\n"
"v_accvgpr_read_b32 v105, a73\n"
"v_accvgpr_read_b32 v106, a74\n"
"v_accvgpr_read_b32 v107, a75\n"
"v_mul_f32 v104, v104, v120\n"
"v_mul_f32 v105, v105, v120\n"
"v_mul_f32 v106, v106, v120\n"
"v_mul_f32 v107, v107, v120\n"
"v_accvgpr_write_b32 a72, v104\n"
"v_accvgpr_write_b32 a73, v105\n"
"v_accvgpr_write_b32 a74, v106\n"
"v_accvgpr_write_b32 a75, v107\n"
"v_accvgpr_read_b32 v108, a76\n"
"v_accvgpr_read_b32 v109, a77\n"
"v_accvgpr_read_b32 v110, a78\n"
"v_accvgpr_read_b32 v111, a79\n"
"v_mul_f32 v108, v108, v124\n"
"v_mul_f32 v109, v109, v124\n"
"v_mul_f32 v110, v110, v124\n"
"v_mul_f32 v111, v111, v124\n"
"v_accvgpr_write_b32 a76, v108\n"
"v_accvgpr_write_b32 a77, v109\n"
"v_accvgpr_write_b32 a78, v110\n"
"v_accvgpr_write_b32 a79, v111\n"
"v_accvgpr_read_b32 v96, a80\n"
"v_accvgpr_read_b32 v97, a81\n"
"v_accvgpr_read_b32 v98, a82\n"
"v_accvgpr_read_b32 v99, a83\n"
"v_mul_f32 v96, v96, v112\n"
"v_mul_f32 v97, v97, v112\n"
"v_mul_f32 v98, v98, v112\n"
"v_mul_f32 v99, v99, v112\n"
"v_accvgpr_write_b32 a80, v96\n"
"v_accvgpr_write_b32 a81, v97\n"
"v_accvgpr_write_b32 a82, v98\n"
"v_accvgpr_write_b32 a83, v99\n"
"v_accvgpr_read_b32 v100, a84\n"
"v_accvgpr_read_b32 v101, a85\n"
"v_accvgpr_read_b32 v102, a86\n"
"v_accvgpr_read_b32 v103, a87\n"
"v_mul_f32 v100, v100, v116\n"
"v_mul_f32 v101, v101, v116\n"
"v_mul_f32 v102, v102, v116\n"
"v_mul_f32 v103, v103, v116\n"
"v_accvgpr_write_b32 a84, v100\n"
"v_accvgpr_write_b32 a85, v101\n"
"v_accvgpr_write_b32 a86, v102\n"
"v_accvgpr_write_b32 a87, v103\n"
"v_accvgpr_read_b32 v104, a88\n"
"v_accvgpr_read_b32 v105, a89\n"
"v_accvgpr_read_b32 v106, a90\n"
"v_accvgpr_read_b32 v107, a91\n"
"v_mul_f32 v104, v104, v120\n"
"v_mul_f32 v105, v105, v120\n"
"v_mul_f32 v106, v106, v120\n"
"v_mul_f32 v107, v107, v120\n"
"v_accvgpr_write_b32 a88, v104\n"
"v_accvgpr_write_b32 a89, v105\n"
"v_accvgpr_write_b32 a90, v106\n"
"v_accvgpr_write_b32 a91, v107\n"
"v_accvgpr_read_b32 v108, a92\n"
"v_accvgpr_read_b32 v109, a93\n"
"v_accvgpr_read_b32 v110, a94\n"
"v_accvgpr_read_b32 v111, a95\n"
"v_mul_f32 v108, v108, v124\n"
"v_mul_f32 v109, v109, v124\n"
"v_mul_f32 v110, v110, v124\n"
"v_mul_f32 v111, v111, v124\n"
"v_accvgpr_write_b32 a92, v108\n"
"v_accvgpr_write_b32 a93, v109\n"
"v_accvgpr_write_b32 a94, v110\n"
"v_accvgpr_write_b32 a95, v111\n"
"v_accvgpr_read_b32 v96, a96\n"
"v_accvgpr_read_b32 v97, a97\n"
"v_accvgpr_read_b32 v98, a98\n"
"v_accvgpr_read_b32 v99, a99\n"
"v_mul_f32 v96, v96, v112\n"
"v_mul_f32 v97, v97, v112\n"
"v_mul_f32 v98, v98, v112\n"
"v_mul_f32 v99, v99, v112\n"
"v_accvgpr_write_b32 a96, v96\n"
"v_accvgpr_write_b32 a97, v97\n"
"v_accvgpr_write_b32 a98, v98\n"
"v_accvgpr_write_b32 a99, v99\n"
"v_accvgpr_read_b32 v100, a100\n"
"v_accvgpr_read_b32 v101, a101\n"
"v_accvgpr_read_b32 v102, a102\n"
"v_accvgpr_read_b32 v103, a103\n"
"v_mul_f32 v100, v100, v116\n"
"v_mul_f32 v101, v101, v116\n"
"v_mul_f32 v102, v102, v116\n"
"v_mul_f32 v103, v103, v116\n"
"v_accvgpr_write_b32 a100, v100\n"
"v_accvgpr_write_b32 a101, v101\n"
"v_accvgpr_write_b32 a102, v102\n"
"v_accvgpr_write_b32 a103, v103\n"
"v_accvgpr_read_b32 v104, a104\n"
"v_accvgpr_read_b32 v105, a105\n"
"v_accvgpr_read_b32 v106, a106\n"
"v_accvgpr_read_b32 v107, a107\n"
"v_mul_f32 v104, v104, v120\n"
"v_mul_f32 v105, v105, v120\n"
"v_mul_f32 v106, v106, v120\n"
"v_mul_f32 v107, v107, v120\n"
"v_accvgpr_write_b32 a104, v104\n"
"v_accvgpr_write_b32 a105, v105\n"
"v_accvgpr_write_b32 a106, v106\n"
"v_accvgpr_write_b32 a107, v107\n"
"v_accvgpr_read_b32 v108, a108\n"
"v_accvgpr_read_b32 v109, a109\n"
"v_accvgpr_read_b32 v110, a110\n"
"v_accvgpr_read_b32 v111, a111\n"
"v_mul_f32 v108, v108, v124\n"
"v_mul_f32 v109, v109, v124\n"
"v_mul_f32 v110, v110, v124\n"
"v_mul_f32 v111, v111, v124\n"
"v_accvgpr_write_b32 a108, v108\n"
"v_accvgpr_write_b32 a109, v109\n"
"v_accvgpr_write_b32 a110, v110\n"
"v_accvgpr_write_b32 a111, v111\n"
"v_accvgpr_read_b32 v96, a112\n"
"v_accvgpr_read_b32 v97, a113\n"
"v_accvgpr_read_b32 v98, a114\n"
"v_accvgpr_read_b32 v99, a115\n"
"v_mul_f32 v96, v96, v112\n"
"v_mul_f32 v97, v97, v112\n"
"v_mul_f32 v98, v98, v112\n"
"v_mul_f32 v99, v99, v112\n"
"v_accvgpr_write_b32 a112, v96\n"
"v_accvgpr_write_b32 a113, v97\n"
"v_accvgpr_write_b32 a114, v98\n"
"v_accvgpr_write_b32 a115, v99\n"
"v_accvgpr_read_b32 v100, a116\n"
"v_accvgpr_read_b32 v101, a117\n"
"v_accvgpr_read_b32 v102, a118\n"
"v_accvgpr_read_b32 v103, a119\n"
"v_mul_f32 v100, v100, v116\n"
"v_mul_f32 v101, v101, v116\n"
"v_mul_f32 v102, v102, v116\n"
"v_mul_f32 v103, v103, v116\n"
"v_accvgpr_write_b32 a116, v100\n"
"v_accvgpr_write_b32 a117, v101\n"
"v_accvgpr_write_b32 a118, v102\n"
"v_accvgpr_write_b32 a119, v103\n"
"v_accvgpr_read_b32 v104, a120\n"
"v_accvgpr_read_b32 v105, a121\n"
"v_accvgpr_read_b32 v106, a122\n"
"v_accvgpr_read_b32 v107, a123\n"
"v_mul_f32 v104, v104, v120\n"
"v_mul_f32 v105, v105, v120\n"
"v_mul_f32 v106, v106, v120\n"
"v_mul_f32 v107, v107, v120\n"
"v_accvgpr_write_b32 a120, v104\n"
"v_accvgpr_write_b32 a121, v105\n"
"v_accvgpr_write_b32 a122, v106\n"
"v_accvgpr_write_b32 a123, v107\n"
"v_accvgpr_read_b32 v108, a124\n"
"v_accvgpr_read_b32 v109, a125\n"
"v_accvgpr_read_b32 v110, a126\n"
"v_accvgpr_read_b32 v111, a127\n"
"v_mul_f32 v108, v108, v124\n"
"v_mul_f32 v109, v109, v124\n"
"v_mul_f32 v110, v110, v124\n"
"v_mul_f32 v111, v111, v124\n"
"v_accvgpr_write_b32 a124, v108\n"
"v_accvgpr_write_b32 a125, v109\n"
"v_accvgpr_write_b32 a126, v110\n"
"v_accvgpr_write_b32 a127, v111\n"
"v_accvgpr_read_b32 v96, a128\n"
"v_accvgpr_read_b32 v97, a129\n"
"v_accvgpr_read_b32 v98, a130\n"
"v_accvgpr_read_b32 v99, a131\n"
"v_mul_f32 v96, v96, v112\n"
"v_mul_f32 v97, v97, v112\n"
"v_mul_f32 v98, v98, v112\n"
"v_mul_f32 v99, v99, v112\n"
"v_accvgpr_write_b32 a128, v96\n"
"v_accvgpr_write_b32 a129, v97\n"
"v_accvgpr_write_b32 a130, v98\n"
"v_accvgpr_write_b32 a131, v99\n"
"v_accvgpr_read_b32 v100, a132\n"
"v_accvgpr_read_b32 v101, a133\n"
"v_accvgpr_read_b32 v102, a134\n"
"v_accvgpr_read_b32 v103, a135\n"
"v_mul_f32 v100, v100, v116\n"
"v_mul_f32 v101, v101, v116\n"
"v_mul_f32 v102, v102, v116\n"
"v_mul_f32 v103, v103, v116\n"
"v_accvgpr_write_b32 a132, v100\n"
"v_accvgpr_write_b32 a133, v101\n"
"v_accvgpr_write_b32 a134, v102\n"
"v_accvgpr_write_b32 a135, v103\n"
"v_accvgpr_read_b32 v104, a136\n"
"v_accvgpr_read_b32 v105, a137\n"
"v_accvgpr_read_b32 v106, a138\n"
"v_accvgpr_read_b32 v107, a139\n"
"v_mul_f32 v104, v104, v120\n"
"v_mul_f32 v105, v105, v120\n"
"v_mul_f32 v106, v106, v120\n"
"v_mul_f32 v107, v107, v120\n"
"v_accvgpr_write_b32 a136, v104\n"
"v_accvgpr_write_b32 a137, v105\n"
"v_accvgpr_write_b32 a138, v106\n"
"v_accvgpr_write_b32 a139, v107\n"
"v_accvgpr_read_b32 v108, a140\n"
"v_accvgpr_read_b32 v109, a141\n"
"v_accvgpr_read_b32 v110, a142\n"
"v_accvgpr_read_b32 v111, a143\n"
"v_mul_f32 v108, v108, v124\n"
"v_mul_f32 v109, v109, v124\n"
"v_mul_f32 v110, v110, v124\n"
"v_mul_f32 v111, v111, v124\n"
"v_accvgpr_write_b32 a140, v108\n"
"v_accvgpr_write_b32 a141, v109\n"
"v_accvgpr_write_b32 a142, v110\n"
"v_accvgpr_write_b32 a143, v111\n"
"v_accvgpr_read_b32 v96, a144\n"
"v_accvgpr_read_b32 v97, a145\n"
"v_accvgpr_read_b32 v98, a146\n"
"v_accvgpr_read_b32 v99, a147\n"
"v_mul_f32 v96, v96, v112\n"
"v_mul_f32 v97, v97, v112\n"
"v_mul_f32 v98, v98, v112\n"
"v_mul_f32 v99, v99, v112\n"
"v_accvgpr_write_b32 a144, v96\n"
"v_accvgpr_write_b32 a145, v97\n"
"v_accvgpr_write_b32 a146, v98\n"
"v_accvgpr_write_b32 a147, v99\n"
"v_accvgpr_read_b32 v100, a148\n"
"v_accvgpr_read_b32 v101, a149\n"
"v_accvgpr_read_b32 v102, a150\n"
"v_accvgpr_read_b32 v103, a151\n"
"v_mul_f32 v100, v100, v116\n"
"v_mul_f32 v101, v101, v116\n"
"v_mul_f32 v102, v102, v116\n"
"v_mul_f32 v103, v103, v116\n"
"v_accvgpr_write_b32 a148, v100\n"
"v_accvgpr_write_b32 a149, v101\n"
"v_accvgpr_write_b32 a150, v102\n"
"v_accvgpr_write_b32 a151, v103\n"
"v_accvgpr_read_b32 v104, a152\n"
"v_accvgpr_read_b32 v105, a153\n"
"v_accvgpr_read_b32 v106, a154\n"
"v_accvgpr_read_b32 v107, a155\n"
"v_mul_f32 v104, v104, v120\n"
"v_mul_f32 v105, v105, v120\n"
"v_mul_f32 v106, v106, v120\n"
"v_mul_f32 v107, v107, v120\n"
"v_accvgpr_write_b32 a152, v104\n"
"v_accvgpr_write_b32 a153, v105\n"
"v_accvgpr_write_b32 a154, v106\n"
"v_accvgpr_write_b32 a155, v107\n"
"v_accvgpr_read_b32 v108, a156\n"
"v_accvgpr_read_b32 v109, a157\n"
"v_accvgpr_read_b32 v110, a158\n"
"v_accvgpr_read_b32 v111, a159\n"
"v_mul_f32 v108, v108, v124\n"
"v_mul_f32 v109, v109, v124\n"
"v_mul_f32 v110, v110, v124\n"
"v_mul_f32 v111, v111, v124\n"
"v_accvgpr_write_b32 a156, v108\n"
"v_accvgpr_write_b32 a157, v109\n"
"v_accvgpr_write_b32 a158, v110\n"
"v_accvgpr_write_b32 a159, v111\n"
"v_accvgpr_read_b32 v96, a160\n"
"v_accvgpr_read_b32 v97, a161\n"
"v_accvgpr_read_b32 v98, a162\n"
"v_accvgpr_read_b32 v99, a163\n"
"v_mul_f32 v96, v96, v112\n"
"v_mul_f32 v97, v97, v112\n"
"v_mul_f32 v98, v98, v112\n"
"v_mul_f32 v99, v99, v112\n"
"v_accvgpr_write_b32 a160, v96\n"
"v_accvgpr_write_b32 a161, v97\n"
"v_accvgpr_write_b32 a162, v98\n"
"v_accvgpr_write_b32 a163, v99\n"
"v_accvgpr_read_b32 v100, a164\n"
"v_accvgpr_read_b32 v101, a165\n"
"v_accvgpr_read_b32 v102, a166\n"
"v_accvgpr_read_b32 v103, a167\n"
"v_mul_f32 v100, v100, v116\n"
"v_mul_f32 v101, v101, v116\n"
"v_mul_f32 v102, v102, v116\n"
"v_mul_f32 v103, v103, v116\n"
"v_accvgpr_write_b32 a164, v100\n"
"v_accvgpr_write_b32 a165, v101\n"
"v_accvgpr_write_b32 a166, v102\n"
"v_accvgpr_write_b32 a167, v103\n"
"v_accvgpr_read_b32 v104, a168\n"
"v_accvgpr_read_b32 v105, a169\n"
"v_accvgpr_read_b32 v106, a170\n"
"v_accvgpr_read_b32 v107, a171\n"
"v_mul_f32 v104, v104, v120\n"
"v_mul_f32 v105, v105, v120\n"
"v_mul_f32 v106, v106, v120\n"
"v_mul_f32 v107, v107, v120\n"
"v_accvgpr_write_b32 a168, v104\n"
"v_accvgpr_write_b32 a169, v105\n"
"v_accvgpr_write_b32 a170, v106\n"
"v_accvgpr_write_b32 a171, v107\n"
"v_accvgpr_read_b32 v108, a172\n"
"v_accvgpr_read_b32 v109, a173\n"
"v_accvgpr_read_b32 v110, a174\n"
"v_accvgpr_read_b32 v111, a175\n"
"v_mul_f32 v108, v108, v124\n"
"v_mul_f32 v109, v109, v124\n"
"v_mul_f32 v110, v110, v124\n"
"v_mul_f32 v111, v111, v124\n"
"v_accvgpr_write_b32 a172, v108\n"
"v_accvgpr_write_b32 a173, v109\n"
"v_accvgpr_write_b32 a174, v110\n"
"v_accvgpr_write_b32 a175, v111\n"
"v_accvgpr_read_b32 v96, a176\n"
"v_accvgpr_read_b32 v97, a177\n"
"v_accvgpr_read_b32 v98, a178\n"
"v_accvgpr_read_b32 v99, a179\n"
"v_mul_f32 v96, v96, v112\n"
"v_mul_f32 v97, v97, v112\n"
"v_mul_f32 v98, v98, v112\n"
"v_mul_f32 v99, v99, v112\n"
"v_accvgpr_write_b32 a176, v96\n"
"v_accvgpr_write_b32 a177, v97\n"
"v_accvgpr_write_b32 a178, v98\n"
"v_accvgpr_write_b32 a179, v99\n"
"v_accvgpr_read_b32 v100, a180\n"
"v_accvgpr_read_b32 v101, a181\n"
"v_accvgpr_read_b32 v102, a182\n"
"v_accvgpr_read_b32 v103, a183\n"
"v_mul_f32 v100, v100, v116\n"
"v_mul_f32 v101, v101, v116\n"
"v_mul_f32 v102, v102, v116\n"
"v_mul_f32 v103, v103, v116\n"
"v_accvgpr_write_b32 a180, v100\n"
"v_accvgpr_write_b32 a181, v101\n"
"v_accvgpr_write_b32 a182, v102\n"
"v_accvgpr_write_b32 a183, v103\n"
"v_accvgpr_read_b32 v104, a184\n"
"v_accvgpr_read_b32 v105, a185\n"
"v_accvgpr_read_b32 v106, a186\n"
"v_accvgpr_read_b32 v107, a187\n"
"v_mul_f32 v104, v104, v120\n"
"v_mul_f32 v105, v105, v120\n"
"v_mul_f32 v106, v106, v120\n"
"v_mul_f32 v107, v107, v120\n"
"v_accvgpr_write_b32 a184, v104\n"
"v_accvgpr_write_b32 a185, v105\n"
"v_accvgpr_write_b32 a186, v106\n"
"v_accvgpr_write_b32 a187, v107\n"
"v_accvgpr_read_b32 v108, a188\n"
"v_accvgpr_read_b32 v109, a189\n"
"v_accvgpr_read_b32 v110, a190\n"
"v_accvgpr_read_b32 v111, a191\n"
"v_mul_f32 v108, v108, v124\n"
"v_mul_f32 v109, v109, v124\n"
"v_mul_f32 v110, v110, v124\n"
"v_mul_f32 v111, v111, v124\n"
"v_accvgpr_write_b32 a188, v108\n"
"v_accvgpr_write_b32 a189, v109\n"
"v_accvgpr_write_b32 a190, v110\n"
"v_accvgpr_write_b32 a191, v111\n"
"v_accvgpr_read_b32 v96, a192\n"
"v_accvgpr_read_b32 v97, a193\n"
"v_accvgpr_read_b32 v98, a194\n"
"v_accvgpr_read_b32 v99, a195\n"
"v_mul_f32 v96, v96, v112\n"
"v_mul_f32 v97, v97, v112\n"
"v_mul_f32 v98, v98, v112\n"
"v_mul_f32 v99, v99, v112\n"
"v_accvgpr_write_b32 a192, v96\n"
"v_accvgpr_write_b32 a193, v97\n"
"v_accvgpr_write_b32 a194, v98\n"
"v_accvgpr_write_b32 a195, v99\n"
"v_accvgpr_read_b32 v100, a196\n"
"v_accvgpr_read_b32 v101, a197\n"
"v_accvgpr_read_b32 v102, a198\n"
"v_accvgpr_read_b32 v103, a199\n"
"v_mul_f32 v100, v100, v116\n"
"v_mul_f32 v101, v101, v116\n"
"v_mul_f32 v102, v102, v116\n"
"v_mul_f32 v103, v103, v116\n"
"v_accvgpr_write_b32 a196, v100\n"
"v_accvgpr_write_b32 a197, v101\n"
"v_accvgpr_write_b32 a198, v102\n"
"v_accvgpr_write_b32 a199, v103\n"
"v_accvgpr_read_b32 v104, a200\n"
"v_accvgpr_read_b32 v105, a201\n"
"v_accvgpr_read_b32 v106, a202\n"
"v_accvgpr_read_b32 v107, a203\n"
"v_mul_f32 v104, v104, v120\n"
"v_mul_f32 v105, v105, v120\n"
"v_mul_f32 v106, v106, v120\n"
"v_mul_f32 v107, v107, v120\n"
"v_accvgpr_write_b32 a200, v104\n"
"v_accvgpr_write_b32 a201, v105\n"
"v_accvgpr_write_b32 a202, v106\n"
"v_accvgpr_write_b32 a203, v107\n"
"v_accvgpr_read_b32 v108, a204\n"
"v_accvgpr_read_b32 v109, a205\n"
"v_accvgpr_read_b32 v110, a206\n"
"v_accvgpr_read_b32 v111, a207\n"
"v_mul_f32 v108, v108, v124\n"
"v_mul_f32 v109, v109, v124\n"
"v_mul_f32 v110, v110, v124\n"
"v_mul_f32 v111, v111, v124\n"
"v_accvgpr_write_b32 a204, v108\n"
"v_accvgpr_write_b32 a205, v109\n"
"v_accvgpr_write_b32 a206, v110\n"
"v_accvgpr_write_b32 a207, v111\n"
"v_accvgpr_read_b32 v96, a208\n"
"v_accvgpr_read_b32 v97, a209\n"
"v_accvgpr_read_b32 v98, a210\n"
"v_accvgpr_read_b32 v99, a211\n"
"v_mul_f32 v96, v96, v112\n"
"v_mul_f32 v97, v97, v112\n"
"v_mul_f32 v98, v98, v112\n"
"v_mul_f32 v99, v99, v112\n"
"v_accvgpr_write_b32 a208, v96\n"
"v_accvgpr_write_b32 a209, v97\n"
"v_accvgpr_write_b32 a210, v98\n"
"v_accvgpr_write_b32 a211, v99\n"
"v_accvgpr_read_b32 v100, a212\n"
"v_accvgpr_read_b32 v101, a213\n"
"v_accvgpr_read_b32 v102, a214\n"
"v_accvgpr_read_b32 v103, a215\n"
"v_mul_f32 v100, v100, v116\n"
"v_mul_f32 v101, v101, v116\n"
"v_mul_f32 v102, v102, v116\n"
"v_mul_f32 v103, v103, v116\n"
"v_accvgpr_write_b32 a212, v100\n"
"v_accvgpr_write_b32 a213, v101\n"
"v_accvgpr_write_b32 a214, v102\n"
"v_accvgpr_write_b32 a215, v103\n"
"v_accvgpr_read_b32 v104, a216\n"
"v_accvgpr_read_b32 v105, a217\n"
"v_accvgpr_read_b32 v106, a218\n"
"v_accvgpr_read_b32 v107, a219\n"
"v_mul_f32 v104, v104, v120\n"
"v_mul_f32 v105, v105, v120\n"
"v_mul_f32 v106, v106, v120\n"
"v_mul_f32 v107, v107, v120\n"
"v_accvgpr_write_b32 a216, v104\n"
"v_accvgpr_write_b32 a217, v105\n"
"v_accvgpr_write_b32 a218, v106\n"
"v_accvgpr_write_b32 a219, v107\n"
"v_accvgpr_read_b32 v108, a220\n"
"v_accvgpr_read_b32 v109, a221\n"
"v_accvgpr_read_b32 v110, a222\n"
"v_accvgpr_read_b32 v111, a223\n"
"v_mul_f32 v108, v108, v124\n"
"v_mul_f32 v109, v109, v124\n"
"v_mul_f32 v110, v110, v124\n"
"v_mul_f32 v111, v111, v124\n"
"v_accvgpr_write_b32 a220, v108\n"
"v_accvgpr_write_b32 a221, v109\n"
"v_accvgpr_write_b32 a222, v110\n"
"v_accvgpr_write_b32 a223, v111\n"
"v_accvgpr_read_b32 v96, a224\n"
"v_accvgpr_read_b32 v97, a225\n"
"v_accvgpr_read_b32 v98, a226\n"
"v_accvgpr_read_b32 v99, a227\n"
"v_mul_f32 v96, v96, v112\n"
"v_mul_f32 v97, v97, v112\n"
"v_mul_f32 v98, v98, v112\n"
"v_mul_f32 v99, v99, v112\n"
"v_accvgpr_write_b32 a224, v96\n"
"v_accvgpr_write_b32 a225, v97\n"
"v_accvgpr_write_b32 a226, v98\n"
"v_accvgpr_write_b32 a227, v99\n"
"v_accvgpr_read_b32 v100, a228\n"
"v_accvgpr_read_b32 v101, a229\n"
"v_accvgpr_read_b32 v102, a230\n"
"v_accvgpr_read_b32 v103, a231\n"
"v_mul_f32 v100, v100, v116\n"
"v_mul_f32 v101, v101, v116\n"
"v_mul_f32 v102, v102, v116\n"
"v_mul_f32 v103, v103, v116\n"
"v_accvgpr_write_b32 a228, v100\n"
"v_accvgpr_write_b32 a229, v101\n"
"v_accvgpr_write_b32 a230, v102\n"
"v_accvgpr_write_b32 a231, v103\n"
"v_accvgpr_read_b32 v104, a232\n"
"v_accvgpr_read_b32 v105, a233\n"
"v_accvgpr_read_b32 v106, a234\n"
"v_accvgpr_read_b32 v107, a235\n"
"v_mul_f32 v104, v104, v120\n"
"v_mul_f32 v105, v105, v120\n"
"v_mul_f32 v106, v106, v120\n"
"v_mul_f32 v107, v107, v120\n"
"v_accvgpr_write_b32 a232, v104\n"
"v_accvgpr_write_b32 a233, v105\n"
"v_accvgpr_write_b32 a234, v106\n"
"v_accvgpr_write_b32 a235, v107\n"
"v_accvgpr_read_b32 v108, a236\n"
"v_accvgpr_read_b32 v109, a237\n"
"v_accvgpr_read_b32 v110, a238\n"
"v_accvgpr_read_b32 v111, a239\n"
"v_mul_f32 v108, v108, v124\n"
"v_mul_f32 v109, v109, v124\n"
"v_mul_f32 v110, v110, v124\n"
"v_mul_f32 v111, v111, v124\n"
"v_accvgpr_write_b32 a236, v108\n"
"v_accvgpr_write_b32 a237, v109\n"
"v_accvgpr_write_b32 a238, v110\n"
"v_accvgpr_write_b32 a239, v111\n"
"v_accvgpr_read_b32 v96, a240\n"
"v_accvgpr_read_b32 v97, a241\n"
"v_accvgpr_read_b32 v98, a242\n"
"v_accvgpr_read_b32 v99, a243\n"
"v_mul_f32 v96, v96, v112\n"
"v_mul_f32 v97, v97, v112\n"
"v_mul_f32 v98, v98, v112\n"
"v_mul_f32 v99, v99, v112\n"
"v_accvgpr_write_b32 a240, v96\n"
"v_accvgpr_write_b32 a241, v97\n"
"v_accvgpr_write_b32 a242, v98\n"
"v_accvgpr_write_b32 a243, v99\n"
"v_accvgpr_read_b32 v100, a244\n"
"v_accvgpr_read_b32 v101, a245\n"
"v_accvgpr_read_b32 v102, a246\n"
"v_accvgpr_read_b32 v103, a247\n"
"v_mul_f32 v100, v100, v116\n"
"v_mul_f32 v101, v101, v116\n"
"v_mul_f32 v102, v102, v116\n"
"v_mul_f32 v103, v103, v116\n"
"v_accvgpr_write_b32 a244, v100\n"
"v_accvgpr_write_b32 a245, v101\n"
"v_accvgpr_write_b32 a246, v102\n"
"v_accvgpr_write_b32 a247, v103\n"
"v_accvgpr_read_b32 v104, a248\n"
"v_accvgpr_read_b32 v105, a249\n"
"v_accvgpr_read_b32 v106, a250\n"
"v_accvgpr_read_b32 v107, a251\n"
"v_mul_f32 v104, v104, v120\n"
"v_mul_f32 v105, v105, v120\n"
"v_mul_f32 v106, v106, v120\n"
"v_mul_f32 v107, v107, v120\n"
"v_accvgpr_write_b32 a248, v104\n"
"v_accvgpr_write_b32 a249, v105\n"
"v_accvgpr_write_b32 a250, v106\n"
"v_accvgpr_write_b32 a251, v107\n"
"v_accvgpr_read_b32 v108, a252\n"
"v_accvgpr_read_b32 v109, a253\n"
"v_accvgpr_read_b32 v110, a254\n"
"v_accvgpr_read_b32 v111, a255\n"
"v_mul_f32 v108, v108, v124\n"
"v_mul_f32 v109, v109, v124\n"
"v_mul_f32 v110, v110, v124\n"
"v_mul_f32 v111, v111, v124\n"
"v_accvgpr_write_b32 a252, v108\n"
"v_accvgpr_write_b32 a253, v109\n"
"v_accvgpr_write_b32 a254, v110\n"
"v_accvgpr_write_b32 a255, v111\n"
"s_nop 3\n"
"s_setpc_b64 s[80:81]\n"
"L_fin_%=:\n"
"s_waitcnt vmcnt(0) lgkmcnt(0)\n"
      :
      : "s"(ka + br * 40), "s"(ka), "s"(b * (128 * 896)), "s"((b * 8 + gk) << 18), "s"(gk * (512 * 896)), "s"(b * (128 * 1792) + br * 896), "v"(toff), "s"(pk)
      : "v1","v2","v3","v4","v5","v6","v7","v8","v9","v10","v11","v12","v13","v14","v15","v16","v17","v18","v19","v20","v21","v22","v23","v24","v25","v26","v27","v28","v29","v30","v31","v32","v33","v34","v35","v36","v37","v38","v39","v40","v41","v42","v43","v44","v45","v46","v47","v48","v49","v50","v51","v52","v53","v54","v55","v56","v57","v58","v59","v60","v61","v62","v63","v64","v65","v66","v67","v68","v69","v70","v71","v72","v73","v74","v75","v76","v77","v78","v79","v80","v81","v82","v83","v84","v85","v86","v87","v88","v89","v90","v91","v92","v93","v94","v95","v96","v97","v98","v99","v100","v101","v102","v103","v104","v105","v106","v107","v108","v109","v110","v111","v112","v113","v114","v115","v116","v117","v118","v119","v120","v121","v122","v123","v124","v125","v126","v127","v128","v129","v130","v131","v132","v133","v134","v135","v136","v137","v138","v139","v140","v141","v142","v143","v144","v145","v146","v147","v148","v149","v150","v151","v152","v153","v154","v155","v156","v157","v158","v159","v160","v161","v162","v163","v164","v165","v166","v167","v168","v169","v170","v171","v172","v173","v174","v175","v176","v177","v178","v179","v180","v181","v182","v183","v184","v185","v186","v187","v188","v189","v190","v191","v192","v193","v194","v195","v196","v197","v198","v199","v200","v201","v202","v203","v204","v205","v206","v207","v208","v209","v210","v211","v212","v213","v214","v215","v216","v217","v218","v219","v220","v221","v222","v223","v224","v225","v226","v227","v228","v229","v230","v231","v232","v233","v234","v235","v236","v237","v238","v239","v240","v241","v242","v243","v244","v245","v246","v247","v248","v249","v250","v251","v252","v253","v254","v255","a0","a1","a2","a3","a4","a5","a6","a7","a8","a9","a10","a11","a12","a13","a14","a15","a16","a17","a18","a19","a20","a21","a22","a23","a24","a25","a26","a27","a28","a29","a30","a31","a32","a33","a34","a35","a36","a37","a38","a39","a40","a41","a42","a43","a44","a45","a46","a47","a48","a49","a50","a51","a52","a53","a54","a55","a56","a57","a58","a59","a60","a61","a62","a63","a64","a65","a66","a67","a68","a69","a70","a71","a72","a73","a74","a75","a76","a77","a78","a79","a80","a81","a82","a83","a84","a85","a86","a87","a88","a89","a90","a91","a92","a93","a94","a95","a96","a97","a98","a99","a100","a101","a102","a103","a104","a105","a106","a107","a108","a109","a110","a111","a112","a113","a114","a115","a116","a117","a118","a119","a120","a121","a122","a123","a124","a125","a126","a127","a128","a129","a130","a131","a132","a133","a134","a135","a136","a137","a138","a139","a140","a141","a142","a143","a144","a145","a146","a147","a148","a149","a150","a151","a152","a153","a154","a155","a156","a157","a158","a159","a160","a161","a162","a163","a164","a165","a166","a167","a168","a169","a170","a171","a172","a173","a174","a175","a176","a177","a178","a179","a180","a181","a182","a183","a184","a185","a186","a187","a188","a189","a190","a191","a192","a193","a194","a195","a196","a197","a198","a199","a200","a201","a202","a203","a204","a205","a206","a207","a208","a209","a210","a211","a212","a213","a214","a215","a216","a217","a218","a219","a220","a221","a222","a223","a224","a225","a226","a227","a228","a229","a230","a231","a232","a233","a234","a235","a236","a237","a238","a239","a240","a241","a242","a243","a244","a245","a246","a247","a248","a249","a250","a251","a252","a253","a254","a255","s4","s5","s6","s7","s8","s9","s10","s11","s12","s13","s14","s15","s16","s17","s18","s19","s28","s29","s30","s31","s32","s33","s34","s35","s36","s37","s38","s39","s40","s41","s42","s43","s44","s45","s46","s47","s48","s49","s50","s51","s52","s53","s54","s55","s56","s57","s58","s59","s60","s61","s62","s63","s64","s65","s66","s67","s68","s69","s70","s71","s72","s73","s74","s75","s76","s77","s78","s79","s80","s81","s82","s83","s84","s85","s86","s87","s88","s89","s90","s91","s92","s93","s94","s95","s96","s97","s98","s99","s100","s101","vcc","scc","memory");
}

extern "C" void kernel_launch(void* const* d_in, const int* in_sizes, int n_in,
                              void* d_out, int out_size, void* d_ws, size_t ws_size,
                              hipStream_t stream) {
  fused_gqa_kernel<<<256, 256, 0, stream>>>(
      (const char*)d_in[0], (const char*)d_in[1], (const char*)d_in[2], (const char*)d_in[3], (const char*)d_in[4],
      (const char*)d_in[5], (const char*)d_in[6], (const char*)d_in[7], (const char*)d_in[8], (const char*)d_in[9],
      (char*)d_out, (char*)d_ws);
}
```
